```python
import torch

def _softmax(x, axis=-1):
    return torch.softmax(x, dim=axis)
def _gelu(x):
    return torch.nn.functional.gelu(x, approximate="tanh")
def _silu(x):
    return torch.nn.functional.silu(x)
def _sigmoid(x):
    return torch.sigmoid(x)
def _layernorm(x, axis=-1, eps=1e-5):
    m = x.mean(dim=axis, keepdim=True)
    v = x.var(dim=axis, keepdim=True, unbiased=False)
    return (x - m) / torch.sqrt(v + eps)
def _scatter_add(tgt, idx, src, axis=0):
    return tgt.clone().index_add_(axis, idx, src)
def _segment_sum(src, seg, n):
    out = torch.zeros((n,) + src.shape[1:], dtype=src.dtype)
    return out.index_add_(0, seg, src)
def _pad(x, pw):
    flat = tuple(v for pair in reversed(pw) for v in pair)
    return torch.nn.functional.pad(x, flat)

kernel_name = 'standalone__self_sup_conv_stem_wavlm__speech'

def setup_inputs():

    _w = (lambda a: (lambda t: t.to(torch.bfloat16) if t.is_floating_point() else t)(torch.as_tensor(a)))
    B, T, D, K = 2, 16000, 512, 31
    scale = (K * D) ** -0.5
    return {
        "x": _w((lambda *s: torch.randn(*s, dtype=torch.bfloat16) * 0.05)(B, T, D)),
        "w": _w((lambda *s: torch.randn(*s, dtype=torch.bfloat16) * 0.05)(K, D, D) * scale),
        "gn_gamma": _w((lambda *s: torch.randn(*s, dtype=torch.bfloat16) * 0.05)(D) * 0.1 + 1.0),
        "gn_beta": _w((lambda *s: torch.randn(*s, dtype=torch.bfloat16) * 0.05)(D) * 0.1),
    }

def reference(x, w, gn_gamma, gn_beta):
    B, T, D = x.shape
    K = w.shape[0]
    s = 2
    L = (T - K) // s + 1
    h = torch.einsum('blc,cd->bld', x[:, 0:s * L:s, :], w[0])
    for i in range(1, K):
        h = h + torch.einsum('blc,cd->bld', x[:, i:i + s * L:s, :], w[i])
    mu = torch.einsum('bld->bd', h)[:, None, :] / L
    var = torch.einsum('bld->bd', (h - mu) ** 2)[:, None, :] / L
    h = (h - mu) / torch.sqrt(var + 1e-5)
    h = h * gn_gamma[None, None, :] + gn_beta[None, None, :]
    return _gelu(h)
```

```cpp
#include <hip/hip_runtime.h>
#include <stdint.h>

typedef __attribute__((ext_vector_type(8))) short bf16x8;
typedef __attribute__((ext_vector_type(4))) short s16x4;
typedef __attribute__((ext_vector_type(4))) float f32x4;
typedef __attribute__((ext_vector_type(2))) float f32x2;
typedef __attribute__((ext_vector_type(2))) __bf16 bf16x2v;
typedef __attribute__((ext_vector_type(4))) unsigned int u32x4;
typedef __attribute__((ext_vector_type(2))) unsigned int u32x2;
constexpr int NH = 6, NST = 3;
typedef __attribute__((ext_vector_type(2 * NH))) unsigned int u32xh;
typedef __attribute__((ext_vector_type(8))) _Float16 f16x8;
typedef __attribute__((ext_vector_type(2))) _Float16 f16x2;
typedef __attribute__((ext_vector_type(2))) unsigned short u16x2;
typedef __attribute__((address_space(1))) unsigned long long gu64;
typedef __attribute__((address_space(1))) unsigned gu32;
typedef __attribute__((address_space(3))) void lds_void;
typedef __attribute__((address_space(3))) s16x4 lds_s16x4;

constexpr int T_ = 16000, D_ = 512, L_ = 7985;
constexpr int HOP = 50, NBLK = 160, ROWS = 2 * NBLK;

constexpr unsigned WS_S = 0;
constexpr unsigned SBLK = 4u << 20;
constexpr unsigned sbase(unsigned sl) { return sl * 131072 + (sl >> 4) * 8192; }
constexpr unsigned FLG = 32u * SBLK;
constexpr unsigned WS_GRP = WS_S + FLG + 16 * 8192;
constexpr unsigned WS_D = WS_S + FLG + 20 * 8192;
constexpr unsigned WS_L = WS_D + ROWS * 1024;
constexpr unsigned WS_Y = 40u << 20;
constexpr unsigned WS_STATS = 129u << 20;
constexpr unsigned WS_FA = WS_STATS + (1u << 17);
constexpr unsigned WS_PUB = WS_STATS + (1u << 19);
static_assert(WS_L + D_ * D_ * 2 <= WS_STATS && FLG % (16 * 8192) == 0, "workspace layout");

#define RLX_AGENT __ATOMIC_RELAXED, __HIP_MEMORY_SCOPE_AGENT

constexpr double COS64[17] = {1.0, 0.99518472667219688624, 0.98078528040323044913, 0.95694033573220886494,
    0.92387953251128675613, 0.88192126434835502971, 0.83146961230254523708, 0.77301045336273696081,
    0.70710678118654752440, 0.63439328416364549822, 0.55557023301960222474, 0.47139673682599764856,
    0.38268343236508977173, 0.29028467725446236764, 0.19509032201612826785, 0.09801714032956060199, 0.0};
constexpr double c64(int k) { k &= 63; if (k > 32) k = 64 - k; return k <= 16 ? COS64[k] : -COS64[32 - k]; }
constexpr double s64(int k) { return c64(k + 48); }
constexpr unsigned short bf_rne(double v) {
  const unsigned u = __builtin_bit_cast(unsigned, (float)v);
  return (unsigned short)((u + 0x7fffu + ((u >> 16) & 1u)) >> 16);
}
constexpr double bf_val(unsigned short h) { return (double)__builtin_bit_cast(float, (unsigned)h << 16); }
constexpr unsigned short hilo(double v, int lo) { return lo ? bf_rne(v - bf_val(bf_rne(v))) : bf_rne(v); }
constexpr double R3 = 0.86602540378443864676;
constexpr double dft(int c, int t, double sgn) {
  return (c >> 1) == 0 ? ((c & t) & 1 ? -1.0 : 1.0)
                       : ((c & 1) ? -0.5 * c64((c >> 1) * t) - R3 * sgn * s64((c >> 1) * t) : c64((c >> 1) * t));
}
constexpr double idft(int c, int t) {
  return (c >> 1) == 0 ? ((c & t) & 1 ? -1.0 : 1.0) / 64.0
                       : ((c & 1) ? -s64((c >> 1) * t) / R3 : c64((c >> 1) * t) * (4.0 / 3.0)) / 32.0;
}
constexpr double UP = 1.0 + 0.72134752 / 256.0;
constexpr int kidx(int g, int j) { return j < 4 ? 4 * g + j : 12 + 4 * g + j; }
struct Tables {
  unsigned short F[4][2][2][64][8];
  unsigned short V[2][4][64][8];
  unsigned short G[4][2][2][64][8];
};
constexpr Tables make_tables() {
  Tables tb{};
  for (int l = 0; l < 64; ++l)
    for (int j = 0; j < 8; ++j) {
      const int g = l >> 4, n = l & 15;
      for (int ct = 0; ct < 4; ++ct) {
        for (int ks = 0; ks < 2; ++ks)
          for (int h = 0; h < 2; ++h) tb.F[ct][ks][h][l][j] = hilo(UP * dft(16 * ct + n, 32 * ks + kidx(g, j), -1.0), h);
        for (int p = 0; p < 2; ++p) {
          const int tap = 4 * g + (j & 3);
          tb.V[p][ct][l][j] = (p == 1 && tap == 15) ? (unsigned short)0 : hilo(UP * dft(16 * ct + n, tap, 1.0), j >> 2);
        }
      }
      for (int tt = 0; tt < 4; ++tt)
        for (int ks = 0; ks < 2; ++ks)
          for (int h = 0; h < 2; ++h) {
            const int c = 32 * ks + kidx(g, j);
            tb.G[tt][ks][h][l][j] = hilo(idft(c, 16 * tt + (tt < 3 ? n : n & 1)), h);
          }
    }
  return tb;
}
__device__ const __attribute__((aligned(16))) Tables TAB = make_tables();

__device__ __forceinline__ unsigned pk_bf16(float lo, float hi) {
  return __builtin_bit_cast(unsigned, __builtin_convertvector(f32x2{lo, hi}, bf16x2v));
}
__device__ __forceinline__ float bf_lo(unsigned v) { return __uint_as_float(v << 16); }
__device__ __forceinline__ float bf_hi(unsigned v) { return __uint_as_float(v & 0xffff0000u); }
__device__ __forceinline__ u32x4 pack8(f32x4 a, f32x4 b) {
  return u32x4{pk_bf16(a[0], a[1]), pk_bf16(a[2], a[3]), pk_bf16(b[0], b[1]), pk_bf16(b[2], b[3])};
}
__device__ __forceinline__ unsigned pk_f16(float lo, float hi) {
  return (__builtin_bit_cast(unsigned, __builtin_amdgcn_cvt_pkrtz(lo, hi)) & 0xfff8fff8u);
}
__device__ __forceinline__ u32x4 pack8h(f32x4 a, f32x4 b) {
  return u32x4{pk_f16(a[0], a[1]), pk_f16(a[2], a[3]), pk_f16(b[0], b[1]), pk_f16(b[2], b[3])};
}
__device__ __forceinline__ unsigned add_h(const bf16x8& a, const bf16x8& b, int d) {
  const unsigned x = __builtin_bit_cast(u32x4, a)[d], y = __builtin_bit_cast(u32x4, b)[d];
  unsigned r = __builtin_bit_cast(unsigned, __builtin_bit_cast(f16x2, x) + __builtin_bit_cast(f16x2, y));
  return r;
}
__device__ __forceinline__ unsigned amax(unsigned m, unsigned v) {
  return __builtin_bit_cast(unsigned, __builtin_elementwise_max(__builtin_bit_cast(u16x2, m), __builtin_bit_cast(u16x2, v & 0x7fff7fffu)));
}
struct Rng { f16x2 hi = {0, 0}, lo = {0, 0}; };
__device__ __forceinline__ void widen(Rng& r, unsigned a, unsigned b) {
  const f16x2 x = __builtin_bit_cast(f16x2, a), y = __builtin_bit_cast(f16x2, b);
  r.hi = __builtin_elementwise_maximum(__builtin_elementwise_maximum(r.hi, x), y);
  r.lo = __builtin_elementwise_minimum(__builtin_elementwise_minimum(r.lo, x), y);
}
__device__ __forceinline__ unsigned amax(const Rng& r) { return amax(amax(0, __builtin_bit_cast(unsigned, r.hi)), __builtin_bit_cast(unsigned, r.lo)); }
__device__ __forceinline__ unsigned expo(unsigned m) { return max(m & 0xffffu, m >> 16) >> 7; }
__device__ __forceinline__ unsigned wave_max(unsigned v) {
  unsigned m = 0;
#pragma unroll 1
  for (int l = 0; l < 64; ++l) m = max(m, (unsigned)__builtin_amdgcn_readlane((int)v, l));
  return m;
}
__device__ __forceinline__ void scale8(bf16x8& v, int e) {
  u32x4 u = __builtin_bit_cast(u32x4, v);
#pragma unroll
  for (int d = 0; d < 4; ++d)
    u[d] = __builtin_amdgcn_perm(__float_as_uint(__builtin_amdgcn_ldexpf(bf_hi(u[d]), e)), __float_as_uint(__builtin_amdgcn_ldexpf(bf_lo(u[d]), e)), 0x07060302u);
  v = __builtin_bit_cast(bf16x8, u);
}
__device__ __forceinline__ void restart(unsigned id) {
  asm volatile("s_waitcnt vmcnt(0)\n\ts_barrier\n\ts_branch conv_fft" :: "{s[0:1]}"(__builtin_amdgcn_kernarg_segment_ptr()), "{s2}"(id), "{v0}"(threadIdx.x) : "memory");
}
__device__ __forceinline__ s16x4 tr_read(const char* p) {
  return __builtin_amdgcn_ds_read_tr16_b64_v4i16((lds_s16x4*)p);
}
__device__ __forceinline__ bf16x8 cat(s16x4 a, s16x4 b) {
  const u32x2 x = __builtin_bit_cast(u32x2, a), y = __builtin_bit_cast(u32x2, b);
  return __builtin_bit_cast(bf16x8, u32x4{x[0], x[1], y[0], y[1]});
}
#define MFMA(a, b, c) __builtin_amdgcn_mfma_f32_16x16x32_bf16(a, b, c, 0, 0, 0)
#define MFMH(a, b, c) __builtin_amdgcn_mfma_f32_16x16x32_f16(__builtin_bit_cast(f16x8, a), __builtin_bit_cast(f16x8, b), c, 0, 0, 0)
#define SB0 __builtin_amdgcn_sched_barrier(0)
#define ZERO4 (f32x4{0.f, 0.f, 0.f, 0.f})
#define DMA16(POL, rs, lds, voff, soff) \
  asm volatile("s_mov_b32 m0, %0\n\ts_nop 0\n\tbuffer_load_dwordx4 %1, %2, %3 offen " POL " lds" \
               :: "s"(lds), "v"(voff), "s"(rs), "s"(soff) : "memory")
__device__ __forceinline__ void st16(const u32x4& v, const u32x4& rs, unsigned voff, unsigned soff) {
  asm volatile("buffer_store_dwordx4 %0, %1, %2, %3 offen sc1\n\ts_nop 1" :: "v"(v), "v"(voff), "s"(rs), "s"(soff) : "memory");
}
__device__ __forceinline__ void ld16(bf16x8& v, const void* p) {
  asm volatile("global_load_dwordx4 %0, %1, off" : "=v"(v) : "v"(p) : "memory");
}
template <int N> __device__ __forceinline__ void wait_vm() { asm volatile("s_waitcnt vmcnt(%0)" :: "i"(N) : "memory"); }
__device__ __forceinline__ u32x4 make_rsrc(const void* p, unsigned bytes) {
  const unsigned long long a = (unsigned long long)p;
  return u32x4{(unsigned)a, (unsigned)(a >> 32) & 0xffffu, bytes, 0x00020000u};
}
__device__ __forceinline__ void spin_flags(const char* p) {
  for (unsigned spins = 0; !__all(__hip_atomic_load((const gu32*)p, RLX_AGENT) != 0u) && spins < (1u << 21); ++spins)
    __builtin_amdgcn_s_sleep(3);
}

constexpr int GW = 10;
constexpr int NQ1 = GW / 2, NQW = 16 - NQ1;
constexpr int STG = 36 * 1024;
constexpr int RAWB = 67 * 1024, XT1 = RAWB + NQ1 * 4096, SINK = XT1 + RAWB;
constexpr int RAWL = 3 * STG;
constexpr int CORR = RAWL + NQW * 4096, KC0 = 6;
constexpr int GAM = RAWL + 36864, CBUF = RAWL + 40960;
constexpr int IH = 8 - NH;

struct Ctx {
  char* smem;
  char* ws;
  const unsigned short *gamma, *beta;
  u32x4 rs;
  unsigned lds0;
  unsigned voff, voff5, crd;
  unsigned xbase, wbase;
  unsigned pwr, prd, poff;
  unsigned xo;
  unsigned lane4, fvo;
  unsigned id;
  unsigned tag[2];
  int es;
  int wv, vb;
  unsigned long long pc;
  unsigned pfo;
};
__device__ __forceinline__ void code_touch(const Ctx& cx, bool first = false) {
  if ((cx.wv ^ (cx.vb >> 3)) & 3) return;
  asm volatile("s_mov_b32 m0, %0\n\ts_nop 0\n\tglobal_load_lds_dword %1, %2" :: "s"((first ? SINK : RAWL + 32768) + cx.wv * 256), "v"(min(__builtin_amdgcn_mbcnt_hi(~0u, __builtin_amdgcn_mbcnt_lo(~0u, cx.wv * 64u)) * 128u, cx.pfo)), "s"(cx.pc) : "memory");
}
template <int NM> constexpr unsigned pofs(int j) { return NM == 6 ? j * 4096 : (j >> 1) * 16384 + (j & 1) * 4096; }
template <int NM> constexpr bool ahead(int j) { return NM == 6 || j == 0 || j == 3; }
template <int NM> constexpr int xtile(int m) { return NM == 5 ? (m + 1) % 5 : m; }
template <int NM>
__device__ __forceinline__ void piece(const Ctx& cx, int j, int kc, unsigned slot) {
  DMA16("sc1", cx.rs, cx.lds0 + slot + j * 4096, cx.voff, (unsigned)(kc + ahead<NM>(j)) * SBLK + pofs<NM>(j));
}
__device__ __forceinline__ void x_store(const Ctx& cx, const u32x4& v, unsigned kb, unsigned blk) {
  st16(v, cx.rs, cx.xo, kb + (blk + (blk & 192u)) * 128u);
}
template <int NM>
__device__ __forceinline__ void stage_issue(const Ctx& cx, int kc) {
  const unsigned slot = (unsigned)((kc + 1) % 3) * STG;
#pragma unroll
  for (int j = 0; j < 10 - NM; ++j)
    if (kc >= 0 || ahead<NM>(j)) piece<NM>(cx, j, kc, slot);
}
template <int V> struct IC { static constexpr int v = V; constexpr operator int() const { return V; } };
template <int A, int B, class F> __device__ __forceinline__ void sfor(F f) {
  if constexpr (A < B) { f(IC<A>{}); sfor<A + 1, B>(f); }
}
struct WF { bf16x8 r[2], i[2]; };
struct XF { bf16x8 r, i; };
struct RQ { u32xh d[2]; unsigned prd; u32x2 raw[NQW]; u32x4 st[5]; f32x4 ca; };
__device__ __forceinline__ unsigned dpp_swap1(unsigned v) {
  return (unsigned)__builtin_amdgcn_update_dpp(0, (int)v, 0xB1, 0xf, 0xf, false);
}
__device__ __forceinline__ f32x4 comb_a(const f32x4 (&p)[3], float c) { return p[0] + (p[1] + p[2]) * (0.5f * c); }
__device__ __forceinline__ f32x4 comb_b(const f32x4 (&p)[3], float c) { return p[1] + p[2] * c; }
__device__ __forceinline__ void y_store(const f32x4 (&a)[2][3], float c, int m, const u32x4& ry, unsigned vo, bool odd, unsigned t0x) {
  const unsigned fl = ((t0x + m) & 2) * 4096;
  const u32x4 re = pack8(comb_a(a[0], c), comb_a(a[1], c)), im = pack8(comb_b(a[0], c), comb_b(a[1], c));
  u32x4 t0, t1;
#pragma unroll
  for (int d = 0; d < 4; ++d) {
    const unsigned sr = dpp_swap1(re[d]), si = dpp_swap1(im[d]);
    t0[d] = odd ? si : re[d];
    t1[d] = odd ? im[d] : sr;
  }
  st16(t0, ry, vo, (unsigned)m * (16 * 65536) + fl);
  st16(t1, ry, vo, (unsigned)m * (16 * 65536) + 65536 + fl);
}
constexpr int NGB = 7;

struct LS { unsigned sa, sp, g, fo, pm, rot, xs, tn, gap; };
template <int NM, int PAR, bool PROD, bool FL, int EX, bool FIRST = false>
__device__ __forceinline__ void k_step(const Ctx& cx, f32x4 (&acc)[5][2][3], const WF& w, WF& wn, XF& x0, XF& x1,
                                       RQ& rq, unsigned& pollv, const bf16x8& vb, LS& ls, bf16x8 (&gb)[16]) {
  constexpr int NP = 10 - NM;
  constexpr bool DMA = FL;
  constexpr bool TR = PROD && PAR == 1, ST = PROD && PAR == 0;
  asm volatile("s_waitcnt vmcnt(%0) lgkmcnt(0)" :: "i"(NP + EX) : "memory");
  if (PAR == 1 && FL && NM == 5) {
    asm volatile("" : "+v"(pollv));
    if (__builtin_expect(!__all(pollv != 0u), 0)) {
      spin_flags(cx.ws + WS_S + ls.fo + cx.fvo);
      pollv = 1u;
    }
  }
  if (FIRST && NM == 5) ls.tn = (unsigned)__builtin_amdgcn_s_memrealtime() + ls.gap + 14u;
  if (PROD && NM == 5 && !FIRST) {
    if (__builtin_expect(ls.gap > 88u, 0)) {
      unsigned now;
      while ((int)((now = (unsigned)__builtin_amdgcn_s_memrealtime()) - ls.tn) < 0) __builtin_amdgcn_s_sleep(1);
      ls.tn = now + ls.gap + PAR * 8u;
      ls.gap -= 14u;
    }
  }
  __builtin_amdgcn_s_barrier();
  SB0;
  s16x4 pv[4], wt[8];
  f32x4 pa[2];
  u32x4 pk, ws[2], xs;
  unsigned xc, wo, sd, gd, dso;
  XF xq[5];
  s16x4 ct[2];
  bf16x8 cb;
  xq[0] = x0;
  xq[1] = x1;
  sfor<0, 5>([&](auto mm) __attribute__((always_inline)) {
    constexpr int m = mm.v;
#pragma unroll
    for (int q = 0; q < 6; ++q) {
      const int s = q & 1, j = q >> 1;
      if (j == 0) acc[m][s][0] = MFMH(w.r[s], xq[m].r, FIRST ? ZERO4 : acc[m][s][0]);
      if (j == 1) acc[m][s][1] = MFMH(w.i[s], xq[m].i, FIRST ? ZERO4 : acc[m][s][1]);
      if (j == 2) acc[m][s][2] = MFMH(ws[s], xs, FIRST ? ZERO4 : acc[m][s][2]);
      SB0;
      if (NM == 5 && m == 2 && q == 5) __builtin_amdgcn_s_setprio(0);
      if (NM == 5 && m == 4 && q == 5 && FL) __builtin_amdgcn_s_setprio(1);
      {
        const int u = q;
        if (u == 1 || u == 2) {
          xs[2 * u - 2] = add_h(xq[m].r, xq[m].i, 2 * u - 2);
          xs[2 * u - 1] = add_h(xq[m].r, xq[m].i, 2 * u - 1);
        }
        if (m == 0 && u < 4) {
          ws[u >> 1][(u & 1) * 2] = add_h(w.r[u >> 1], w.i[u >> 1], (u & 1) * 2);
          ws[u >> 1][(u & 1) * 2 + 1] = add_h(w.r[u >> 1], w.i[u >> 1], (u & 1) * 2 + 1);
        }
        if (m == 0 && u == 0) {
          asm volatile("" : "+s"(ls.sp), "+s"(ls.g), "+s"(ls.fo), "+s"(ls.pm), "+s"(ls.rot));
          sd = cx.lds0 + ls.sp;
          gd = ls.g + (2 + PAR) * SBLK;
        }
        if (m == 0 && u == 3) {
          asm volatile("" : "+s"(ls.sa));
          xc = cx.xbase + ls.sa;
        }
        if (m == 0 && u == 5) {
          asm volatile("" : "+s"(ls.sa));
          wo = cx.wbase + ls.sa;
        }
        constexpr int XU = m < 4 ? 4 : 1;
        if (u == XU || u == XU + 1) {
          const bool nxt = m >= 3;
          const bf16x8 v = *reinterpret_cast<const bf16x8*>(cx.smem + (xc ^ (u != XU ? 64u : 0u)) + xtile<NM>((m + 2) % 5) * 2048);
          XF& d = !nxt ? xq[nxt ? 0 : m + 2] : (m == 3 ? x0 : x1);
          if (u != XU) d.i = v; else d.r = v;
        }
        if (m >= 1 && m < 4 && u < 3 && 3 * m + u < 11) {
          const int idx = 3 * (m - 1) + u;
          const int ri = idx >> 2, ss = (idx >> 1) & 1, hf = idx & 1;
          wt[idx] = tr_read(cx.smem + (wo ^ (unsigned)(ri * 64 + ss * 32)) + hf * 8192);
          if (hf) { if (ri) wn.i[ss] = cat(wt[idx - 1], wt[idx]); else wn.r[ss] = cat(wt[idx - 1], wt[idx]); }
        }
        if (PAR == 0) {
          if (FL && !PROD && m == 0 && u == 0) { if (ls.fo == FLG + 12 * 8192) code_touch(cx); }
          if (FL && m == 0 && u == 0 && NM == 5) {
            if (ls.pm & 1) asm volatile("buffer_load_dword %0, %1, %2, %3 offen sc1" : "+v"(pollv) : "v"(cx.fvo), "s"(cx.rs), "s"(ls.fo) : "memory");
          }
        }
        if (PROD && !FIRST && m == 0 && u == 2) {
          asm volatile("" : "+s"(ls.fo));
          const unsigned e = (ls.fo >> 12) - (FLG >> 12) + PAR - KC0;
          if (e < 16 && cx.wv < 5) {
            DMA16("sc1", cx.rs, NM == 6 ? CORR + PAR * 4096 + cx.wv * 1024 : RAWL + PAR * 1024, cx.voff5, e * (NM == 6 ? 32768 : 64));
          }
        }
        if (PROD && !FIRST && NM == 6 && m == 3) {
          if (u == 2) cb = *reinterpret_cast<const bf16x8*>(cx.smem + cx.lane4 * 4 + RAWL + (1 - PAR) * 1024);
          if (u == 3) {
            ct[0] = tr_read(cx.smem + cx.crd + (1 - PAR) * 4096);
            ct[1] = tr_read(cx.smem + cx.crd + (1 - PAR) * 4096 + 512);
          }
        }
        if (PROD && !FIRST && NM == 6 && m == 4 && u == 3) {
          asm volatile("" : "+s"(ls.fo));
          if ((ls.fo >> 12) - (FLG >> 12) + PAR - KC0 - 1 < 16) rq.ca = MFMA(cat(ct[0], ct[1]), cb, rq.ca);
        }
        if (!PROD && PAR == 0 && NM == 5 && !ahead<NM>(m) && u == 5) { if (ls.fo == FLG + 14 * 8192) piece<NM>(cx, m, 31, RAWL - 16384); }
        if (FIRST && m < NST && u == 4) {
          constexpr int n = 10 - NH - NST + m;
          x_store(cx, rq.st[m % 5], (ls.xs & ~255u) + (n / 5) * (8 * SBLK), (ls.xs & 255u) + n % 5);
        }
        if (FIRST && m < 4 && u < 3 && 3 * m + u < NQW) *reinterpret_cast<u32x2*>(cx.smem + (cx.pwr + (RAWL - RAWB)) + (3 * m + u) * 4096) = rq.raw[(3 * m + u) % NQW];
        if (DMA && m < NP && u == 2) {
          dso = gd + pofs<NM>(m) + ahead<NM>(m) * SBLK;
          asm volatile("s_mov_b32 m0, %1" : "+s"(dso) : "s"(sd + m * 4096));
        }
        if (DMA && m < NP && u == 3)
          asm volatile("buffer_load_dwordx4 %0, %1, %2 offen sc1 lds" :: "v"(cx.voff), "s"(cx.rs), "s"(dso) : "memory");
        if (!DMA && m < 4 && (u == 1 || u == 3 || u == 5) && 3 * m + (u >> 1) < NGB)
          asm volatile("global_load_dwordx4 %0, %1, %2" : "=v"(gb[3 * m + (u >> 1)]) : "v"(cx.lane4 * 4), "s"(&TAB.G[0][0][0][0][0] + (3 * m + (u >> 1)) * 512) : "memory");
        if (!DMA && NM == 5 && m == 3 && u == 1) {
          if (cx.wv == 7) {
            const unsigned co = (unsigned)(((cx.vb >> 3) & 3) * 2 + ((cx.vb >> 2) & 1)) * 128u + (cx.lane4 & 127u);
            asm volatile("s_mov_b32 m0, %0\n\ts_nop 0\n\tglobal_load_lds_dword %1, off"
                         :: "s"(GAM), "v"((const char*)(cx.lane4 < 128 ? cx.gamma : cx.beta) + co) : "memory");
          }
        }
        if (PAR == 1 && FL && m == 0 && u == 1 && NM == 6)
          asm volatile("buffer_store_dword %0, %1, %2, %3 offen sc1" :: "v"(cx.xbase), "v"(cx.fvo), "s"(cx.rs), "s"(ls.fo + 16384 + (ls.rot & 3) * 256) : "memory");
        if (ST && !FIRST && m == 0 && u == 1) {
          asm volatile("" : "+s"(ls.fo));
          const unsigned e = (ls.fo >> 13) - (FLG >> 13) - IH;
          if (e < NH) {
            const bool r3 = NH <= 5 || e >= NH - 5;
            x_store(cx, u32x4{rq.d[0][2 * e], rq.d[0][2 * e + 1], rq.d[1][2 * e], rq.d[1][2 * e + 1]}, (ls.xs & ~255u) + (r3 ? 8 * SBLK : 0u), (ls.xs & 255u) + e + (r3 ? 5 : 10) - NH);
          }
        }
        if (TR && (m == 0 || m == 2) && u >= 4) pv[m + (u & 1)] = tr_read(cx.smem + (rq.prd ^ (unsigned)((m + (u & 1)) << 5)));
        if (TR && m == 3 && u == 3) {
          rq.prd += 4096;
          asm volatile("" : "+v"(rq.prd));
        }
        if (TR && (m == 1 || m == 3) && u >= 4) pa[u & 1] = MFMA(cat(pv[m - 1 + (u & 1)], pv[m - 1 + (u & 1)]), vb, ZERO4);
        if (TR && (m == 2 || m == 4) && u < 2) {
          pk[2 * u] = pk_f16(pa[u][0], pa[u][1]);
          pk[2 * u + 1] = pk_f16(pa[u][2], pa[u][3]);
          if (u) st16(pk, cx.rs, cx.poff, ls.g + NQ1 * (2 * SBLK) + ((ls.rot + NQ1) & 3) * 16384u + (m - 2) * 64u);
        }
        if (m == 3 && u == 5) {
          ls.sp = ls.sa;
          ls.sa = ls.sa == 2 * STG ? 0u : ls.sa + STG;
          asm volatile("" : "+s"(ls.sa), "+s"(ls.sp));
        }
        if (PAR == 1 && m == 4 && u == 5) {
          ls.g += 2 * SBLK;
          ls.fo += 8192;
          if (NM == 5) ls.pm >>= 1;
          ls.rot += 1;
          asm volatile("" : "+s"(ls.g), "+s"(ls.fo), "+s"(ls.pm), "+s"(ls.rot));
        }
      }
      SB0;
    }
  });
}

template <int NM>
__device__ __forceinline__ void k_tail(const Ctx& cx, f32x4 (&acc)[5][2][3], const WF& w0, WF& w1, const XF& x0, const XF& x1, bf16x8 (&gb)[16]) {
  const int bid = cx.vb;
  wait_vm<NGB>();
  __builtin_amdgcn_s_barrier();
  SB0;
  const int idx = bid >> 3, slot = (bid & 7) * 4 + (idx >> 3), lane = cx.lane4 >> 2, h = (idx >> 2) & 1;
  const float cf = slot ? -1.0f : 0.0f;
  const u32x4 ry = make_rsrc(cx.ws + WS_Y, ROWS * 65536);
  const int cgo = ((idx & 3) * 4 + (cx.wv & 1)) * 4096 + (lane >> 4) * 16;
  const unsigned t0x = (unsigned)(h * 10 + (cx.wv >> 2) * 5 + (cx.wv & 2));
  const unsigned xa = cx.xbase + STG, xb = cx.xbase + RAWL - 16384, wo = cx.wbase + STG;
  unsigned yo;
  XF xq[12];
  s16x4 wt[8];
  u32x4 ws[2][2], xs;
  xq[0] = x0;
  xq[1] = x1;
  sfor<0, 10>([&](auto jj) __attribute__((always_inline)) {
    constexpr int j = jj.v;
    constexpr int m = j < 4 ? j & 1 : j >> 1, kk = j < 4 ? j >> 1 : j & 1;
    constexpr int m2 = j < 2 ? j : (j + 2) >> 1, k2 = j < 2 ? 1 : j & 1;
    constexpr int mp = j - 1 < 4 ? (j - 1) & 1 : (j - 1) >> 1, kp = j - 1 < 4 ? (j - 1) >> 1 : (j - 1) & 1;
    const WF& w = kk ? w1 : w0;
#pragma unroll
    for (int q = 0; q < 6; ++q) {
      const int s = q & 1, p = q >> 1;
      if (p == 0) acc[m][s][0] = MFMH(w.r[s], xq[j].r, acc[m][s][0]);
      if (p == 1) acc[m][s][1] = MFMH(w.i[s], xq[j].i, acc[m][s][1]);
      if (p == 2) acc[m][s][2] = MFMH(ws[kk][s], xs, acc[m][s][2]);
      SB0;
      {
        const int u = q;
        if (u < 4) xs[u] = add_h(xq[j].r, xq[j].i, u);
        if ((j == 0 || j == 2) && u < 4) {
          ws[kk][u >> 1][(u & 1) * 2] = add_h(w.r[u >> 1], w.i[u >> 1], (u & 1) * 2);
          ws[kk][u >> 1][(u & 1) * 2 + 1] = add_h(w.r[u >> 1], w.i[u >> 1], (u & 1) * 2 + 1);
        }
        if (u >= 4 && j < 8) {
          const unsigned o = (k2 && m2 >= 2 ? xb : xa) ^ (u & 1 ? 64u : 0u);
          const bf16x8 v = *reinterpret_cast<const bf16x8*>(cx.smem + o + xtile<NM>(m2 % 5) * 2048);
          if (u & 1) xq[(j + 2) % 12].i = v; else xq[(j + 2) % 12].r = v;
        }
        if (j == 0 || (j == 1 && u < 2)) {
          const int ix = 6 * j + u;
          const int ri = ix >> 2, ss = (ix >> 1) & 1, hf = ix & 1;
          wt[ix] = tr_read(cx.smem + (wo ^ (unsigned)(ri * 64 + ss * 32)) + hf * 8192);
          if (hf) { if (ri) w1.i[ss] = cat(wt[ix - 1], wt[ix]); else w1.r[ss] = cat(wt[ix - 1], wt[ix]); }
        }
        if (j == 3 && u == 1) yo = (unsigned)((h * NBLK + (cx.wv >> 2) * 80 + (lane & 14)) * 65536 + cgo + (2 * slot + (lane & 1)) * 64);
        if (j >= 3 && kp == 1 && u == 2) y_store(acc[mp], cf, xtile<NM>(mp), ry, yo, cx.lane4 & 4, t0x);
        const int gi = NGB + 6 * (j - 3) + u - 3;
        if (gi >= NGB && gi < 12)
          asm volatile("global_load_dwordx4 %0, %1, %2" : "=v"(gb[gi & 15]) : "v"(cx.lane4 * 4), "s"(&TAB.G[0][0][0][0][0] + gi * 512) : "memory");
      }
      SB0;
    }
  });
  y_store(acc[4], cf, xtile<NM>(4), ry, yo, cx.lane4 & 4, t0x);
}

__device__ __forceinline__ int run_bs(int vb, int t) { return 5 * ((vb >> 4) + 16 * ((vb ^ t) & 1)); }
__device__ __forceinline__ int run_q(int vb, int t) { return (((vb >> 1) & 1) + ((vb >> 2) & 2) + 2 * t) & 3; }
__device__ __forceinline__ unsigned x_addr(int vb, int t, int wv) {
  const int lane = threadIdx.x & 63, q = run_q(vb, t);
  const unsigned c = (unsigned)((lane & 15) ^ (((4 * wv + (lane >> 4)) & 7) << 1));
  return (unsigned)(2 * (HOP * run_bs(vb, t) + 4 * wv + (lane >> 4)) + ((t ^ q ^ (q >> 1)) & 1)) * 1024u + c * 16 + q * 256;
}
__device__ __forceinline__ u32x4 x_rsrc(const unsigned short* x, int vb) { return make_rsrc(x + (size_t)((vb >> 2) & 1) * T_ * D_, T_ * D_ * 2); }
__device__ __forceinline__ void issue_x(int t, int wv, unsigned a, const u32x4& rx, unsigned lds0, int j0 = 0, int j1 = 9) {
#pragma unroll
  for (int jj = 0; jj < 9; ++jj)
    if (jj >= j0 && jj < j1 && (jj < 8 || wv < 3)) DMA16("nt", rx, lds0 + t * XT1 + wv * 1024 + jj * 8192, a, jj * 65536u);
}
struct XR { s16x4 t[8]; };
constexpr int P0 = 3, P1 = 23;
constexpr int PG = 20, NFA = 1;
constexpr int TE = 4;
constexpr int NIT = 23, NS = 30 - NH - NST, NA = 7, NB = 13;
constexpr int ord(int p) { return p < 0 || p >= NIT ? -1 : p == 0 ? 0 : p < 6 ? p + 2 : p < 8 ? p - 5 : p; }
constexpr int st_w(int j) { return j < 2 ? j : j < 7 ? -1 : j < 15 ? j - 5 : -1; }
constexpr int st_x(int j) { return j < 7 ? j - 2 : j - 10; }
constexpr int st_pos(int j) { return st_w(j) >= 0 ? (j < 9 ? 0 : j < 13 ? 6 : 7) : j < 7 ? j - 1 : j - 7; }
constexpr int st_first(int p) {
  int j = p <= P0 ? 0 : p >= P1 ? NS : (p - P0) * NS / (P1 - P0);
  while (j > 0 && p <= NIT && st_pos(j - 1) + 2 > p - 1) --j;
  return j;
}
template <int NM>
__device__ __forceinline__ void phase1(Ctx& cx, const unsigned short* __restrict__ x, const unsigned short* __restrict__ w,
                                       RQ& rqv, unsigned& xs, bf16x8& vb_, WF& wa, XF& x0, XF& x1) {
  u32x2 rq[NQW];
  static_assert(NQ1 == 5, "phase-1 wait counts assume five quanta");
  char* smem = cx.smem;
  const unsigned lds0 = (unsigned)(uintptr_t)(lds_void*)smem;
  const int idx = cx.vb >> 3, wv = cx.wv, lane = threadIdx.x & 63;
  const u32x4 rw = make_rsrc(w, 31 * D_ * D_ * 2);
  const int tap = threadIdx.x >> 5, pc = threadIdx.x & 31;
  const unsigned roff = (unsigned)(tap * (2 * D_ * D_ * 2) + pc * 8);
  const int ng = wv & 3, mg = wv >> 2;
  const int g = lane >> 4, qq = (lane >> 2) & 3, pp = lane & 3;
  u32x2 r1[NQ1];
  bf16x8 fb[2][2];
  unsigned long long tag;
  const char* rec = cx.ws + WS_PUB + ((cx.id & 7) * 8 + wv) * 16;
  u32x4 pr, wl;
  {
    const int vb = cx.vb;
    const int kr = (vb >> 7) * 32 + (vb & 4) * 4 + (idx & 15);
    auto raw_load = [&](u32x2& r, int q) {
      const unsigned so = (unsigned)(kr + 64 * (2 * (q & 3) + ((q >> 2) & 1)) + 512 * (((q >> 3) ^ q ^ (q >> 1)) & 1)) * 1024u + (unsigned)((vb + q) & 3) * 256u;
      asm volatile("buffer_load_dwordx2 %0, %1, %2, %3 offen nt" : "=v"(r) : "v"(roff), "s"(rw), "s"(so) : "memory");
    };
#pragma unroll
    for (int k = 0; k < NQ1; ++k) raw_load(r1[k], k);
    const unsigned xa1 = x_addr(vb, 1, wv);
    issue_x(1, wv, xa1, x_rsrc(x, vb), lds0, 0, TE);
    if (NM == 6 && (cx.id | wv) == 0) {
      unsigned g[2] = {~0u, ~0u};
#pragma unroll 1
      for (int k = -1; k < 16 && ((k & 1) || min(g[0], g[1]) > 440u); ++k) {
        unsigned long long t0, t1;
        unsigned d;
        asm volatile("s_memtime %0\n\ts_waitcnt lgkmcnt(0)\n\ts_load_dword %2, %3, 0x0\n\ts_waitcnt lgkmcnt(0)\n\ts_memtime %1\n\ts_waitcnt lgkmcnt(0)"
                     : "=&s"(t0), "=&s"(t1), "=&s"(d) : "s"(rec + 32768 + (k < 0 ? 128 : (k & 1) * 8192 + (k & 2) * 2048 + (k & 4) * 4096 + (k & 8) * 128)) : "memory");
        if (k >= 0) { if (k & 1) g[1] = min(g[1], (unsigned)(t1 - t0)); else g[0] = min(g[0], (unsigned)(t1 - t0)); }
      }
      const unsigned lo = cx.tag[0] | (((g[0] < g[1] ? 0u : 1u) ^ (cx.vb >> 2)) & 1u), hi = cx.tag[1];
      asm volatile("global_store_dwordx4 %0, %1, %2 sc1\n\ts_nop 1" :: "v"(lane * 16 + 2048), "v"(u32x4{lo, hi, lo, hi}), "s"(rec) : "memory");
    }
    {
#pragma unroll
      for (int i = 0; i < 4; ++i) ld16(fb[i >> 1][i & 1], TAB.F[ng][i >> 1][i & 1][lane]);
      ld16(vb_, TAB.V[0][ng][lane]);
      asm volatile("global_load_dwordx4 %0, %1, %2 sc1" : "=v"(pr) : "v"(0u), "s"(rec) : "memory");
    }
    if (NM == 5 && wv >= 6)
      asm volatile("buffer_load_dwordx4 %0, %1, %2, %3 offen nt" : "=v"(wl) : "v"((threadIdx.x & 127) * 16), "s"(rw), "s"(30 * D_ * D_ * 2 + vb * 2048) : "memory");
    if (cx.id == 0 && wv >= TE - 1)
      DMA16("nt", x_rsrc(x, vb), lds0 + XT1 + (wv + 1) * 8192, x_addr(vb, 1, 0), (wv + 1) * 65536u);
    if (NM == 5 || (cx.id | wv)) issue_x(1, wv, xa1, x_rsrc(x, vb), lds0, TE, 9);
#pragma unroll
    for (int k = 0; k < NQW; ++k) raw_load(rq[k], NQ1 + k);
    {
      const int bid = vb, xb = (bid >> 2) & 1, krow = kr;
      const int slot = (bid & 7) * 4 + (idx >> 3), b = (idx >> 2) & 1, nt = idx & 3;
      const int rt = 4 * g + qq;
      cx.lane4 = lane * 4;
      cx.fvo = NM == 5 ? (unsigned)((bid & 7) * 1024 + ng * 256 + lane * 4) : wv == 0 && lane < 8 ? (unsigned)(lane * 1024 + (bid >> 2) * 4) : 0x80000000u;
      cx.pwr = (unsigned)(RAWB + tap * 256 + (((pc >> 2) ^ (tap & 7)) << 5) + (pc & 3) * 8);
      cx.prd = (unsigned)(RAWB + rt * 256 + (((4 * mg) ^ (rt & 7)) << 5) + pp * 8);
      cx.poff = (unsigned)(sbase(8 * ng + ((lane >> 1) & 7)) + (krow >> 5) * SBLK + (krow & 31) * 512 +
                           mg * 256 + (lane & 1) * 64 + g * 16);
      cx.xo = (unsigned)(sbase(8 * ng + ((lane >> 1) & 7)) + mg * SBLK + 65536 + xb * 8192 + (lane & 1) * 64 + g * 16);
      cx.xbase = (unsigned)(16384 + mg * 5 * 2048 + (lane & 15) * 128 + ((g ^ ((lane >> 1) & 7)) << 4));
      cx.wbase = (unsigned)(rt * 512 + (((4 * ng) ^ (rt & 7)) << 5) + pp * 8);
      cx.lds0 = lds0 + (NM == 6 ? ng * 1024 : 16384 + ng * 1024);
      if (NM == 6) {
        const int row = 2 * ng + (lane >> 5);
        cx.voff = (unsigned)(sbase(slot) + (nt * 32 + row) * 512 + (((lane & 31) ^ ((row & 7) << 1)) << 4));
        const int kr8 = lane >> 3;
        cx.voff5 = (unsigned)(WS_L - WS_S + (8 * ng + kr8) * 1024 + (nt * 2 + xb) * 128 + (((lane & 7) ^ ((kr8 & 2) | (ng & 1) << 2)) << 4));
        cx.crd = (unsigned)(CORR + (8 * g + qq) * 128 + ((ng ^ ((qq >> 1) | (g & 1) << 1)) << 5) + pp * 8);
      } else {
        const int row = 8 * ng + (lane >> 3), rg = (bid & 1) * 8 + (slot & 1) * 4 + (bid & 2) + (idx >> 4);
        cx.voff = (unsigned)(sbase(slot) + 65536 + b * 8192 + row * 128 + (((lane & 7) ^ ((row >> 1) & 7)) << 4));
        cx.voff5 = (unsigned)(WS_D - WS_S + (b * NBLK + 10 * rg + min(lane & 15, 9)) * 1024 + g * 16);
      }
    }
    asm volatile("" : "+v"(cx.fvo), "+v"(cx.poff), "+v"(cx.xo), "+v"(cx.voff), "+v"(cx.voff5), "+v"(cx.lane4));
  }
  Rng gx, gw;
  auto scan = [&](int t) __attribute__((always_inline)) {
    u32x4 v[9];
#pragma unroll
    for (int j = 0; j < 9; ++j)
      if (j < 8 || NM == 6) v[j] = *reinterpret_cast<const u32x4*>(smem + t * XT1 + (j < 8 || wv < 3 ? j : 7) * 8192 + threadIdx.x * 16);
    SB0;
#pragma unroll
    for (int j = 0; j < 9; ++j)
      if (j < 8 || NM == 6) {
        widen(gx, v[j][0], v[j][1]);
        widen(gx, v[j][2], v[j][3]);
      }
    asm volatile("" : "+v"(gx.hi), "+v"(gx.lo));
    SB0;
  };
  if (NM == 6 && (cx.id | wv) == 0) wait_vm<NQW>();
  wait_vm<8 + 6 + NQW>();
  unsigned near;
  scan(0);
#pragma unroll
  for (int q = 0; q < NQ1; ++q) {
    asm volatile("" : "+v"(r1[q]));
    widen(gw, r1[q][0], r1[q][1]);
  }
  {
    wait_vm<NQW + 8 - TE>();
    unsigned d0, d1;
    for (unsigned spins = 0;; ++spins) {
      asm volatile("" : "+v"(pr));
      d0 = __builtin_amdgcn_readfirstlane(pr[0]);
      d1 = __builtin_amdgcn_readfirstlane(pr[1]);
      const unsigned d2 = __builtin_amdgcn_readfirstlane(pr[2]), d3 = __builtin_amdgcn_readfirstlane(pr[3]);
      if (__builtin_expect((d0 == ~d2 && d1 == ~d3) || spins > (1u << 21), 1)) break;
      __builtin_amdgcn_s_sleep(2);
      asm volatile("global_load_dwordx4 %0, %1, %2 sc1\n\ts_waitcnt vmcnt(0)" : "=v"(pr) : "v"(0u), "s"(rec) : "memory");
    }
    tag = d0 | (unsigned long long)d1 << 32;
    const unsigned vn = (cx.id + (d0 >> 1)) & 7u;
    if (__builtin_expect(vn != (cx.vb & 7u), 0)) restart(cx.id | 256u | vn << 10);
  }
  const unsigned ee = (cx.id & 512u ? cx.id : (unsigned)(tag >> 32)) >> 16, ex = ee & 255u, ew = ee >> 8;
  cx.es = 256 - (int)(ex + ew);
  char* ws;
  const int bid = cx.vb;
  bf16x8& vb = vb_;
  u32x4 f0, f1;
  char* fab;
  const u32x4* fa;
  u32x2 ov;
  unsigned pollv;
  const int rb[2] = {run_bs(bid, 0), run_bs(bid, 1)};
  unsigned xb0[4];
#pragma unroll
  for (int k = 0; k < 4; ++k) xb0[k] = (unsigned)((4 * g + qq) * 256 + (((2 * mg) ^ ((4 * g + qq + 2 * k) & 7)) << 5) + pp * 8);
#pragma unroll
  for (int q = 0; q < NQ1; ++q) *reinterpret_cast<u32x2*>(smem + cx.pwr + q * 4096) = r1[q];
  asm volatile("" : "+v"(fb[0][0]), "+v"(fb[0][1]), "+v"(fb[1][0]), "+v"(fb[1][1]), "+v"(vb));
  scale8(vb, 128 - (int)ew);
#pragma unroll
  for (int i = 0; i < 4; ++i) scale8(fb[i >> 1][i & 1], 128 - (int)ex);
  asm volatile("" : "+v"(fb[0][0]), "+v"(fb[0][1]), "+v"(fb[1][0]), "+v"(fb[1][1]), "+v"(vb));
  asm volatile("s_waitcnt lgkmcnt(0)" : "+v"(xb0[0]), "+v"(xb0[1]), "+v"(xb0[2]), "+v"(xb0[3]) :: "memory");
  wait_vm<NQW>();
  asm volatile("global_load_dwordx2 %0, %1, %2 sc1" : "=v"(ov) : "v"(2048u), "s"(rec) : "memory");
  __builtin_amdgcn_s_barrier();
  SB0;
  u32x4 hw[6][2], hx[20];
  f32x4 pend[2];
  auto rd = [&](int it, int i) __attribute__((always_inline)) {
    if (it < 3) return tr_read(smem + ((cx.prd ^ (unsigned)((i & 3) << 5)) + (2 * it + (i >> 2)) * 4096));
    const int n = it - 3;
    const unsigned base = (xb0[n % 5 & 3] ^ (unsigned)((4 * ((n / 5) & 1) + (i & 1)) << 5)) + n / 10 * XT1;
    return tr_read(smem + (base + n % 5 * (HOP * 256) + (i >> 1) * (16 * 256)));
  };
  auto dst = [&](int it) -> u32x4& { return it < 3 ? hw[it == 2 ? 4 : 2 * it + 1][1] : hx[it - 3]; };
  auto item = [&](int it, int nx, int pv, XR& xr, auto side) __attribute__((always_inline)) {
    const XR c = xr;
    const int nm = it == 2 ? 4 : 8, nr = nx == 2 ? 4 : 8;
    f32x4 r[8];
#pragma unroll
    for (int i = 0; i < 8; ++i) {
      if (i >= nm) break;
      const int s2 = i & 1, ks = (i >> 1) & 1;
      if (it < 3) r[i] = MFMA(cat(c.t[i], c.t[i]), vb, ZERO4);
      else r[i] = MFMA(cat(c.t[4 * ks + s2], c.t[4 * ks + 2 + s2]), fb[ks][i < 4], i < 2 ? ZERO4 : r[i - 2]);
      SB0;
      if (nx >= 0) {
#pragma unroll
        for (int j = 0; j < 8; ++j) if (j < nr && j * nm / nr == i) xr.t[j] = rd(nx, j);
      }
      if (pv >= 0 && (nm == 4 ? i >= 2 : i >= 1 && i <= 4)) {
        u32x4& d = dst(pv);
#pragma unroll
        for (int e = 0; e < 4; ++e)
          if ((nm == 4 ? 2 + (e >> 1) : 1 + e) == i) d[e] = pk_f16(pend[e >> 1][(e & 1) * 2], pend[e >> 1][(e & 1) * 2 + 1]);
        if (i == (nm == 4 ? 3 : 4)) asm volatile("" : "+v"(d));
      }
      if (it < 3 && i >= 3 && (i & 1)) {
        u32x4& d = hw[2 * it + ((i - 3) >> 2)][((i - 3) >> 1) & 1];
        d = pack8h(r[i - 3], r[i - 2]);
        asm volatile("" : "+v"(d));
      }
      side(i * (8 / nm));
      if (nm == 4) side(2 * i + 1);
      SB0;
    }
    pend[0] = r[nm - 2];
    pend[1] = r[nm - 1];
  };
  const unsigned so0 = (unsigned)(2 * run_q(bid, 0)) * SBLK, so1 = (unsigned)(16 + 2 * run_q(bid, 1)) * SBLK;
  auto store = [&](int j) __attribute__((always_inline)) {
    if (j == 0) {
      wait_vm<0>();
      for (unsigned spins = 0;; ++spins) {
        asm volatile("" : "+v"(ov));
        near = __builtin_amdgcn_readfirstlane(ov[0]) ^ (unsigned)tag;
        if (__builtin_expect((near < 2u && __builtin_amdgcn_readfirstlane(ov[1]) == (unsigned)(tag >> 32)) || spins > (1u << 21), 1)) break;
        __builtin_amdgcn_s_sleep(2);
        asm volatile("global_load_dwordx2 %0, %1, %2 sc1\n\ts_waitcnt vmcnt(0)" : "=v"(ov) : "v"(2048u), "s"(rec) : "memory");
      }
      ws = cx.ws += (near & 1u) * 8192;
      cx.rs = make_rsrc(ws + WS_S, WS_L - WS_S + D_ * D_ * 2);
      fab = ws + WS_FA + (cx.id & 512u) * 32;
      fa = reinterpret_cast<const u32x4*>(fab + (bid & 7) * 2048) + lane;
      if (NM == 6 && wv == 1) {
        const unsigned o = (lane < 32 ? WS_STATS : WS_S + FLG + 16384 - 512) + bid * 512 + lane * 16;
        const unsigned v = lane < 32 ? ~0u : 0u;
        asm volatile("global_store_dwordx4 %0, %1, %2 sc1\n\ts_nop 1" :: "v"(o), "v"(u32x4{v, v, v, v}), "s"(ws) : "memory");
        if (lane < 8) __hip_atomic_store((gu32*)(ws + WS_S + FLG + lane * 1024 + ((bid + 2) & 3) * 256 + (bid >> 2) * 4), 0u, RLX_AGENT);
      }
    }
    if (j == NB + 1 && NM == 6) {
      if (((bid >> 1) ^ (bid >> 3) ^ 1) & 1 && threadIdx.x < 160) {
        const int t = threadIdx.x >= 80, blk = (threadIdx.x >> 4) - 5 * t;
        const unsigned o = (unsigned)(t * XT1 + blk * (HOP * 256) + (((lane & 15) ^ ((blk & 3) << 2)) << 4));
        const u32x4 lo = *reinterpret_cast<const u32x4*>(smem + o), hi = *reinterpret_cast<const u32x4*>(smem + o + 64 * 256);
        u32x4 d;
#pragma unroll
        for (int e = 0; e < 4; ++e) d[e] = pk_bf16(bf_lo(hi[e]) - bf_lo(lo[e]), bf_hi(hi[e]) - bf_hi(lo[e]));
        st16(d, cx.rs, (unsigned)((((bid >> 2) & 1) * NBLK + (t ? rb[1] : rb[0]) + blk) * 1024 + run_q(bid, t) * 256 + (lane & 15) * 16), WS_D - WS_S);
      }
    }
    if (j == NB + 1 && NM == 5) {
      if (wv >= 6) {
        asm volatile("" : "+v"(wl));
        st16(wl, cx.rs, (threadIdx.x & 127) * 16, WS_L - WS_S + bid * 2048);
      }
    }
    const int wq = st_w(j), xn = st_x(j);
    if (wq >= 0) st16(hw[wq >> 1][wq & 1], cx.rs, cx.poff, (unsigned)(wq >> 1) * (2 * SBLK) + (unsigned)((bid + (wq >> 1)) & 3) * 16384u + (wq & 1) * 128u);
    else x_store(cx, hx[xn], (xn < 10 ? so0 : so1) + ((xn / 5) & 1) * (8 * SBLK), (unsigned)(rb[xn / 10] + xn % 5));
    if (j == NA + NFA - 1) {
      scan(1);
#pragma unroll
      for (int k = 0; k < NQW; ++k) {
        asm volatile("" : "+v"(rq[k]));
        widen(gw, rq[k][0], rq[k][1]);
      }
      const unsigned mx = amax(gx), mw = amax(gw);
      if (__builtin_expect(__any(expo(mx) > ex + 7 || expo(mw) > ew + 9) && !(cx.id & 512u), 0)) {
        const u32x2 m = {wave_max(expo(mx)), wave_max(expo(mw))};
        asm volatile("global_atomic_umax %0, %1, %3 sc1\n\tglobal_atomic_umax %0, %2, %3 offset:4 sc1\n\ts_waitcnt vmcnt(0)"
                     :: "v"(lane * 16 + 1024), "v"(m[0]), "v"(m[1]), "s"(rec - ((cx.id & 7) * 8 + wv) * 16) : "memory");
      }
    }
    if (j == NA + NFA - 1 || j == NB + NFA - 1) {
      wait_vm<NFA>();
      __builtin_amdgcn_s_barrier();
      if (wv == 0 && lane < 8) {
        if (j < NB) __hip_atomic_store((gu64*)(fab + lane * 2048) + bid, tag, RLX_AGENT);
        else __hip_atomic_store((gu32*)(ws + WS_S + FLG + lane * 1024 + ((bid + 2) & 3) * 256 + (bid >> 2) * 4), 1u, RLX_AGENT);
      }
    }
    SB0;
  };
  XR xr;
#pragma unroll
  for (int j = 0; j < 8; ++j) xr.t[j] = rd(0, j);
  SB0;
  static_assert(st_first(NIT) == NS && PG >= 14 && st_first(PG - 2) >= NB + 2 && NA + NFA <= NB && NFA >= 1 && NH <= 7, "order of the hand-off steps");
  sfor<0, NIT>([&](auto p) __attribute__((always_inline)) {
    constexpr int j0 = st_first(p.v), j1 = st_first(p.v + 1);
    if (p >= 12 && p < PG) { if (!(bid & 1) && cx.id) __builtin_amdgcn_s_sleep(6); }
    if (p == PG - 2) {
      code_touch(cx, true);
      if (NM == 6 && wv == 0)
        asm volatile("global_load_dwordx4 %0, %2, off sc1\n\tglobal_load_dwordx4 %1, %2, off offset:1024 sc1" : "=&v"(f0), "=&v"(f1) : "v"(fa) : "memory");
    }
    if (p == PG) {
      if (NM == 6 && wv == 0) {
        wait_vm<st_first(PG) - st_first(PG - 2)>();
        for (unsigned spins = 0; spins < (1u << 21); ++spins) {
          asm volatile("" : "+v"(f0), "+v"(f1));
          const unsigned t0 = (unsigned)tag, t1 = (unsigned)(tag >> 32);
          if (__all(f0[0] == t0 && f0[1] == t1 && f0[2] == t0 && f0[3] == t1 && f1[0] == t0 && f1[1] == t1 && f1[2] == t0 && f1[3] == t1)) break;
          __builtin_amdgcn_s_sleep(2);
          asm volatile("global_load_dwordx4 %0, %2, off sc1\n\tglobal_load_dwordx4 %1, %2, off offset:1024 sc1\n\ts_waitcnt vmcnt(0)"
                       : "=&v"(f0), "=&v"(f1) : "v"(fa) : "memory");
        }
      }
      __builtin_amdgcn_s_barrier();
      stage_issue<NM>(cx, -1);
      asm volatile("global_load_dwordx2 %0, %1, %2 sc1" : "=v"(ov) : "v"(1024u), "s"(rec) : "memory");
      stage_issue<NM>(cx, 0);
      SB0;
    }
    if (p == NIT - 1 && NM == 5) {
      asm volatile("buffer_load_dword %0, %1, %2, %3 offen sc1" : "=v"(pollv) : "v"(cx.fvo), "s"(cx.rs), "s"(FLG) : "memory");
    }
    item(ord(p), ord(p + 1), ord(p - 1), xr, [&](int i) __attribute__((always_inline)) {
      if ((i * (j1 - j0) + 7) / 8 < ((i + 1) * (j1 - j0) + 7) / 8) store(j0 + (i * (j1 - j0) + 7) / 8);
    });
  });
  hx[19] = pack8h(pend[0], pend[1]);
#pragma unroll
  for (int e = 0; e < NH; ++e)
#pragma unroll
    for (int d = 0; d < 4; ++d) rqv.d[d >> 1][2 * e + (d & 1)] = hx[20 - NH + e][d];
  asm volatile("" : "+v"(rqv.d[0]), "+v"(rqv.d[1]));
  xs = so1 + (unsigned)rb[1];
#pragma unroll
  for (int e = 0; e < NST; ++e) rqv.st[e] = hx[20 - NH - NST + e];
  rqv.prd = cx.prd + (RAWL - RAWB);
  rqv.ca = ZERO4;
  wait_vm<NM == 5 ? NS - st_first(NIT - 1) : 10 - NM + NS - st_first(PG)>();
  {
    asm volatile("" : "+v"(ov));
    const unsigned ox = __builtin_amdgcn_readfirstlane(ov[0]), ow = __builtin_amdgcn_readfirstlane(ov[1]);
    if (__builtin_expect((ox | ow) && !(cx.id & 512u), 0))
      restart((cx.id & 255u) | 768u | (bid & 7u) << 10 | min(max(ex + 7, ox) - 7, 220u) << 16 | min(max(ew + 9, ow) - 9, 220u) << 24);
    if (NM == 6 && (cx.id & 255u) == 0 && wv == 0)
      asm volatile("global_store_dwordx4 %0, %1, %2 sc1\n\ts_nop 1" :: "v"(lane * 16), "v"(u32x4{0u, 0u, 0u, 0u}), "s"(rec) : "memory");
  }
  if (NM == 5) {
    asm volatile("" : "+v"(pollv));
    if (!__all(pollv != 0u)) spin_flags(ws + WS_S + FLG + cx.fvo);
  }
  __builtin_amdgcn_s_barrier();
  SB0;
#pragma unroll
  for (int s = 0; s < 2; ++s) {
    const char* pr = cx.smem + (cx.wbase ^ (unsigned)(s * 32));
    const char* pi = cx.smem + (cx.wbase ^ (unsigned)(64 + s * 32));
    wa.r[s] = cat(tr_read(pr), tr_read(pr + 8192));
    wa.i[s] = cat(tr_read(pi), tr_read(pi + 8192));
  }
  const unsigned xh = cx.xbase + xtile<NM>(0) * 2048;
  x0.r = *reinterpret_cast<const bf16x8*>(cx.smem + xh);
  x0.i = *reinterpret_cast<const bf16x8*>(cx.smem + (xh ^ 64u));
  x1.r = *reinterpret_cast<const bf16x8*>(cx.smem + xh + 2048);
  x1.i = *reinterpret_cast<const bf16x8*>(cx.smem + (xh ^ 64u) + 2048);
  SB0;
  stage_issue<NM>(cx, 1);
#pragma unroll
  for (int k = 0; k < NQW; ++k) rqv.raw[k] = rq[k];
}

template <int NM>
__device__ __forceinline__ void run12(Ctx& cx, const unsigned short* __restrict__ x, const unsigned short* __restrict__ w, bf16x8 (&gb)[16]) {
  RQ rq;
  bf16x8 vb;
  unsigned xs;
  f32x4 acc[5][2][3];
  WF wa, wb;
  XF x0, x1;
  phase1<NM>(cx, x, w, rq, xs, vb, wa, x0, x1);
  unsigned pollv = 1u;
  SB0;
  LS ls{STG, 0u, 0u, FLG, (cx.wv & 3) == ((cx.vb >> 3) & 3) ? 0x3ffcu : 0x444u, (unsigned)cx.vb, xs,
        0u, cx.wv == 7 ? (cx.vb & 1 ? 125u : 137u) - 14u : 0u};
  if (NM == 5) __builtin_amdgcn_s_setprio(1);
  k_step<NM, 0, true, true, NS - st_first(PG), true>(cx, acc, wa, wb, x0, x1, rq, pollv, vb, ls, gb);
#pragma unroll 1
  for (int it = 0; it < NQW; ++it) {
    k_step<NM, 1, true, true, 0>(cx, acc, wb, wa, x0, x1, rq, pollv, vb, ls, gb);
    k_step<NM, 0, true, true, 2>(cx, acc, wa, wb, x0, x1, rq, pollv, vb, ls, gb);
  }
  if (NM == 6) *reinterpret_cast<f32x4*>(cx.smem + CBUF + (cx.lane4 & 60) * 64 + cx.wv * 64 + (cx.lane4 >> 6) * 16) = rq.ca;
#pragma unroll 1
  for (int it = NQW; it < 14; ++it) {
    k_step<NM, 1, false, true, 0>(cx, acc, wb, wa, x0, x1, rq, pollv, vb, ls, gb);
    k_step<NM, 0, false, true, 0>(cx, acc, wa, wb, x0, x1, rq, pollv, vb, ls, gb);
  }
  k_step<NM, 1, false, false, NM == 5 ? 3 : 0>(cx, acc, wb, wa, x0, x1, rq, pollv, vb, ls, gb);
  k_tail<NM>(cx, acc, wa, wb, x0, x1, gb);
  wait_vm<0>();
#pragma unroll
  for (int i = 0; i < 12; i += 4) asm volatile("" : "+v"(gb[i]), "+v"(gb[i + 1]), "+v"(gb[i + 2]), "+v"(gb[i + 3]));
}

template <int CTRL>
__device__ __forceinline__ float bfly(float a, float b, bool hi) {
  const float give = hi ? a : b, keep = hi ? b : a;
  return keep + __builtin_bit_cast(float, __builtin_amdgcn_update_dpp(0, __builtin_bit_cast(int, give), CTRL, 0xf, 0xf, false));
}
template <int CTRL>
__device__ __forceinline__ float dpp_add(float v) {
  asm("" : "+v"(v));
  return v + __builtin_bit_cast(float, __builtin_amdgcn_update_dpp(0, __builtin_bit_cast(int, v), CTRL, 0xf, 0xf, true));
}
__device__ __forceinline__ void row16_sum(float (&v)[8]) {
#pragma unroll
  for (int r = 0; r < 8; ++r) v[r] = dpp_add<0x128>(v[r]);
  SB0;
#pragma unroll
  for (int r = 0; r < 8; ++r) v[r] = dpp_add<0x124>(v[r]);
  SB0;
#pragma unroll
  for (int r = 0; r < 8; ++r) v[r] = dpp_add<0x122>(v[r]);
  SB0;
#pragma unroll
  for (int r = 0; r < 8; ++r) v[r] = dpp_add<0x121>(v[r]);
#pragma unroll
  for (int r = 0; r < 8; ++r) asm("" : "+v"(v[r]));
  SB0;
}

extern "C" __global__ __launch_bounds__(512, 2) void conv_fft(char* ws, const unsigned short* __restrict__ x,
                                                   const unsigned short* __restrict__ w,
                                                   const unsigned short* __restrict__ gamma,
                                                   const unsigned short* __restrict__ beta,
                                                   unsigned short* __restrict__ out) {
  __shared__ __attribute__((aligned(16))) char smem[160 * 1024];
  const unsigned lds0 = (unsigned)(uintptr_t)(lds_void*)smem;
  const int wv = __builtin_amdgcn_readfirstlane(threadIdx.x >> 6);
  Ctx cx;
  {
    unsigned len;
    asm volatile("s_getpc_b64 %0\n.Lcf_a:\n\ts_mov_b32 %1, .Lcf_b-.Lcf_a" : "=s"(cx.pc), "=s"(len));
    cx.wv = wv;
    cx.pfo = len;
    unsigned xcc;
    asm volatile("s_getreg_b32 %0, hwreg(HW_REG_XCC_ID, 0, 3)" : "=s"(xcc));
    unsigned id = blockIdx.x;
    asm volatile("" : "+s"(id));
    cx.id = id;
    cx.vb = (int)((id & 248u) | (id & 256u ? (id >> 10) & 7u : xcc));
    const char* pb = ws + WS_PUB;
    u32x4 sx, sw;
    if ((id | wv) == 0) {
      const unsigned l = threadIdx.x;
      const u32x4 z = {0u, 0u, 0u, 0u};
      {
#pragma unroll
        for (int k = 0; k < 2; ++k)
          asm volatile("global_store_dwordx4 %0, %1, %2 sc1" :: "v"(32768 + (l & 7) * 16 + (l & 8) * 512 + (l & 16) * 1024 + (l & 32) * 32 + k * 8192), "v"(z), "s"(pb) : "memory");
      }
      {
        const unsigned so = l * (31u * 1024u + 16u) + (4u << 20);
        asm volatile("global_store_dwordx4 %2, %3, %4 sc1\n\tglobal_load_dwordx4 %0, %5, %6\n\tglobal_load_dwordx4 %1, %7, %8"
                     : "=&v"(sx), "=&v"(sw) : "v"(l * 16 + 1024), "v"(z), "s"(pb), "v"(so + (l & 3u) * (8u << 20)), "s"(x), "v"(so + (l & 1u) * (6u << 20)), "s"(w) : "memory");
      }
    }
    if (id) code_touch(cx, true);
    SB0;
    issue_x(0, wv, x_addr(cx.vb, 0, wv), x_rsrc(x, cx.vb), lds0);
    SB0;
    if ((id | wv) == 0) {
      wait_vm<9>();
      asm volatile("" : "+v"(sx), "+v"(sw));
      unsigned m = expo(amax(amax(amax(amax(0, sx[0]), sx[1]), sx[2]), sx[3])) | expo(amax(amax(amax(amax(0, sw[0]), sw[1]), sw[2]), sw[3])) << 16;
      m = amax(m, (unsigned)__builtin_amdgcn_update_dpp(0, (int)m, 0x128, 0xf, 0xf, false));
      m = amax(m, (unsigned)__builtin_amdgcn_update_dpp(0, (int)m, 0x124, 0xf, 0xf, false));
      m = amax(m, (unsigned)__builtin_amdgcn_update_dpp(0, (int)m, 0x122, 0xf, 0xf, false));
      m = amax(m, (unsigned)__builtin_amdgcn_update_dpp(0, (int)m, 0x121, 0xf, 0xf, false));
      unsigned ex = 28, ew = 28;
#pragma unroll
      for (int l = 0; l < 64; l += 16) {
        const unsigned v = (unsigned)__builtin_amdgcn_readlane((int)m, l);
        ex = max(ex + 2, v & 255u) - 2;
        ew = max(ew + 3, v >> 16) - 3;
      }
      const unsigned long long tg = __builtin_amdgcn_s_memrealtime();
      const unsigned lo = (unsigned)tg << 4 | xcc << 1, hi = ((unsigned)(tg >> 28) & 0xffffu) | min(ex, 220u) << 16 | min(ew, 220u) << 24;
      asm volatile("global_store_dwordx4 %0, %1, %2 sc1\n\ts_nop 1" :: "v"(threadIdx.x * 16), "v"(u32x4{lo, hi, ~lo, ~hi}), "s"(pb) : "memory");
      cx.tag[0] = lo;
      cx.tag[1] = hi;
    }
  }
  cx.smem = smem;
  cx.ws = ws;
  cx.gamma = gamma;
  cx.beta = beta;
  bf16x8 gb[16];
  if (wv < 4) run12<6>(cx, x, w, gb); else run12<5>(cx, x, w, gb);
  ws = cx.ws;
  const int bid = cx.vb, idx = bid >> 3;
  const int slot = (bid & 7) * 4 + (idx >> 3), b = (idx >> 2) & 1, nt = idx & 3;
  const int lane = (int)__builtin_amdgcn_mbcnt_hi(~0u, __builtin_amdgcn_mbcnt_lo(~0u, 0u));
  __builtin_amdgcn_s_barrier();
  gu32* fl = (gu32*)(ws + WS_GRP + nt * 256);
  if (wv == 1 && lane < 8) __hip_atomic_store(fl + (lane + (lane & 4)) * 256 + b * 32 + slot, 1u, RLX_AGENT);
  const int c64i = nt * 2 + ((bid >> 2) & 1), rg = (bid & 1) * 8 + (slot & 1) * 4 + (bid & 2) + (idx >> 4);
  const int hw = wv >> 2, wp = hw ? 7 - wv : wv, cg = c64i * 2 + hw;
  const int bs = 10 * rg;
  const int ntk = wp < 2 ? 3 : 2;
  const int g = lane >> 4, qq = (lane >> 2) & 3, pp = lane & 3, fr = lane & 15;
  const int ch0 = cg * 32 + 8 * g;
  const int fi = b * 32 + (lane & 31);
  const unsigned cnt = lds0 + 8 * 12288 + 3072 + hw * 8;
  if (wv == 2) asm volatile("ds_write_b32 %0, %1\n\ts_waitcnt lgkmcnt(0)" :: "v"(cnt + (lane & 3) * 4), "v"(0u) : "memory");
#pragma unroll
  for (int i = 12; i < 16; ++i)
    asm volatile("global_load_dwordx4 %0, %1, %2" : "=v"(gb[i]) : "v"(lane * 16), "s"(&TAB.G[0][0][0][0][0] + i * 512) : "memory");
  const int l0 = HOP * (bs + wp) + fr, le = (fr >> 1) < ntk ? HOP * (bs + wp + 4 * (fr >> 1)) + 48 + (fr & 1) : 1 << 20;
  const int lend = min(HOP * (bs + 10), L_);
  f32x4 nk, rk;
  float rn = 1.0f / (float)(lend - HOP * bs);
#pragma unroll
  for (int k = 0; k < 4; ++k) {
    nk[k] = (float)(2 * HOP + min(max(lend - HOP * (bs + k + 8), 0), HOP));
    rk[k] = 1.0f / nk[k];
  }
  asm volatile("" : "+v"(nk), "+v"(rk), "+v"(rn));
  f32x4 cin[2];
#pragma unroll
    for (int s = 0; s < 2; ++s) {
      cin[s] = *reinterpret_cast<const f32x4*>(smem + CBUF + ((wp + 4 * (fr >> 1)) & 15) * 256 + hw * 128 + g * 32 + s * 16);
#pragma unroll
      for (int r = 0; r < 4; ++r) cin[s][r] = fr & 1 ? __builtin_amdgcn_ldexpf(cin[s][r], cx.es) : 0.f;
    }
    asm volatile("" : "+v"(cin[0]), "+v"(cin[1]));
  SB0;
  if (wv == 0) spin_flags((const char*)(fl + ((bid & 7) + (bid & 4)) * 256 + (fi ^ (fi == b * 32 + slot))));
  __builtin_amdgcn_s_barrier();
  char* my = smem + wv * 12288;
  {
    const u32x4 rs = make_rsrc(ws + WS_Y, ROWS * 65536);
    const unsigned vo = (unsigned)((lane >> 2) * 64 + (((lane & 3) ^ ((lane >> 3) & 2)) << 4));
#pragma unroll
    for (int i = 0; i < 3; ++i) {
      const unsigned yr = (unsigned)(b * NBLK + bs + wp + 4 * i);
      const unsigned so = i < ntk ? (yr * 16 + (cg ^ ((yr >> 4) & 2))) * 4096 : 0xf0000000u;
#pragma unroll
      for (int j = 0; j < 4; ++j) DMA16("sc1", rs, lds0 + wv * 12288 + i * 4096 + j * 1024, vo, so + j * 1024);
    }
  }
  f32x4 y[24], npv[2];
  float s1[8], s2[8];
  {
    bf16x8 a[12];
    const char* src = my + (4 * g + qq) * 64 + ((g & 1) << 5) + pp * 8;
    auto rd = [&](int q) __attribute__((always_inline)) {
      const char* p = reinterpret_cast<const char*>(smem + (((unsigned)(src - smem)) ^ (unsigned)((q & 2) << 4)) + (q >> 2) * 4096 + (q & 1) * 2048);
      a[q] = cat(tr_read(p), tr_read(p + 1024));
    };
    auto sums = [&](int cp, int q) __attribute__((always_inline)) {
      const int e = (cp & 4) + q;
      if ((cp & 3) == 3) {
        if (cp >= 8) y[cp & 7][q] = (fr >> 1) == (cp >> 3) ? y[cp][q] : y[cp & 7][q];
        return;
      }
      float d = y[cp][q];
      if (cp >= 16 && l0 + 8 * HOP + 16 * (cp & 3) >= lend) d = 0.f;
      if (cp == 0 || cp == 4) { s1[e] = d; s2[e] = d * d; }
      else { s1[e] += d; s2[e] = __builtin_fmaf(d, d, s2[e]); }
      asm volatile("" : "+v"(s1[e]), "+v"(s2[e]));
    };
    wait_vm<8>();
    asm volatile("" : "+v"(gb[12]), "+v"(gb[13]), "+v"(gb[14]), "+v"(gb[15]));
    SB0;
    u32x2 pv[2];
#pragma unroll
    for (int s = 0; s < 2; ++s) pv[s] = *reinterpret_cast<const u32x2*>(my + s * 32 + g * 8);
    SB0;
#pragma unroll
    for (int q = 0; q < 4; ++q) { rd(q); SB0; }
#pragma unroll
    for (int s = 0; s < 2; ++s) {
      npv[s] = f32x4{bf_lo(pv[s][0]), bf_hi(pv[s][0]), bf_lo(pv[s][1]), bf_hi(pv[s][1])} * (-1.0f / 64.0f);
      cin[s] += npv[s];
    }
    SB0;
    auto chain = [&](auto cc) __attribute__((always_inline)) {
      constexpr int c = cc.v, is = c >> 2, tt = c & 3;
      if (c == 7) wait_vm<4>();
      if (c == 15) wait_vm<0>();
#pragma unroll
      for (int q = 0; q < 4; ++q) {
        if (c < 24) y[c % 24] = MFMA(a[(2 * is + (q >> 1)) % 12], gb[4 * tt + (q ^ 1)], q ? y[c % 24] : tt == 3 ? cin[is & 1] : npv[is & 1]);
        SB0;
        if (c == 7 || c == 15) rd((c == 7 ? 4 : 8) + q);
        if (c >= 2) sums(c - 2, q);
        SB0;
      }
    };
    sfor<0, 16>(chain);
    if (ntk == 3) {
      sfor<16, 26>(chain);
    } else {
#pragma unroll
      for (int q = 0; q < 8; ++q) sums(14 + (q >> 2), q & 3);
    }
#pragma unroll
    for (int e = 0; e < 8; ++e) {
      const float d = le < lend ? y[3 + (e & 4)][e & 3] : 0.f;
      s1[e] += d;
      s2[e] = __builtin_fmaf(d, d, s2[e]);
    }
  }
  float* red = reinterpret_cast<float*>(smem + 8 * 12288);
  {
    float v[8];
#pragma unroll
    for (int j = 0; j < 8; ++j) v[j] = bfly<0x140>(s1[j], s2[j], fr & 8);
#pragma unroll
    for (int j = 0; j < 4; ++j) v[j] = bfly<0x141>(v[j], v[j + 4], fr & 4);
#pragma unroll
    for (int j = 0; j < 2; ++j) v[j] = bfly<0x4E>(v[j], v[j + 2], fr & 2);
    red[((fr >> 3) * 8 + 2 * wp + hw) * 32 + 8 * g + (fr & 7)] = bfly<0xB1>(v[0], v[1], fr & 1);
    if (fr < 2) *reinterpret_cast<f32x4*>(red + (16 + 2 * wp + hw) * 32 + 8 * g + 4 * fr) = fr ? npv[1] : npv[0];
  }
  unsigned arr = 1u;
  if (lane == 0) asm volatile("ds_add_rtn_u32 %0, %1, %0\n\ts_waitcnt lgkmcnt(0)" : "+v"(arr) : "v"(cnt) : "memory");
  unsigned long long* stats = reinterpret_cast<unsigned long long*>(ws + WS_STATS);
  if (__builtin_amdgcn_readfirstlane(arr) == 3u && lane < 32) {
    const int tid = hw * 32 + lane;
    float mk[4], sm = 0.f, m2 = 0.f;
#pragma unroll
    for (int k = 0; k < 4; ++k) {
      const float* e = red + 64 * k + tid;
      const float dm = e[0] * rk[k];
      mk[k] = dm - e[512];
      m2 += fmaxf(e[256] - e[0] * dm, 0.f);
      sm += nk[k] * mk[k];
    }
    const float mean = sm * rn;
#pragma unroll
    for (int k = 0; k < 4; ++k) m2 += nk[k] * (mk[k] - mean) * (mk[k] - mean);
    unsigned long long pk = (unsigned long long)__float_as_uint(mean) | ((unsigned long long)__float_as_uint(m2) << 32);
    if (pk == ~0ull) pk = ~1ull;
    __hip_atomic_store((gu64*)(stats + (b * 16 + rg + (rg & 12) + (c64i & 1) * 4 + b * 16) * 256 + nt * 64 + tid), pk, RLX_AGENT);
  }
  const u32x4 gam = *reinterpret_cast<const u32x4*>(smem + GAM + hw * 64 + g * 16);
  const u32x4 bet = *reinterpret_cast<const u32x4*>(smem + GAM + 128 + hw * 64 + g * 16);
  f32x4 ga[2], be[2];
#pragma unroll
  for (int r = 0; r < 8; ++r) {
    ga[r >> 2][r & 3] = (r & 1) ? bf_hi(gam[r >> 1]) : bf_lo(gam[r >> 1]);
    be[r >> 2][r & 3] = (r & 1) ? bf_hi(bet[r >> 1]) : bf_lo(bet[r >> 1]);
  }
  const float eps = fmaxf(__builtin_amdgcn_ldexpf(1e-5f, 2 * cx.es), 1e-30f);
  float na = fr < 15 ? 500.f : 485.f;
  asm volatile("" : "+v"(ga[0]), "+v"(ga[1]), "+v"(be[0]), "+v"(be[1]), "+v"(na));
  unsigned long long* gl = reinterpret_cast<unsigned long long*>(smem + 8 * 12288 + 4096);
  {
    unsigned long long pk[2];
    const int row = 4 * wp + (lane >> 5), col = hw * 32 + (lane & 31);
    for (unsigned spins = 0;; ++spins) {
#pragma unroll
      for (int r = 0; r < 2; ++r)
        pk[r] = __hip_atomic_load((gu64*)(stats + (b * 32 + row + 2 * r + 4 * wp + (c64i & 1) * 4) * 256 + nt * 64 + col), RLX_AGENT);
      if (__all(pk[0] != ~0ull && pk[1] != ~0ull) || spins > (1u << 20)) break;
      __builtin_amdgcn_s_sleep(1);
    }
    gl[row * 66 + col] = pk[0];
    gl[(row + 2) * 66 + col] = pk[1];
    if (lane == 0) asm volatile("ds_add_u32 %0, %1" :: "v"(cnt + 4), "v"(1u) : "memory");
    unsigned c;
    do asm volatile("ds_read_b32 %0, %1\n\ts_waitcnt lgkmcnt(0)" : "=v"(c) : "v"(cnt + 4) : "memory");
    while (__builtin_amdgcn_readfirstlane(c) < 4u);
  }
  {
    u32x4 pk[4];
#pragma unroll
    for (int r = 0; r < 4; ++r)
      pk[r] = *reinterpret_cast<const u32x4*>(gl + fr * 66 + hw * 32 + 8 * g + 2 * r);
    float mu[8], vs[8];
#pragma unroll
    for (int r = 0; r < 8; ++r) mu[r] = na * __uint_as_float(pk[r >> 1][(r & 1) * 2]);
    row16_sum(mu);
#pragma unroll
    for (int r = 0; r < 8; ++r) {
      mu[r] *= 1.0f / (float)L_;
      const float da = __uint_as_float(pk[r >> 1][(r & 1) * 2]) - mu[r];
      vs[r] = __uint_as_float(pk[r >> 1][(r & 1) * 2 + 1]) + na * da * da;
    }
    row16_sum(vs);
#pragma unroll
    for (int r = 0; r < 8; ++r) {
      const float mean = mu[r], var = vs[r] * (1.0f / (float)L_);
      const float rstd = __builtin_amdgcn_rsqf(var + eps);
      const float gg = ga[r >> 2][r & 3] * rstd;
      ga[r >> 2][r & 3] = gg;
      be[r >> 2][r & 3] -= (mean + npv[r >> 2][r & 3]) * gg;
    }
  }
  {
    const float k1 = -2.0f * 0.7978845608028654f * 1.4426950408889634f, k2 = k1 * 0.044715f;
    f32x4 kk1 = {k1, k1, k1, k1}, kk2 = {k2, k2, k2, k2};
    asm volatile("" : "+v"(kk1), "+v"(kk2));
    const u32x4 ro = make_rsrc(out + (size_t)b * L_ * D_, lend * (D_ * 2));
    const unsigned oo = (unsigned)((l0 * D_ + ch0) * 2);
#pragma unroll
    for (int i = 0; i < 3; ++i)
      if (i < 2 || ntk == 3)
#pragma unroll
      for (int tt = 0; tt < 4; ++tt) {
        if (tt == 3 && i) break;
        f32x4 o[2];
#pragma unroll
        for (int s = 0; s < 2; ++s) {
          const f32x4 z = y[8 * i + 4 * s + tt] * ga[s] + be[s];
          const f32x4 a = z * (z * z * kk2 + kk1);
          f32x4 e;
#pragma unroll
          for (int r = 0; r < 4; ++r) e[r] = __builtin_amdgcn_exp2f(a[r]);
          e += 1.0f;
#pragma unroll
          for (int r = 0; r < 4; ++r) e[r] = __builtin_amdgcn_rcpf(e[r]);
          o[s] = z * e;
        }
        st16(pack8(o[0], o[1]), ro, tt < 3 ? oo : (unsigned)((le * D_ + ch0) * 2), tt < 3 ? (4 * HOP * i + 16 * tt) * (D_ * 2) : 0);
      }
  }
  asm volatile(".Lcf_b:\n\ts_nop 0" ::: "memory");
}

extern "C" void kernel_launch(void* const* d_in, const int* in_sizes, int n_in,
                              void* d_out, int out_size, void* d_ws, size_t ws_size,
                              hipStream_t stream) {
  const unsigned short* x = (const unsigned short*)d_in[0];
  const unsigned short* w = (const unsigned short*)d_in[1];
  const unsigned short* gamma = (const unsigned short*)d_in[2];
  const unsigned short* beta = (const unsigned short*)d_in[3];
  char* ws = (char*)(((uintptr_t)d_ws + (2u << 20) - 1) & ~(uintptr_t)((2u << 20) - 1));
  conv_fft<<<256, 512, 0, stream>>>(ws, x, w, gamma, beta, (unsigned short*)d_out);
}
```

```cpp
#include <hip/hip_runtime.h>
#include <stdint.h>

typedef __attribute__((ext_vector_type(8))) short bf16x8;
typedef __attribute__((ext_vector_type(4))) short s16x4;
typedef __attribute__((ext_vector_type(4))) float f32x4;
typedef __attribute__((ext_vector_type(2))) float f32x2;
typedef __attribute__((ext_vector_type(2))) __bf16 bf16x2v;
typedef __attribute__((ext_vector_type(4))) unsigned int u32x4;
typedef __attribute__((ext_vector_type(2))) unsigned int u32x2;
constexpr int NH = 6, NST = 3;
typedef __attribute__((ext_vector_type(2 * NH))) unsigned int u32xh;
typedef __attribute__((ext_vector_type(8))) _Float16 f16x8;
typedef __attribute__((ext_vector_type(2))) _Float16 f16x2;
typedef __attribute__((ext_vector_type(2))) unsigned short u16x2;
typedef __attribute__((address_space(1))) unsigned long long gu64;
typedef __attribute__((address_space(1))) unsigned gu32;
typedef __attribute__((address_space(3))) void lds_void;
typedef __attribute__((address_space(3))) s16x4 lds_s16x4;

constexpr int T_ = 16000, D_ = 512, L_ = 7985;
constexpr int HOP = 50, NBLK = 160, ROWS = 2 * NBLK;

constexpr unsigned WS_S = 0;
constexpr unsigned SBLK = 4u << 20;
constexpr unsigned sbase(unsigned sl) { return sl * 131072 + (sl >> 4) * 8192; }
constexpr unsigned FLG = 32u * SBLK;
constexpr unsigned WS_GRP = WS_S + FLG + 16 * 8192;
constexpr unsigned WS_D = WS_S + FLG + 20 * 8192;
constexpr unsigned WS_L = WS_D + ROWS * 1024;
constexpr unsigned WS_Y = 40u << 20;
constexpr unsigned WS_STATS = 129u << 20;
constexpr unsigned WS_FA = WS_STATS + (1u << 17);
constexpr unsigned WS_PUB = WS_STATS + (1u << 19);
static_assert(WS_L + D_ * D_ * 2 <= WS_STATS && FLG % (16 * 8192) == 0, "workspace layout");

#define RLX_AGENT __ATOMIC_RELAXED, __HIP_MEMORY_SCOPE_AGENT

constexpr double COS64[17] = {1.0, 0.99518472667219688624, 0.98078528040323044913, 0.95694033573220886494,
    0.92387953251128675613, 0.88192126434835502971, 0.83146961230254523708, 0.77301045336273696081,
    0.70710678118654752440, 0.63439328416364549822, 0.55557023301960222474, 0.47139673682599764856,
    0.38268343236508977173, 0.29028467725446236764, 0.19509032201612826785, 0.09801714032956060199, 0.0};
constexpr double c64(int k) { k &= 63; if (k > 32) k = 64 - k; return k <= 16 ? COS64[k] : -COS64[32 - k]; }
constexpr double s64(int k) { return c64(k + 48); }
constexpr unsigned short bf_rne(double v) {
  const unsigned u = __builtin_bit_cast(unsigned, (float)v);
  return (unsigned short)((u + 0x7fffu + ((u >> 16) & 1u)) >> 16);
}
constexpr double bf_val(unsigned short h) { return (double)__builtin_bit_cast(float, (unsigned)h << 16); }
constexpr unsigned short hilo(double v, int lo) { return lo ? bf_rne(v - bf_val(bf_rne(v))) : bf_rne(v); }
constexpr unsigned short nz(unsigned short h) { return h & 0x7fff ? h : (unsigned short)(97 << 7); }
constexpr double R3 = 0.86602540378443864676;
constexpr double dft(int c, int t, double sgn) {
  return (c >> 1) == 0 ? ((c & t) & 1 ? -1.0 : 1.0)
                       : ((c & 1) ? -0.5 * c64((c >> 1) * t) - R3 * sgn * s64((c >> 1) * t) : c64((c >> 1) * t));
}
constexpr double idft(int c, int t) {
  return (c >> 1) == 0 ? ((c & t) & 1 ? -1.0 : 1.0) / 64.0
                       : ((c & 1) ? -s64((c >> 1) * t) / R3 - c64((c >> 1) * t) * (2.0 / 3.0) : c64((c >> 1) * t) * (4.0 / 3.0)) / 32.0;
}
constexpr double UP = 1.0 + 0.72134752 / 256.0;
constexpr int kidx(int g, int j) { return j < 4 ? 4 * g + j : 12 + 4 * g + j; }
struct Tables {
  unsigned short F[4][2][2][64][8];
  unsigned short V[2][4][64][8];
  unsigned short G[4][2][2][64][8];
};
constexpr Tables make_tables() {
  Tables tb{};
  for (int l = 0; l < 64; ++l)
    for (int j = 0; j < 8; ++j) {
      const int g = l >> 4, n = l & 15;
      for (int ct = 0; ct < 4; ++ct) {
        for (int ks = 0; ks < 2; ++ks)
          for (int h = 0; h < 2; ++h) tb.F[ct][ks][h][l][j] = nz(hilo(UP * dft(16 * ct + n, 32 * ks + kidx(g, j), -1.0), h));
        for (int p = 0; p < 2; ++p) {
          const int tap = 4 * g + (j & 3);
          tb.V[p][ct][l][j] = (p == 1 && tap == 15) ? (unsigned short)0 : nz(hilo(UP * dft(16 * ct + n, tap, 1.0), j >> 2));
        }
      }
      for (int tt = 0; tt < 4; ++tt)
        for (int ks = 0; ks < 2; ++ks)
          for (int h = 0; h < 2; ++h) {
            const int c = 32 * ks + kidx(g, j);
            tb.G[tt][ks][h][l][j] = hilo(idft(c, 16 * tt + (tt < 3 ? n : n & 1)), h);
          }
    }
  return tb;
}
__device__ const __attribute__((aligned(16))) Tables TAB = make_tables();

__device__ __forceinline__ unsigned pk_bf16(float lo, float hi) {
  return __builtin_bit_cast(unsigned, __builtin_convertvector(f32x2{lo, hi}, bf16x2v));
}
__device__ __forceinline__ float bf_lo(unsigned v) { return __uint_as_float(v << 16); }
__device__ __forceinline__ float bf_hi(unsigned v) { return __uint_as_float(v & 0xffff0000u); }
__device__ __forceinline__ u32x4 pack8(f32x4 a, f32x4 b) {
  return u32x4{pk_bf16(a[0], a[1]), pk_bf16(a[2], a[3]), pk_bf16(b[0], b[1]), pk_bf16(b[2], b[3])};
}
__device__ __forceinline__ unsigned pk_f16(float lo, float hi) {
  return (__builtin_bit_cast(unsigned, __builtin_amdgcn_cvt_pkrtz(lo, hi)) & 0xfff8fff8u);
}
__device__ __forceinline__ u32x4 pack8h(f32x4 a, f32x4 b) {
  return u32x4{pk_f16(a[0], a[1]), pk_f16(a[2], a[3]), pk_f16(b[0], b[1]), pk_f16(b[2], b[3])};
}
__device__ __forceinline__ unsigned add_h(const bf16x8& a, const bf16x8& b, int d) {
  const unsigned x = __builtin_bit_cast(u32x4, a)[d], y = __builtin_bit_cast(u32x4, b)[d];
  unsigned r = __builtin_bit_cast(unsigned, __builtin_bit_cast(f16x2, x) + __builtin_bit_cast(f16x2, y));
  return r;
}
__device__ __forceinline__ unsigned amax(unsigned m, unsigned v) {
  return __builtin_bit_cast(unsigned, __builtin_elementwise_max(__builtin_bit_cast(u16x2, m), __builtin_bit_cast(u16x2, v & 0x7fff7fffu)));
}
struct Rng { f16x2 hi = {0, 0}, lo = {0, 0}; };
__device__ __forceinline__ void widen(Rng& r, unsigned a, unsigned b) {
  const f16x2 x = __builtin_bit_cast(f16x2, a), y = __builtin_bit_cast(f16x2, b);
  r.hi = __builtin_elementwise_maximum(__builtin_elementwise_maximum(r.hi, x), y);
  r.lo = __builtin_elementwise_minimum(__builtin_elementwise_minimum(r.lo, x), y);
}
__device__ __forceinline__ unsigned amax(const Rng& r) { return amax(amax(0, __builtin_bit_cast(unsigned, r.hi)), __builtin_bit_cast(unsigned, r.lo)); }
__device__ __forceinline__ unsigned expo(unsigned m) { return max(m & 0xffffu, m >> 16) >> 7; }
__device__ __forceinline__ unsigned wave_max(unsigned v) {
  unsigned m = 0;
#pragma unroll 1
  for (int l = 0; l < 64; ++l) m = max(m, (unsigned)__builtin_amdgcn_readlane((int)v, l));
  return m;
}
__device__ __forceinline__ void scale8(bf16x8& v, int e) {
  u32x4 u = __builtin_bit_cast(u32x4, v);
#pragma unroll
  for (int d = 0; d < 4; ++d)
    u[d] = __builtin_amdgcn_perm(__float_as_uint(__builtin_amdgcn_ldexpf(bf_hi(u[d]), e)), __float_as_uint(__builtin_amdgcn_ldexpf(bf_lo(u[d]), e)), 0x07060302u);
  v = __builtin_bit_cast(bf16x8, u);
}
__device__ __forceinline__ void restart(unsigned id) {
  asm volatile("s_waitcnt vmcnt(0)\n\ts_barrier\n\ts_branch conv_fft" :: "{s[0:1]}"(__builtin_amdgcn_kernarg_segment_ptr()), "{s2}"(id), "{v0}"(threadIdx.x) : "memory");
}
__device__ __forceinline__ s16x4 tr_read(const char* p) {
  return __builtin_amdgcn_ds_read_tr16_b64_v4i16((lds_s16x4*)p);
}
__device__ __forceinline__ bf16x8 cat(s16x4 a, s16x4 b) {
  const u32x2 x = __builtin_bit_cast(u32x2, a), y = __builtin_bit_cast(u32x2, b);
  return __builtin_bit_cast(bf16x8, u32x4{x[0], x[1], y[0], y[1]});
}
#define MFMA(a, b, c) __builtin_amdgcn_mfma_f32_16x16x32_bf16(a, b, c, 0, 0, 0)
#define MFMH(a, b, c) __builtin_amdgcn_mfma_f32_16x16x32_f16(__builtin_bit_cast(f16x8, a), __builtin_bit_cast(f16x8, b), c, 0, 0, 0)
#define SB0 __builtin_amdgcn_sched_barrier(0)
#define ZERO4 (f32x4{0.f, 0.f, 0.f, 0.f})
#define DMA16(POL, rs, lds, voff, soff) \
  asm volatile("s_mov_b32 m0, %0\n\ts_nop 0\n\tbuffer_load_dwordx4 %1, %2, %3 offen " POL " lds" \
               :: "s"(lds), "v"(voff), "s"(rs), "s"(soff) : "memory")
__device__ __forceinline__ void st16(const u32x4& v, const u32x4& rs, unsigned voff, unsigned soff) {
  asm volatile("buffer_store_dwordx4 %0, %1, %2, %3 offen sc1\n\ts_nop 1" :: "v"(v), "v"(voff), "s"(rs), "s"(soff) : "memory");
}
__device__ __forceinline__ void ld16(bf16x8& v, const void* p) {
  asm volatile("global_load_dwordx4 %0, %1, off" : "=v"(v) : "v"(p) : "memory");
}
template <int N> __device__ __forceinline__ void wait_vm() { asm volatile("s_waitcnt vmcnt(%0)" :: "i"(N) : "memory"); }
__device__ __forceinline__ u32x4 make_rsrc(const void* p, unsigned bytes) {
  const unsigned long long a = (unsigned long long)p;
  return u32x4{(unsigned)a, (unsigned)(a >> 32) & 0xffffu, bytes, 0x00020000u};
}
__device__ __forceinline__ void spin_flags(const char* p) {
  for (unsigned spins = 0; !__all(__hip_atomic_load((const gu32*)p, RLX_AGENT) != 0u) && spins < (1u << 21); ++spins)
    __builtin_amdgcn_s_sleep(3);
}

typedef __attribute__((ext_vector_type(8))) unsigned int u32x8;
typedef __attribute__((ext_vector_type(16))) unsigned int u32x16;
typedef __attribute__((ext_vector_type(32))) unsigned int u32x32;
constexpr int GW = 10;
constexpr int NQ1 = GW / 2, NQW = 16 - NQ1;
constexpr int STG = 36 * 1024;
constexpr int RAWB = 67 * 1024, XT1 = RAWB + NQ1 * 4096, SINK = XT1 + RAWB;
constexpr int RAWL = 3 * STG;
constexpr int RAW0 = RAWL + 12 * 4096;
constexpr int GAM = RAWL + 36864, CBUF = RAWL + 40960;

struct Ctx {

  char* smem;
  char* ws;
  const unsigned short *gamma, *beta;
  u32x4 rs;
  unsigned lds0;
  unsigned voffw, voffx;
  unsigned poff, xo;
  unsigned id;
  unsigned tag[2];
  int es;
  int wv, vb;
  unsigned long long pc;
  unsigned pfo;
};
__device__ __forceinline__ void code_touch(const Ctx& cx) {
  if ((cx.wv ^ (cx.vb >> 3)) & 3) return;
  const unsigned l = __builtin_amdgcn_mbcnt_hi(~0u, __builtin_amdgcn_mbcnt_lo(~0u, cx.wv * 64u));
#pragma unroll
  for (int k = 0; k < 2; ++k)
    asm volatile("s_mov_b32 m0, %0\n\ts_nop 0\n\tglobal_load_lds_dword %1, %2" :: "s"(SINK + cx.wv * 512 + k * 256), "v"(min((l + k * 256) * 128u, cx.pfo)), "s"(cx.pc) : "memory");
}
__device__ __forceinline__ void stage_issue(const Ctx& cx, int kc) {
  const unsigned slot = (unsigned)((kc + 1) % 3) * STG;
#pragma unroll
  for (int i = 0; i < 9; ++i) {
    const int j = i < 4 ? i : i - 4;
    const bool ahead = i < 4 || j == 0 || j == 3;
    const unsigned lo = i < 4 ? j * 4096 : j < 3 ? 16384 + j * 4096 : j == 3 ? 16384 + 10240 : 16384 + 14336;
    const unsigned go = i < 4 ? j * 4096 : (j >> 1) * 16384 + (j & 1) * 4096;
    if (kc >= 0 || ahead)
      DMA16("sc1", cx.rs, cx.lds0 + slot + lo + (i == 6 ? (cx.wv & 2) * 4096 : 0), i < 4 ? cx.voffw : cx.voffx, (unsigned)(kc + ahead) * SBLK + go);
  }
}
__device__ __forceinline__ unsigned x_place(unsigned kb, unsigned blk) { return kb + (blk + (blk & 192u)) * 128u; }
template <int V> struct IC { static constexpr int v = V; constexpr operator int() const { return V; } };
template <int A, int B, class F> __device__ __forceinline__ void sfor(F f) {
  if constexpr (A < B) { f(IC<A>{}); sfor<A + 1, B>(f); }
}
__device__ __forceinline__ unsigned dpp_swap1(unsigned v) {
  return (unsigned)__builtin_amdgcn_update_dpp(0, (int)v, 0xB1, 0xf, 0xf, false);
}

__device__ __forceinline__ int run_bs(int vb, int t) { return 5 * ((vb >> 4) + 16 * ((vb ^ t) & 1)); }
__device__ __forceinline__ int run_q(int vb, int t) { return (((vb >> 1) & 1) + ((vb >> 2) & 2) + 2 * t) & 3; }
__device__ __forceinline__ unsigned x_addr(int vb, int t, int wv) {
  const int lane = threadIdx.x & 63, q = run_q(vb, t);
  const unsigned c = (unsigned)((lane & 15) ^ (((4 * wv + (lane >> 4)) & 7) << 1));
  return (unsigned)(2 * (HOP * run_bs(vb, t) + 4 * wv + (lane >> 4)) + ((t ^ q ^ (q >> 1)) & 1)) * 1024u + c * 16 + q * 256;
}
__device__ __forceinline__ u32x4 x_rsrc(const unsigned short* x, int vb) { return make_rsrc(x + (size_t)((vb >> 2) & 1) * T_ * D_, T_ * D_ * 2); }
__device__ __forceinline__ void issue_x(int t, int wv, unsigned a, const u32x4& rx, unsigned lds0, int j0 = 0, int j1 = 17) {
#pragma unroll
  for (int jj = 0; jj < 17; ++jj)
    if (jj >= j0 && jj < j1 && (jj < 16 || wv < 3)) DMA16("nt", rx, lds0 + t * XT1 + wv * 1024 + jj * 4096, a, jj * 32768u);
}
__device__ __forceinline__ void mfma0(f32x4& d, const bf16x8& a, const bf16x8& b) {
  asm volatile("v_mfma_f32_16x16x32_bf16 %0, %1, %2, 0" : "=&v"(d) : "v"(a), "v"(b));
}
__device__ __forceinline__ void mfma1(f32x4& d, const bf16x8& a, const bf16x8& b) {
  asm volatile("v_mfma_f32_16x16x32_bf16 %0, %1, %2, %0" : "+v"(d) : "v"(a), "v"(b));
}
__device__ __forceinline__ bf16x8 dup(s16x4 a) {
  u32x2 lo = __builtin_bit_cast(u32x2, a), hi;
  asm volatile("v_mov_b64 %0, %1" : "=v"(hi) : "v"(lo));
  return __builtin_bit_cast(bf16x8, u32x4{lo[0], lo[1], hi[0], hi[1]});
}
__device__ __forceinline__ void scale8p(bf16x8& v, int e) {
  typedef __attribute__((ext_vector_type(8))) unsigned short u16x8;
  const unsigned short h = (unsigned short)(e << 7);
  v = __builtin_bit_cast(bf16x8, (u16x8)(__builtin_bit_cast(u16x8, v) + h));
}
struct XR { s16x4 t[8]; };
#define P0_ 10
#define NA_ 14
#define SX0_ P0_
#define SCN_ 12
#define PG_ 40
#define NFA_ 2
constexpr int P0 = P0_, P1 = 46;
constexpr int PG = PG_, NFA = NFA_;
constexpr int TE = 8;
constexpr int NIT = 46, NS = 2 * (30 - NH - NST), NA = NA_, NB = 26;
constexpr int ord(int p) { return p == 0 ? 0 : p < 6 ? p + 2 : p < 8 ? p - 5 : p; }
constexpr int st_w(int j) { return j < 2 ? j : j < 7 ? -1 : j < 15 ? j - 5 : -1; }
constexpr int st_x(int j) { return j < 7 ? j - 2 : j - 10; }
constexpr int st_pos(int j) { return st_w(j) >= 0 ? (j < 9 ? 0 : j < 13 ? 6 : 7) : j < 7 ? j - 1 : j - 7; }
constexpr int st_first(int h) {
  int J = h <= P0 ? 0 : h >= P1 ? NS : (h - P0) * NS / (P1 - P0);
  for (int k = 0; k < J; ++k)
    if (2 * st_pos(k >> 1) + (k & 1) + 2 > h - 1) return k;
  return J;
}
constexpr int SCN = SCN_;
constexpr int sc_pos(int k) {
  constexpr int s1 = SX0_ > P0 + 1 ? SX0_ : P0 + 1;
  return k < 17 ? k * SX0_ / 17 : s1 + (k - 17) * (P0 + 1 + SCN - s1) / 17;
}
constexpr int sc_raw(int k) { return k < NQ1 ? k : P0 + 1 + (k - NQ1) * SCN / NQW; }
constexpr int sc_cnt(int h) { int c = 0; for (int k = 0; k < 34; ++k) c += sc_pos(k) == h; return c; }
constexpr bool sc_ok() { for (int h = 0; h < NIT; ++h) if (sc_cnt(h) > 2) return false; return true; }
constexpr int XBAR = st_first(25) > NA + NFA - 1 ? -1 : 24;
static_assert(sc_ok() && st_first(P0 + 1 + SCN) <= NA + NFA - 1 && SX0_ <= P0 + 1 + SCN, "range check before the first hand-off");
constexpr int MV0 = P0 + 1 + SCN + 1;
struct Hand {
  u32x4 held[2 * NH], st[2 * NST], raw[NQW - 1];
  bf16x8 vb;
  unsigned hst, pwr, prd;
};
__device__ __forceinline__ void phase1(Ctx& cx, const unsigned short* __restrict__ x, const unsigned short* __restrict__ w, Hand& hd) {
  u32x4 rq[NQW];
  static_assert(NQ1 == 5, "phase-1 wait counts assume five quanta");
  char* smem = cx.smem;
  const unsigned lds0 = (unsigned)(uintptr_t)(lds_void*)smem;
  const int idx = cx.vb >> 3, wv = cx.wv, lane = threadIdx.x & 63;
  const u32x4 rw = make_rsrc(w, 31 * D_ * D_ * 2);
  const int tap = threadIdx.x >> 4, pc = threadIdx.x & 15;
  const unsigned roff = (unsigned)(tap * (2 * D_ * D_ * 2) + pc * 16);
  const int ng = wv;
  const int g = lane >> 4, qq = (lane >> 2) & 3, pp = lane & 3;
  u32x4 r1[NQ1];
  bf16x8 fb[2][2];
  unsigned long long tag;
  const char* rec = cx.ws + WS_PUB + ((cx.id & 7) * 8 + wv) * 16;
  u32x4 pr, wl;
  unsigned pwr, prd, pvo, pww, xb0[4], pq[4], xa[2];
  {
    const int vb = cx.vb;
    const int kr = (vb >> 7) * 32 + (vb & 4) * 4 + (idx & 15);
    auto raw_load = [&](u32x4& r, int q) {
      const unsigned so = (unsigned)(kr + 64 * (2 * (q & 3) + ((q >> 2) & 1)) + 512 * (((q >> 3) ^ q ^ (q >> 1)) & 1)) * 1024u + (unsigned)((vb + q) & 3) * 256u;
      asm volatile("buffer_load_dwordx4 %0, %1, %2, %3 offen nt" : "=v"(r) : "v"(roff), "s"(rw), "s"(so) : "memory");
    };
#pragma unroll
    for (int k = 0; k < NQ1; ++k) raw_load(r1[k], k);
    const unsigned xa1 = x_addr(vb, 1, wv);
    if (cx.id | wv) issue_x(1, wv, xa1, x_rsrc(x, vb), lds0, 0, TE);
    if (cx.id == 0 && wv) {
#pragma unroll
      for (int k = 0; k < 6; ++k)
        if (k < 5 || wv < 3) DMA16("nt", x_rsrc(x, vb), lds0 + XT1 + (3 * k + wv - 1) * 4096, x_addr(vb, 1, 0), (3 * k + wv - 1) * 32768u);
    }
    if ((cx.id | wv) == 0) {
      unsigned g[2] = {~0u, ~0u};
#pragma unroll 1
      for (int k = -1; k < 16 && ((k & 1) || min(g[0], g[1]) > 440u); ++k) {
        unsigned long long t0, t1;
        unsigned d;
        asm volatile("s_memtime %0\n\ts_waitcnt lgkmcnt(0)\n\ts_load_dword %2, %3, 0x0\n\ts_waitcnt lgkmcnt(0)\n\ts_memtime %1\n\ts_waitcnt lgkmcnt(0)"
                     : "=&s"(t0), "=&s"(t1), "=&s"(d) : "s"(rec + 32768 + (k < 0 ? 128 : (k & 1) * 8192 + (k & 2) * 2048 + (k & 4) * 4096 + (k & 8) * 128)) : "memory");
        if (k >= 0) { if (k & 1) g[1] = min(g[1], (unsigned)(t1 - t0)); else g[0] = min(g[0], (unsigned)(t1 - t0)); }
      }
      const unsigned lo = cx.tag[0] | (((g[0] < g[1] ? 0u : 1u) ^ (cx.vb >> 2)) & 1u), hi = cx.tag[1];
      asm volatile("global_store_dwordx4 %0, %1, %2 sc1\n\ts_nop 1" :: "v"(lane * 16 + 2048), "v"(u32x4{lo, hi, lo, hi}), "s"(rec) : "memory");
    }
    {
#pragma unroll
      for (int i = 0; i < 4; ++i) ld16(fb[i >> 1][i & 1], TAB.F[ng][i >> 1][i & 1][lane]);
      ld16(hd.vb, TAB.V[0][ng][lane]);
      asm volatile("global_load_dwordx4 %0, %1, %2 sc1" : "=v"(pr) : "v"(0u), "s"(rec) : "memory");
    }
    const unsigned lo_ = wv >= 2 ? (threadIdx.x & 127) * 16 : 0x80000000u;
    asm volatile("buffer_load_dwordx4 %0, %1, %2, %3 offen nt" : "=a"(wl) : "v"(lo_), "s"(rw), "s"(30 * D_ * D_ * 2 + vb * 2048) : "memory");
    if (cx.id | wv) issue_x(1, wv, xa1, x_rsrc(x, vb), lds0, TE, 17);
#pragma unroll
    for (int k = 0; k < NQW; ++k) raw_load(rq[k], NQ1 + k);
    {
      const int bid = vb, xb = (bid >> 2) & 1, krow = kr;
      const int slot = (bid & 7) * 4 + (idx >> 3), b = (idx >> 2) & 1, nt = idx & 3;
      const int rt = 4 * g + qq;
      pvo = (unsigned)((bid & 7) * 1024 + wv * 256 + lane * 4);
      pwr = (unsigned)((pc >> 3) * 2048 + tap * 128 + ((((pc >> 1) ^ (tap >> 1)) & 3) << 5) + (pc & 1) * 16);
      prd = (unsigned)(rt * 128 + (((rt >> 1) & 3) << 5) + pp * 8);
      cx.poff = (unsigned)(sbase(8 * ng + ((lane >> 1) & 7)) + (krow >> 5) * SBLK + (krow & 31) * 512 + (lane & 1) * 64 + g * 16);
      cx.xo = (unsigned)(sbase(8 * ng + ((lane >> 1) & 7)) + 65536 + xb * 8192 + (lane & 1) * 64 + g * 16);
      cx.lds0 = lds0 + wv * 1024;
#pragma unroll
      for (int k = 0; k < 4; ++k) {
        xb0[k] = (unsigned)((4 * g + qq) * 256 + (((4 * g + qq + 2 * k) & 7) << 5) + pp * 8);
        pq[k] = (prd + RAWB) ^ (unsigned)(k << 5);
      }
      pww = pwr + RAWB;
      asm volatile("" : "+v"(xb0[0]), "+v"(xb0[1]), "+v"(xb0[2]), "+v"(xb0[3]), "+v"(pq[0]), "+v"(pq[1]), "+v"(pq[2]), "+v"(pq[3]), "+v"(pww));
      {
        const int row = 2 * wv + (lane >> 5), p = ((lane & 31) >> 1) ^ (row & 7);
        const int chunk = ((p >> 2) & 1) * 8 + ((p >> 1) & 1) * 4 + ((p >> 3) & 1) * 2 + (p & 1);
        cx.voffw = (unsigned)(sbase(slot) + (nt * 32 + row) * 512 + chunk * 32 + (lane & 1) * 16);
        cx.voffx = (unsigned)(sbase(slot) + 65536 + b * 8192 + (8 * wv + ((lane >> 2) & 7)) * 128 + (lane >> 5) * 64 + (((lane & 3) ^ ((wv & 1) << 1)) << 4));
      }
    }
    asm volatile("" : "+v"(pvo), "+v"(cx.poff), "+v"(cx.xo), "+v"(cx.voffw), "+v"(cx.voffx));
  }
  Rng gx, gw;
  if ((cx.id | wv) == 0) wait_vm<NQW>();
  wait_vm<16 + 6 + NQW>();
  unsigned near;
#pragma unroll
  for (int q = 0; q < NQ1; ++q) {
    asm volatile("" : "+v"(r1[q]));
    *reinterpret_cast<u32x4*>(smem + (pww + q * 4096)) = r1[q];
  }
  SB0;
  {
    wait_vm<NQW + 16 - TE>();
    unsigned d0, d1;
    for (unsigned spins = 0;; ++spins) {
      asm volatile("" : "+v"(pr));
      d0 = __builtin_amdgcn_readfirstlane(pr[0]);
      d1 = __builtin_amdgcn_readfirstlane(pr[1]);
      const unsigned d2 = __builtin_amdgcn_readfirstlane(pr[2]), d3 = __builtin_amdgcn_readfirstlane(pr[3]);
      if (__builtin_expect((d0 == ~d2 && d1 == ~d3) || spins > (1u << 21), 1)) break;
      __builtin_amdgcn_s_sleep(2);
      asm volatile("global_load_dwordx4 %0, %1, %2 sc1\n\ts_waitcnt vmcnt(0)" : "=v"(pr) : "v"(0u), "s"(rec) : "memory");
    }
    tag = d0 | (unsigned long long)d1 << 32;
    const unsigned vn = (cx.id + (d0 >> 1)) & 7u;
    if (__builtin_expect(vn != (cx.vb & 7u), 0)) restart(cx.id | 256u | vn << 10);
  }
  const unsigned ee = (cx.id & 512u ? cx.id : (unsigned)(tag >> 32)) >> 16, ex = ee & 255u, ew = ee >> 8;
  cx.es = 256 - (int)(ex + ew);
  char* ws;
  const int bid = cx.vb;
  bf16x8& vb = hd.vb;
  u32x4 f0, f1;
  const u32x4* fa;
  u32x2 ov;
  unsigned pollv;
  const int rb[2] = {run_bs(bid, 0), run_bs(bid, 1)};
  asm volatile("" : "+v"(fb[0][0]), "+v"(fb[0][1]), "+v"(fb[1][0]), "+v"(fb[1][1]), "+v"(vb));
  scale8p(vb, 128 - (int)ew);
#pragma unroll
  for (int i = 0; i < 4; ++i) scale8p(fb[i >> 1][i & 1], 128 - (int)ex);
  asm volatile("" : "+v"(fb[0][0]), "+v"(fb[0][1]), "+v"(fb[1][0]), "+v"(fb[1][1]), "+v"(vb));
  asm volatile("s_waitcnt lgkmcnt(0)" ::: "memory");
  asm volatile("global_load_dwordx2 %0, %1, %2 sc1" : "=v"(ov) : "v"(2048u), "s"(rec) : "memory");
  __builtin_amdgcn_s_barrier();
  SB0;
  u32x4 hw[2][5][2], hx[2][20];
  f32x4 pend[2];
  auto rd = [&](int h, int i) __attribute__((always_inline)) {
    const int it = ord(h >> 1), mg = h & 1;
    if (it < 3) return tr_read(smem + (pq[i & 3] + mg * 2048 + (2 * it + (i >> 2)) * 4096));
    const int n = it - 3;
    const int c = 4 * ((n / 5) & 1) + 2 * mg + (i & 1);
    return tr_read(smem + ((c ? xa[i & 1] : xb0[n % 5 & 3]) + n % 5 * (HOP * 256) + (i >> 1) * (16 * 256)));
  };
  auto xaddr = [&](int h) __attribute__((always_inline)) {
    const int n = ord(h >> 1) - 3, mg = h & 1;
    if (h >= NIT || n < 0) return;
    if (h == 26) {
#pragma unroll
      for (int k = 0; k < 4; ++k) xb0[k] += XT1;
      asm volatile("" : "+v"(xb0[0]), "+v"(xb0[1]), "+v"(xb0[2]), "+v"(xb0[3]));
    }
#pragma unroll
    for (int i = 0; i < 2; ++i) {
      const int c = 4 * ((n / 5) & 1) + 2 * mg + i;
      if (c) { xa[i] = xb0[n % 5 & 3] ^ (unsigned)(c << 5); asm volatile("" : "+v"(xa[i])); }
    }
  };
  auto dst = [&](int h) -> u32x4& { const int it = ord(h >> 1); return it < 3 ? hw[h & 1][it == 2 ? 4 : 2 * it + 1][1] : hx[h & 1][it - 3]; };
  auto item = [&](int h, XR& xr, auto side) __attribute__((always_inline)) {
    const XR c = xr;
    const int it = ord(h >> 1), mg = h & 1;
    const int nm = it == 2 ? 4 : 8, nr = h + 1 < NIT && ord((h + 1) >> 1) == 2 ? 4 : 8;
    const int pn = h > 0 && ord((h - 1) >> 1) == 2 ? 4 : 8;
    f32x4 r[8];
    bf16x8 dq[3];
    if (it < 3) { dq[0] = dup(c.t[0]); dq[1] = dup(c.t[1]); asm volatile("s_nop 0"); }
#pragma unroll
    for (int i = 0; i < 8; ++i) {
      if (i >= nm) break;
      const int s2 = i & 1, ks = (i >> 1) & 1;
      if (it < 3) mfma0(r[i], dq[i % 3], vb);
      else if (i < 2) mfma0(r[6 + i], cat(c.t[4 * ks + s2], c.t[4 * ks + 2 + s2]), fb[ks][1]);
      else mfma1(r[6 + s2], cat(c.t[4 * ks + s2], c.t[4 * ks + 2 + s2]), fb[ks][i < 4]);
      SB0;
      if (it < 3 && i + 2 < nm) dq[(i + 2) % 3] = dup(c.t[i + 2]);
      if (h + 1 < NIT && i < 4) {
        const bool nq = ord((h + 1) >> 1) < 3;
#pragma unroll
        for (int u = 0; u < 2; ++u) {
          const int k = 2 * i + u, j = nq ? k : (k & 4) + ((k >> 1) & 1) + (k & 1) * 2;
          if (j < nr) xr.t[j] = rd(h + 1, j);
        }
      }
      if (i == (nm == 4 ? 3 : 4)) xaddr(h + 2);
      if (h > 0 && (nm == 4 ? i >= 2 : i >= 4)) {
        u32x4& d = dst(h - 1);
#pragma unroll
        for (int e = 0; e < 4; ++e)
          if ((nm == 4 ? 2 + (e >> 1) : 4 + e) == i) d[e] = pk_f16(pend[e >> 1][(e & 1) * 2], pend[e >> 1][(e & 1) * 2 + 1]);
        if (i == nm - 1) asm volatile("" : "+v"(d));
      }
      if (it < 3 && i >= 3 && (i & 1)) {
        u32x4& d = hw[mg][2 * it + ((i - 3) >> 2)][((i - 3) >> 1) & 1];
        d = pack8h(r[i - 3], r[i - 2]);
        asm volatile("" : "+v"(d));
      }
      side(i * (8 / nm));
      if (nm == 4) side(2 * i + 1);
      SB0;
    }
    (void)pn;
    pend[0] = r[it < 3 ? nm - 2 : 6];
    pend[1] = r[it < 3 ? nm - 1 : 7];
  };
  const unsigned so0 = (unsigned)(2 * run_q(bid, 0)) * SBLK, so1 = (unsigned)(16 + 2 * run_q(bid, 1)) * SBLK;
  auto store = [&](int J) __attribute__((always_inline)) {
    const int j = J >> 1, mg = J & 1;
    if (J == 0) {
      wait_vm<0>();
      if (cx.id == 0) __builtin_amdgcn_s_barrier();
      for (unsigned spins = 0;; ++spins) {
        asm volatile("" : "+v"(ov));
        near = __builtin_amdgcn_readfirstlane(ov[0]) ^ (unsigned)tag;
        if (__builtin_expect((near < 2u && __builtin_amdgcn_readfirstlane(ov[1]) == (unsigned)(tag >> 32)) || spins > (1u << 21), 1)) break;
        __builtin_amdgcn_s_sleep(2);
        asm volatile("global_load_dwordx2 %0, %1, %2 sc1\n\ts_waitcnt vmcnt(0)" : "=v"(ov) : "v"(2048u), "s"(rec) : "memory");
      }
      ws = cx.ws += (near & 1u) * 8192;
      cx.rs = make_rsrc(ws + WS_S, WS_L - WS_S + D_ * D_ * 2);
    }
    if (J == 1) {
      if (wv == 1) {
        const unsigned o = (lane < 32 ? WS_STATS : WS_S + FLG + 16384 - 512) + bid * 512 + lane * 16;
        const unsigned v = lane < 32 ? ~0u : 0u;
        asm volatile("global_store_dwordx4 %0, %1, %2 sc1\n\ts_nop 1" :: "v"(o), "v"(u32x4{v, v, v, v}), "s"(ws) : "memory");
        if (lane < 8) __hip_atomic_store((gu32*)(ws + WS_S + FLG + lane * 1024 + ((bid + 2) & 3) * 256 + (bid >> 2) * 4), 0u, RLX_AGENT);
      }
    }
    if (J == NB + NFA) {
      if (((bid >> 1) ^ (bid >> 3) ^ 1) & 1 && threadIdx.x < 160) {
        const int t = threadIdx.x >= 80, blk = (threadIdx.x >> 4) - 5 * t;
        const unsigned o = (unsigned)(t * XT1 + blk * (HOP * 256) + (((lane & 15) ^ ((blk & 3) << 2)) << 4));
        const u32x4 lo = *reinterpret_cast<const u32x4*>(smem + o), hi = *reinterpret_cast<const u32x4*>(smem + o + 64 * 256);
        u32x4 d;
#pragma unroll
        for (int e = 0; e < 4; ++e) d[e] = pk_bf16(bf_lo(hi[e]) - bf_lo(lo[e]), bf_hi(hi[e]) - bf_hi(lo[e]));
        st16(d, cx.rs, (unsigned)((((bid >> 2) & 1) * NBLK + (t ? rb[1] : rb[0]) + blk) * 1024 + run_q(bid, t) * 256 + (lane & 15) * 16), WS_D - WS_S);
      }
      asm volatile("buffer_store_dwordx4 %0, %1, %2, %3 offen sc1" :: "a"(wl), "v"(wv >= 2 ? (threadIdx.x & 127) * 16 : 0x80000000u), "s"(cx.rs), "s"(WS_L - WS_S + bid * 2048) : "memory");
    }
    const int wq = st_w(j), xn = st_x(j);
    if (wq >= 0) st16(hw[mg][wq >> 1][wq & 1], cx.rs, cx.poff, (unsigned)(wq >> 1) * (2 * SBLK) + (unsigned)((bid + (wq >> 1)) & 3) * 16384u + (wq & 1) * 128u + mg * 256u);
    else st16(hx[mg][xn], cx.rs, cx.xo, x_place((xn < 10 ? so0 : so1) + ((xn / 5) & 1) * (8 * SBLK) + mg * SBLK, (unsigned)(rb[xn / 10] + xn % 5)));
    if (J == NA + NFA - 1) {
      const unsigned mx = amax(gx), mw = amax(gw);
      if (__builtin_expect(__any(expo(mx) > ex + 7 || expo(mw) > ew + 9) && !(cx.id & 512u), 0)) {
        const u32x2 m = {wave_max(expo(mx)), wave_max(expo(mw))};
        asm volatile("global_atomic_umax %0, %1, %3 sc1\n\tglobal_atomic_umax %0, %2, %3 offset:4 sc1\n\ts_waitcnt vmcnt(0)"
                     :: "v"(lane * 16 + 1024), "v"(m[0]), "v"(m[1]), "s"(rec - ((cx.id & 7) * 8 + wv) * 16) : "memory");
      }
    }
    if (J == NA + NFA - 1 || J == NB + NFA - 1) {
      if (J < NB) {
      } else {
      }
      wait_vm<NFA>();
      if (J < NB) {
      } else {
      }
      __builtin_amdgcn_s_barrier();
      if (J < NB) {
      } else {
      }
      if (wv == 0 && lane < 8) {
        if (J < NB) __hip_atomic_store((gu64*)(ws + WS_FA + (cx.id & 512u) * 32 + lane * 2048) + bid, tag, RLX_AGENT);
        else __hip_atomic_store((gu32*)(ws + WS_S + FLG + lane * 1024 + ((bid + 2) & 3) * 256 + (bid >> 2) * 4), 1u, RLX_AGENT);
      }
    }
    SB0;
  };
  u32x4 sv[2];
  auto check = [&](int h, int s) __attribute__((always_inline)) {
#pragma unroll
    for (int k = 0; k < 34; ++k) {
      if (sc_pos(k) != h) continue;
      int o = 0;
#pragma unroll
      for (int k2 = 0; k2 < k; ++k2) o += sc_pos(k2) == h;
      const int t = k / 17, j = k % 17;
      if (s == o) sv[o] = *reinterpret_cast<const u32x4*>(smem + (t * XT1 + (j < 16 || wv < 3 ? j : 15) * 4096 + threadIdx.x * 16));
      if (s == 6 + o) {
        widen(gx, sv[o][0], sv[o][1]);
        widen(gx, sv[o][2], sv[o][3]);
      }
    }
#pragma unroll
    for (int k = 0; k < 16; ++k) {
      if (s != 5 || sc_raw(k) != h) continue;
      u32x4& r = k < NQ1 ? r1[k] : rq[k - NQ1];
      asm volatile("" : "+v"(r));
      widen(gw, r[0], r[1]);
      widen(gw, r[2], r[3]);
    }
    if (s == 5 && h >= MV0 && h < MV0 + NQW - 1) asm volatile("" : "+a"(rq[h - MV0 + 1]));
    if (s == 1 && h <= P0 + 2 + SCN) asm volatile("" : "+v"(gx.hi), "+v"(gx.lo), "+v"(gw.hi), "+v"(gw.lo));
  };
  XR xr;
#pragma unroll
  for (int j = 0; j < 8; ++j) xr.t[j] = rd(0, j);
  static_assert(ord(0) < 3 && ord(1) >= 3 && ord(0) != 2, "positions 0, 1: weights");
  SB0;
  static_assert(st_first(NIT) == NS && PG >= 28 && st_first(PG - 4) > NB + NFA && NA + NFA <= NB && NFA >= 1 && NH <= 7, "order of the hand-off steps");
  sfor<0, NIT>([&](auto p) __attribute__((always_inline)) {
    constexpr int j0 = st_first(p.v), j1 = st_first(p.v + 1);
    if (p >= 24 && p < PG && !(p & 1)) { if (!(bid & 1) && cx.id) __builtin_amdgcn_s_sleep(6); }
    if (p == XBAR) __builtin_amdgcn_s_barrier();
    if (p == PG - 4) {
      code_touch(cx);
      fa = reinterpret_cast<const u32x4*>(ws + WS_FA + (cx.id & 512u) * 32 + (bid & 7) * 2048) + lane;
      if (wv == 0)
        asm volatile("global_load_dwordx4 %0, %2, off sc1\n\tglobal_load_dwordx4 %1, %2, off offset:1024 sc1" : "=&v"(f0), "=&v"(f1) : "v"(fa) : "memory");
    }
    if (p == PG) {
      if (wv == 0) {
        wait_vm<st_first(PG) - st_first(PG - 4)>();
        for (unsigned spins = 0; spins < (1u << 21); ++spins) {
          asm volatile("" : "+v"(f0), "+v"(f1));
          const unsigned t0 = (unsigned)tag, t1 = (unsigned)(tag >> 32);
          if (__all(f0[0] == t0 && f0[1] == t1 && f0[2] == t0 && f0[3] == t1 && f1[0] == t0 && f1[1] == t1 && f1[2] == t0 && f1[3] == t1)) break;
          __builtin_amdgcn_s_sleep(2);
          asm volatile("global_load_dwordx4 %0, %2, off sc1\n\tglobal_load_dwordx4 %1, %2, off offset:1024 sc1\n\ts_waitcnt vmcnt(0)"
                       : "=&v"(f0), "=&v"(f1) : "v"(fa) : "memory");
        }
      }
      __builtin_amdgcn_s_barrier();
      stage_issue(cx, -1);
      asm volatile("global_load_dwordx2 %0, %1, %2 sc1" : "=v"(ov) : "v"(1024u), "s"(rec) : "memory");
      stage_issue(cx, 0);
      SB0;
    }
    if (p == NIT - 2)
      asm volatile("buffer_load_dword %0, %1, %2, %3 offen sc1" : "=v"(pollv) : "v"(pvo), "s"(cx.rs), "s"(FLG) : "memory");
    item(p, xr, [&](int i) __attribute__((always_inline)) {
      if ((i * (j1 - j0) + 7) / 8 < ((i + 1) * (j1 - j0) + 7) / 8) store(j0 + (i * (j1 - j0) + 7) / 8);
      check(p, i);
    });
  });
  hx[1][19] = pack8h(pend[0], pend[1]);
#pragma unroll
  for (int e = 0; e < 2 * NH; ++e) hd.held[e] = hx[e & 1][20 - NH + (e >> 1)];
#pragma unroll
  for (int e = 0; e < 2 * NST; ++e) hd.st[e] = hx[e & 1][20 - NH - NST + (e >> 1)];
  {
    const int e = lane & 31, n = (lane < 32 ? 20 - NH : 20 - NH - NST) + (e >> 1);
    hd.hst = e < (lane < 32 ? 2 * NH : 2 * NST) ? x_place(so1 + (n >= 15 ? 8 * SBLK : 0) + (e & 1) * SBLK, (unsigned)(rb[1] + n % 5)) : 0xf0000000u;
  }
  *reinterpret_cast<u32x4*>(smem + RAW0 + pwr) = rq[0];
#pragma unroll
  for (int k = 1; k < NQW; ++k) hd.raw[k - 1] = rq[k];
  hd.pwr = RAWL + pwr;
  hd.prd = RAW0 + prd;
  wait_vm<NS - st_first(NIT - 2)>();
  {
    asm volatile("" : "+v"(ov));
    const unsigned ox = __builtin_amdgcn_readfirstlane(ov[0]), ow = __builtin_amdgcn_readfirstlane(ov[1]);
    if (__builtin_expect((ox | ow) && !(cx.id & 512u), 0))
      restart((cx.id & 255u) | 768u | (bid & 7u) << 10 | min(max(ex + 7, ox) - 7, 220u) << 16 | min(max(ew + 9, ow) - 9, 220u) << 24);
    if ((cx.id & 255u) == 0 && wv == 0)
      asm volatile("global_store_dwordx4 %0, %1, %2 sc1\n\ts_nop 1" :: "v"(lane * 16), "v"(u32x4{0u, 0u, 0u, 0u}), "s"(rec) : "memory");
  }
  asm volatile("" : "+v"(pollv));
  if (!__all(pollv != 0u)) spin_flags(ws + WS_S + FLG + pvo);
  asm volatile("s_waitcnt lgkmcnt(0)" ::: "memory");
  __builtin_amdgcn_s_barrier();
  SB0;
}

__device__ __forceinline__ void run12(Ctx& cx, const unsigned short* __restrict__ x, const unsigned short* __restrict__ w) {
  Hand hd;
  phase1(cx, x, w, hd);
  const int bid = cx.vb, idx = bid >> 3, wv = cx.wv, lane = threadIdx.x & 63;
  const int slot = (bid & 7) * 4 + (idx >> 3), b = (idx >> 2) & 1, nt = idx & 3, xb = (bid >> 2) & 1;
  const int mg = wv >> 1, nh = wv & 1, g = lane >> 4, qq = (lane >> 2) & 3, pp = lane & 3, rt = 4 * g + qq;
  const int rg = (bid & 1) * 8 + (slot & 1) * 4 + (bid & 2) + (idx >> 4);
  u32x16 vA, sA, sB, sC, h1, st0;
  u32x8 vB, raw1, st1;
  u32x32 h0, raw0;
  u32x2 vC, sD;
#pragma unroll
  for (int c = 0; c < 4; ++c) {
    vA[c] = (unsigned)(rt * 512 + (((nh * 4 + c) ^ (rt & 7)) << 5) + pp * 8);
    vA[5 + c] = hd.prd ^ (unsigned)(c << 5);
  }
  { const int r = lane & 15; vA[4] = (unsigned)(16384 + mg * 5 * 2048 + (r >> 3) * 1024 + (r & 7) * 64 + ((g ^ ((r >> 3) << 1)) << 4)); }
  vA[9] = cx.voffw;
  vA[10] = cx.voffx;
  {
    const int kr8 = lane >> 3;
    vA[11] = (unsigned)(WS_L - WS_S + (8 * wv + kr8) * 1024 + (nt * 2 + xb) * 128 + (((lane & 7) ^ ((kr8 & 2) | (wv & 1) << 2)) << 4));
  }
  vA[12] = (unsigned)(WS_D - WS_S + (b * NBLK + 10 * rg + min(lane & 15, 9)) * 1024 + g * 16);
  vA[13] = (unsigned)(RAWL + (8 * g + qq) * 128 + ((wv ^ ((qq >> 1) | (g & 1) << 1)) << 5) + pp * 8);
  vA[14] = cx.poff;
  vA[15] = cx.xo;
  vB[0] = hd.hst;
  vB[1] = wv == 0 && lane < 8 ? (unsigned)(lane * 1024 + (bid >> 2) * 4) : 0x80000000u;
  vB[2] = (unsigned)((bid & 7) * 1024 + wv * 256 + lane * 4);
  vB[3] = 1u;
  {
    const char* gp = (const char*)(lane < 32 ? cx.gamma : cx.beta) + (nt * 2 + xb) * 128 + (lane & 31) * 4;
    vB[4] = (unsigned)(uintptr_t)gp;
    vB[5] = (unsigned)((uintptr_t)gp >> 32);
  }
  vB[6] = (unsigned)((b * NBLK + mg * 80 + (lane & 14)) * 65536 + nt * 16384 + g * 16 + (2 * slot + (lane & 1)) * 64);
  {
    unsigned len;
    asm volatile("s_mov_b32 %0, .Lcf_b-.Lcf_t-64" : "=s"(len));
    vB[7] = min((unsigned)(wv * 64 + lane) * 128u, len);
  }
  vC[0] = hd.pwr;
  vC[1] = (unsigned)(CBUF + (lane & 15) * 256 + wv * 64 + g * 16);
  const unsigned w23 = (unsigned)(wv & 2) * 4096u;
#pragma unroll
  for (int i = 0; i < 4; ++i) sA[i] = cx.rs[i];
  sA[4] = cx.lds0; sA[5] = w23; sA[6] = STG; sA[7] = cx.lds0; sA[8] = cx.lds0 + w23; sA[9] = STG; sA[10] = 2 * SBLK;
  sA[11] = FLG + 2 * 8192 + (bid & 3) * 256; sA[12] = wv == nt ? 0x3ffcu : 0x444u; sA[13] = FLG; sA[14] = FLG;
  sA[15] = NQ1 * 2 * SBLK + ((bid + NQ1) & 3) * 16384;
  sB[0] = sA[15]; sB[1] = (unsigned)(-4 * 32768); sB[2] = 0xf0000000u; sB[3] = 0xf0000000u; sB[4] = ~0u; sB[5] = 0;
  sB[6] = wv == 3 ? (bid & 1 ? 125u : 137u) - 14u : 0u; sB[7] = 0; sB[8] = sB[9] = sB[10] = sB[11] = 0;
  sB[12] = (unsigned)(-2 * STG); sB[13] = STG; sB[14] = sB[15] = 0;
  {
    const u32x4 ry = make_rsrc(cx.ws + WS_Y, ROWS * 65536);
#pragma unroll
    for (int i = 0; i < 4; ++i) sC[i] = ry[i];
    sC[4] = sC[5] = slot ? 0xbf800000u : 0u;
    sC[6] = sC[7] = 0;
#pragma unroll
    for (int m = 0; m < 5; ++m) {
      const int xt = mg ? (m + 1) % 5 : m;
      sC[8 + m] = (unsigned)(xt * 16 * 65536 + ((b * 10 + mg * 5 + 2 * nh + xt) & 2) * 4096);
    }
    sC[13] = ((wv ^ idx) & 3) == 0 ? RAWL + 32768 + wv * 256 : 0;
    sC[14] = sC[15] = 0;
    const unsigned long long gt = (unsigned long long)&TAB.G[0][0][0][0][0];
    sD[0] = __builtin_amdgcn_readfirstlane((unsigned)gt);
    sD[1] = __builtin_amdgcn_readfirstlane((unsigned)(gt >> 32));
  }
#pragma unroll
  for (int i = 0; i < 32; ++i) {
    h0[i] = hd.held[i >> 2][i & 3];
    raw0[i] = hd.raw[i >> 2][i & 3];
    if (i < 16) { h1[i] = hd.held[8 + (i >> 2)][i & 3]; st0[i] = hd.st[i >> 2][i & 3]; }
    if (i < 8) { raw1[i] = hd.raw[8 + (i >> 2)][i & 3]; st1[i] = hd.st[4 + (i >> 2)][i & 3]; }
  }
#pragma unroll
  for (int i = 0; i < 16; ++i) {
    sA[i] = __builtin_amdgcn_readfirstlane(sA[i]);
    sB[i] = __builtin_amdgcn_readfirstlane(sB[i]);
    sC[i] = __builtin_amdgcn_readfirstlane(sC[i]);
  }
  u32x16 o0, o4, o8;
  u32x8 o1, o7, o9;
  u32x2 o2;
  u32x4 o5;
  u32x32 o6, o3;
  asm volatile(
  "ds_read_b64_tr_b16 v[0:1], v228 offset:0\n"
  "ds_read_b64_tr_b16 v[2:3], v228 offset:8192\n"
  "ds_read_b64_tr_b16 v[16:17], v228 offset:256\n"
  "ds_read_b64_tr_b16 v[18:19], v228 offset:8448\n"
  "ds_read_b64_tr_b16 v[4:5], v229 offset:0\n"
  "ds_read_b64_tr_b16 v[6:7], v229 offset:8192\n"
  "ds_read_b64_tr_b16 v[20:21], v229 offset:256\n"
  "ds_read_b64_tr_b16 v[22:23], v229 offset:8448\n"
  "ds_read_b64_tr_b16 v[8:9], v230 offset:0\n"
  "ds_read_b64_tr_b16 v[10:11], v230 offset:8192\n"
  "ds_read_b64_tr_b16 v[24:25], v230 offset:256\n"
  "ds_read_b64_tr_b16 v[26:27], v230 offset:8448\n"
  "ds_read_b64_tr_b16 v[12:13], v231 offset:0\n"
  "ds_read_b64_tr_b16 v[14:15], v231 offset:8192\n"
  "ds_read_b64_tr_b16 v[28:29], v231 offset:256\n"
  "ds_read_b64_tr_b16 v[30:31], v231 offset:8448\n"
  "ds_read_b128 v[96:99], v232 offset:0\n"
  "ds_read_b128 v[100:103], v232 offset:512\n"
  "ds_read_b128 v[104:107], v232 offset:2048\n"
  "ds_read_b128 v[108:111], v232 offset:2560\n"
  "s_add_i32 s67, s44, s45\n"
  "s_add_i32 m0, s44, 73728\n"
  "s_mov_b32 s64, 8388608\n"
  "buffer_load_dwordx4 v237, s[40:43], s64 offen sc1 lds\n"
  "s_add_i32 m0, s44, 77824\n"
  "s_mov_b32 s64, 8392704\n"
  "buffer_load_dwordx4 v237, s[40:43], s64 offen sc1 lds\n"
  "s_add_i32 m0, s44, 81920\n"
  "s_mov_b32 s64, 8396800\n"
  "buffer_load_dwordx4 v237, s[40:43], s64 offen sc1 lds\n"
  "s_add_i32 m0, s44, 86016\n"
  "s_mov_b32 s64, 8400896\n"
  "buffer_load_dwordx4 v237, s[40:43], s64 offen sc1 lds\n"
  "s_add_i32 m0, s44, 90112\n"
  "s_mov_b32 s64, 8388608\n"
  "buffer_load_dwordx4 v238, s[40:43], s64 offen sc1 lds\n"
  "s_add_i32 m0, s44, 94208\n"
  "s_mov_b32 s64, 4198400\n"
  "buffer_load_dwordx4 v238, s[40:43], s64 offen sc1 lds\n"
  "s_add_i32 m0, s67, 98304\n"
  "s_mov_b32 s64, 4210688\n"
  "buffer_load_dwordx4 v238, s[40:43], s64 offen sc1 lds\n"
  "s_add_i32 m0, s44, 100352\n"
  "s_mov_b32 s64, 8409088\n"
  "buffer_load_dwordx4 v238, s[40:43], s64 offen sc1 lds\n"
  "s_add_i32 m0, s44, 104448\n"
  "s_mov_b32 s64, 4227072\n"
  "buffer_load_dwordx4 v238, s[40:43], s64 offen sc1 lds\n"
  "v_mov_b32 v172, 0\n"
  "v_mov_b32 v173, 0\n"
  "v_mov_b32 v174, 0\n"
  "v_mov_b32 v175, 0\n"
  "s_memrealtime s[70:71]\n"
  "s_waitcnt lgkmcnt(0)\n"
  "s_add_i32 s61, s70, s62\n"
  "s_add_i32 s61, s61, 14\n"
  "v_pk_add_f16 v32, v0, v16\n"
  "v_pk_add_f16 v33, v1, v17\n"
  "v_pk_add_f16 v34, v2, v18\n"
  "v_pk_add_f16 v35, v3, v19\n"
  "v_pk_add_f16 v36, v4, v20\n"
  "v_pk_add_f16 v37, v5, v21\n"
  "v_pk_add_f16 v38, v6, v22\n"
  "v_pk_add_f16 v39, v7, v23\n"
  "v_pk_add_f16 v40, v8, v24\n"
  "v_pk_add_f16 v41, v9, v25\n"
  "v_pk_add_f16 v42, v10, v26\n"
  "v_pk_add_f16 v43, v11, v27\n"
  "v_pk_add_f16 v44, v12, v28\n"
  "v_pk_add_f16 v45, v13, v29\n"
  "v_pk_add_f16 v46, v14, v30\n"
  "v_pk_add_f16 v47, v15, v31\n"
  "v_add_u32 v228, 36864, v228\n"
  "v_add_u32 v229, 36864, v229\n"
  "v_add_u32 v230, 36864, v230\n"
  "v_add_u32 v231, 36864, v231\n"
  "v_add_u32 v232, 36864, v232\n"
  "s_barrier\n"
  "v_mfma_f32_16x16x32_f16 a[0:3], v[0:3], v[96:99], 0\n"
  "v_readlane_b32 s65, v244, 32\n"
  "v_pk_add_f16 v120, v96, v100\n"
  "v_mfma_f32_16x16x32_f16 a[12:15], v[4:7], v[96:99], 0\n"
  "v_pk_add_f16 v121, v97, v101\n"
  "v_pk_add_f16 v122, v98, v102\n"
  "v_mfma_f32_16x16x32_f16 a[24:27], v[8:11], v[96:99], 0\n"
  "s_add_i32 m0, s47, 0\n"
  "ds_read_b64_tr_b16 v[48:49], v228 offset:0\n"
  "v_pk_add_f16 v123, v99, v103\n"
  "v_mfma_f32_16x16x32_f16 a[36:39], v[12:15], v[96:99], 0\n"
  "s_add_i32 s64, s50, 4194304\n"
  "buffer_load_dwordx4 v237, s[40:43], s64 offen sc1 lds\n"
  "v_readlane_b32 s66, v244, 33\n"
  "v_mfma_f32_16x16x32_f16 a[4:7], v[16:19], v[100:103], 0\n"
  "s_cmp_eq_u32 s46, 73728\n"
  "s_cselect_b32 s49, s68, s69\n"
  "ds_read_b64_tr_b16 v[124:125], v233 offset:0\n"
  "v_mfma_f32_16x16x32_f16 a[16:19], v[20:23], v[100:103], 0\n"
  "ds_read_b64_tr_b16 v[128:129], v234 offset:0\n"
  "ds_read_b64_tr_b16 v[50:51], v228 offset:8192\n"
  "v_mfma_f32_16x16x32_f16 a[28:31], v[24:27], v[100:103], 0\n"
  "buffer_store_dwordx4 a[192:195], v243, s[40:43], s65 offen sc1\n"
  "ds_read_b128 v[96:99], v232 offset:4096\n"
  "v_mfma_f32_16x16x32_f16 a[40:43], v[28:31], v[100:103], 0\n"
  "ds_read_b64_tr_b16 v[132:133], v235 offset:0\n"
  "v_readlane_b32 s65, v244, 34\n"
  "v_mfma_f32_16x16x32_f16 a[8:11], v[32:35], v[120:123], 0\n"
  "s_add_i32 m0, s47, 4096\n"
  "ds_read_b64_tr_b16 v[64:65], v228 offset:256\n"
  "ds_read_b128 v[100:103], v232 offset:4608\n"
  "v_mfma_f32_16x16x32_f16 a[20:23], v[36:39], v[120:123], 0\n"
  "s_add_i32 s64, s50, 4198400\n"
  "buffer_load_dwordx4 v237, s[40:43], s64 offen sc1 lds\n"
  "ds_read_b64_tr_b16 v[136:137], v236 offset:0\n"
  "v_mfma_f32_16x16x32_f16 a[32:35], v[40:43], v[120:123], 0\n"
  "ds_read_b64_tr_b16 v[66:67], v228 offset:8448\n"
  "v_add_u32 v228, s49, v228\n"
  "v_mfma_f32_16x16x32_f16 a[44:47], v[44:47], v[120:123], 0\n"
  "ds_read_b64_tr_b16 v[52:53], v229 offset:0\n"
  "ds_read_b64_tr_b16 v[54:55], v229 offset:8192\n"
  "v_mfma_f32_16x16x32_f16 a[48:51], v[0:3], v[104:107], 0\n"
  "buffer_store_dwordx4 a[196:199], v243, s[40:43], s66 offen sc1\n"
  "v_pk_add_f16 v120, v104, v108\n"
  "v_mfma_f32_16x16x32_f16 a[60:63], v[4:7], v[104:107], 0\n"
  "v_pk_add_f16 v121, v105, v109\n"
  "v_pk_add_f16 v122, v106, v110\n"
  "v_mfma_f32_16x16x32_f16 a[72:75], v[8:11], v[104:107], 0\n"
  "s_add_i32 m0, s47, 8192\n"
  "ds_read_b64_tr_b16 v[68:69], v229 offset:256\n"
  "v_pk_add_f16 v123, v107, v111\n"
  "v_mfma_f32_16x16x32_f16 a[84:87], v[12:15], v[104:107], 0\n"
  "s_add_i32 s64, s50, 4202496\n"
  "buffer_load_dwordx4 v237, s[40:43], s64 offen sc1 lds\n"
  "v_readlane_b32 s66, v244, 35\n"
  "v_mfma_f32_16x16x32_f16 a[52:55], v[16:19], v[108:111], 0\n"
  "s_waitcnt lgkmcnt(4)\n"
  "ds_read_b64_tr_b16 v[70:71], v229 offset:8448\n"
  "v_mov_b64 v[126:127], v[124:125]\n"
  "v_mfma_f32_16x16x32_f16 a[64:67], v[20:23], v[108:111], 0\n"
  "s_waitcnt lgkmcnt(4)\n"
  "ds_read_b128 v[104:107], v232 offset:6144\n"
  "v_mov_b64 v[130:131], v[128:129]\n"
  "v_mfma_f32_16x16x32_f16 a[76:79], v[24:27], v[108:111], 0\n"
  "buffer_store_dwordx4 a[200:203], v243, s[40:43], s65 offen sc1\n"
  "v_mov_b64 v[134:135], v[132:133]\n"
  "v_mfma_f32_16x16x32_f16 a[88:91], v[28:31], v[108:111], 0\n"
  "ds_read_b64_tr_b16 v[56:57], v230 offset:0\n"
  "v_readlane_b32 s65, v244, 36\n"
  "v_mfma_f32_16x16x32_f16 a[56:59], v[32:35], v[120:123], 0\n"
  "s_add_i32 m0, s47, 12288\n"
  "ds_read_b128 v[108:111], v232 offset:6656\n"
  "v_mov_b64 v[138:139], v[136:137]\n"
  "v_mfma_f32_16x16x32_f16 a[68:71], v[36:39], v[120:123], 0\n"
  "s_add_i32 s64, s50, 4206592\n"
  "buffer_load_dwordx4 v237, s[40:43], s64 offen sc1 lds\n"
  "v_mfma_f32_16x16x32_f16 a[80:83], v[40:43], v[120:123], 0\n"
  "ds_read_b64_tr_b16 v[58:59], v230 offset:8192\n"
  "v_pk_add_f16 v80, v48, v64\n"
  "v_mfma_f32_16x16x32_f16 a[92:95], v[44:47], v[120:123], 0\n"
  "s_waitcnt lgkmcnt(4)\n"
  "ds_read_b64_tr_b16 v[72:73], v230 offset:256\n"
  "v_pk_add_f16 v81, v49, v65\n"
  "v_mfma_f32_16x16x32_f16 a[96:99], v[0:3], v[96:99], 0\n"
  "buffer_store_dwordx4 a[204:207], v243, s[40:43], s66 offen sc1\n"
  "v_pk_add_f16 v120, v96, v100\n"
  "v_mfma_f32_16x16x32_bf16 v[140:143], v[124:127], v[176:179], 0\n"
  "ds_read_b64_tr_b16 v[74:75], v230 offset:8448\n"
  "v_pk_add_f16 v121, v97, v101\n"
  "v_mfma_f32_16x16x32_f16 a[108:111], v[4:7], v[96:99], 0\n"
  "s_add_i32 m0, s47, 16384\n"
  "v_pk_add_f16 v122, v98, v102\n"
  "v_pk_add_f16 v123, v99, v103\n"
  "v_mfma_f32_16x16x32_f16 a[120:123], v[8:11], v[96:99], 0\n"
  "s_add_i32 s64, s50, 4194304\n"
  "buffer_load_dwordx4 v238, s[40:43], s64 offen sc1 lds\n"
  "v_readlane_b32 s66, v244, 37\n"
  "v_mfma_f32_16x16x32_f16 a[132:135], v[12:15], v[96:99], 0\n"
  "ds_read_b64_tr_b16 v[60:61], v231 offset:0\n"
  "v_cvt_pkrtz_f16_f32 v124, v140, v141\n"
  "v_mfma_f32_16x16x32_bf16 v[144:147], v[128:131], v[176:179], 0\n"
  "ds_read_b64_tr_b16 v[62:63], v231 offset:8192\n"
  "v_cvt_pkrtz_f16_f32 v125, v142, v143\n"
  "v_mfma_f32_16x16x32_f16 a[100:103], v[16:19], v[100:103], 0\n"
  "buffer_store_dwordx4 a[208:211], v243, s[40:43], s65 offen sc1\n"
  "v_and_b32 v124, 0xfff8fff8, v124\n"
  "v_mfma_f32_16x16x32_f16 a[112:115], v[20:23], v[100:103], 0\n"
  "ds_read_b128 v[96:99], v232 offset:0\n"
  "v_and_b32 v125, 0xfff8fff8, v125\n"
  "v_mfma_f32_16x16x32_f16 a[124:127], v[24:27], v[100:103], 0\n"
  "s_add_i32 m0, s47, 20480\n"
  "ds_read_b128 v[112:115], v232 offset:8192\n"
  "v_cvt_pkrtz_f16_f32 v126, v144, v145\n"
  "v_mfma_f32_16x16x32_bf16 v[148:151], v[132:135], v[176:179], 0\n"
  "s_add_i32 s64, s50, 4096\n"
  "buffer_load_dwordx4 v238, s[40:43], s64 offen sc1 lds\n"
  "v_cvt_pkrtz_f16_f32 v127, v146, v147\n"
  "v_mfma_f32_16x16x32_f16 a[136:139], v[28:31], v[100:103], 0\n"
  "s_waitcnt lgkmcnt(4)\n"
  "ds_write_b128 v252, a[144:147] offset:4096\n"
  "v_and_b32 v126, 0xfff8fff8, v126\n"
  "v_mfma_f32_16x16x32_f16 a[104:107], v[32:35], v[120:123], 0\n"
  "ds_read_b64_tr_b16 v[76:77], v231 offset:256\n"
  "v_and_b32 v127, 0xfff8fff8, v127\n"
  "v_mfma_f32_16x16x32_f16 a[116:119], v[36:39], v[120:123], 0\n"
  "buffer_store_dwordx4 a[212:215], v243, s[40:43], s66 offen sc1\n"
  "v_cvt_pkrtz_f16_f32 v128, v148, v149\n"
  "v_mfma_f32_16x16x32_bf16 v[152:155], v[136:139], v[176:179], 0\n"
  "ds_write_b128 v252, a[156:159] offset:16384\n"
  "v_cvt_pkrtz_f16_f32 v129, v150, v151\n"
  "v_mfma_f32_16x16x32_f16 a[128:131], v[40:43], v[120:123], 0\n"
  "s_add_i32 m0, s48, 24576\n"
  "ds_read_b128 v[100:103], v232 offset:512\n"
  "v_and_b32 v128, 0xfff8fff8, v128\n"
  "v_mfma_f32_16x16x32_f16 a[140:143], v[44:47], v[120:123], 0\n"
  "s_add_i32 s64, s50, 16384\n"
  "buffer_load_dwordx4 v238, s[40:43], s64 offen sc1 lds\n"
  "v_and_b32 v129, 0xfff8fff8, v129\n"
  "v_mfma_f32_16x16x32_f16 a[144:147], v[0:3], v[104:107], 0\n"
  "ds_read_b128 v[116:119], v232 offset:8704\n"
  "v_pk_add_f16 v120, v104, v108\n"
  "v_mfma_f32_16x16x32_f16 a[156:159], v[4:7], v[104:107], 0\n"
  "ds_write_b128 v252, a[168:171] offset:28672\n"
  "v_pk_add_f16 v121, v105, v109\n"
  "v_mfma_f32_16x16x32_f16 a[168:171], v[8:11], v[104:107], 0\n"
  "buffer_store_dwordx4 v[124:127], v242, s[40:43], s56 offen offset:0 sc1\n"
  "ds_read_b64_tr_b16 v[78:79], v231 offset:8448\n"
  "ds_write_b128 v252, a[180:183] offset:40960\n"
  "v_mfma_f32_16x16x32_f16 a[180:183], v[12:15], v[104:107], 0\n"
  "ds_write_b128 v252, a[148:151] offset:8192\n"
  "v_pk_add_f16 v122, v106, v110\n"
  "v_mfma_f32_16x16x32_f16 a[148:151], v[16:19], v[108:111], 0\n"
  "s_add_i32 m0, s47, 26624\n"
  "ds_write_b128 v252, a[160:163] offset:20480\n"
  "v_pk_add_f16 v123, v107, v111\n"
  "v_mfma_f32_16x16x32_f16 a[160:163], v[20:23], v[108:111], 0\n"
  "s_add_i32 s64, s50, 4214784\n"
  "buffer_load_dwordx4 v238, s[40:43], s64 offen sc1 lds\n"
  "ds_write_b128 v252, a[172:175] offset:32768\n"
  "v_mfma_f32_16x16x32_f16 a[172:175], v[24:27], v[108:111], 0\n"
  "ds_write_b128 v252, a[152:155] offset:12288\n"
  "v_cvt_pkrtz_f16_f32 v130, v152, v153\n"
  "v_mfma_f32_16x16x32_f16 a[184:187], v[28:31], v[108:111], 0\n"
  "s_add_i32 m0, s47, 30720\n"
  "ds_write_b128 v252, a[164:167] offset:24576\n"
  "v_cvt_pkrtz_f16_f32 v131, v154, v155\n"
  "v_mfma_f32_16x16x32_f16 a[152:155], v[32:35], v[120:123], 0\n"
  "s_add_i32 s64, s50, 32768\n"
  "buffer_load_dwordx4 v238, s[40:43], s64 offen sc1 lds\n"
  "ds_write_b128 v252, a[176:179] offset:36864\n"
  "v_mfma_f32_16x16x32_f16 a[164:167], v[36:39], v[120:123], 0\n"
  "s_waitcnt lgkmcnt(9)\n"
  "v_and_b32 v130, 0xfff8fff8, v130\n"
  "v_and_b32 v131, 0xfff8fff8, v131\n"
  "v_mfma_f32_16x16x32_f16 a[176:179], v[40:43], v[120:123], 0\n"
  "s_add_i32 s47, s44, s46\n"
  "s_waitcnt lgkmcnt(7)\n"
  "ds_read_b128 v[104:107], v232 offset:2048\n"
  "v_mfma_f32_16x16x32_f16 a[188:191], v[44:47], v[120:123], 0\n"
  "s_add_i32 s48, s47, s45\n"
  "buffer_store_dwordx4 v[128:131], v242, s[40:43], s56 offen offset:128 sc1\n"
  "v_pk_add_f16 v82, v50, v66\n"
  "v_mfma_f32_16x16x32_f16 a[192:195], v[0:3], v[112:115], 0\n"
  "s_add_i32 s46, s46, s49\n"
  "ds_read_b128 v[108:111], v232 offset:2560\n"
  "v_pk_add_f16 v120, v112, v116\n"
  "v_mfma_f32_16x16x32_f16 a[204:207], v[4:7], v[112:115], 0\n"
  "s_add_i32 s50, s50, 4194304\n"
  "v_pk_add_f16 v121, v113, v117\n"
  "v_pk_add_f16 v122, v114, v118\n"
  "v_mfma_f32_16x16x32_f16 a[216:219], v[8:11], v[112:115], 0\n"
  "v_pk_add_f16 v123, v115, v119\n"
  "v_pk_add_f16 v83, v51, v67\n"
  "v_mfma_f32_16x16x32_f16 a[228:231], v[12:15], v[112:115], 0\n"
  "v_pk_add_f16 v84, v52, v68\n"
  "v_pk_add_f16 v85, v53, v69\n"
  "v_mfma_f32_16x16x32_f16 a[196:199], v[16:19], v[116:119], 0\n"
  "v_pk_add_f16 v86, v54, v70\n"
  "v_pk_add_f16 v87, v55, v71\n"
  "v_mfma_f32_16x16x32_f16 a[208:211], v[20:23], v[116:119], 0\n"
  "v_pk_add_f16 v88, v56, v72\n"
  "v_pk_add_f16 v89, v57, v73\n"
  "v_mfma_f32_16x16x32_f16 a[220:223], v[24:27], v[116:119], 0\n"
  "v_pk_add_f16 v90, v58, v74\n"
  "v_pk_add_f16 v91, v59, v75\n"
  "v_mfma_f32_16x16x32_f16 a[232:235], v[28:31], v[116:119], 0\n"
  "v_pk_add_f16 v92, v60, v76\n"
  "v_pk_add_f16 v93, v61, v77\n"
  "v_mfma_f32_16x16x32_f16 a[200:203], v[32:35], v[120:123], 0\n"
  "v_pk_add_f16 v94, v62, v78\n"
  "v_pk_add_f16 v95, v63, v79\n"
  "s_waitcnt vmcnt(17) lgkmcnt(0)\n"
  "v_mfma_f32_16x16x32_f16 a[212:215], v[36:39], v[120:123], 0\n"
  "s_cmp_gt_u32 s62, 88\n"
  "s_cbranch_scc1 .Lpace_s0_None_%=\n"
  ".Lpaced_s0_None_%=:\n"
  "v_mfma_f32_16x16x32_f16 a[224:227], v[40:43], v[120:123], 0\n"
  "v_add_u32 v229, s49, v229\n"
  "v_add_u32 v230, s49, v230\n"
  "s_barrier\n"
  "v_mfma_f32_16x16x32_f16 a[236:239], v[44:47], v[120:123], 0\n"
  "v_add_u32 v231, s49, v231\n"
  "v_add_u32 v232, s49, v232\n"
  "v_mfma_f32_16x16x32_f16 a[0:3], v[48:51], v[96:99], a[0:3]\n"
  "buffer_load_dwordx4 v[164:167], v240, s[40:43], s59 offen sc1\n"
  "v_pk_add_f16 v120, v96, v100\n"
  "v_mfma_f32_16x16x32_f16 a[12:15], v[52:55], v[96:99], a[12:15]\n"
  "v_pk_add_f16 v121, v97, v101\n"
  "v_pk_add_f16 v122, v98, v102\n"
  "v_mfma_f32_16x16x32_f16 a[24:27], v[56:59], v[96:99], a[24:27]\n"
  "s_and_b32 s65, s51, 0xffffe300\n"
  "ds_read_b64_tr_b16 v[0:1], v228 offset:0\n"
  "v_pk_add_f16 v123, v99, v103\n"
  "v_mfma_f32_16x16x32_f16 a[36:39], v[60:63], v[96:99], a[36:39]\n"
  "s_add_i32 m0, s44, 155648\n"
  "ds_read_b64_tr_b16 v[2:3], v228 offset:8192\n"
  "ds_read_b128 v[96:99], v232 offset:4096\n"
  "v_mfma_f32_16x16x32_f16 a[4:7], v[64:67], v[100:103], a[4:7]\n"
  "s_cmp_eq_u32 s46, 73728\n"
  "s_cselect_b32 s49, s68, s69\n"
  "buffer_load_dwordx4 v239, s[40:43], s58 offen sc1 lds\n"
  "v_mfma_f32_16x16x32_f16 a[16:19], v[68:71], v[100:103], a[16:19]\n"
  "s_add_i32 s57, s57, 32768\n"
  "ds_read_b64_tr_b16 v[124:125], v233 offset:2048\n"
  "v_add_u32 v233, -45056, v233\n"
  "v_mfma_f32_16x16x32_f16 a[28:31], v[72:75], v[100:103], a[28:31]\n"
  "s_lshr_b32 s59, s57, 9\n"
  "ds_read_b64_tr_b16 v[128:129], v234 offset:2048\n"
  "v_add_u32 v234, -45056, v234\n"
  "v_mfma_f32_16x16x32_f16 a[40:43], v[76:79], v[100:103], a[40:43]\n"
  "s_add_i32 m0, s47, 0\n"
  "ds_read_b64_tr_b16 v[132:133], v235 offset:2048\n"
  "v_add_u32 v235, -45056, v235\n"
  "v_mfma_f32_16x16x32_f16 a[8:11], v[80:83], v[120:123], a[8:11]\n"
  "s_add_i32 s64, s50, 4194304\n"
  "buffer_load_dwordx4 v237, s[40:43], s64 offen sc1 lds\n"
  "ds_read_b64_tr_b16 v[16:17], v228 offset:256\n"
  "v_mfma_f32_16x16x32_f16 a[20:23], v[84:87], v[120:123], a[20:23]\n"
  "ds_read_b128 v[100:103], v232 offset:4608\n"
  "ds_read_b64_tr_b16 v[136:137], v236 offset:2048\n"
  "v_mfma_f32_16x16x32_f16 a[32:35], v[88:91], v[120:123], a[32:35]\n"
  "s_add_i32 s53, s53, 8192\n"
  "s_lshr_b32 s52, s52, 1\n"
  "ds_read_b64_tr_b16 v[18:19], v228 offset:8448\n"
  "v_mfma_f32_16x16x32_f16 a[44:47], v[92:95], v[120:123], a[44:47]\n"
  "s_bitcmp1_b32 s52, 0\n"
  "s_cselect_b32 s54, s53, 134217728\n"
  "ds_read_b64_tr_b16 v[4:5], v229 offset:0\n"
  "v_mfma_f32_16x16x32_f16 a[48:51], v[48:51], v[104:107], a[48:51]\n"
  "s_add_i32 s51, s51, 8448\n"
  "buffer_store_dword v232, v245, s[40:43], s65 offen sc1\n"
  "v_pk_add_f16 v120, v104, v108\n"
  "v_mfma_f32_16x16x32_f16 a[60:63], v[52:55], v[104:107], a[60:63]\n"
  "v_pk_add_f16 v121, v105, v109\n"
  "v_pk_add_f16 v122, v106, v110\n"
  "v_mfma_f32_16x16x32_f16 a[72:75], v[56:59], v[104:107], a[72:75]\n"
  "ds_read_b64_tr_b16 v[6:7], v229 offset:8192\n"
  "v_pk_add_f16 v123, v107, v111\n"
  "v_mfma_f32_16x16x32_f16 a[84:87], v[60:63], v[104:107], a[84:87]\n"
  "s_add_i32 m0, s47, 4096\n"
  "ds_read_b64_tr_b16 v[20:21], v229 offset:256\n"
  "v_add_u32 v228, s49, v228\n"
  "v_mfma_f32_16x16x32_f16 a[52:55], v[64:67], v[108:111], a[52:55]\n"
  "s_waitcnt lgkmcnt(4)\n"
  "s_add_i32 s64, s50, 4198400\n"
  "buffer_load_dwordx4 v237, s[40:43], s64 offen sc1 lds\n"
  "v_mfma_f32_16x16x32_f16 a[64:67], v[68:71], v[108:111], a[64:67]\n"
  "s_waitcnt lgkmcnt(3)\n"
  "ds_read_b64_tr_b16 v[22:23], v229 offset:8448\n"
  "v_mov_b64 v[126:127], v[124:125]\n"
  "v_mfma_f32_16x16x32_f16 a[76:79], v[72:75], v[108:111], a[76:79]\n"
  "ds_read_b128 v[104:107], v232 offset:6144\n"
  "v_mov_b64 v[130:131], v[128:129]\n"
  "v_mfma_f32_16x16x32_f16 a[88:91], v[76:79], v[108:111], a[88:91]\n"
  "s_add_i32 m0, s47, 8192\n"
  "ds_read_b64_tr_b16 v[8:9], v230 offset:0\n"
  "v_mov_b64 v[134:135], v[132:133]\n"
  "v_mfma_f32_16x16x32_f16 a[56:59], v[80:83], v[120:123], a[56:59]\n"
  "s_add_i32 s64, s50, 4202496\n"
  "buffer_load_dwordx4 v237, s[40:43], s64 offen sc1 lds\n"
  "v_mov_b64 v[138:139], v[136:137]\n"
  "v_mfma_f32_16x16x32_f16 a[68:71], v[84:87], v[120:123], a[68:71]\n"
  "ds_read_b128 v[108:111], v232 offset:6656\n"
  "v_pk_add_f16 v32, v0, v16\n"
  "v_mfma_f32_16x16x32_f16 a[80:83], v[88:91], v[120:123], a[80:83]\n"
  "ds_read_b64_tr_b16 v[10:11], v230 offset:8192\n"
  "v_pk_add_f16 v33, v1, v17\n"
  "v_mfma_f32_16x16x32_f16 a[92:95], v[92:95], v[120:123], a[92:95]\n"
  "s_add_i32 m0, s47, 12288\n"
  "ds_read_b64_tr_b16 v[24:25], v230 offset:256\n"
  "v_pk_add_f16 v34, v2, v18\n"
  "v_mfma_f32_16x16x32_f16 a[96:99], v[48:51], v[96:99], a[96:99]\n"
  "s_add_i32 s64, s50, 4206592\n"
  "buffer_load_dwordx4 v237, s[40:43], s64 offen sc1 lds\n"
  "v_pk_add_f16 v120, v96, v100\n"
  "v_mfma_f32_16x16x32_bf16 v[140:143], v[124:127], v[176:179], 0\n"
  "s_waitcnt lgkmcnt(5)\n"
  "ds_read_b64_tr_b16 v[26:27], v230 offset:8448\n"
  "v_pk_add_f16 v121, v97, v101\n"
  "v_mfma_f32_16x16x32_f16 a[108:111], v[52:55], v[96:99], a[108:111]\n"
  "v_pk_add_f16 v122, v98, v102\n"
  "v_pk_add_f16 v123, v99, v103\n"
  "v_mfma_f32_16x16x32_f16 a[120:123], v[56:59], v[96:99], a[120:123]\n"
  "s_add_i32 m0, s47, 16384\n"
  "ds_read_b64_tr_b16 v[12:13], v231 offset:0\n"
  "v_pk_add_f16 v35, v3, v19\n"
  "v_mfma_f32_16x16x32_f16 a[132:135], v[60:63], v[96:99], a[132:135]\n"
  "s_add_i32 s64, s50, 4194304\n"
  "buffer_load_dwordx4 v238, s[40:43], s64 offen sc1 lds\n"
  "v_cvt_pkrtz_f16_f32 v124, v140, v141\n"
  "v_mfma_f32_16x16x32_bf16 v[144:147], v[128:131], v[176:179], 0\n"
  "ds_read_b64_tr_b16 v[14:15], v231 offset:8192\n"
  "v_cvt_pkrtz_f16_f32 v125, v142, v143\n"
  "v_mfma_f32_16x16x32_f16 a[100:103], v[64:67], v[100:103], a[100:103]\n"
  "ds_read_b128 v[96:99], v232 offset:0\n"
  "v_and_b32 v124, 0xfff8fff8, v124\n"
  "v_mfma_f32_16x16x32_f16 a[112:115], v[68:71], v[100:103], a[112:115]\n"
  "s_add_i32 m0, s47, 20480\n"
  "ds_read_b128 v[112:115], v232 offset:8192\n"
  "v_and_b32 v125, 0xfff8fff8, v125\n"
  "v_mfma_f32_16x16x32_f16 a[124:127], v[72:75], v[100:103], a[124:127]\n"
  "s_add_i32 s64, s50, 4096\n"
  "buffer_load_dwordx4 v238, s[40:43], s64 offen sc1 lds\n"
  "v_cvt_pkrtz_f16_f32 v126, v144, v145\n"
  "v_mfma_f32_16x16x32_bf16 v[148:151], v[132:135], v[176:179], 0\n"
  "s_waitcnt lgkmcnt(4)\n"
  "v_cvt_pkrtz_f16_f32 v127, v146, v147\n"
  "v_and_b32 v126, 0xfff8fff8, v126\n"
  "v_mfma_f32_16x16x32_f16 a[136:139], v[76:79], v[100:103], a[136:139]\n"
  "ds_read_b64_tr_b16 v[28:29], v231 offset:256\n"
  "v_and_b32 v127, 0xfff8fff8, v127\n"
  "v_mfma_f32_16x16x32_f16 a[104:107], v[80:83], v[120:123], a[104:107]\n"
  "s_add_i32 m0, s48, 24576\n"
  "ds_read_b128 v[100:103], v232 offset:512\n"
  "v_pk_add_f16 v36, v4, v20\n"
  "v_mfma_f32_16x16x32_f16 a[116:119], v[84:87], v[120:123], a[116:119]\n"
  "s_add_i32 s64, s50, 16384\n"
  "buffer_load_dwordx4 v238, s[40:43], s64 offen sc1 lds\n"
  "v_mfma_f32_16x16x32_bf16 v[152:155], v[136:139], v[176:179], 0\n"
  "ds_read_b128 v[116:119], v232 offset:8704\n"
  "v_cvt_pkrtz_f16_f32 v128, v148, v149\n"
  "v_mfma_f32_16x16x32_f16 a[128:131], v[88:91], v[120:123], a[128:131]\n"
  "ds_read_b64_tr_b16 v[30:31], v231 offset:8448\n"
  "v_cvt_pkrtz_f16_f32 v129, v150, v151\n"
  "v_mfma_f32_16x16x32_f16 a[140:143], v[92:95], v[120:123], a[140:143]\n"
  "v_and_b32 v128, 0xfff8fff8, v128\n"
  "v_and_b32 v129, 0xfff8fff8, v129\n"
  "v_mfma_f32_16x16x32_f16 a[144:147], v[48:51], v[104:107], a[144:147]\n"
  "buffer_store_dwordx4 v[124:127], v242, s[40:43], s56 offen offset:256 sc1\n"
  "v_pk_add_f16 v120, v104, v108\n"
  "v_mfma_f32_16x16x32_f16 a[156:159], v[52:55], v[104:107], a[156:159]\n"
  "v_pk_add_f16 v121, v105, v109\n"
  "v_pk_add_f16 v122, v106, v110\n"
  "v_mfma_f32_16x16x32_f16 a[168:171], v[56:59], v[104:107], a[168:171]\n"
  "v_pk_add_f16 v123, v107, v111\n"
  "v_cvt_pkrtz_f16_f32 v130, v152, v153\n"
  "v_mfma_f32_16x16x32_f16 a[180:183], v[60:63], v[104:107], a[180:183]\n"
  "s_add_i32 m0, s47, 26624\n"
  "ds_read_b128 v[104:107], v232 offset:2048\n"
  "v_cvt_pkrtz_f16_f32 v131, v154, v155\n"
  "v_mfma_f32_16x16x32_f16 a[148:151], v[64:67], v[108:111], a[148:151]\n"
  "s_add_i32 s64, s50, 4214784\n"
  "buffer_load_dwordx4 v238, s[40:43], s64 offen sc1 lds\n"
  "v_and_b32 v130, 0xfff8fff8, v130\n"
  "v_mfma_f32_16x16x32_f16 a[160:163], v[68:71], v[108:111], a[160:163]\n"
  "s_waitcnt lgkmcnt(1)\n"
  "v_and_b32 v131, 0xfff8fff8, v131\n"
  "v_pk_add_f16 v37, v5, v21\n"
  "v_mfma_f32_16x16x32_f16 a[172:175], v[72:75], v[108:111], a[172:175]\n"
  "s_cmp_lt_u32 s57, 0x80000\n"
  "s_cselect_b32 s58, s57, 0xf0000000\n"
  "s_cselect_b32 s59, s59, 0xf0000000\n"
  "v_mfma_f32_16x16x32_f16 a[184:187], v[76:79], v[108:111], a[184:187]\n"
  "s_add_i32 m0, s47, 30720\n"
  "ds_read_b128 v[108:111], v232 offset:2560\n"
  "v_pk_add_f16 v38, v6, v22\n"
  "v_mfma_f32_16x16x32_f16 a[152:155], v[80:83], v[120:123], a[152:155]\n"
  "s_add_i32 s64, s50, 32768\n"
  "buffer_load_dwordx4 v238, s[40:43], s64 offen sc1 lds\n"
  "v_pk_add_f16 v39, v7, v23\n"
  "v_mfma_f32_16x16x32_f16 a[164:167], v[84:87], v[120:123], a[164:167]\n"
  "s_add_i32 s47, s44, s46\n"
  "v_pk_add_f16 v40, v8, v24\n"
  "v_mfma_f32_16x16x32_f16 a[176:179], v[88:91], v[120:123], a[176:179]\n"
  "s_add_i32 s48, s47, s45\n"
  "v_pk_add_f16 v41, v9, v25\n"
  "v_pk_add_f16 v42, v10, v26\n"
  "v_mfma_f32_16x16x32_f16 a[188:191], v[92:95], v[120:123], a[188:191]\n"
  "s_add_i32 s46, s46, s49\n"
  "v_pk_add_f16 v43, v11, v27\n"
  "v_pk_add_f16 v44, v12, v28\n"
  "v_mfma_f32_16x16x32_f16 a[192:195], v[48:51], v[112:115], a[192:195]\n"
  "s_add_i32 s50, s50, 4194304\n"
  "buffer_store_dwordx4 v[128:131], v242, s[40:43], s56 offen offset:384 sc1\n"
  "v_pk_add_f16 v120, v112, v116\n"
  "v_mfma_f32_16x16x32_f16 a[204:207], v[52:55], v[112:115], a[204:207]\n"
  "s_add_i32 s55, s55, 8404992\n"
  "v_pk_add_f16 v121, v113, v117\n"
  "v_pk_add_f16 v122, v114, v118\n"
  "v_mfma_f32_16x16x32_f16 a[216:219], v[56:59], v[112:115], a[216:219]\n"
  "s_and_b32 s56, s55, 0xff80c000\n"
  "v_pk_add_f16 v123, v115, v119\n"
  "v_pk_add_f16 v45, v13, v29\n"
  "v_mfma_f32_16x16x32_f16 a[228:231], v[60:63], v[112:115], a[228:231]\n"
  "v_pk_add_f16 v46, v14, v30\n"
  "v_pk_add_f16 v47, v15, v31\n"
  "v_mfma_f32_16x16x32_f16 a[196:199], v[64:67], v[116:119], a[196:199]\n"
  "v_add_u32 v229, s49, v229\n"
  "v_add_u32 v230, s49, v230\n"
  "v_mfma_f32_16x16x32_f16 a[208:211], v[68:71], v[116:119], a[208:211]\n"
  "v_add_u32 v231, s49, v231\n"
  "v_add_u32 v232, s49, v232\n"
  "v_mfma_f32_16x16x32_f16 a[220:223], v[72:75], v[116:119], a[220:223]\n"
  "v_add_u32 v236, -45056, v236\n"
  "v_mfma_f32_16x16x32_f16 a[232:235], v[76:79], v[116:119], a[232:235]\n"
  "v_mfma_f32_16x16x32_f16 a[200:203], v[80:83], v[120:123], a[200:203]\n"
  "s_waitcnt vmcnt(12) lgkmcnt(0)\n"
  "v_mfma_f32_16x16x32_f16 a[212:215], v[84:87], v[120:123], a[212:215]\n"
  "s_cmp_gt_u32 s62, 88\n"
  "s_cbranch_scc1 .Lpace_s1_None_%=\n"
  ".Lpaced_s1_None_%=:\n"
  "v_mfma_f32_16x16x32_f16 a[224:227], v[88:91], v[120:123], a[224:227]\n"
  "s_barrier\n"
  "v_mfma_f32_16x16x32_f16 a[236:239], v[92:95], v[120:123], a[236:239]\n"
  "s_mov_b32 s63, 10\n"
  ".p2align 6\n"
  ".LloopP_%=:\n"
  "v_mfma_f32_16x16x32_f16 a[0:3], v[0:3], v[96:99], a[0:3]\n"
  "buffer_load_dword v247, v246, s[40:43], s54 offen sc1\n"
  "v_pk_add_f16 v120, v96, v100\n"
  "v_mfma_f32_16x16x32_f16 a[12:15], v[4:7], v[96:99], a[12:15]\n"
  "v_pk_add_f16 v121, v97, v101\n"
  "v_pk_add_f16 v122, v98, v102\n"
  "v_mfma_f32_16x16x32_f16 a[24:27], v[8:11], v[96:99], a[24:27]\n"
  "s_min_u32 s67, s60, 11\n"
  "s_lshl_b32 s67, s67, 2\n"
  "v_pk_add_f16 v123, v99, v103\n"
  "v_mfma_f32_16x16x32_f16 a[36:39], v[12:15], v[96:99], a[36:39]\n"
  "ds_read_b64_tr_b16 v[48:49], v228 offset:0\n"
  "v_readlane_b32 s66, v244, s60\n"
  "v_mfma_f32_16x16x32_f16 a[4:7], v[16:19], v[100:103], a[4:7]\n"
  "s_cmp_eq_u32 s46, 73728\n"
  "s_cselect_b32 s49, s68, s69\n"
  "buffer_load_dwordx4 v[168:171], v240, s[40:43], s59 offen sc1\n"
  "v_mfma_f32_16x16x32_f16 a[16:19], v[20:23], v[100:103], a[16:19]\n"
  "ds_read_b64_tr_b16 v[124:125], v233 offset:0\n"
  "ds_read_b64_tr_b16 v[128:129], v234 offset:0\n"
  "v_mfma_f32_16x16x32_f16 a[28:31], v[24:27], v[100:103], a[28:31]\n"
  "ds_read_b64_tr_b16 v[50:51], v228 offset:8192\n"
  "ds_read_b128 v[96:99], v232 offset:4096\n"
  "v_mfma_f32_16x16x32_f16 a[40:43], v[28:31], v[100:103], a[40:43]\n"
  "s_add_i32 m0, s44, 110592\n"
  "ds_read_b64_tr_b16 v[160:161], v241 offset:45056\n"
  "ds_read_b64_tr_b16 v[162:163], v241 offset:45568\n"
  "v_mfma_f32_16x16x32_f16 a[8:11], v[32:35], v[120:123], a[8:11]\n"
  "buffer_load_dwordx4 v239, s[40:43], s58 offen sc1 lds\n"
  "ds_read_b64_tr_b16 v[132:133], v235 offset:0\n"
  "v_mfma_f32_16x16x32_f16 a[20:23], v[36:39], v[120:123], a[20:23]\n"
  "s_add_i32 s57, s57, 32768\n"
  "ds_read_b64_tr_b16 v[64:65], v228 offset:256\n"
  "ds_read_b128 v[100:103], v232 offset:4608\n"
  "v_mfma_f32_16x16x32_f16 a[32:35], v[40:43], v[120:123], a[32:35]\n"
  "s_lshr_b32 s59, s57, 9\n"
  "s_add_i32 s60, s60, 1\n"
  "ds_read_b64_tr_b16 v[136:137], v236 offset:0\n"
  "v_mfma_f32_16x16x32_f16 a[44:47], v[44:47], v[120:123], a[44:47]\n"
  "s_add_i32 m0, s47, 0\n"
  "ds_read_b64_tr_b16 v[66:67], v228 offset:8448\n"
  "v_add_u32 v228, s49, v228\n"
  "v_mfma_f32_16x16x32_f16 a[48:51], v[0:3], v[104:107], a[48:51]\n"
  "s_add_i32 s64, s50, 4194304\n"
  "buffer_load_dwordx4 v237, s[40:43], s64 offen sc1 lds\n"
  "v_pk_add_f16 v120, v104, v108\n"
  "v_mfma_f32_16x16x32_f16 a[60:63], v[4:7], v[104:107], a[60:63]\n"
  "s_set_gpr_idx_on s67, gpr_idx(SRC0)\n"
  "v_mov_b64 v[156:157], v[180:181]\n"
  "v_mov_b64 v[158:159], v[182:183]\n"
  "s_set_gpr_idx_off\n"
  "v_mfma_f32_16x16x32_f16 a[72:75], v[8:11], v[104:107], a[72:75]\n"
  "s_waitcnt lgkmcnt(5)\n"
  "ds_read_b64_tr_b16 v[52:53], v229 offset:0\n"
  "v_pk_add_f16 v121, v105, v109\n"
  "v_mfma_f32_16x16x32_f16 a[84:87], v[12:15], v[104:107], a[84:87]\n"
  "v_pk_add_f16 v122, v106, v110\n"
  "v_pk_add_f16 v123, v107, v111\n"
  "v_mfma_f32_16x16x32_f16 a[52:55], v[16:19], v[108:111], a[52:55]\n"
  "buffer_store_dwordx4 v[156:159], v243, s[40:43], s66 offen sc1\n"
  "ds_read_b64_tr_b16 v[54:55], v229 offset:8192\n"
  "v_mfma_f32_16x16x32_f16 a[64:67], v[20:23], v[108:111], a[64:67]\n"
  "s_waitcnt lgkmcnt(3)\n"
  "ds_read_b64_tr_b16 v[68:69], v229 offset:256\n"
  "v_mov_b64 v[126:127], v[124:125]\n"
  "v_mfma_f32_16x16x32_f16 a[76:79], v[24:27], v[108:111], a[76:79]\n"
  "s_waitcnt lgkmcnt(3)\n"
  "ds_read_b64_tr_b16 v[70:71], v229 offset:8448\n"
  "v_mov_b64 v[130:131], v[128:129]\n"
  "v_mfma_f32_16x16x32_bf16 v[172:175], v[160:163], v[164:167], v[172:175]\n"
  "s_add_i32 m0, s47, 4096\n"
  "ds_read_b128 v[104:107], v232 offset:6144\n"
  "v_mov_b64 v[134:135], v[132:133]\n"
  "v_mfma_f32_16x16x32_f16 a[88:91], v[28:31], v[108:111], a[88:91]\n"
  "s_add_i32 s64, s50, 4198400\n"
  "buffer_load_dwordx4 v237, s[40:43], s64 offen sc1 lds\n"
  "v_mov_b64 v[138:139], v[136:137]\n"
  "v_mfma_f32_16x16x32_f16 a[56:59], v[32:35], v[120:123], a[56:59]\n"
  "ds_read_b64_tr_b16 v[56:57], v230 offset:0\n"
  "v_pk_add_f16 v80, v48, v64\n"
  "v_mfma_f32_16x16x32_f16 a[68:71], v[36:39], v[120:123], a[68:71]\n"
  "ds_read_b128 v[108:111], v232 offset:6656\n"
  "v_pk_add_f16 v81, v49, v65\n"
  "v_mfma_f32_16x16x32_f16 a[80:83], v[40:43], v[120:123], a[80:83]\n"
  "s_add_i32 m0, s47, 8192\n"
  "ds_read_b64_tr_b16 v[58:59], v230 offset:8192\n"
  "v_pk_add_f16 v82, v50, v66\n"
  "v_mfma_f32_16x16x32_f16 a[92:95], v[44:47], v[120:123], a[92:95]\n"
  "s_add_i32 s64, s50, 4202496\n"
  "buffer_load_dwordx4 v237, s[40:43], s64 offen sc1 lds\n"
  "v_pk_add_f16 v83, v51, v67\n"
  "v_mfma_f32_16x16x32_f16 a[96:99], v[0:3], v[96:99], a[96:99]\n"
  "s_waitcnt lgkmcnt(4)\n"
  "v_pk_add_f16 v120, v96, v100\n"
  "v_pk_add_f16 v121, v97, v101\n"
  "v_mfma_f32_16x16x32_bf16 v[140:143], v[124:127], v[176:179], 0\n"
  "ds_read_b64_tr_b16 v[72:73], v230 offset:256\n"
  "v_pk_add_f16 v84, v52, v68\n"
  "v_mfma_f32_16x16x32_f16 a[108:111], v[4:7], v[96:99], a[108:111]\n"
  "s_add_i32 m0, s47, 12288\n"
  "v_pk_add_f16 v122, v98, v102\n"
  "v_pk_add_f16 v123, v99, v103\n"
  "v_mfma_f32_16x16x32_f16 a[120:123], v[8:11], v[96:99], a[120:123]\n"
  "s_add_i32 s64, s50, 4206592\n"
  "buffer_load_dwordx4 v237, s[40:43], s64 offen sc1 lds\n"
  "v_pk_add_f16 v85, v53, v69\n"
  "v_mfma_f32_16x16x32_f16 a[132:135], v[12:15], v[96:99], a[132:135]\n"
  "ds_read_b64_tr_b16 v[74:75], v230 offset:8448\n"
  "v_cvt_pkrtz_f16_f32 v124, v140, v141\n"
  "v_mfma_f32_16x16x32_bf16 v[144:147], v[128:131], v[176:179], 0\n"
  "ds_read_b64_tr_b16 v[60:61], v231 offset:0\n"
  "v_cvt_pkrtz_f16_f32 v125, v142, v143\n"
  "v_mfma_f32_16x16x32_f16 a[100:103], v[16:19], v[100:103], a[100:103]\n"
  "s_add_i32 m0, s47, 16384\n"
  "ds_read_b64_tr_b16 v[62:63], v231 offset:8192\n"
  "v_and_b32 v124, 0xfff8fff8, v124\n"
  "v_mfma_f32_16x16x32_f16 a[112:115], v[20:23], v[100:103], a[112:115]\n"
  "s_add_i32 s64, s50, 4194304\n"
  "buffer_load_dwordx4 v238, s[40:43], s64 offen sc1 lds\n"
  "v_and_b32 v125, 0xfff8fff8, v125\n"
  "v_mfma_f32_16x16x32_f16 a[124:127], v[24:27], v[100:103], a[124:127]\n"
  "ds_read_b128 v[96:99], v232 offset:0\n"
  "v_cvt_pkrtz_f16_f32 v126, v144, v145\n"
  "v_mfma_f32_16x16x32_bf16 v[148:151], v[132:135], v[176:179], 0\n"
  "ds_read_b128 v[112:115], v232 offset:8192\n"
  "v_cvt_pkrtz_f16_f32 v127, v146, v147\n"
  "v_mfma_f32_16x16x32_f16 a[136:139], v[28:31], v[100:103], a[136:139]\n"
  "s_add_i32 m0, s47, 20480\n"
  "ds_read_b64_tr_b16 v[76:77], v231 offset:256\n"
  "v_and_b32 v126, 0xfff8fff8, v126\n"
  "v_mfma_f32_16x16x32_f16 a[104:107], v[32:35], v[120:123], a[104:107]\n"
  "s_add_i32 s64, s50, 4096\n"
  "buffer_load_dwordx4 v238, s[40:43], s64 offen sc1 lds\n"
  "v_and_b32 v127, 0xfff8fff8, v127\n"
  "v_mfma_f32_16x16x32_f16 a[116:119], v[36:39], v[120:123], a[116:119]\n"
  "s_waitcnt lgkmcnt(8)\n"
  "ds_read_b128 v[100:103], v232 offset:512\n"
  "v_cvt_pkrtz_f16_f32 v128, v148, v149\n"
  "v_mfma_f32_16x16x32_bf16 v[152:155], v[136:139], v[176:179], 0\n"
  "s_waitcnt lgkmcnt(6)\n"
  "ds_read_b128 v[116:119], v232 offset:8704\n"
  "v_cvt_pkrtz_f16_f32 v129, v150, v151\n"
  "v_mfma_f32_16x16x32_f16 a[128:131], v[40:43], v[120:123], a[128:131]\n"
  "s_add_i32 m0, s48, 24576\n"
  "ds_read_b64_tr_b16 v[78:79], v231 offset:8448\n"
  "v_and_b32 v128, 0xfff8fff8, v128\n"
  "v_mfma_f32_16x16x32_f16 a[140:143], v[44:47], v[120:123], a[140:143]\n"
  "s_add_i32 s64, s50, 16384\n"
  "buffer_load_dwordx4 v238, s[40:43], s64 offen sc1 lds\n"
  "v_and_b32 v129, 0xfff8fff8, v129\n"
  "v_mfma_f32_16x16x32_f16 a[144:147], v[0:3], v[104:107], a[144:147]\n"
  "v_pk_add_f16 v120, v104, v108\n"
  "v_pk_add_f16 v121, v105, v109\n"
  "v_mfma_f32_16x16x32_f16 a[156:159], v[4:7], v[104:107], a[156:159]\n"
  "v_pk_add_f16 v122, v106, v110\n"
  "v_pk_add_f16 v123, v107, v111\n"
  "v_mfma_f32_16x16x32_f16 a[168:171], v[8:11], v[104:107], a[168:171]\n"
  "v_cvt_pkrtz_f16_f32 v130, v152, v153\n"
  "v_cvt_pkrtz_f16_f32 v131, v154, v155\n"
  "v_mfma_f32_16x16x32_f16 a[180:183], v[12:15], v[104:107], a[180:183]\n"
  "buffer_store_dwordx4 v[124:127], v242, s[40:43], s56 offen offset:0 sc1\n"
  "v_and_b32 v130, 0xfff8fff8, v130\n"
  "v_mfma_f32_16x16x32_f16 a[148:151], v[16:19], v[108:111], a[148:151]\n"
  "ds_read_b128 v[104:107], v232 offset:2048\n"
  "v_and_b32 v131, 0xfff8fff8, v131\n"
  "v_mfma_f32_16x16x32_f16 a[160:163], v[20:23], v[108:111], a[160:163]\n"
  "s_cmp_lt_u32 s57, 0x80000\n"
  "s_cselect_b32 s58, s57, 0xf0000000\n"
  "s_cselect_b32 s59, s59, 0xf0000000\n"
  "v_mfma_f32_16x16x32_f16 a[172:175], v[24:27], v[108:111], a[172:175]\n"
  "s_add_i32 m0, s47, 26624\n"
  "s_waitcnt lgkmcnt(1)\n"
  "v_pk_add_f16 v86, v54, v70\n"
  "v_mfma_f32_16x16x32_f16 a[184:187], v[28:31], v[108:111], a[184:187]\n"
  "s_add_i32 s64, s50, 4214784\n"
  "buffer_load_dwordx4 v238, s[40:43], s64 offen sc1 lds\n"
  "v_pk_add_f16 v87, v55, v71\n"
  "v_mfma_f32_16x16x32_f16 a[152:155], v[32:35], v[120:123], a[152:155]\n"
  "ds_read_b128 v[108:111], v232 offset:2560\n"
  "v_pk_add_f16 v88, v56, v72\n"
  "v_mfma_f32_16x16x32_f16 a[164:167], v[36:39], v[120:123], a[164:167]\n"
  "v_pk_add_f16 v89, v57, v73\n"
  "v_pk_add_f16 v90, v58, v74\n"
  "v_mfma_f32_16x16x32_f16 a[176:179], v[40:43], v[120:123], a[176:179]\n"
  "s_add_i32 m0, s47, 30720\n"
  "v_pk_add_f16 v91, v59, v75\n"
  "v_pk_add_f16 v92, v60, v76\n"
  "v_mfma_f32_16x16x32_f16 a[188:191], v[44:47], v[120:123], a[188:191]\n"
  "s_add_i32 s64, s50, 32768\n"
  "buffer_load_dwordx4 v238, s[40:43], s64 offen sc1 lds\n"
  "v_pk_add_f16 v93, v61, v77\n"
  "v_mfma_f32_16x16x32_f16 a[192:195], v[0:3], v[112:115], a[192:195]\n"
  "s_add_i32 s47, s44, s46\n"
  "v_pk_add_f16 v120, v112, v116\n"
  "v_pk_add_f16 v121, v113, v117\n"
  "v_mfma_f32_16x16x32_f16 a[204:207], v[4:7], v[112:115], a[204:207]\n"
  "s_add_i32 s48, s47, s45\n"
  "v_pk_add_f16 v122, v114, v118\n"
  "v_pk_add_f16 v123, v115, v119\n"
  "v_mfma_f32_16x16x32_f16 a[216:219], v[8:11], v[112:115], a[216:219]\n"
  "s_add_i32 s46, s46, s49\n"
  "v_pk_add_f16 v94, v62, v78\n"
  "v_pk_add_f16 v95, v63, v79\n"
  "v_mfma_f32_16x16x32_f16 a[228:231], v[12:15], v[112:115], a[228:231]\n"
  "s_add_i32 s50, s50, 4194304\n"
  "buffer_store_dwordx4 v[128:131], v242, s[40:43], s56 offen offset:128 sc1\n"
  "v_add_u32 v229, s49, v229\n"
  "v_mfma_f32_16x16x32_f16 a[196:199], v[16:19], v[116:119], a[196:199]\n"
  "v_add_u32 v230, s49, v230\n"
  "v_add_u32 v231, s49, v231\n"
  "v_mfma_f32_16x16x32_f16 a[208:211], v[20:23], v[116:119], a[208:211]\n"
  "v_add_u32 v232, s49, v232\n"
  "v_mfma_f32_16x16x32_f16 a[220:223], v[24:27], v[116:119], a[220:223]\n"
  "v_mfma_f32_16x16x32_f16 a[232:235], v[28:31], v[116:119], a[232:235]\n"
  "v_mfma_f32_16x16x32_f16 a[200:203], v[32:35], v[120:123], a[200:203]\n"
  "s_waitcnt vmcnt(12) lgkmcnt(0)\n"
  "v_mfma_f32_16x16x32_f16 a[212:215], v[36:39], v[120:123], a[212:215]\n"
  "v_cmp_eq_u32 vcc, 0, v247\n"
  "s_cbranch_vccnz .Lspin_pe_None_%=\n"
  ".Lspun_pe_None_%=:\n"
  "s_cmp_gt_u32 s62, 88\n"
  "s_cbranch_scc1 .Lpace_pe_None_%=\n"
  ".Lpaced_pe_None_%=:\n"
  "v_mfma_f32_16x16x32_f16 a[224:227], v[40:43], v[120:123], a[224:227]\n"
  "s_barrier\n"
  "v_mfma_f32_16x16x32_f16 a[236:239], v[44:47], v[120:123], a[236:239]\n"
  "v_mfma_f32_16x16x32_f16 a[0:3], v[48:51], v[96:99], a[0:3]\n"
  "buffer_load_dwordx4 v[164:167], v240, s[40:43], s59 offen sc1\n"
  "v_pk_add_f16 v120, v96, v100\n"
  "v_mfma_f32_16x16x32_f16 a[12:15], v[52:55], v[96:99], a[12:15]\n"
  "v_pk_add_f16 v121, v97, v101\n"
  "v_pk_add_f16 v122, v98, v102\n"
  "v_mfma_f32_16x16x32_f16 a[24:27], v[56:59], v[96:99], a[24:27]\n"
  "s_min_u32 s67, s60, 11\n"
  "s_lshl_b32 s67, s67, 2\n"
  "v_pk_add_f16 v123, v99, v103\n"
  "v_mfma_f32_16x16x32_f16 a[36:39], v[60:63], v[96:99], a[36:39]\n"
  "s_add_i32 m0, s44, 155648\n"
  "s_and_b32 s65, s51, 0xffffe300\n"
  "ds_read_b64_tr_b16 v[0:1], v228 offset:0\n"
  "v_mfma_f32_16x16x32_f16 a[4:7], v[64:67], v[100:103], a[4:7]\n"
  "buffer_load_dwordx4 v239, s[40:43], s58 offen sc1 lds\n"
  "v_readlane_b32 s66, v244, s60\n"
  "v_mfma_f32_16x16x32_f16 a[16:19], v[68:71], v[100:103], a[16:19]\n"
  "s_cmp_eq_u32 s46, 73728\n"
  "s_cselect_b32 s49, s68, s69\n"
  "ds_read_b64_tr_b16 v[124:125], v233 offset:2048\n"
  "v_mfma_f32_16x16x32_f16 a[28:31], v[72:75], v[100:103], a[28:31]\n"
  "s_add_i32 s57, s57, 32768\n"
  "ds_read_b64_tr_b16 v[128:129], v234 offset:2048\n"
  "v_add_u32 v233, 4096, v233\n"
  "v_mfma_f32_16x16x32_f16 a[40:43], v[76:79], v[100:103], a[40:43]\n"
  "s_add_i32 m0, s47, 0\n"
  "s_lshr_b32 s59, s57, 9\n"
  "ds_read_b64_tr_b16 v[2:3], v228 offset:8192\n"
  "v_mfma_f32_16x16x32_f16 a[8:11], v[80:83], v[120:123], a[8:11]\n"
  "s_add_i32 s64, s50, 4194304\n"
  "buffer_load_dwordx4 v237, s[40:43], s64 offen sc1 lds\n"
  "v_add_u32 v234, 4096, v234\n"
  "v_mfma_f32_16x16x32_f16 a[20:23], v[84:87], v[120:123], a[20:23]\n"
  "ds_read_b128 v[96:99], v232 offset:4096\n"
  "ds_read_b64_tr_b16 v[160:161], v241 offset:0\n"
  "v_mfma_f32_16x16x32_f16 a[32:35], v[88:91], v[120:123], a[32:35]\n"
  "s_add_i32 s53, s53, 8192\n"
  "s_lshr_b32 s52, s52, 1\n"
  "ds_read_b64_tr_b16 v[162:163], v241 offset:512\n"
  "v_mfma_f32_16x16x32_f16 a[44:47], v[92:95], v[120:123], a[44:47]\n"
  "s_add_i32 s60, s60, 1\n"
  "ds_read_b64_tr_b16 v[132:133], v235 offset:2048\n"
  "v_add_u32 v235, 4096, v235\n"
  "v_mfma_f32_16x16x32_f16 a[48:51], v[48:51], v[104:107], a[48:51]\n"
  "s_add_i32 s51, s51, 8448\n"
  "buffer_store_dword v232, v245, s[40:43], s65 offen sc1\n"
  "ds_read_b64_tr_b16 v[16:17], v228 offset:256\n"
  "v_mfma_f32_16x16x32_f16 a[60:63], v[52:55], v[104:107], a[60:63]\n"
  "ds_read_b128 v[100:103], v232 offset:4608\n"
  "v_pk_add_f16 v120, v104, v108\n"
  "v_mfma_f32_16x16x32_f16 a[72:75], v[56:59], v[104:107], a[72:75]\n"
  "ds_read_b64_tr_b16 v[136:137], v236 offset:2048\n"
  "ds_read_b64_tr_b16 v[18:19], v228 offset:8448\n"
  "s_set_gpr_idx_on s67, gpr_idx(SRC0)\n"
  "v_mov_b64 v[156:157], v[180:181]\n"
  "v_mov_b64 v[158:159], v[182:183]\n"
  "s_set_gpr_idx_off\n"
  "v_mfma_f32_16x16x32_f16 a[84:87], v[60:63], v[104:107], a[84:87]\n"
  "ds_read_b64_tr_b16 v[4:5], v229 offset:0\n"
  "v_pk_add_f16 v121, v105, v109\n"
  "v_mfma_f32_16x16x32_f16 a[52:55], v[64:67], v[108:111], a[52:55]\n"
  "buffer_store_dwordx4 v[156:159], v243, s[40:43], s66 offen sc1\n"
  "v_pk_add_f16 v122, v106, v110\n"
  "v_mfma_f32_16x16x32_f16 a[64:67], v[68:71], v[108:111], a[64:67]\n"
  "s_waitcnt lgkmcnt(6)\n"
  "ds_read_b64_tr_b16 v[6:7], v229 offset:8192\n"
  "v_pk_add_f16 v123, v107, v111\n"
  "v_mfma_f32_16x16x32_f16 a[76:79], v[72:75], v[108:111], a[76:79]\n"
  "s_bitcmp1_b32 s52, 0\n"
  "s_cselect_b32 s54, s53, 134217728\n"
  "ds_read_b64_tr_b16 v[20:21], v229 offset:256\n"
  "v_mfma_f32_16x16x32_bf16 v[172:175], v[160:163], v[168:171], v[172:175]\n"
  "s_add_i32 m0, s47, 4096\n"
  "ds_read_b64_tr_b16 v[22:23], v229 offset:8448\n"
  "v_add_u32 v228, s49, v228\n"
  "v_mfma_f32_16x16x32_f16 a[88:91], v[76:79], v[108:111], a[88:91]\n"
  "s_add_i32 s64, s50, 4198400\n"
  "buffer_load_dwordx4 v237, s[40:43], s64 offen sc1 lds\n"
  "v_add_u32 v229, s49, v229\n"
  "v_mfma_f32_16x16x32_f16 a[56:59], v[80:83], v[120:123], a[56:59]\n"
  "s_waitcnt lgkmcnt(5)\n"
  "ds_read_b128 v[104:107], v232 offset:6144\n"
  "v_mov_b64 v[126:127], v[124:125]\n"
  "v_mfma_f32_16x16x32_f16 a[68:71], v[84:87], v[120:123], a[68:71]\n"
  "ds_read_b64_tr_b16 v[8:9], v230 offset:0\n"
  "v_mov_b64 v[130:131], v[128:129]\n"
  "v_mfma_f32_16x16x32_f16 a[80:83], v[88:91], v[120:123], a[80:83]\n"
  "s_add_i32 m0, s47, 8192\n"
  "ds_read_b128 v[108:111], v232 offset:6656\n"
  "v_mov_b64 v[134:135], v[132:133]\n"
  "v_mfma_f32_16x16x32_f16 a[92:95], v[92:95], v[120:123], a[92:95]\n"
  "s_add_i32 s64, s50, 4202496\n"
  "buffer_load_dwordx4 v237, s[40:43], s64 offen sc1 lds\n"
  "v_mov_b64 v[138:139], v[136:137]\n"
  "v_mfma_f32_16x16x32_f16 a[96:99], v[48:51], v[96:99], a[96:99]\n"
  "s_waitcnt lgkmcnt(7)\n"
  "ds_read_b64_tr_b16 v[10:11], v230 offset:8192\n"
  "v_pk_add_f16 v120, v96, v100\n"
  "v_mfma_f32_16x16x32_bf16 v[140:143], v[124:127], v[176:179], 0\n"
  "s_waitcnt lgkmcnt(4)\n"
  "ds_read_b64_tr_b16 v[24:25], v230 offset:256\n"
  "v_pk_add_f16 v121, v97, v101\n"
  "v_mfma_f32_16x16x32_f16 a[108:111], v[52:55], v[96:99], a[108:111]\n"
  "s_add_i32 m0, s47, 12288\n"
  "v_pk_add_f16 v122, v98, v102\n"
  "v_pk_add_f16 v123, v99, v103\n"
  "v_mfma_f32_16x16x32_f16 a[120:123], v[56:59], v[96:99], a[120:123]\n"
  "s_add_i32 s64, s50, 4206592\n"
  "buffer_load_dwordx4 v237, s[40:43], s64 offen sc1 lds\n"
  "v_pk_add_f16 v32, v0, v16\n"
  "v_mfma_f32_16x16x32_f16 a[132:135], v[60:63], v[96:99], a[132:135]\n"
  "ds_read_b64_tr_b16 v[26:27], v230 offset:8448\n"
  "v_cvt_pkrtz_f16_f32 v124, v140, v141\n"
  "v_mfma_f32_16x16x32_bf16 v[144:147], v[128:131], v[176:179], 0\n"
  "ds_read_b64_tr_b16 v[12:13], v231 offset:0\n"
  "v_cvt_pkrtz_f16_f32 v125, v142, v143\n"
  "v_mfma_f32_16x16x32_f16 a[100:103], v[64:67], v[100:103], a[100:103]\n"
  "s_add_i32 m0, s47, 16384\n"
  "ds_read_b64_tr_b16 v[14:15], v231 offset:8192\n"
  "v_and_b32 v124, 0xfff8fff8, v124\n"
  "v_mfma_f32_16x16x32_f16 a[112:115], v[68:71], v[100:103], a[112:115]\n"
  "s_add_i32 s64, s50, 4194304\n"
  "buffer_load_dwordx4 v238, s[40:43], s64 offen sc1 lds\n"
  "v_and_b32 v125, 0xfff8fff8, v125\n"
  "v_mfma_f32_16x16x32_f16 a[124:127], v[72:75], v[100:103], a[124:127]\n"
  "ds_read_b128 v[96:99], v232 offset:0\n"
  "v_cvt_pkrtz_f16_f32 v126, v144, v145\n"
  "v_mfma_f32_16x16x32_bf16 v[148:151], v[132:135], v[176:179], 0\n"
  "ds_read_b128 v[112:115], v232 offset:8192\n"
  "v_cvt_pkrtz_f16_f32 v127, v146, v147\n"
  "v_mfma_f32_16x16x32_f16 a[136:139], v[76:79], v[100:103], a[136:139]\n"
  "s_add_i32 m0, s47, 20480\n"
  "ds_read_b64_tr_b16 v[28:29], v231 offset:256\n"
  "v_and_b32 v126, 0xfff8fff8, v126\n"
  "v_mfma_f32_16x16x32_f16 a[104:107], v[80:83], v[120:123], a[104:107]\n"
  "s_add_i32 s64, s50, 4096\n"
  "buffer_load_dwordx4 v238, s[40:43], s64 offen sc1 lds\n"
  "v_and_b32 v127, 0xfff8fff8, v127\n"
  "v_mfma_f32_16x16x32_f16 a[116:119], v[84:87], v[120:123], a[116:119]\n"
  "s_waitcnt lgkmcnt(8)\n"
  "ds_read_b128 v[100:103], v232 offset:512\n"
  "v_cvt_pkrtz_f16_f32 v128, v148, v149\n"
  "v_mfma_f32_16x16x32_bf16 v[152:155], v[136:139], v[176:179], 0\n"
  "s_waitcnt lgkmcnt(6)\n"
  "ds_read_b128 v[116:119], v232 offset:8704\n"
  "v_cvt_pkrtz_f16_f32 v129, v150, v151\n"
  "v_mfma_f32_16x16x32_f16 a[128:131], v[88:91], v[120:123], a[128:131]\n"
  "s_add_i32 m0, s48, 24576\n"
  "ds_read_b64_tr_b16 v[30:31], v231 offset:8448\n"
  "v_and_b32 v128, 0xfff8fff8, v128\n"
  "v_mfma_f32_16x16x32_f16 a[140:143], v[92:95], v[120:123], a[140:143]\n"
  "s_add_i32 s64, s50, 16384\n"
  "buffer_load_dwordx4 v238, s[40:43], s64 offen sc1 lds\n"
  "v_and_b32 v129, 0xfff8fff8, v129\n"
  "v_mfma_f32_16x16x32_f16 a[144:147], v[48:51], v[104:107], a[144:147]\n"
  "v_pk_add_f16 v120, v104, v108\n"
  "v_pk_add_f16 v121, v105, v109\n"
  "v_mfma_f32_16x16x32_f16 a[156:159], v[52:55], v[104:107], a[156:159]\n"
  "v_pk_add_f16 v122, v106, v110\n"
  "v_pk_add_f16 v123, v107, v111\n"
  "v_mfma_f32_16x16x32_f16 a[168:171], v[56:59], v[104:107], a[168:171]\n"
  "v_cvt_pkrtz_f16_f32 v130, v152, v153\n"
  "v_cvt_pkrtz_f16_f32 v131, v154, v155\n"
  "v_mfma_f32_16x16x32_f16 a[180:183], v[60:63], v[104:107], a[180:183]\n"
  "buffer_store_dwordx4 v[124:127], v242, s[40:43], s56 offen offset:256 sc1\n"
  "v_and_b32 v130, 0xfff8fff8, v130\n"
  "v_mfma_f32_16x16x32_f16 a[148:151], v[64:67], v[108:111], a[148:151]\n"
  "ds_read_b128 v[104:107], v232 offset:2048\n"
  "v_and_b32 v131, 0xfff8fff8, v131\n"
  "v_mfma_f32_16x16x32_f16 a[160:163], v[68:71], v[108:111], a[160:163]\n"
  "s_cmp_lt_u32 s57, 0x80000\n"
  "s_cselect_b32 s58, s57, 0xf0000000\n"
  "s_cselect_b32 s59, s59, 0xf0000000\n"
  "v_mfma_f32_16x16x32_f16 a[172:175], v[72:75], v[108:111], a[172:175]\n"
  "s_add_i32 m0, s47, 26624\n"
  "s_waitcnt lgkmcnt(1)\n"
  "v_pk_add_f16 v33, v1, v17\n"
  "v_mfma_f32_16x16x32_f16 a[184:187], v[76:79], v[108:111], a[184:187]\n"
  "s_add_i32 s64, s50, 4214784\n"
  "buffer_load_dwordx4 v238, s[40:43], s64 offen sc1 lds\n"
  "v_pk_add_f16 v34, v2, v18\n"
  "v_mfma_f32_16x16x32_f16 a[152:155], v[80:83], v[120:123], a[152:155]\n"
  "ds_read_b128 v[108:111], v232 offset:2560\n"
  "v_pk_add_f16 v35, v3, v19\n"
  "v_mfma_f32_16x16x32_f16 a[164:167], v[84:87], v[120:123], a[164:167]\n"
  "v_pk_add_f16 v36, v4, v20\n"
  "v_pk_add_f16 v37, v5, v21\n"
  "v_mfma_f32_16x16x32_f16 a[176:179], v[88:91], v[120:123], a[176:179]\n"
  "s_add_i32 m0, s47, 30720\n"
  "v_pk_add_f16 v38, v6, v22\n"
  "v_pk_add_f16 v39, v7, v23\n"
  "v_mfma_f32_16x16x32_f16 a[188:191], v[92:95], v[120:123], a[188:191]\n"
  "s_add_i32 s64, s50, 32768\n"
  "buffer_load_dwordx4 v238, s[40:43], s64 offen sc1 lds\n"
  "v_pk_add_f16 v40, v8, v24\n"
  "v_mfma_f32_16x16x32_f16 a[192:195], v[48:51], v[112:115], a[192:195]\n"
  "s_add_i32 s47, s44, s46\n"
  "v_pk_add_f16 v120, v112, v116\n"
  "v_pk_add_f16 v121, v113, v117\n"
  "v_mfma_f32_16x16x32_f16 a[204:207], v[52:55], v[112:115], a[204:207]\n"
  "s_add_i32 s48, s47, s45\n"
  "v_pk_add_f16 v122, v114, v118\n"
  "v_pk_add_f16 v123, v115, v119\n"
  "v_mfma_f32_16x16x32_f16 a[216:219], v[56:59], v[112:115], a[216:219]\n"
  "s_add_i32 s46, s46, s49\n"
  "v_pk_add_f16 v41, v9, v25\n"
  "v_pk_add_f16 v42, v10, v26\n"
  "v_mfma_f32_16x16x32_f16 a[228:231], v[60:63], v[112:115], a[228:231]\n"
  "s_add_i32 s50, s50, 4194304\n"
  "buffer_store_dwordx4 v[128:131], v242, s[40:43], s56 offen offset:384 sc1\n"
  "v_pk_add_f16 v43, v11, v27\n"
  "v_mfma_f32_16x16x32_f16 a[196:199], v[64:67], v[116:119], a[196:199]\n"
  "s_add_i32 s55, s55, 8404992\n"
  "v_pk_add_f16 v44, v12, v28\n"
  "v_pk_add_f16 v45, v13, v29\n"
  "v_mfma_f32_16x16x32_f16 a[208:211], v[68:71], v[116:119], a[208:211]\n"
  "s_and_b32 s56, s55, 0xff80c000\n"
  "v_pk_add_f16 v46, v14, v30\n"
  "v_pk_add_f16 v47, v15, v31\n"
  "v_mfma_f32_16x16x32_f16 a[220:223], v[72:75], v[116:119], a[220:223]\n"
  "v_add_u32 v230, s49, v230\n"
  "v_add_u32 v231, s49, v231\n"
  "v_mfma_f32_16x16x32_f16 a[232:235], v[76:79], v[116:119], a[232:235]\n"
  "v_add_u32 v232, s49, v232\n"
  "v_add_u32 v236, 4096, v236\n"
  "v_mfma_f32_16x16x32_f16 a[200:203], v[80:83], v[120:123], a[200:203]\n"
  "s_waitcnt vmcnt(13) lgkmcnt(0)\n"
  "v_mfma_f32_16x16x32_f16 a[212:215], v[84:87], v[120:123], a[212:215]\n"
  "s_cmp_gt_u32 s62, 88\n"
  "s_cbranch_scc1 .Lpace_po_None_%=\n"
  ".Lpaced_po_None_%=:\n"
  "v_mfma_f32_16x16x32_f16 a[224:227], v[88:91], v[120:123], a[224:227]\n"
  "s_barrier\n"
  "v_mfma_f32_16x16x32_f16 a[236:239], v[92:95], v[120:123], a[236:239]\n"
  "s_sub_i32 s63, s63, 1\n"
  "s_cmp_lg_u32 s63, 0\n"
  "s_cbranch_scc1 .LloopP_%=\n"
  ".Lcf_t:\n"
  "s_cmp_eq_u32 s85, 0\n"
  "s_cbranch_scc1 .Lnt_%=\n"
  "s_getpc_b64 s[86:87]\n"
  "s_mov_b32 m0, s85\n"
  "s_nop 0\n"
  "global_load_lds_dword v251, s[86:87]\n"
  ".Lnt_%=:\n"
  "v_mfma_f32_16x16x32_f16 a[0:3], v[0:3], v[96:99], a[0:3]\n"
  "buffer_load_dword v247, v246, s[40:43], s54 offen sc1\n"
  "v_pk_add_f16 v120, v96, v100\n"
  "v_mfma_f32_16x16x32_f16 a[12:15], v[4:7], v[96:99], a[12:15]\n"
  "v_pk_add_f16 v121, v97, v101\n"
  "v_pk_add_f16 v122, v98, v102\n"
  "v_mfma_f32_16x16x32_f16 a[24:27], v[8:11], v[96:99], a[24:27]\n"
  "ds_read_b64_tr_b16 v[48:49], v228 offset:0\n"
  "v_pk_add_f16 v123, v99, v103\n"
  "v_mfma_f32_16x16x32_f16 a[36:39], v[12:15], v[96:99], a[36:39]\n"
  "s_add_i32 m0, s47, 0\n"
  "ds_read_b64_tr_b16 v[50:51], v228 offset:8192\n"
  "v_accvgpr_read_b32 v124, a0\n"
  "v_mfma_f32_16x16x32_f16 a[4:7], v[16:19], v[100:103], a[4:7]\n"
  "s_add_i32 s64, s50, 4194304\n"
  "buffer_load_dwordx4 v237, s[40:43], s64 offen sc1 lds\n"
  "v_accvgpr_read_b32 v125, a1\n"
  "v_mfma_f32_16x16x32_f16 a[16:19], v[20:23], v[100:103], a[16:19]\n"
  "s_cmp_eq_u32 s46, 73728\n"
  "s_cselect_b32 s49, s68, s69\n"
  "ds_read_b128 v[96:99], v232 offset:4096\n"
  "v_mfma_f32_16x16x32_f16 a[28:31], v[24:27], v[100:103], a[28:31]\n"
  "ds_read_b64_tr_b16 v[64:65], v228 offset:256\n"
  "v_accvgpr_read_b32 v126, a2\n"
  "v_mfma_f32_16x16x32_f16 a[40:43], v[28:31], v[100:103], a[40:43]\n"
  "s_add_i32 m0, s47, 4096\n"
  "ds_read_b128 v[100:103], v232 offset:4608\n"
  "v_accvgpr_read_b32 v127, a3\n"
  "v_mfma_f32_16x16x32_f16 a[8:11], v[32:35], v[120:123], a[8:11]\n"
  "s_add_i32 s64, s50, 4198400\n"
  "buffer_load_dwordx4 v237, s[40:43], s64 offen sc1 lds\n"
  "v_accvgpr_read_b32 v128, a4\n"
  "v_mfma_f32_16x16x32_f16 a[20:23], v[36:39], v[120:123], a[20:23]\n"
  "ds_read_b64_tr_b16 v[66:67], v228 offset:8448\n"
  "v_accvgpr_read_b32 v129, a5\n"
  "v_mfma_f32_16x16x32_f16 a[32:35], v[40:43], v[120:123], a[32:35]\n"
  "ds_read_b64_tr_b16 v[52:53], v229 offset:0\n"
  "v_accvgpr_read_b32 v130, a6\n"
  "v_mfma_f32_16x16x32_f16 a[44:47], v[44:47], v[120:123], a[44:47]\n"
  "s_add_i32 m0, s47, 8192\n"
  "ds_read_b64_tr_b16 v[54:55], v229 offset:8192\n"
  "v_accvgpr_read_b32 v131, a7\n"
  "v_mfma_f32_16x16x32_f16 a[48:51], v[0:3], v[104:107], a[48:51]\n"
  "s_add_i32 s64, s50, 4202496\n"
  "buffer_load_dwordx4 v237, s[40:43], s64 offen sc1 lds\n"
  "v_pk_add_f16 v120, v104, v108\n"
  "v_mfma_f32_16x16x32_f16 a[60:63], v[4:7], v[104:107], a[60:63]\n"
  "v_pk_add_f16 v121, v105, v109\n"
  "v_pk_add_f16 v122, v106, v110\n"
  "v_mfma_f32_16x16x32_f16 a[72:75], v[8:11], v[104:107], a[72:75]\n"
  "ds_read_b64_tr_b16 v[68:69], v229 offset:256\n"
  "v_pk_add_f16 v123, v107, v111\n"
  "v_mfma_f32_16x16x32_f16 a[84:87], v[12:15], v[104:107], a[84:87]\n"
  "s_add_i32 m0, s47, 12288\n"
  "ds_read_b64_tr_b16 v[70:71], v229 offset:8448\n"
  "v_accvgpr_read_b32 v132, a8\n"
  "v_mfma_f32_16x16x32_f16 a[52:55], v[16:19], v[108:111], a[52:55]\n"
  "s_add_i32 s64, s50, 4206592\n"
  "buffer_load_dwordx4 v237, s[40:43], s64 offen sc1 lds\n"
  "v_accvgpr_read_b32 v133, a9\n"
  "v_mfma_f32_16x16x32_f16 a[64:67], v[20:23], v[108:111], a[64:67]\n"
  "s_waitcnt lgkmcnt(4)\n"
  "ds_read_b128 v[104:107], v232 offset:6144\n"
  "v_accvgpr_read_b32 v134, a10\n"
  "v_mfma_f32_16x16x32_f16 a[76:79], v[24:27], v[108:111], a[76:79]\n"
  "ds_read_b64_tr_b16 v[56:57], v230 offset:0\n"
  "v_accvgpr_read_b32 v135, a11\n"
  "v_mfma_f32_16x16x32_f16 a[88:91], v[28:31], v[108:111], a[88:91]\n"
  "s_add_i32 m0, s47, 16384\n"
  "ds_read_b128 v[108:111], v232 offset:6656\n"
  "v_accvgpr_read_b32 v136, a12\n"
  "v_mfma_f32_16x16x32_f16 a[56:59], v[32:35], v[120:123], a[56:59]\n"
  "s_add_i32 s64, s50, 4194304\n"
  "buffer_load_dwordx4 v238, s[40:43], s64 offen sc1 lds\n"
  "v_accvgpr_read_b32 v137, a13\n"
  "v_mfma_f32_16x16x32_f16 a[68:71], v[36:39], v[120:123], a[68:71]\n"
  "ds_read_b64_tr_b16 v[58:59], v230 offset:8192\n"
  "v_accvgpr_read_b32 v138, a14\n"
  "v_mfma_f32_16x16x32_f16 a[80:83], v[40:43], v[120:123], a[80:83]\n"
  "s_waitcnt lgkmcnt(4)\n"
  "ds_read_b64_tr_b16 v[72:73], v230 offset:256\n"
  "v_accvgpr_read_b32 v139, a15\n"
  "v_mfma_f32_16x16x32_f16 a[92:95], v[44:47], v[120:123], a[92:95]\n"
  "s_add_i32 m0, s47, 20480\n"
  "ds_read_b64_tr_b16 v[74:75], v230 offset:8448\n"
  "v_accvgpr_read_b32 v140, a16\n"
  "v_mfma_f32_16x16x32_f16 a[96:99], v[0:3], v[96:99], a[96:99]\n"
  "s_add_i32 s64, s50, 4096\n"
  "buffer_load_dwordx4 v238, s[40:43], s64 offen sc1 lds\n"
  "v_pk_add_f16 v120, v96, v100\n"
  "v_mfma_f32_16x16x32_f16 a[108:111], v[4:7], v[96:99], a[108:111]\n"
  "v_pk_add_f16 v121, v97, v101\n"
  "v_pk_add_f16 v122, v98, v102\n"
  "v_mfma_f32_16x16x32_f16 a[120:123], v[8:11], v[96:99], a[120:123]\n"
  "ds_read_b64_tr_b16 v[60:61], v231 offset:0\n"
  "v_pk_add_f16 v123, v99, v103\n"
  "v_mfma_f32_16x16x32_f16 a[132:135], v[12:15], v[96:99], a[132:135]\n"
  "s_add_i32 m0, s48, 24576\n"
  "ds_read_b64_tr_b16 v[62:63], v231 offset:8192\n"
  "v_accvgpr_read_b32 v141, a17\n"
  "v_mfma_f32_16x16x32_f16 a[100:103], v[16:19], v[100:103], a[100:103]\n"
  "s_add_i32 s64, s50, 16384\n"
  "buffer_load_dwordx4 v238, s[40:43], s64 offen sc1 lds\n"
  "v_accvgpr_read_b32 v142, a18\n"
  "v_mfma_f32_16x16x32_f16 a[112:115], v[20:23], v[100:103], a[112:115]\n"
  "ds_read_b128 v[96:99], v232 offset:0\n"
  "v_accvgpr_read_b32 v143, a19\n"
  "v_mfma_f32_16x16x32_f16 a[124:127], v[24:27], v[100:103], a[124:127]\n"
  "s_waitcnt lgkmcnt(3)\n"
  "ds_read_b128 v[112:115], v232 offset:8192\n"
  "v_accvgpr_read_b32 v144, a20\n"
  "v_mfma_f32_16x16x32_f16 a[136:139], v[28:31], v[100:103], a[136:139]\n"
  "s_add_i32 m0, s47, 26624\n"
  "ds_read_b64_tr_b16 v[76:77], v231 offset:256\n"
  "v_accvgpr_read_b32 v145, a21\n"
  "v_mfma_f32_16x16x32_f16 a[104:107], v[32:35], v[120:123], a[104:107]\n"
  "s_add_i32 s64, s50, 4214784\n"
  "buffer_load_dwordx4 v238, s[40:43], s64 offen sc1 lds\n"
  "v_accvgpr_read_b32 v146, a22\n"
  "v_mfma_f32_16x16x32_f16 a[116:119], v[36:39], v[120:123], a[116:119]\n"
  "ds_read_b128 v[100:103], v232 offset:512\n"
  "v_accvgpr_read_b32 v147, a23\n"
  "v_mfma_f32_16x16x32_f16 a[128:131], v[40:43], v[120:123], a[128:131]\n"
  "ds_read_b128 v[116:119], v232 offset:8704\n"
  "v_accvgpr_read_b32 v148, a24\n"
  "v_mfma_f32_16x16x32_f16 a[140:143], v[44:47], v[120:123], a[140:143]\n"
  "s_add_i32 m0, s47, 30720\n"
  "ds_read_b64_tr_b16 v[78:79], v231 offset:8448\n"
  "v_accvgpr_read_b32 v149, a25\n"
  "v_mfma_f32_16x16x32_f16 a[144:147], v[0:3], v[104:107], a[144:147]\n"
  "s_add_i32 s64, s50, 32768\n"
  "buffer_load_dwordx4 v238, s[40:43], s64 offen sc1 lds\n"
  "v_pk_add_f16 v120, v104, v108\n"
  "v_mfma_f32_16x16x32_f16 a[156:159], v[4:7], v[104:107], a[156:159]\n"
  "v_pk_add_f16 v121, v105, v109\n"
  "v_pk_add_f16 v122, v106, v110\n"
  "v_mfma_f32_16x16x32_f16 a[168:171], v[8:11], v[104:107], a[168:171]\n"
  "v_pk_add_f16 v123, v107, v111\n"
  "v_accvgpr_read_b32 v150, a26\n"
  "v_mfma_f32_16x16x32_f16 a[180:183], v[12:15], v[104:107], a[180:183]\n"
  "ds_read_b128 v[104:107], v232 offset:2048\n"
  "v_accvgpr_read_b32 v151, a27\n"
  "v_mfma_f32_16x16x32_f16 a[148:151], v[16:19], v[108:111], a[148:151]\n"
  "s_add_i32 s47, s44, s46\n"
  "v_accvgpr_read_b32 v152, a28\n"
  "v_accvgpr_read_b32 v153, a29\n"
  "v_mfma_f32_16x16x32_f16 a[160:163], v[20:23], v[108:111], a[160:163]\n"
  "s_add_i32 s48, s47, s45\n"
  "v_accvgpr_read_b32 v154, a30\n"
  "v_accvgpr_read_b32 v155, a31\n"
  "v_mfma_f32_16x16x32_f16 a[172:175], v[24:27], v[108:111], a[172:175]\n"
  "s_waitcnt lgkmcnt(1)\n"
  "v_accvgpr_read_b32 v156, a32\n"
  "v_accvgpr_read_b32 v157, a33\n"
  "v_mfma_f32_16x16x32_f16 a[184:187], v[28:31], v[108:111], a[184:187]\n"
  "s_add_i32 s46, s46, s49\n"
  "ds_read_b128 v[108:111], v232 offset:2560\n"
  "v_accvgpr_read_b32 v158, a34\n"
  "v_mfma_f32_16x16x32_f16 a[152:155], v[32:35], v[120:123], a[152:155]\n"
  "s_add_i32 s50, s50, 4194304\n"
  "v_accvgpr_read_b32 v159, a35\n"
  "v_accvgpr_read_b32 v160, a36\n"
  "v_mfma_f32_16x16x32_f16 a[164:167], v[36:39], v[120:123], a[164:167]\n"
  "v_accvgpr_read_b32 v161, a37\n"
  "v_accvgpr_read_b32 v162, a38\n"
  "v_mfma_f32_16x16x32_f16 a[176:179], v[40:43], v[120:123], a[176:179]\n"
  "v_accvgpr_read_b32 v163, a39\n"
  "v_accvgpr_read_b32 v164, a40\n"
  "v_mfma_f32_16x16x32_f16 a[188:191], v[44:47], v[120:123], a[188:191]\n"
  "v_accvgpr_read_b32 v165, a41\n"
  "v_accvgpr_read_b32 v166, a42\n"
  "v_mfma_f32_16x16x32_f16 a[192:195], v[0:3], v[112:115], a[192:195]\n"
  "v_pk_add_f16 v120, v112, v116\n"
  "v_pk_add_f16 v121, v113, v117\n"
  "v_mfma_f32_16x16x32_f16 a[204:207], v[4:7], v[112:115], a[204:207]\n"
  "v_pk_add_f16 v122, v114, v118\n"
  "v_pk_add_f16 v123, v115, v119\n"
  "v_mfma_f32_16x16x32_f16 a[216:219], v[8:11], v[112:115], a[216:219]\n"
  "v_accvgpr_read_b32 v167, a43\n"
  "v_accvgpr_read_b32 v168, a44\n"
  "v_mfma_f32_16x16x32_f16 a[228:231], v[12:15], v[112:115], a[228:231]\n"
  "v_accvgpr_read_b32 v169, a45\n"
  "v_accvgpr_read_b32 v170, a46\n"
  "v_mfma_f32_16x16x32_f16 a[196:199], v[16:19], v[116:119], a[196:199]\n"
  "v_accvgpr_read_b32 v171, a47\n"
  "v_pk_add_f16 v80, v48, v64\n"
  "v_mfma_f32_16x16x32_f16 a[208:211], v[20:23], v[116:119], a[208:211]\n"
  "v_pk_add_f16 v81, v49, v65\n"
  "v_pk_add_f16 v82, v50, v66\n"
  "v_mfma_f32_16x16x32_f16 a[220:223], v[24:27], v[116:119], a[220:223]\n"
  "v_pk_add_f16 v83, v51, v67\n"
  "v_pk_add_f16 v84, v52, v68\n"
  "v_mfma_f32_16x16x32_f16 a[232:235], v[28:31], v[116:119], a[232:235]\n"
  "v_pk_add_f16 v85, v53, v69\n"
  "v_pk_add_f16 v86, v54, v70\n"
  "v_mfma_f32_16x16x32_f16 a[200:203], v[32:35], v[120:123], a[200:203]\n"
  "v_pk_add_f16 v87, v55, v71\n"
  "v_pk_add_f16 v88, v56, v72\n"
  "s_waitcnt vmcnt(9) lgkmcnt(0)\n"
  "v_mfma_f32_16x16x32_f16 a[212:215], v[36:39], v[120:123], a[212:215]\n"
  "v_cmp_eq_u32 vcc, 0, v247\n"
  "s_cbranch_vccnz .Lspin_s22_None_%=\n"
  ".Lspun_s22_None_%=:\n"
  "v_mfma_f32_16x16x32_f16 a[224:227], v[40:43], v[120:123], a[224:227]\n"
  "v_pk_add_f16 v89, v57, v73\n"
  "v_pk_add_f16 v90, v58, v74\n"
  "s_barrier\n"
  "v_mfma_f32_16x16x32_f16 a[236:239], v[44:47], v[120:123], a[236:239]\n"
  "v_pk_add_f16 v91, v59, v75\n"
  "v_pk_add_f16 v92, v60, v76\n"
  "v_pk_add_f16 v93, v61, v77\n"
  "v_pk_add_f16 v94, v62, v78\n"
  "v_pk_add_f16 v95, v63, v79\n"
  "v_add_u32 v228, s49, v228\n"
  "v_add_u32 v229, s49, v229\n"
  "v_add_u32 v230, s49, v230\n"
  "v_add_u32 v231, s49, v231\n"
  "v_add_u32 v232, s49, v232\n"
  "v_mfma_f32_16x16x32_f16 v[124:127], v[48:51], v[96:99], v[124:127]\n"
  "v_pk_add_f16 v120, v96, v100\n"
  "v_pk_add_f16 v121, v97, v101\n"
  "v_mfma_f32_16x16x32_f16 v[136:139], v[52:55], v[96:99], v[136:139]\n"
  "v_pk_add_f16 v122, v98, v102\n"
  "v_pk_add_f16 v123, v99, v103\n"
  "v_mfma_f32_16x16x32_f16 v[148:151], v[56:59], v[96:99], v[148:151]\n"
  "s_and_b32 s65, s51, 0xffffe300\n"
  "ds_read_b64_tr_b16 v[0:1], v228 offset:0\n"
  "ds_read_b64_tr_b16 v[2:3], v228 offset:8192\n"
  "v_mfma_f32_16x16x32_f16 v[160:163], v[60:63], v[96:99], v[160:163]\n"
  "s_add_i32 m0, s47, 0\n"
  "ds_read_b128 v[96:99], v232 offset:4096\n"
  "ds_read_b64_tr_b16 v[16:17], v228 offset:256\n"
  "v_mfma_f32_16x16x32_f16 v[128:131], v[64:67], v[100:103], v[128:131]\n"
  "s_add_i32 s64, s50, 4194304\n"
  "buffer_load_dwordx4 v237, s[40:43], s64 offen sc1 lds\n"
  "ds_read_b64_tr_b16 v[18:19], v228 offset:8448\n"
  "v_mfma_f32_16x16x32_f16 v[140:143], v[68:71], v[100:103], v[140:143]\n"
  "s_cmp_eq_u32 s46, 73728\n"
  "s_cselect_b32 s49, s68, s69\n"
  "ds_read_b64_tr_b16 v[4:5], v229 offset:0\n"
  "v_mfma_f32_16x16x32_f16 v[152:155], v[72:75], v[100:103], v[152:155]\n"
  "ds_read_b64_tr_b16 v[6:7], v229 offset:8192\n"
  "v_add_u32 v228, s49, v228\n"
  "v_mfma_f32_16x16x32_f16 v[164:167], v[76:79], v[100:103], v[164:167]\n"
  "ds_read_b128 v[100:103], v232 offset:4608\n"
  "ds_read_b64_tr_b16 v[20:21], v229 offset:256\n"
  "v_mfma_f32_16x16x32_f16 v[132:135], v[80:83], v[120:123], v[132:135]\n"
  "buffer_store_dword v232, v245, s[40:43], s65 offen sc1\n"
  "ds_read_b64_tr_b16 v[22:23], v229 offset:8448\n"
  "v_mfma_f32_16x16x32_f16 v[144:147], v[84:87], v[120:123], v[144:147]\n"
  "ds_read_b64_tr_b16 v[8:9], v230 offset:0\n"
  "v_add_u32 v229, s49, v229\n"
  "v_mfma_f32_16x16x32_f16 v[156:159], v[88:91], v[120:123], v[156:159]\n"
  "s_add_i32 s53, s53, 8192\n"
  "s_lshr_b32 s52, s52, 1\n"
  "ds_read_b64_tr_b16 v[10:11], v230 offset:8192\n"
  "v_mfma_f32_16x16x32_f16 v[168:171], v[92:95], v[120:123], v[168:171]\n"
  "s_add_i32 m0, s47, 4096\n"
  "s_waitcnt lgkmcnt(7)\n"
  "ds_read_b64_tr_b16 v[24:25], v230 offset:256\n"
  "v_mfma_f32_16x16x32_f16 a[48:51], v[48:51], v[104:107], a[48:51]\n"
  "s_add_i32 s64, s50, 4198400\n"
  "buffer_load_dwordx4 v237, s[40:43], s64 offen sc1 lds\n"
  "v_pk_add_f16 v120, v104, v108\n"
  "v_mfma_f32_16x16x32_f16 a[60:63], v[52:55], v[104:107], a[60:63]\n"
  "s_add_i32 s51, s51, 8448\n"
  "v_pk_add_f16 v121, v105, v109\n"
  "v_pk_add_f16 v122, v106, v110\n"
  "v_mfma_f32_16x16x32_f16 a[72:75], v[56:59], v[104:107], a[72:75]\n"
  "ds_read_b64_tr_b16 v[26:27], v230 offset:8448\n"
  "v_pk_add_f16 v123, v107, v111\n"
  "v_mfma_f32_16x16x32_f16 a[84:87], v[60:63], v[104:107], a[84:87]\n"
  "s_add_i32 m0, s47, 8192\n"
  "ds_read_b128 v[104:107], v232 offset:6144\n"
  "v_accvgpr_read_b32 v180, a48\n"
  "v_mfma_f32_16x16x32_f16 a[52:55], v[64:67], v[108:111], a[52:55]\n"
  "s_add_i32 s64, s50, 4202496\n"
  "buffer_load_dwordx4 v237, s[40:43], s64 offen sc1 lds\n"
  "v_accvgpr_read_b32 v181, a49\n"
  "v_mfma_f32_16x16x32_f16 a[64:67], v[68:71], v[108:111], a[64:67]\n"
  "s_bitcmp1_b32 s52, 0\n"
  "s_cselect_b32 s54, s53, 134217728\n"
  "ds_read_b64_tr_b16 v[12:13], v231 offset:0\n"
  "v_mfma_f32_16x16x32_f16 a[76:79], v[72:75], v[108:111], a[76:79]\n"
  "s_waitcnt lgkmcnt(6)\n"
  "ds_read_b64_tr_b16 v[14:15], v231 offset:8192\n"
  "v_accvgpr_read_b32 v182, a50\n"
  "v_mfma_f32_16x16x32_f16 a[88:91], v[76:79], v[108:111], a[88:91]\n"
  "s_add_i32 m0, s47, 12288\n"
  "ds_read_b128 v[108:111], v232 offset:6656\n"
  "v_accvgpr_read_b32 v183, a51\n"
  "v_mfma_f32_16x16x32_f16 a[56:59], v[80:83], v[120:123], a[56:59]\n"
  "s_add_i32 s64, s50, 4206592\n"
  "buffer_load_dwordx4 v237, s[40:43], s64 offen sc1 lds\n"
  "v_accvgpr_read_b32 v184, a52\n"
  "v_mfma_f32_16x16x32_f16 a[68:71], v[84:87], v[120:123], a[68:71]\n"
  "ds_read_b128 v[112:115], v232 offset:8192\n"
  "v_accvgpr_read_b32 v185, a53\n"
  "v_mfma_f32_16x16x32_f16 a[80:83], v[88:91], v[120:123], a[80:83]\n"
  "s_waitcnt lgkmcnt(5)\n"
  "ds_read_b64_tr_b16 v[28:29], v231 offset:256\n"
  "v_accvgpr_read_b32 v186, a54\n"
  "v_mfma_f32_16x16x32_f16 a[92:95], v[92:95], v[120:123], a[92:95]\n"
  "s_add_i32 m0, s47, 16384\n"
  "ds_read_b128 v[116:119], v232 offset:8704\n"
  "v_accvgpr_read_b32 v187, a55\n"
  "v_mfma_f32_16x16x32_f16 a[96:99], v[48:51], v[96:99], a[96:99]\n"
  "s_add_i32 s64, s50, 4194304\n"
  "buffer_load_dwordx4 v238, s[40:43], s64 offen sc1 lds\n"
  "v_pk_add_f16 v120, v96, v100\n"
  "v_mfma_f32_16x16x32_f16 a[108:111], v[52:55], v[96:99], a[108:111]\n"
  "v_pk_add_f16 v121, v97, v101\n"
  "v_pk_add_f16 v122, v98, v102\n"
  "v_mfma_f32_16x16x32_f16 a[120:123], v[56:59], v[96:99], a[120:123]\n"
  "ds_read_b64_tr_b16 v[30:31], v231 offset:8448\n"
  "v_pk_add_f16 v123, v99, v103\n"
  "v_mfma_f32_16x16x32_f16 a[132:135], v[60:63], v[96:99], a[132:135]\n"
  "s_add_i32 m0, s47, 20480\n"
  "ds_read_b128 v[96:99], v232 offset:0\n"
  "v_accvgpr_read_b32 v188, a56\n"
  "v_mfma_f32_16x16x32_f16 a[100:103], v[64:67], v[100:103], a[100:103]\n"
  "s_add_i32 s64, s50, 4096\n"
  "buffer_load_dwordx4 v238, s[40:43], s64 offen sc1 lds\n"
  "v_accvgpr_read_b32 v189, a57\n"
  "v_mfma_f32_16x16x32_f16 a[112:115], v[68:71], v[100:103], a[112:115]\n"
  "v_accvgpr_read_b32 v190, a58\n"
  "v_accvgpr_read_b32 v191, a59\n"
  "v_mfma_f32_16x16x32_f16 a[124:127], v[72:75], v[100:103], a[124:127]\n"
  "v_accvgpr_read_b32 v192, a60\n"
  "v_accvgpr_read_b32 v193, a61\n"
  "v_mfma_f32_16x16x32_f16 a[136:139], v[76:79], v[100:103], a[136:139]\n"
  "s_add_i32 m0, s48, 24576\n"
  "ds_read_b128 v[100:103], v232 offset:512\n"
  "v_accvgpr_read_b32 v194, a62\n"
  "v_mfma_f32_16x16x32_f16 a[104:107], v[80:83], v[120:123], a[104:107]\n"
  "s_add_i32 s64, s50, 16384\n"
  "buffer_load_dwordx4 v238, s[40:43], s64 offen sc1 lds\n"
  "v_accvgpr_read_b32 v195, a63\n"
  "v_mfma_f32_16x16x32_f16 a[116:119], v[84:87], v[120:123], a[116:119]\n"
  "s_waitcnt lgkmcnt(6)\n"
  "v_accvgpr_read_b32 v196, a64\n"
  "v_accvgpr_read_b32 v197, a65\n"
  "v_mfma_f32_16x16x32_f16 a[128:131], v[88:91], v[120:123], a[128:131]\n"
  "s_waitcnt lgkmcnt(2)\n"
  "v_accvgpr_read_b32 v198, a66\n"
  "v_accvgpr_read_b32 v199, a67\n"
  "v_mfma_f32_16x16x32_f16 a[140:143], v[92:95], v[120:123], a[140:143]\n"
  "s_add_i32 m0, s47, 26624\n"
  "v_accvgpr_read_b32 v200, a68\n"
  "v_accvgpr_read_b32 v201, a69\n"
  "v_mfma_f32_16x16x32_f16 a[144:147], v[48:51], v[104:107], a[144:147]\n"
  "s_add_i32 s64, s50, 4214784\n"
  "buffer_load_dwordx4 v238, s[40:43], s64 offen sc1 lds\n"
  "v_pk_add_f16 v120, v104, v108\n"
  "v_mfma_f32_16x16x32_f16 a[156:159], v[52:55], v[104:107], a[156:159]\n"
  "v_pk_add_f16 v121, v105, v109\n"
  "v_pk_add_f16 v122, v106, v110\n"
  "v_mfma_f32_16x16x32_f16 a[168:171], v[56:59], v[104:107], a[168:171]\n"
  "v_pk_add_f16 v123, v107, v111\n"
  "v_accvgpr_read_b32 v202, a70\n"
  "v_mfma_f32_16x16x32_f16 a[180:183], v[60:63], v[104:107], a[180:183]\n"
  "s_add_i32 m0, s47, 30720\n"
  "ds_read_b128 v[104:107], v232 offset:2048\n"
  "v_accvgpr_read_b32 v203, a71\n"
  "v_mfma_f32_16x16x32_f16 a[148:151], v[64:67], v[108:111], a[148:151]\n"
  "s_add_i32 s64, s50, 32768\n"
  "buffer_load_dwordx4 v238, s[40:43], s64 offen sc1 lds\n"
  "v_accvgpr_read_b32 v204, a72\n"
  "v_mfma_f32_16x16x32_f16 a[160:163], v[68:71], v[108:111], a[160:163]\n"
  "s_add_i32 s47, s44, s46\n"
  "v_accvgpr_read_b32 v205, a73\n"
  "v_accvgpr_read_b32 v206, a74\n"
  "v_mfma_f32_16x16x32_f16 a[172:175], v[72:75], v[108:111], a[172:175]\n"
  "s_add_i32 s48, s47, s45\n"
  "v_accvgpr_read_b32 v207, a75\n"
  "v_accvgpr_read_b32 v208, a76\n"
  "v_mfma_f32_16x16x32_f16 a[184:187], v[76:79], v[108:111], a[184:187]\n"
  "s_add_i32 s46, s46, s49\n"
  "ds_read_b128 v[108:111], v232 offset:2560\n"
  "v_accvgpr_read_b32 v209, a77\n"
  "v_mfma_f32_16x16x32_f16 a[152:155], v[80:83], v[120:123], a[152:155]\n"
  "s_add_i32 s50, s50, 4194304\n"
  "v_accvgpr_read_b32 v210, a78\n"
  "v_accvgpr_read_b32 v211, a79\n"
  "v_mfma_f32_16x16x32_f16 a[164:167], v[84:87], v[120:123], a[164:167]\n"
  "v_accvgpr_read_b32 v212, a80\n"
  "v_accvgpr_read_b32 v213, a81\n"
  "v_mfma_f32_16x16x32_f16 a[176:179], v[88:91], v[120:123], a[176:179]\n"
  "v_accvgpr_read_b32 v214, a82\n"
  "v_accvgpr_read_b32 v215, a83\n"
  "v_mfma_f32_16x16x32_f16 a[188:191], v[92:95], v[120:123], a[188:191]\n"
  "v_accvgpr_read_b32 v216, a84\n"
  "v_accvgpr_read_b32 v217, a85\n"
  "v_mfma_f32_16x16x32_f16 a[192:195], v[48:51], v[112:115], a[192:195]\n"
  "v_pk_add_f16 v120, v112, v116\n"
  "v_pk_add_f16 v121, v113, v117\n"
  "v_mfma_f32_16x16x32_f16 a[204:207], v[52:55], v[112:115], a[204:207]\n"
  "v_pk_add_f16 v122, v114, v118\n"
  "v_pk_add_f16 v123, v115, v119\n"
  "v_mfma_f32_16x16x32_f16 a[216:219], v[56:59], v[112:115], a[216:219]\n"
  "v_accvgpr_read_b32 v218, a86\n"
  "v_accvgpr_read_b32 v219, a87\n"
  "v_mfma_f32_16x16x32_f16 a[228:231], v[60:63], v[112:115], a[228:231]\n"
  "v_accvgpr_read_b32 v220, a88\n"
  "v_accvgpr_read_b32 v221, a89\n"
  "v_mfma_f32_16x16x32_f16 a[196:199], v[64:67], v[116:119], a[196:199]\n"
  "v_accvgpr_read_b32 v222, a90\n"
  "v_accvgpr_read_b32 v223, a91\n"
  "v_mfma_f32_16x16x32_f16 a[208:211], v[68:71], v[116:119], a[208:211]\n"
  "v_accvgpr_read_b32 v224, a92\n"
  "v_accvgpr_read_b32 v225, a93\n"
  "v_mfma_f32_16x16x32_f16 a[220:223], v[72:75], v[116:119], a[220:223]\n"
  "v_accvgpr_read_b32 v226, a94\n"
  "v_accvgpr_read_b32 v227, a95\n"
  "v_mfma_f32_16x16x32_f16 a[232:235], v[76:79], v[116:119], a[232:235]\n"
  "v_pk_add_f16 v32, v0, v16\n"
  "v_pk_add_f16 v33, v1, v17\n"
  "v_mfma_f32_16x16x32_f16 a[200:203], v[80:83], v[120:123], a[200:203]\n"
  "v_pk_add_f16 v34, v2, v18\n"
  "v_pk_add_f16 v35, v3, v19\n"
  "s_waitcnt vmcnt(10) lgkmcnt(0)\n"
  "v_mfma_f32_16x16x32_f16 a[212:215], v[84:87], v[120:123], a[212:215]\n"
  "v_pk_add_f16 v36, v4, v20\n"
  "v_pk_add_f16 v37, v5, v21\n"
  "v_mfma_f32_16x16x32_f16 a[224:227], v[88:91], v[120:123], a[224:227]\n"
  "v_pk_add_f16 v38, v6, v22\n"
  "v_pk_add_f16 v39, v7, v23\n"
  "s_barrier\n"
  "v_mfma_f32_16x16x32_f16 a[236:239], v[92:95], v[120:123], a[236:239]\n"
  "v_pk_add_f16 v40, v8, v24\n"
  "v_pk_add_f16 v41, v9, v25\n"
  "v_pk_add_f16 v42, v10, v26\n"
  "v_pk_add_f16 v43, v11, v27\n"
  "v_pk_add_f16 v44, v12, v28\n"
  "v_pk_add_f16 v45, v13, v29\n"
  "v_pk_add_f16 v46, v14, v30\n"
  "v_pk_add_f16 v47, v15, v31\n"
  "v_add_u32 v230, s49, v230\n"
  "v_add_u32 v231, s49, v231\n"
  "v_add_u32 v232, s49, v232\n"
  "s_mov_b32 s63, 2\n"
  ".p2align 6\n"
  ".LloopN_%=:\n"
  "v_mfma_f32_16x16x32_f16 v[124:127], v[0:3], v[96:99], v[124:127]\n"
  "buffer_load_dword v247, v246, s[40:43], s54 offen sc1\n"
  "v_pk_add_f16 v120, v96, v100\n"
  "v_mfma_f32_16x16x32_f16 v[136:139], v[4:7], v[96:99], v[136:139]\n"
  "v_pk_add_f16 v121, v97, v101\n"
  "v_pk_add_f16 v122, v98, v102\n"
  "v_mfma_f32_16x16x32_f16 v[148:151], v[8:11], v[96:99], v[148:151]\n"
  "ds_read_b64_tr_b16 v[48:49], v228 offset:0\n"
  "v_pk_add_f16 v123, v99, v103\n"
  "v_mfma_f32_16x16x32_f16 v[160:163], v[12:15], v[96:99], v[160:163]\n"
  "s_add_i32 m0, s47, 0\n"
  "ds_read_b64_tr_b16 v[50:51], v228 offset:8192\n"
  "ds_read_b128 v[96:99], v232 offset:4096\n"
  "v_mfma_f32_16x16x32_f16 v[128:131], v[16:19], v[100:103], v[128:131]\n"
  "s_add_i32 s64, s50, 4194304\n"
  "buffer_load_dwordx4 v237, s[40:43], s64 offen sc1 lds\n"
  "ds_read_b64_tr_b16 v[64:65], v228 offset:256\n"
  "v_mfma_f32_16x16x32_f16 v[140:143], v[20:23], v[100:103], v[140:143]\n"
  "s_cmp_eq_u32 s46, 73728\n"
  "s_cselect_b32 s49, s68, s69\n"
  "ds_read_b64_tr_b16 v[66:67], v228 offset:8448\n"
  "v_mfma_f32_16x16x32_f16 v[152:155], v[24:27], v[100:103], v[152:155]\n"
  "ds_read_b64_tr_b16 v[52:53], v229 offset:0\n"
  "v_add_u32 v228, s49, v228\n"
  "v_mfma_f32_16x16x32_f16 v[164:167], v[28:31], v[100:103], v[164:167]\n"
  "s_add_i32 m0, s47, 4096\n"
  "ds_read_b128 v[100:103], v232 offset:4608\n"
  "ds_read_b64_tr_b16 v[54:55], v229 offset:8192\n"
  "v_mfma_f32_16x16x32_f16 v[132:135], v[32:35], v[120:123], v[132:135]\n"
  "s_add_i32 s64, s50, 4198400\n"
  "buffer_load_dwordx4 v237, s[40:43], s64 offen sc1 lds\n"
  "ds_read_b64_tr_b16 v[68:69], v229 offset:256\n"
  "v_mfma_f32_16x16x32_f16 v[144:147], v[36:39], v[120:123], v[144:147]\n"
  "ds_read_b64_tr_b16 v[70:71], v229 offset:8448\n"
  "v_add_u32 v229, s49, v229\n"
  "v_mfma_f32_16x16x32_f16 v[156:159], v[40:43], v[120:123], v[156:159]\n"
  "ds_read_b64_tr_b16 v[56:57], v230 offset:0\n"
  "ds_read_b64_tr_b16 v[58:59], v230 offset:8192\n"
  "v_mfma_f32_16x16x32_f16 v[168:171], v[44:47], v[120:123], v[168:171]\n"
  "s_add_i32 m0, s47, 8192\n"
  "ds_read_b64_tr_b16 v[72:73], v230 offset:256\n"
  "ds_read_b64_tr_b16 v[74:75], v230 offset:8448\n"
  "v_mfma_f32_16x16x32_f16 v[180:183], v[0:3], v[104:107], v[180:183]\n"
  "s_add_i32 s64, s50, 4202496\n"
  "buffer_load_dwordx4 v237, s[40:43], s64 offen sc1 lds\n"
  "v_pk_add_f16 v120, v104, v108\n"
  "v_mfma_f32_16x16x32_f16 v[192:195], v[4:7], v[104:107], v[192:195]\n"
  "s_waitcnt lgkmcnt(9)\n"
  "v_pk_add_f16 v121, v105, v109\n"
  "v_pk_add_f16 v122, v106, v110\n"
  "v_mfma_f32_16x16x32_f16 v[204:207], v[8:11], v[104:107], v[204:207]\n"
  "ds_read_b64_tr_b16 v[60:61], v231 offset:0\n"
  "v_pk_add_f16 v123, v107, v111\n"
  "v_mfma_f32_16x16x32_f16 v[216:219], v[12:15], v[104:107], v[216:219]\n"
  "s_add_i32 m0, s47, 12288\n"
  "ds_read_b128 v[104:107], v232 offset:6144\n"
  "v_pk_add_f16 v80, v48, v64\n"
  "v_mfma_f32_16x16x32_f16 v[184:187], v[16:19], v[108:111], v[184:187]\n"
  "s_waitcnt lgkmcnt(6)\n"
  "s_add_i32 s64, s50, 4206592\n"
  "buffer_load_dwordx4 v237, s[40:43], s64 offen sc1 lds\n"
  "v_mfma_f32_16x16x32_f16 v[196:199], v[20:23], v[108:111], v[196:199]\n"
  "ds_read_b64_tr_b16 v[62:63], v231 offset:8192\n"
  "v_pk_add_f16 v81, v49, v65\n"
  "v_mfma_f32_16x16x32_f16 v[208:211], v[24:27], v[108:111], v[208:211]\n"
  "s_waitcnt lgkmcnt(3)\n"
  "ds_read_b128 v[112:115], v232 offset:8192\n"
  "v_pk_add_f16 v82, v50, v66\n"
  "v_mfma_f32_16x16x32_f16 v[220:223], v[28:31], v[108:111], v[220:223]\n"
  "s_add_i32 m0, s47, 16384\n"
  "ds_read_b128 v[108:111], v232 offset:6656\n"
  "v_pk_add_f16 v83, v51, v67\n"
  "v_mfma_f32_16x16x32_f16 v[188:191], v[32:35], v[120:123], v[188:191]\n"
  "s_add_i32 s64, s50, 4194304\n"
  "buffer_load_dwordx4 v238, s[40:43], s64 offen sc1 lds\n"
  "v_pk_add_f16 v84, v52, v68\n"
  "v_mfma_f32_16x16x32_f16 v[200:203], v[36:39], v[120:123], v[200:203]\n"
  "ds_read_b64_tr_b16 v[76:77], v231 offset:256\n"
  "v_pk_add_f16 v85, v53, v69\n"
  "v_mfma_f32_16x16x32_f16 v[212:215], v[40:43], v[120:123], v[212:215]\n"
  "ds_read_b128 v[116:119], v232 offset:8704\n"
  "v_pk_add_f16 v86, v54, v70\n"
  "v_mfma_f32_16x16x32_f16 v[224:227], v[44:47], v[120:123], v[224:227]\n"
  "s_add_i32 m0, s47, 20480\n"
  "ds_read_b64_tr_b16 v[78:79], v231 offset:8448\n"
  "v_pk_add_f16 v87, v55, v71\n"
  "v_mfma_f32_16x16x32_f16 a[96:99], v[0:3], v[96:99], a[96:99]\n"
  "s_add_i32 s64, s50, 4096\n"
  "buffer_load_dwordx4 v238, s[40:43], s64 offen sc1 lds\n"
  "v_pk_add_f16 v120, v96, v100\n"
  "v_mfma_f32_16x16x32_f16 a[108:111], v[4:7], v[96:99], a[108:111]\n"
  "v_pk_add_f16 v121, v97, v101\n"
  "v_pk_add_f16 v122, v98, v102\n"
  "v_mfma_f32_16x16x32_f16 a[120:123], v[8:11], v[96:99], a[120:123]\n"
  "v_pk_add_f16 v123, v99, v103\n"
  "v_pk_add_f16 v88, v56, v72\n"
  "v_mfma_f32_16x16x32_f16 a[132:135], v[12:15], v[96:99], a[132:135]\n"
  "s_add_i32 m0, s48, 24576\n"
  "ds_read_b128 v[96:99], v232 offset:0\n"
  "v_pk_add_f16 v89, v57, v73\n"
  "v_mfma_f32_16x16x32_f16 a[100:103], v[16:19], v[100:103], a[100:103]\n"
  "s_add_i32 s64, s50, 16384\n"
  "buffer_load_dwordx4 v238, s[40:43], s64 offen sc1 lds\n"
  "v_pk_add_f16 v90, v58, v74\n"
  "v_mfma_f32_16x16x32_f16 a[112:115], v[20:23], v[100:103], a[112:115]\n"
  "v_pk_add_f16 v91, v59, v75\n"
  "v_add_u32 v230, s49, v230\n"
  "v_mfma_f32_16x16x32_f16 a[124:127], v[24:27], v[100:103], a[124:127]\n"
  "s_waitcnt lgkmcnt(1)\n"
  "v_pk_add_f16 v92, v60, v76\n"
  "v_pk_add_f16 v93, v61, v77\n"
  "v_mfma_f32_16x16x32_f16 a[136:139], v[28:31], v[100:103], a[136:139]\n"
  "s_add_i32 m0, s47, 26624\n"
  "ds_read_b128 v[100:103], v232 offset:512\n"
  "v_pk_add_f16 v94, v62, v78\n"
  "v_mfma_f32_16x16x32_f16 a[104:107], v[32:35], v[120:123], a[104:107]\n"
  "s_add_i32 s64, s50, 4214784\n"
  "buffer_load_dwordx4 v238, s[40:43], s64 offen sc1 lds\n"
  "v_pk_add_f16 v95, v63, v79\n"
  "v_mfma_f32_16x16x32_f16 a[116:119], v[36:39], v[120:123], a[116:119]\n"
  "v_add_u32 v231, s49, v231\n"
  "v_mfma_f32_16x16x32_f16 a[128:131], v[40:43], v[120:123], a[128:131]\n"
  "v_mfma_f32_16x16x32_f16 a[140:143], v[44:47], v[120:123], a[140:143]\n"
  "s_add_i32 m0, s47, 30720\n"
  "v_mfma_f32_16x16x32_f16 a[144:147], v[0:3], v[104:107], a[144:147]\n"
  "s_add_i32 s64, s50, 32768\n"
  "buffer_load_dwordx4 v238, s[40:43], s64 offen sc1 lds\n"
  "v_pk_add_f16 v120, v104, v108\n"
  "v_mfma_f32_16x16x32_f16 a[156:159], v[4:7], v[104:107], a[156:159]\n"
  "v_pk_add_f16 v121, v105, v109\n"
  "v_pk_add_f16 v122, v106, v110\n"
  "v_mfma_f32_16x16x32_f16 a[168:171], v[8:11], v[104:107], a[168:171]\n"
  "v_pk_add_f16 v123, v107, v111\n"
  "v_mfma_f32_16x16x32_f16 a[180:183], v[12:15], v[104:107], a[180:183]\n"
  "ds_read_b128 v[104:107], v232 offset:2048\n"
  "v_mfma_f32_16x16x32_f16 a[148:151], v[16:19], v[108:111], a[148:151]\n"
  "s_add_i32 s47, s44, s46\n"
  "s_add_i32 s50, s50, 4194304\n"
  "v_mfma_f32_16x16x32_f16 a[160:163], v[20:23], v[108:111], a[160:163]\n"
  "s_add_i32 s48, s47, s45\n"
  "s_add_i32 s46, s46, s49\n"
  "v_mfma_f32_16x16x32_f16 a[172:175], v[24:27], v[108:111], a[172:175]\n"
  "v_mfma_f32_16x16x32_f16 a[184:187], v[28:31], v[108:111], a[184:187]\n"
  "ds_read_b128 v[108:111], v232 offset:2560\n"
  "v_add_u32 v232, s49, v232\n"
  "v_mfma_f32_16x16x32_f16 a[152:155], v[32:35], v[120:123], a[152:155]\n"
  "v_mfma_f32_16x16x32_f16 a[164:167], v[36:39], v[120:123], a[164:167]\n"
  "v_mfma_f32_16x16x32_f16 a[176:179], v[40:43], v[120:123], a[176:179]\n"
  "v_mfma_f32_16x16x32_f16 a[188:191], v[44:47], v[120:123], a[188:191]\n"
  "v_mfma_f32_16x16x32_f16 a[192:195], v[0:3], v[112:115], a[192:195]\n"
  "v_pk_add_f16 v120, v112, v116\n"
  "v_pk_add_f16 v121, v113, v117\n"
  "v_mfma_f32_16x16x32_f16 a[204:207], v[4:7], v[112:115], a[204:207]\n"
  "v_pk_add_f16 v122, v114, v118\n"
  "v_pk_add_f16 v123, v115, v119\n"
  "v_mfma_f32_16x16x32_f16 a[216:219], v[8:11], v[112:115], a[216:219]\n"
  "v_mfma_f32_16x16x32_f16 a[228:231], v[12:15], v[112:115], a[228:231]\n"
  "v_mfma_f32_16x16x32_f16 a[196:199], v[16:19], v[116:119], a[196:199]\n"
  "v_mfma_f32_16x16x32_f16 a[208:211], v[20:23], v[116:119], a[208:211]\n"
  "v_mfma_f32_16x16x32_f16 a[220:223], v[24:27], v[116:119], a[220:223]\n"
  "v_mfma_f32_16x16x32_f16 a[232:235], v[28:31], v[116:119], a[232:235]\n"
  "v_mfma_f32_16x16x32_f16 a[200:203], v[32:35], v[120:123], a[200:203]\n"
  "s_waitcnt vmcnt(9) lgkmcnt(0)\n"
  "v_mfma_f32_16x16x32_f16 a[212:215], v[36:39], v[120:123], a[212:215]\n"
  "v_cmp_eq_u32 vcc, 0, v247\n"
  "s_cbranch_vccnz .Lspin_ne_None_%=\n"
  ".Lspun_ne_None_%=:\n"
  "v_mfma_f32_16x16x32_f16 a[224:227], v[40:43], v[120:123], a[224:227]\n"
  "s_barrier\n"
  "v_mfma_f32_16x16x32_f16 a[236:239], v[44:47], v[120:123], a[236:239]\n"
  "v_mfma_f32_16x16x32_f16 v[124:127], v[48:51], v[96:99], v[124:127]\n"
  "v_pk_add_f16 v120, v96, v100\n"
  "v_pk_add_f16 v121, v97, v101\n"
  "v_mfma_f32_16x16x32_f16 v[136:139], v[52:55], v[96:99], v[136:139]\n"
  "v_pk_add_f16 v122, v98, v102\n"
  "v_pk_add_f16 v123, v99, v103\n"
  "v_mfma_f32_16x16x32_f16 v[148:151], v[56:59], v[96:99], v[148:151]\n"
  "s_and_b32 s65, s51, 0xffffe300\n"
  "ds_read_b64_tr_b16 v[0:1], v228 offset:0\n"
  "ds_read_b64_tr_b16 v[2:3], v228 offset:8192\n"
  "v_mfma_f32_16x16x32_f16 v[160:163], v[60:63], v[96:99], v[160:163]\n"
  "s_add_i32 m0, s47, 0\n"
  "ds_read_b128 v[96:99], v232 offset:4096\n"
  "ds_read_b64_tr_b16 v[16:17], v228 offset:256\n"
  "v_mfma_f32_16x16x32_f16 v[128:131], v[64:67], v[100:103], v[128:131]\n"
  "s_add_i32 s64, s50, 4194304\n"
  "buffer_load_dwordx4 v237, s[40:43], s64 offen sc1 lds\n"
  "ds_read_b64_tr_b16 v[18:19], v228 offset:8448\n"
  "v_mfma_f32_16x16x32_f16 v[140:143], v[68:71], v[100:103], v[140:143]\n"
  "s_cmp_eq_u32 s46, 73728\n"
  "s_cselect_b32 s49, s68, s69\n"
  "ds_read_b64_tr_b16 v[4:5], v229 offset:0\n"
  "v_mfma_f32_16x16x32_f16 v[152:155], v[72:75], v[100:103], v[152:155]\n"
  "ds_read_b64_tr_b16 v[6:7], v229 offset:8192\n"
  "v_add_u32 v228, s49, v228\n"
  "v_mfma_f32_16x16x32_f16 v[164:167], v[76:79], v[100:103], v[164:167]\n"
  "ds_read_b128 v[100:103], v232 offset:4608\n"
  "ds_read_b64_tr_b16 v[20:21], v229 offset:256\n"
  "v_mfma_f32_16x16x32_f16 v[132:135], v[80:83], v[120:123], v[132:135]\n"
  "buffer_store_dword v232, v245, s[40:43], s65 offen sc1\n"
  "ds_read_b64_tr_b16 v[22:23], v229 offset:8448\n"
  "v_mfma_f32_16x16x32_f16 v[144:147], v[84:87], v[120:123], v[144:147]\n"
  "ds_read_b64_tr_b16 v[8:9], v230 offset:0\n"
  "v_add_u32 v229, s49, v229\n"
  "v_mfma_f32_16x16x32_f16 v[156:159], v[88:91], v[120:123], v[156:159]\n"
  "s_add_i32 s53, s53, 8192\n"
  "s_lshr_b32 s52, s52, 1\n"
  "ds_read_b64_tr_b16 v[10:11], v230 offset:8192\n"
  "v_mfma_f32_16x16x32_f16 v[168:171], v[92:95], v[120:123], v[168:171]\n"
  "s_add_i32 m0, s47, 4096\n"
  "s_waitcnt lgkmcnt(7)\n"
  "ds_read_b64_tr_b16 v[24:25], v230 offset:256\n"
  "v_mfma_f32_16x16x32_f16 v[180:183], v[48:51], v[104:107], v[180:183]\n"
  "s_add_i32 s64, s50, 4198400\n"
  "buffer_load_dwordx4 v237, s[40:43], s64 offen sc1 lds\n"
  "v_pk_add_f16 v120, v104, v108\n"
  "v_mfma_f32_16x16x32_f16 v[192:195], v[52:55], v[104:107], v[192:195]\n"
  "s_add_i32 s51, s51, 8448\n"
  "v_pk_add_f16 v121, v105, v109\n"
  "v_pk_add_f16 v122, v106, v110\n"
  "v_mfma_f32_16x16x32_f16 v[204:207], v[56:59], v[104:107], v[204:207]\n"
  "ds_read_b64_tr_b16 v[26:27], v230 offset:8448\n"
  "v_pk_add_f16 v123, v107, v111\n"
  "v_mfma_f32_16x16x32_f16 v[216:219], v[60:63], v[104:107], v[216:219]\n"
  "s_add_i32 m0, s47, 8192\n"
  "s_waitcnt lgkmcnt(4)\n"
  "ds_read_b128 v[104:107], v232 offset:6144\n"
  "v_mfma_f32_16x16x32_f16 v[184:187], v[64:67], v[108:111], v[184:187]\n"
  "s_add_i32 s64, s50, 4202496\n"
  "buffer_load_dwordx4 v237, s[40:43], s64 offen sc1 lds\n"
  "v_pk_add_f16 v32, v0, v16\n"
  "v_mfma_f32_16x16x32_f16 v[196:199], v[68:71], v[108:111], v[196:199]\n"
  "s_bitcmp1_b32 s52, 0\n"
  "s_cselect_b32 s54, s53, 134217728\n"
  "ds_read_b64_tr_b16 v[12:13], v231 offset:0\n"
  "v_mfma_f32_16x16x32_f16 v[208:211], v[72:75], v[108:111], v[208:211]\n"
  "ds_read_b64_tr_b16 v[14:15], v231 offset:8192\n"
  "v_pk_add_f16 v33, v1, v17\n"
  "v_mfma_f32_16x16x32_f16 v[220:223], v[76:79], v[108:111], v[220:223]\n"
  "s_add_i32 m0, s47, 12288\n"
  "ds_read_b128 v[108:111], v232 offset:6656\n"
  "v_pk_add_f16 v34, v2, v18\n"
  "v_mfma_f32_16x16x32_f16 v[188:191], v[80:83], v[120:123], v[188:191]\n"
  "s_add_i32 s64, s50, 4206592\n"
  "buffer_load_dwordx4 v237, s[40:43], s64 offen sc1 lds\n"
  "v_pk_add_f16 v35, v3, v19\n"
  "v_mfma_f32_16x16x32_f16 v[200:203], v[84:87], v[120:123], v[200:203]\n"
  "s_waitcnt lgkmcnt(4)\n"
  "ds_read_b128 v[112:115], v232 offset:8192\n"
  "v_mfma_f32_16x16x32_f16 v[212:215], v[88:91], v[120:123], v[212:215]\n"
  "ds_read_b64_tr_b16 v[28:29], v231 offset:256\n"
  "v_pk_add_f16 v36, v4, v20\n"
  "v_mfma_f32_16x16x32_f16 v[224:227], v[92:95], v[120:123], v[224:227]\n"
  "s_add_i32 m0, s47, 16384\n"
  "ds_read_b128 v[116:119], v232 offset:8704\n"
  "v_pk_add_f16 v37, v5, v21\n"
  "v_mfma_f32_16x16x32_f16 a[96:99], v[48:51], v[96:99], a[96:99]\n"
  "s_add_i32 s64, s50, 4194304\n"
  "buffer_load_dwordx4 v238, s[40:43], s64 offen sc1 lds\n"
  "v_pk_add_f16 v120, v96, v100\n"
  "v_mfma_f32_16x16x32_f16 a[108:111], v[52:55], v[96:99], a[108:111]\n"
  "v_pk_add_f16 v121, v97, v101\n"
  "v_pk_add_f16 v122, v98, v102\n"
  "v_mfma_f32_16x16x32_f16 a[120:123], v[56:59], v[96:99], a[120:123]\n"
  "ds_read_b64_tr_b16 v[30:31], v231 offset:8448\n"
  "v_pk_add_f16 v123, v99, v103\n"
  "v_mfma_f32_16x16x32_f16 a[132:135], v[60:63], v[96:99], a[132:135]\n"
  "s_add_i32 m0, s47, 20480\n"
  "ds_read_b128 v[96:99], v232 offset:0\n"
  "v_pk_add_f16 v38, v6, v22\n"
  "v_mfma_f32_16x16x32_f16 a[100:103], v[64:67], v[100:103], a[100:103]\n"
  "s_add_i32 s64, s50, 4096\n"
  "buffer_load_dwordx4 v238, s[40:43], s64 offen sc1 lds\n"
  "v_pk_add_f16 v39, v7, v23\n"
  "v_mfma_f32_16x16x32_f16 a[112:115], v[68:71], v[100:103], a[112:115]\n"
  "v_pk_add_f16 v40, v8, v24\n"
  "v_pk_add_f16 v41, v9, v25\n"
  "v_mfma_f32_16x16x32_f16 a[124:127], v[72:75], v[100:103], a[124:127]\n"
  "v_pk_add_f16 v42, v10, v26\n"
  "v_pk_add_f16 v43, v11, v27\n"
  "v_mfma_f32_16x16x32_f16 a[136:139], v[76:79], v[100:103], a[136:139]\n"
  "s_add_i32 m0, s48, 24576\n"
  "ds_read_b128 v[100:103], v232 offset:512\n"
  "v_add_u32 v230, s49, v230\n"
  "v_mfma_f32_16x16x32_f16 a[104:107], v[80:83], v[120:123], a[104:107]\n"
  "s_add_i32 s64, s50, 16384\n"
  "buffer_load_dwordx4 v238, s[40:43], s64 offen sc1 lds\n"
  "v_add_u32 v231, s49, v231\n"
  "v_mfma_f32_16x16x32_f16 a[116:119], v[84:87], v[120:123], a[116:119]\n"
  "s_waitcnt lgkmcnt(6)\n"
  "s_waitcnt lgkmcnt(2)\n"
  "v_pk_add_f16 v44, v12, v28\n"
  "v_mfma_f32_16x16x32_f16 a[128:131], v[88:91], v[120:123], a[128:131]\n"
  "v_pk_add_f16 v45, v13, v29\n"
  "v_pk_add_f16 v46, v14, v30\n"
  "v_mfma_f32_16x16x32_f16 a[140:143], v[92:95], v[120:123], a[140:143]\n"
  "s_add_i32 m0, s47, 26624\n"
  "v_pk_add_f16 v47, v15, v31\n"
  "v_mfma_f32_16x16x32_f16 a[144:147], v[48:51], v[104:107], a[144:147]\n"
  "s_add_i32 s64, s50, 4214784\n"
  "buffer_load_dwordx4 v238, s[40:43], s64 offen sc1 lds\n"
  "v_pk_add_f16 v120, v104, v108\n"
  "v_mfma_f32_16x16x32_f16 a[156:159], v[52:55], v[104:107], a[156:159]\n"
  "v_pk_add_f16 v121, v105, v109\n"
  "v_pk_add_f16 v122, v106, v110\n"
  "v_mfma_f32_16x16x32_f16 a[168:171], v[56:59], v[104:107], a[168:171]\n"
  "v_pk_add_f16 v123, v107, v111\n"
  "v_mfma_f32_16x16x32_f16 a[180:183], v[60:63], v[104:107], a[180:183]\n"
  "s_add_i32 m0, s47, 30720\n"
  "ds_read_b128 v[104:107], v232 offset:2048\n"
  "v_mfma_f32_16x16x32_f16 a[148:151], v[64:67], v[108:111], a[148:151]\n"
  "s_add_i32 s64, s50, 32768\n"
  "buffer_load_dwordx4 v238, s[40:43], s64 offen sc1 lds\n"
  "v_mfma_f32_16x16x32_f16 a[160:163], v[68:71], v[108:111], a[160:163]\n"
  "s_add_i32 s47, s44, s46\n"
  "s_add_i32 s50, s50, 4194304\n"
  "v_mfma_f32_16x16x32_f16 a[172:175], v[72:75], v[108:111], a[172:175]\n"
  "s_add_i32 s48, s47, s45\n"
  "s_add_i32 s46, s46, s49\n"
  "v_mfma_f32_16x16x32_f16 a[184:187], v[76:79], v[108:111], a[184:187]\n"
  "ds_read_b128 v[108:111], v232 offset:2560\n"
  "v_add_u32 v232, s49, v232\n"
  "v_mfma_f32_16x16x32_f16 a[152:155], v[80:83], v[120:123], a[152:155]\n"
  "v_mfma_f32_16x16x32_f16 a[164:167], v[84:87], v[120:123], a[164:167]\n"
  "v_mfma_f32_16x16x32_f16 a[176:179], v[88:91], v[120:123], a[176:179]\n"
  "v_mfma_f32_16x16x32_f16 a[188:191], v[92:95], v[120:123], a[188:191]\n"
  "v_mfma_f32_16x16x32_f16 a[192:195], v[48:51], v[112:115], a[192:195]\n"
  "v_pk_add_f16 v120, v112, v116\n"
  "v_pk_add_f16 v121, v113, v117\n"
  "v_mfma_f32_16x16x32_f16 a[204:207], v[52:55], v[112:115], a[204:207]\n"
  "v_pk_add_f16 v122, v114, v118\n"
  "v_pk_add_f16 v123, v115, v119\n"
  "v_mfma_f32_16x16x32_f16 a[216:219], v[56:59], v[112:115], a[216:219]\n"
  "v_mfma_f32_16x16x32_f16 a[228:231], v[60:63], v[112:115], a[228:231]\n"
  "v_mfma_f32_16x16x32_f16 a[196:199], v[64:67], v[116:119], a[196:199]\n"
  "v_mfma_f32_16x16x32_f16 a[208:211], v[68:71], v[116:119], a[208:211]\n"
  "v_mfma_f32_16x16x32_f16 a[220:223], v[72:75], v[116:119], a[220:223]\n"
  "v_mfma_f32_16x16x32_f16 a[232:235], v[76:79], v[116:119], a[232:235]\n"
  "v_mfma_f32_16x16x32_f16 a[200:203], v[80:83], v[120:123], a[200:203]\n"
  "s_waitcnt vmcnt(10) lgkmcnt(0)\n"
  "v_mfma_f32_16x16x32_f16 a[212:215], v[84:87], v[120:123], a[212:215]\n"
  "v_mfma_f32_16x16x32_f16 a[224:227], v[88:91], v[120:123], a[224:227]\n"
  "s_barrier\n"
  "v_mfma_f32_16x16x32_f16 a[236:239], v[92:95], v[120:123], a[236:239]\n"
  "s_sub_i32 s63, s63, 1\n"
  "s_cmp_lg_u32 s63, 0\n"
  "s_cbranch_scc1 .LloopN_%=\n"
  "v_mfma_f32_16x16x32_f16 v[124:127], v[0:3], v[96:99], v[124:127]\n"
  "s_add_i32 s67, s44, s45\n"
  "buffer_load_dword v247, v246, s[40:43], s54 offen sc1\n"
  "v_pk_add_f16 v120, v96, v100\n"
  "v_mfma_f32_16x16x32_f16 v[136:139], v[4:7], v[96:99], v[136:139]\n"
  "v_pk_add_f16 v121, v97, v101\n"
  "v_pk_add_f16 v122, v98, v102\n"
  "v_mfma_f32_16x16x32_f16 v[148:151], v[8:11], v[96:99], v[148:151]\n"
  "ds_read_b64_tr_b16 v[48:49], v228 offset:0\n"
  "v_pk_add_f16 v123, v99, v103\n"
  "v_mfma_f32_16x16x32_f16 v[160:163], v[12:15], v[96:99], v[160:163]\n"
  "s_add_i32 m0, s47, 0\n"
  "ds_read_b64_tr_b16 v[50:51], v228 offset:8192\n"
  "ds_read_b128 v[96:99], v232 offset:4096\n"
  "v_mfma_f32_16x16x32_f16 v[128:131], v[16:19], v[100:103], v[128:131]\n"
  "s_add_i32 s64, s50, 4194304\n"
  "buffer_load_dwordx4 v237, s[40:43], s64 offen sc1 lds\n"
  "ds_read_b64_tr_b16 v[64:65], v228 offset:256\n"
  "v_mfma_f32_16x16x32_f16 v[140:143], v[20:23], v[100:103], v[140:143]\n"
  "s_cmp_eq_u32 s46, 73728\n"
  "s_cselect_b32 s49, s68, s69\n"
  "ds_read_b64_tr_b16 v[66:67], v228 offset:8448\n"
  "v_mfma_f32_16x16x32_f16 v[152:155], v[24:27], v[100:103], v[152:155]\n"
  "ds_read_b64_tr_b16 v[52:53], v229 offset:0\n"
  "v_add_u32 v228, s49, v228\n"
  "v_mfma_f32_16x16x32_f16 v[164:167], v[28:31], v[100:103], v[164:167]\n"
  "s_add_i32 m0, s47, 4096\n"
  "ds_read_b128 v[100:103], v232 offset:4608\n"
  "ds_read_b64_tr_b16 v[54:55], v229 offset:8192\n"
  "v_mfma_f32_16x16x32_f16 v[132:135], v[32:35], v[120:123], v[132:135]\n"
  "s_add_i32 s64, s50, 4198400\n"
  "buffer_load_dwordx4 v237, s[40:43], s64 offen sc1 lds\n"
  "ds_read_b64_tr_b16 v[68:69], v229 offset:256\n"
  "v_mfma_f32_16x16x32_f16 v[144:147], v[36:39], v[120:123], v[144:147]\n"
  "ds_read_b64_tr_b16 v[70:71], v229 offset:8448\n"
  "v_add_u32 v229, s49, v229\n"
  "v_mfma_f32_16x16x32_f16 v[156:159], v[40:43], v[120:123], v[156:159]\n"
  "ds_read_b64_tr_b16 v[56:57], v230 offset:0\n"
  "ds_read_b64_tr_b16 v[58:59], v230 offset:8192\n"
  "v_mfma_f32_16x16x32_f16 v[168:171], v[44:47], v[120:123], v[168:171]\n"
  "s_add_i32 m0, s47, 8192\n"
  "ds_read_b64_tr_b16 v[72:73], v230 offset:256\n"
  "ds_read_b64_tr_b16 v[74:75], v230 offset:8448\n"
  "v_mfma_f32_16x16x32_f16 v[180:183], v[0:3], v[104:107], v[180:183]\n"
  "s_add_i32 s64, s50, 4202496\n"
  "buffer_load_dwordx4 v237, s[40:43], s64 offen sc1 lds\n"
  "v_pk_add_f16 v120, v104, v108\n"
  "v_mfma_f32_16x16x32_f16 v[192:195], v[4:7], v[104:107], v[192:195]\n"
  "s_waitcnt lgkmcnt(9)\n"
  "v_pk_add_f16 v121, v105, v109\n"
  "v_pk_add_f16 v122, v106, v110\n"
  "v_mfma_f32_16x16x32_f16 v[204:207], v[8:11], v[104:107], v[204:207]\n"
  "ds_read_b64_tr_b16 v[60:61], v231 offset:0\n"
  "v_pk_add_f16 v123, v107, v111\n"
  "v_mfma_f32_16x16x32_f16 v[216:219], v[12:15], v[104:107], v[216:219]\n"
  "s_add_i32 m0, s47, 12288\n"
  "ds_read_b128 v[104:107], v232 offset:6144\n"
  "v_pk_add_f16 v80, v48, v64\n"
  "v_mfma_f32_16x16x32_f16 v[184:187], v[16:19], v[108:111], v[184:187]\n"
  "s_waitcnt lgkmcnt(6)\n"
  "s_add_i32 s64, s50, 4206592\n"
  "buffer_load_dwordx4 v237, s[40:43], s64 offen sc1 lds\n"
  "v_mfma_f32_16x16x32_f16 v[196:199], v[20:23], v[108:111], v[196:199]\n"
  "ds_read_b64_tr_b16 v[62:63], v231 offset:8192\n"
  "v_pk_add_f16 v81, v49, v65\n"
  "v_mfma_f32_16x16x32_f16 v[208:211], v[24:27], v[108:111], v[208:211]\n"
  "s_waitcnt lgkmcnt(3)\n"
  "ds_read_b128 v[112:115], v232 offset:8192\n"
  "v_pk_add_f16 v82, v50, v66\n"
  "v_mfma_f32_16x16x32_f16 v[220:223], v[28:31], v[108:111], v[220:223]\n"
  "s_add_i32 m0, s47, 16384\n"
  "ds_read_b128 v[108:111], v232 offset:6656\n"
  "v_pk_add_f16 v83, v51, v67\n"
  "v_mfma_f32_16x16x32_f16 v[188:191], v[32:35], v[120:123], v[188:191]\n"
  "s_add_i32 s64, s50, 4194304\n"
  "buffer_load_dwordx4 v238, s[40:43], s64 offen sc1 lds\n"
  "v_pk_add_f16 v84, v52, v68\n"
  "v_mfma_f32_16x16x32_f16 v[200:203], v[36:39], v[120:123], v[200:203]\n"
  "ds_read_b64_tr_b16 v[76:77], v231 offset:256\n"
  "v_pk_add_f16 v85, v53, v69\n"
  "v_mfma_f32_16x16x32_f16 v[212:215], v[40:43], v[120:123], v[212:215]\n"
  "ds_read_b128 v[116:119], v232 offset:8704\n"
  "v_pk_add_f16 v86, v54, v70\n"
  "v_mfma_f32_16x16x32_f16 v[224:227], v[44:47], v[120:123], v[224:227]\n"
  "s_add_i32 m0, s47, 20480\n"
  "ds_read_b64_tr_b16 v[78:79], v231 offset:8448\n"
  "v_pk_add_f16 v87, v55, v71\n"
  "v_mfma_f32_16x16x32_f16 a[96:99], v[0:3], v[96:99], a[96:99]\n"
  "s_add_i32 s64, s50, 4096\n"
  "buffer_load_dwordx4 v238, s[40:43], s64 offen sc1 lds\n"
  "v_pk_add_f16 v120, v96, v100\n"
  "v_mfma_f32_16x16x32_f16 a[108:111], v[4:7], v[96:99], a[108:111]\n"
  "v_pk_add_f16 v121, v97, v101\n"
  "v_pk_add_f16 v122, v98, v102\n"
  "v_mfma_f32_16x16x32_f16 a[120:123], v[8:11], v[96:99], a[120:123]\n"
  "ds_write_b128 v253, v[172:175]\n"
  "v_pk_add_f16 v123, v99, v103\n"
  "v_mfma_f32_16x16x32_f16 a[132:135], v[12:15], v[96:99], a[132:135]\n"
  "s_add_i32 m0, s48, 24576\n"
  "ds_read_b128 v[96:99], v232 offset:0\n"
  "v_pk_add_f16 v88, v56, v72\n"
  "v_mfma_f32_16x16x32_f16 a[100:103], v[16:19], v[100:103], a[100:103]\n"
  "s_add_i32 s64, s50, 16384\n"
  "buffer_load_dwordx4 v238, s[40:43], s64 offen sc1 lds\n"
  "v_pk_add_f16 v89, v57, v73\n"
  "v_mfma_f32_16x16x32_f16 a[112:115], v[20:23], v[100:103], a[112:115]\n"
  "v_pk_add_f16 v90, v58, v74\n"
  "v_pk_add_f16 v91, v59, v75\n"
  "v_mfma_f32_16x16x32_f16 a[124:127], v[24:27], v[100:103], a[124:127]\n"
  "s_waitcnt lgkmcnt(2)\n"
  "v_pk_add_f16 v92, v60, v76\n"
  "v_pk_add_f16 v93, v61, v77\n"
  "v_mfma_f32_16x16x32_f16 a[136:139], v[28:31], v[100:103], a[136:139]\n"
  "s_add_i32 m0, s47, 26624\n"
  "ds_read_b128 v[100:103], v232 offset:512\n"
  "v_pk_add_f16 v94, v62, v78\n"
  "v_mfma_f32_16x16x32_f16 a[104:107], v[32:35], v[120:123], a[104:107]\n"
  "s_add_i32 s64, s50, 4214784\n"
  "buffer_load_dwordx4 v238, s[40:43], s64 offen sc1 lds\n"
  "v_pk_add_f16 v95, v63, v79\n"
  "v_mfma_f32_16x16x32_f16 a[116:119], v[36:39], v[120:123], a[116:119]\n"
  "v_add_u32 v230, s49, v230\n"
  "v_add_u32 v231, s49, v231\n"
  "v_mfma_f32_16x16x32_f16 a[128:131], v[40:43], v[120:123], a[128:131]\n"
  "v_mfma_f32_16x16x32_f16 a[140:143], v[44:47], v[120:123], a[140:143]\n"
  "s_add_i32 m0, s47, 30720\n"
  "v_mfma_f32_16x16x32_f16 a[144:147], v[0:3], v[104:107], a[144:147]\n"
  "s_add_i32 s64, s50, 32768\n"
  "buffer_load_dwordx4 v238, s[40:43], s64 offen sc1 lds\n"
  "v_pk_add_f16 v120, v104, v108\n"
  "v_mfma_f32_16x16x32_f16 a[156:159], v[4:7], v[104:107], a[156:159]\n"
  "v_pk_add_f16 v121, v105, v109\n"
  "v_pk_add_f16 v122, v106, v110\n"
  "v_mfma_f32_16x16x32_f16 a[168:171], v[8:11], v[104:107], a[168:171]\n"
  "v_pk_add_f16 v123, v107, v111\n"
  "v_mfma_f32_16x16x32_f16 a[180:183], v[12:15], v[104:107], a[180:183]\n"
  "s_add_i32 m0, s44, 114688\n"
  "ds_read_b128 v[104:107], v232 offset:2048\n"
  "v_mfma_f32_16x16x32_f16 a[148:151], v[16:19], v[108:111], a[148:151]\n"
  "s_add_i32 s47, s44, s46\n"
  "s_add_i32 s64, s50, 4198400\n"
  "buffer_load_dwordx4 v238, s[40:43], s64 offen sc1 lds\n"
  "v_mfma_f32_16x16x32_f16 a[160:163], v[20:23], v[108:111], a[160:163]\n"
  "s_add_i32 s48, s47, s45\n"
  "s_add_i32 s46, s46, s49\n"
  "v_mfma_f32_16x16x32_f16 a[172:175], v[24:27], v[108:111], a[172:175]\n"
  "v_mfma_f32_16x16x32_f16 a[184:187], v[28:31], v[108:111], a[184:187]\n"
  "s_add_i32 m0, s67, 118784\n"
  "ds_read_b128 v[108:111], v232 offset:2560\n"
  "v_add_u32 v232, s49, v232\n"
  "v_mfma_f32_16x16x32_f16 a[152:155], v[32:35], v[120:123], a[152:155]\n"
  "s_add_i32 s64, s50, 4210688\n"
  "buffer_load_dwordx4 v238, s[40:43], s64 offen sc1 lds\n"
  "v_mfma_f32_16x16x32_f16 a[164:167], v[36:39], v[120:123], a[164:167]\n"
  "v_mfma_f32_16x16x32_f16 a[176:179], v[40:43], v[120:123], a[176:179]\n"
  "v_mfma_f32_16x16x32_f16 a[188:191], v[44:47], v[120:123], a[188:191]\n"
  "s_add_i32 m0, s44, 124928\n"
  "v_mfma_f32_16x16x32_f16 a[192:195], v[0:3], v[112:115], a[192:195]\n"
  "s_add_i32 s64, s50, 4227072\n"
  "buffer_load_dwordx4 v238, s[40:43], s64 offen sc1 lds\n"
  "v_pk_add_f16 v120, v112, v116\n"
  "v_mfma_f32_16x16x32_f16 a[204:207], v[4:7], v[112:115], a[204:207]\n"
  "s_add_i32 s50, s50, 4194304\n"
  "v_pk_add_f16 v121, v113, v117\n"
  "v_pk_add_f16 v122, v114, v118\n"
  "v_mfma_f32_16x16x32_f16 a[216:219], v[8:11], v[112:115], a[216:219]\n"
  "v_pk_add_f16 v123, v115, v119\n"
  "v_mfma_f32_16x16x32_f16 a[228:231], v[12:15], v[112:115], a[228:231]\n"
  "v_mfma_f32_16x16x32_f16 a[196:199], v[16:19], v[116:119], a[196:199]\n"
  "v_mfma_f32_16x16x32_f16 a[208:211], v[20:23], v[116:119], a[208:211]\n"
  "v_mfma_f32_16x16x32_f16 a[220:223], v[24:27], v[116:119], a[220:223]\n"
  "v_mfma_f32_16x16x32_f16 a[232:235], v[28:31], v[116:119], a[232:235]\n"
  "v_mfma_f32_16x16x32_f16 a[200:203], v[32:35], v[120:123], a[200:203]\n"
  "s_waitcnt vmcnt(12) lgkmcnt(0)\n"
  "v_mfma_f32_16x16x32_f16 a[212:215], v[36:39], v[120:123], a[212:215]\n"
  "v_cmp_eq_u32 vcc, 0, v247\n"
  "s_cbranch_vccnz .Lspin_s28_None_%=\n"
  ".Lspun_s28_None_%=:\n"
  "v_mfma_f32_16x16x32_f16 a[224:227], v[40:43], v[120:123], a[224:227]\n"
  "s_barrier\n"
  "v_mfma_f32_16x16x32_f16 a[236:239], v[44:47], v[120:123], a[236:239]\n"
  "v_mfma_f32_16x16x32_f16 v[124:127], v[48:51], v[96:99], v[124:127]\n"
  "v_mbcnt_lo_u32_b32 v252, -1, 0\n"
  "v_mbcnt_hi_u32_b32 v252, -1, v252\n"
  "v_mfma_f32_16x16x32_f16 v[136:139], v[52:55], v[96:99], v[136:139]\n"
  "v_pk_add_f16 v120, v96, v100\n"
  "v_pk_add_f16 v121, v97, v101\n"
  "v_mfma_f32_16x16x32_f16 v[148:151], v[56:59], v[96:99], v[148:151]\n"
  "v_lshlrev_b32 v252, 7, v252\n"
  "v_pk_add_f16 v122, v98, v102\n"
  "v_mfma_f32_16x16x32_f16 v[160:163], v[60:63], v[96:99], v[160:163]\n"
  "s_mov_b32 m0, 147456\n"
  "v_pk_add_f16 v123, v99, v103\n"
  "v_add_u32 v252, s45, v252\n"
  "v_mfma_f32_16x16x32_f16 v[128:131], v[64:67], v[100:103], v[128:131]\n"
  "s_cmp_eq_u32 s46, 73728\n"
  "s_cselect_b32 s49, s68, s69\n"
  "global_load_lds_dword v[248:249], off\n"
  "v_mfma_f32_16x16x32_f16 v[140:143], v[68:71], v[100:103], v[140:143]\n"
  "ds_read_b64_tr_b16 v[0:1], v228 offset:0\n"
  "ds_read_b64_tr_b16 v[2:3], v228 offset:8192\n"
  "v_mfma_f32_16x16x32_f16 v[152:155], v[72:75], v[100:103], v[152:155]\n"
  "ds_read_b128 v[96:99], v232 offset:4096\n"
  "ds_read_b64_tr_b16 v[16:17], v228 offset:256\n"
  "v_mfma_f32_16x16x32_f16 v[164:167], v[76:79], v[100:103], v[164:167]\n"
  "ds_read_b128 v[100:103], v232 offset:4608\n"
  "ds_read_b64_tr_b16 v[18:19], v228 offset:8448\n"
  "v_mfma_f32_16x16x32_f16 v[132:135], v[80:83], v[120:123], v[132:135]\n"
  "ds_read_b64_tr_b16 v[4:5], v229 offset:0\n"
  "v_add_u32 v228, s49, v228\n"
  "v_mfma_f32_16x16x32_f16 v[144:147], v[84:87], v[120:123], v[144:147]\n"
  "ds_read_b64_tr_b16 v[6:7], v229 offset:8192\n"
  "ds_read_b64_tr_b16 v[20:21], v229 offset:256\n"
  "v_mfma_f32_16x16x32_f16 v[156:159], v[88:91], v[120:123], v[156:159]\n"
  "ds_read_b64_tr_b16 v[22:23], v229 offset:8448\n"
  "v_add_u32 v229, s49, v229\n"
  "v_mfma_f32_16x16x32_f16 v[168:171], v[92:95], v[120:123], v[168:171]\n"
  "s_mov_b32 m0, 144384\n"
  "ds_read_b64_tr_b16 v[8:9], v230 offset:0\n"
  "ds_read_b64_tr_b16 v[10:11], v230 offset:8192\n"
  "v_mfma_f32_16x16x32_f16 v[180:183], v[48:51], v[104:107], v[180:183]\n"
  "global_load_lds_dword v252, s[88:89]\n"
  "v_pk_add_f16 v120, v104, v108\n"
  "v_mfma_f32_16x16x32_f16 v[192:195], v[52:55], v[104:107], v[192:195]\n"
  "v_pk_add_f16 v121, v105, v109\n"
  "v_pk_add_f16 v122, v106, v110\n"
  "v_mfma_f32_16x16x32_f16 v[204:207], v[56:59], v[104:107], v[204:207]\n"
  "s_waitcnt lgkmcnt(6)\n"
  "ds_read_b64_tr_b16 v[24:25], v230 offset:256\n"
  "v_pk_add_f16 v123, v107, v111\n"
  "v_mfma_f32_16x16x32_f16 v[216:219], v[60:63], v[104:107], v[216:219]\n"
  "ds_read_b128 v[104:107], v232 offset:6144\n"
  "v_pk_add_f16 v32, v0, v16\n"
  "v_mfma_f32_16x16x32_f16 v[184:187], v[64:67], v[108:111], v[184:187]\n"
  "ds_read_b64_tr_b16 v[26:27], v230 offset:8448\n"
  "v_pk_add_f16 v33, v1, v17\n"
  "v_mfma_f32_16x16x32_f16 v[196:199], v[68:71], v[108:111], v[196:199]\n"
  "s_waitcnt lgkmcnt(5)\n"
  "ds_read_b64_tr_b16 v[12:13], v231 offset:0\n"
  "v_pk_add_f16 v34, v2, v18\n"
  "v_mfma_f32_16x16x32_f16 v[208:211], v[72:75], v[108:111], v[208:211]\n"
  "ds_read_b64_tr_b16 v[14:15], v231 offset:8192\n"
  "v_pk_add_f16 v35, v3, v19\n"
  "v_mfma_f32_16x16x32_f16 v[220:223], v[76:79], v[108:111], v[220:223]\n"
  "ds_read_b128 v[108:111], v232 offset:6656\n"
  "v_pk_add_f16 v36, v4, v20\n"
  "v_mfma_f32_16x16x32_f16 v[188:191], v[80:83], v[120:123], v[188:191]\n"
  "ds_read_b128 v[112:115], v232 offset:8192\n"
  "v_pk_add_f16 v37, v5, v21\n"
  "v_mfma_f32_16x16x32_f16 v[200:203], v[84:87], v[120:123], v[200:203]\n"
  "ds_read_b64_tr_b16 v[28:29], v231 offset:256\n"
  "v_pk_add_f16 v38, v6, v22\n"
  "v_mfma_f32_16x16x32_f16 v[212:215], v[88:91], v[120:123], v[212:215]\n"
  "ds_read_b128 v[116:119], v232 offset:8704\n"
  "v_pk_add_f16 v39, v7, v23\n"
  "v_mfma_f32_16x16x32_f16 v[224:227], v[92:95], v[120:123], v[224:227]\n"
  "s_waitcnt lgkmcnt(6)\n"
  "ds_read_b64_tr_b16 v[30:31], v231 offset:8448\n"
  "v_pk_add_f16 v40, v8, v24\n"
  "v_mfma_f32_16x16x32_f16 a[96:99], v[48:51], v[96:99], a[96:99]\n"
  "v_pk_add_f16 v120, v96, v100\n"
  "v_pk_add_f16 v121, v97, v101\n"
  "v_mfma_f32_16x16x32_f16 a[108:111], v[52:55], v[96:99], a[108:111]\n"
  "v_pk_add_f16 v122, v98, v102\n"
  "v_pk_add_f16 v123, v99, v103\n"
  "v_mfma_f32_16x16x32_f16 a[120:123], v[56:59], v[96:99], a[120:123]\n"
  "v_pk_add_f16 v41, v9, v25\n"
  "v_pk_add_f16 v42, v10, v26\n"
  "v_mfma_f32_16x16x32_f16 a[132:135], v[60:63], v[96:99], a[132:135]\n"
  "ds_read_b128 v[96:99], v232 offset:0\n"
  "v_pk_add_f16 v43, v11, v27\n"
  "v_mfma_f32_16x16x32_f16 a[100:103], v[64:67], v[100:103], a[100:103]\n"
  "v_add_u32 v230, s49, v230\n"
  "v_add_u32 v231, s49, v231\n"
  "v_mfma_f32_16x16x32_f16 a[112:115], v[68:71], v[100:103], a[112:115]\n"
  "v_mfma_f32_16x16x32_f16 a[124:127], v[72:75], v[100:103], a[124:127]\n"
  "s_waitcnt lgkmcnt(1)\n"
  "v_pk_add_f16 v44, v12, v28\n"
  "v_pk_add_f16 v45, v13, v29\n"
  "v_mfma_f32_16x16x32_f16 a[136:139], v[76:79], v[100:103], a[136:139]\n"
  "ds_read_b128 v[100:103], v232 offset:512\n"
  "v_pk_add_f16 v46, v14, v30\n"
  "v_mfma_f32_16x16x32_f16 a[104:107], v[80:83], v[120:123], a[104:107]\n"
  "v_pk_add_f16 v47, v15, v31\n"
  "v_mfma_f32_16x16x32_f16 a[116:119], v[84:87], v[120:123], a[116:119]\n"
  "v_mfma_f32_16x16x32_f16 a[128:131], v[88:91], v[120:123], a[128:131]\n"
  "v_mfma_f32_16x16x32_f16 a[140:143], v[92:95], v[120:123], a[140:143]\n"
  "v_mfma_f32_16x16x32_f16 a[144:147], v[48:51], v[104:107], a[144:147]\n"
  "v_pk_add_f16 v120, v104, v108\n"
  "v_pk_add_f16 v121, v105, v109\n"
  "v_mfma_f32_16x16x32_f16 a[156:159], v[52:55], v[104:107], a[156:159]\n"
  "v_pk_add_f16 v122, v106, v110\n"
  "v_pk_add_f16 v123, v107, v111\n"
  "v_mfma_f32_16x16x32_f16 a[168:171], v[56:59], v[104:107], a[168:171]\n"
  "v_mfma_f32_16x16x32_f16 a[180:183], v[60:63], v[104:107], a[180:183]\n"
  "ds_read_b128 v[104:107], v232 offset:2048\n"
  "v_mfma_f32_16x16x32_f16 a[148:151], v[64:67], v[108:111], a[148:151]\n"
  "s_add_i32 s47, s44, s46\n"
  "s_add_i32 s50, s50, 4194304\n"
  "v_mfma_f32_16x16x32_f16 a[160:163], v[68:71], v[108:111], a[160:163]\n"
  "s_add_i32 s48, s47, s45\n"
  "s_add_i32 s46, s46, s49\n"
  "v_mfma_f32_16x16x32_f16 a[172:175], v[72:75], v[108:111], a[172:175]\n"
  "v_mfma_f32_16x16x32_f16 a[184:187], v[76:79], v[108:111], a[184:187]\n"
  "ds_read_b128 v[108:111], v232 offset:2560\n"
  "v_add_u32 v232, s49, v232\n"
  "v_mfma_f32_16x16x32_f16 a[152:155], v[80:83], v[120:123], a[152:155]\n"
  "v_mfma_f32_16x16x32_f16 a[164:167], v[84:87], v[120:123], a[164:167]\n"
  "v_mfma_f32_16x16x32_f16 a[176:179], v[88:91], v[120:123], a[176:179]\n"
  "v_mfma_f32_16x16x32_f16 a[188:191], v[92:95], v[120:123], a[188:191]\n"
  "v_mfma_f32_16x16x32_f16 a[192:195], v[48:51], v[112:115], a[192:195]\n"
  "v_pk_add_f16 v120, v112, v116\n"
  "v_pk_add_f16 v121, v113, v117\n"
  "v_mfma_f32_16x16x32_f16 a[204:207], v[52:55], v[112:115], a[204:207]\n"
  "v_pk_add_f16 v122, v114, v118\n"
  "v_pk_add_f16 v123, v115, v119\n"
  "v_mfma_f32_16x16x32_f16 a[216:219], v[56:59], v[112:115], a[216:219]\n"
  "v_mfma_f32_16x16x32_f16 a[228:231], v[60:63], v[112:115], a[228:231]\n"
  "v_mfma_f32_16x16x32_f16 a[196:199], v[64:67], v[116:119], a[196:199]\n"
  "v_mfma_f32_16x16x32_f16 a[208:211], v[68:71], v[116:119], a[208:211]\n"
  "v_mfma_f32_16x16x32_f16 a[220:223], v[72:75], v[116:119], a[220:223]\n"
  "v_mfma_f32_16x16x32_f16 a[232:235], v[76:79], v[116:119], a[232:235]\n"
  "v_mfma_f32_16x16x32_f16 a[200:203], v[80:83], v[120:123], a[200:203]\n"
  "s_waitcnt vmcnt(2) lgkmcnt(0)\n"
  "v_mfma_f32_16x16x32_f16 a[212:215], v[84:87], v[120:123], a[212:215]\n"
  "v_mfma_f32_16x16x32_f16 a[224:227], v[88:91], v[120:123], a[224:227]\n"
  "s_barrier\n"
  "v_mfma_f32_16x16x32_f16 a[236:239], v[92:95], v[120:123], a[236:239]\n"
  "v_mfma_f32_16x16x32_f16 v[124:127], v[0:3], v[96:99], v[124:127]\n"
  "v_pk_add_f16 v120, v96, v100\n"
  "v_pk_add_f16 v121, v97, v101\n"
  "v_mfma_f32_16x16x32_f16 v[136:139], v[4:7], v[96:99], v[136:139]\n"
  "v_pk_add_f16 v122, v98, v102\n"
  "v_pk_add_f16 v123, v99, v103\n"
  "v_mfma_f32_16x16x32_f16 v[148:151], v[8:11], v[96:99], v[148:151]\n"
  "ds_read_b128 v[112:115], v232 offset:0\n"
  "v_add_u32 v242, 57344, v232\n"
  "v_mfma_f32_16x16x32_f16 v[160:163], v[12:15], v[96:99], v[160:163]\n"
  "s_mov_b32 s66, 0x55555555\n"
  "s_mov_b32 s67, 0x55555555\n"
  "ds_read_b128 v[116:119], v232 offset:512\n"
  "v_mfma_f32_16x16x32_f16 v[128:131], v[16:19], v[100:103], v[128:131]\n"
  "ds_read_b64_tr_b16 v[48:49], v228 offset:0\n"
  "ds_read_b64_tr_b16 v[50:51], v228 offset:8192\n"
  "v_mfma_f32_16x16x32_f16 v[140:143], v[20:23], v[100:103], v[140:143]\n"
  "ds_read_b64_tr_b16 v[64:65], v228 offset:256\n"
  "ds_read_b64_tr_b16 v[66:67], v228 offset:8448\n"
  "v_mfma_f32_16x16x32_f16 v[152:155], v[24:27], v[100:103], v[152:155]\n"
  "ds_read_b64_tr_b16 v[52:53], v229 offset:0\n"
  "ds_read_b64_tr_b16 v[54:55], v229 offset:8192\n"
  "v_mfma_f32_16x16x32_f16 v[164:167], v[28:31], v[100:103], v[164:167]\n"
  "ds_read_b64_tr_b16 v[68:69], v229 offset:256\n"
  "ds_read_b64_tr_b16 v[70:71], v229 offset:8448\n"
  "v_mfma_f32_16x16x32_f16 v[132:135], v[32:35], v[120:123], v[132:135]\n"
  "ds_read_b64_tr_b16 v[56:57], v230 offset:0\n"
  "ds_read_b64_tr_b16 v[58:59], v230 offset:8192\n"
  "v_mfma_f32_16x16x32_f16 v[144:147], v[36:39], v[120:123], v[144:147]\n"
  "ds_read_b64_tr_b16 v[72:73], v230 offset:256\n"
  "ds_read_b64_tr_b16 v[74:75], v230 offset:8448\n"
  "v_mfma_f32_16x16x32_f16 v[156:159], v[40:43], v[120:123], v[156:159]\n"
  "ds_read_b64_tr_b16 v[60:61], v231 offset:0\n"
  "ds_read_b64_tr_b16 v[62:63], v231 offset:8192\n"
  "v_mfma_f32_16x16x32_f16 v[168:171], v[44:47], v[120:123], v[168:171]\n"
  "ds_read_b64_tr_b16 v[76:77], v231 offset:256\n"
  "ds_read_b64_tr_b16 v[78:79], v231 offset:8448\n"
  "v_mfma_f32_16x16x32_f16 v[180:183], v[0:3], v[104:107], v[180:183]\n"
  "s_waitcnt lgkmcnt(12)\n"
  "v_pk_add_f16 v120, v104, v108\n"
  "v_pk_add_f16 v121, v105, v109\n"
  "v_mfma_f32_16x16x32_f16 v[192:195], v[4:7], v[104:107], v[192:195]\n"
  "v_pk_add_f16 v122, v106, v110\n"
  "v_pk_add_f16 v123, v107, v111\n"
  "v_mfma_f32_16x16x32_f16 v[204:207], v[8:11], v[104:107], v[204:207]\n"
  "s_waitcnt lgkmcnt(8)\n"
  "v_pk_add_f16 v80, v48, v64\n"
  "v_pk_add_f16 v81, v49, v65\n"
  "v_mfma_f32_16x16x32_f16 v[216:219], v[12:15], v[104:107], v[216:219]\n"
  "v_pk_add_f16 v82, v50, v66\n"
  "v_pk_add_f16 v83, v51, v67\n"
  "v_mfma_f32_16x16x32_f16 v[184:187], v[16:19], v[108:111], v[184:187]\n"
  "s_waitcnt lgkmcnt(4)\n"
  "ds_read_b128 v[96:99], v232 offset:2048\n"
  "v_pk_add_f16 v84, v52, v68\n"
  "v_mfma_f32_16x16x32_f16 v[196:199], v[20:23], v[108:111], v[196:199]\n"
  "ds_read_b128 v[100:103], v232 offset:2560\n"
  "v_pk_add_f16 v85, v53, v69\n"
  "v_mfma_f32_16x16x32_f16 v[208:211], v[24:27], v[108:111], v[208:211]\n"
  "s_waitcnt lgkmcnt(2)\n"
  "v_pk_add_f16 v86, v54, v70\n"
  "v_pk_add_f16 v87, v55, v71\n"
  "v_mfma_f32_16x16x32_f16 v[220:223], v[28:31], v[108:111], v[220:223]\n"
  "v_pk_add_f16 v88, v56, v72\n"
  "v_pk_add_f16 v89, v57, v73\n"
  "v_mfma_f32_16x16x32_f16 v[188:191], v[32:35], v[120:123], v[188:191]\n"
  "v_pk_add_f16 v90, v58, v74\n"
  "v_pk_add_f16 v91, v59, v75\n"
  "v_mfma_f32_16x16x32_f16 v[200:203], v[36:39], v[120:123], v[200:203]\n"
  "v_pk_add_f16 v92, v60, v76\n"
  "v_pk_add_f16 v93, v61, v77\n"
  "v_mfma_f32_16x16x32_f16 v[212:215], v[40:43], v[120:123], v[212:215]\n"
  "v_pk_add_f16 v94, v62, v78\n"
  "v_pk_add_f16 v95, v63, v79\n"
  "v_mfma_f32_16x16x32_f16 v[224:227], v[44:47], v[120:123], v[224:227]\n"
  "v_mfma_f32_16x16x32_f16 v[124:127], v[48:51], v[112:115], v[124:127]\n"
  "v_pk_add_f16 v120, v112, v116\n"
  "v_pk_add_f16 v121, v113, v117\n"
  "v_mfma_f32_16x16x32_f16 v[136:139], v[52:55], v[112:115], v[136:139]\n"
  "v_pk_add_f16 v122, v114, v118\n"
  "v_pk_add_f16 v123, v115, v119\n"
  "v_mfma_f32_16x16x32_f16 v[128:131], v[64:67], v[116:119], v[128:131]\n"
  "ds_read_b128 v[104:107], v232 offset:4096\n"
  "ds_read_b128 v[108:111], v232 offset:4608\n"
  "v_mfma_f32_16x16x32_f16 v[140:143], v[68:71], v[116:119], v[140:143]\n"
  "v_mfma_f32_16x16x32_f16 v[132:135], v[80:83], v[120:123], v[132:135]\n"
  "v_mfma_f32_16x16x32_f16 v[144:147], v[84:87], v[120:123], v[144:147]\n"
  "v_mfma_f32_16x16x32_f16 v[148:151], v[56:59], v[112:115], v[148:151]\n"
  "v_mfma_f32_16x16x32_f16 v[160:163], v[60:63], v[112:115], v[160:163]\n"
  "v_fmac_f32 v124, s76, v132\n"
  "v_fmac_f32 v128, s76, v132\n"
  "v_mfma_f32_16x16x32_f16 v[152:155], v[72:75], v[116:119], v[152:155]\n"
  "s_waitcnt lgkmcnt(2)\n"
  "v_fmac_f32 v125, s76, v133\n"
  "v_fmac_f32 v129, s76, v133\n"
  "v_mfma_f32_16x16x32_f16 v[164:167], v[76:79], v[116:119], v[164:167]\n"
  "v_fmac_f32 v126, s76, v134\n"
  "v_fmac_f32 v130, s76, v134\n"
  "v_mfma_f32_16x16x32_f16 v[156:159], v[88:91], v[120:123], v[156:159]\n"
  "v_fmac_f32 v127, s76, v135\n"
  "v_fmac_f32 v131, s76, v135\n"
  "v_mfma_f32_16x16x32_f16 v[168:171], v[92:95], v[120:123], v[168:171]\n"
  "v_cvt_pk_bf16_f32 v172, v124, v125\n"
  "v_cvt_pk_bf16_f32 v173, v126, v127\n"
  "v_mfma_f32_16x16x32_f16 v[180:183], v[48:51], v[96:99], v[180:183]\n"
  "v_pk_add_f16 v120, v96, v100\n"
  "v_pk_add_f16 v121, v97, v101\n"
  "v_mfma_f32_16x16x32_f16 v[192:195], v[52:55], v[96:99], v[192:195]\n"
  "v_pk_add_f16 v122, v98, v102\n"
  "v_pk_add_f16 v123, v99, v103\n"
  "v_mfma_f32_16x16x32_f16 v[184:187], v[64:67], v[100:103], v[184:187]\n"
  "ds_read_b128 v[112:115], v242 offset:4096\n"
  "v_cvt_pk_bf16_f32 v176, v128, v129\n"
  "v_cvt_pk_bf16_f32 v177, v130, v131\n"
  "v_fmac_f32 v136, s76, v144\n"
  "v_fmac_f32 v140, s76, v144\n"
  "v_mfma_f32_16x16x32_f16 v[196:199], v[68:71], v[100:103], v[196:199]\n"
  "ds_read_b128 v[116:119], v242 offset:4608\n"
  "v_fmac_f32 v137, s76, v145\n"
  "v_fmac_f32 v141, s76, v145\n"
  "v_fmac_f32 v138, s76, v146\n"
  "v_fmac_f32 v142, s76, v146\n"
  "v_mfma_f32_16x16x32_f16 v[188:191], v[80:83], v[120:123], v[188:191]\n"
  "v_fmac_f32 v139, s76, v147\n"
  "v_fmac_f32 v143, s76, v147\n"
  "v_cvt_pk_bf16_f32 v174, v136, v137\n"
  "v_cvt_pk_bf16_f32 v175, v138, v139\n"
  "v_cvt_pk_bf16_f32 v178, v140, v141\n"
  "v_mfma_f32_16x16x32_f16 v[200:203], v[84:87], v[120:123], v[200:203]\n"
  "v_cvt_pk_bf16_f32 v179, v142, v143\n"
  "v_fmac_f32 v148, s76, v156\n"
  "v_fmac_f32 v152, s76, v156\n"
  "v_fmac_f32 v149, s76, v157\n"
  "v_fmac_f32 v153, s76, v157\n"
  "v_mfma_f32_16x16x32_f16 v[204:207], v[56:59], v[96:99], v[204:207]\n"
  "s_mov_b64 vcc, s[66:67]\n"
  "v_cndmask_b32_dpp v234, v176, v172, vcc quad_perm:[1,0,3,2] row_mask:0xf bank_mask:0xf\n"
  "v_cndmask_b32_dpp v235, v177, v173, vcc quad_perm:[1,0,3,2] row_mask:0xf bank_mask:0xf\n"
  "v_cndmask_b32_dpp v236, v178, v174, vcc quad_perm:[1,0,3,2] row_mask:0xf bank_mask:0xf\n"
  "v_cndmask_b32_dpp v237, v179, v175, vcc quad_perm:[1,0,3,2] row_mask:0xf bank_mask:0xf\n"
  "v_fmac_f32 v150, s76, v158\n"
  "v_mfma_f32_16x16x32_f16 v[216:219], v[60:63], v[96:99], v[216:219]\n"
  "s_add_i32 s64, s80, 0\n"
  "buffer_store_dwordx4 v[234:237], v250, s[72:75], s64 offen sc1\n"
  "s_not_b64 vcc, vcc\n"
  "v_cndmask_b32_dpp v238, v172, v176, vcc quad_perm:[1,0,3,2] row_mask:0xf bank_mask:0xf\n"
  "v_cndmask_b32_dpp v239, v173, v177, vcc quad_perm:[1,0,3,2] row_mask:0xf bank_mask:0xf\n"
  "v_cndmask_b32_dpp v240, v174, v178, vcc quad_perm:[1,0,3,2] row_mask:0xf bank_mask:0xf\n"
  "v_cndmask_b32_dpp v241, v175, v179, vcc quad_perm:[1,0,3,2] row_mask:0xf bank_mask:0xf\n"
  "v_mfma_f32_16x16x32_f16 v[208:211], v[72:75], v[100:103], v[208:211]\n"
  "s_waitcnt lgkmcnt(2)\n"
  "s_add_i32 s65, s80, 65536\n"
  "buffer_store_dwordx4 v[238:241], v250, s[72:75], s65 offen sc1\n"
  "v_fmac_f32 v154, s76, v158\n"
  "v_fmac_f32 v151, s76, v159\n"
  "v_fmac_f32 v155, s76, v159\n"
  "v_cvt_pk_bf16_f32 v172, v148, v149\n"
  "v_mfma_f32_16x16x32_f16 v[220:223], v[76:79], v[100:103], v[220:223]\n"
  "v_cvt_pk_bf16_f32 v173, v150, v151\n"
  "v_cvt_pk_bf16_f32 v176, v152, v153\n"
  "v_cvt_pk_bf16_f32 v177, v154, v155\n"
  "v_fmac_f32 v160, s76, v168\n"
  "v_fmac_f32 v164, s76, v168\n"
  "v_mfma_f32_16x16x32_f16 v[212:215], v[88:91], v[120:123], v[212:215]\n"
  "v_fmac_f32 v161, s76, v169\n"
  "v_fmac_f32 v165, s76, v169\n"
  "v_fmac_f32 v162, s76, v170\n"
  "v_fmac_f32 v166, s76, v170\n"
  "v_fmac_f32 v163, s76, v171\n"
  "v_mfma_f32_16x16x32_f16 v[224:227], v[92:95], v[120:123], v[224:227]\n"
  "v_fmac_f32 v167, s76, v171\n"
  "v_cvt_pk_bf16_f32 v174, v160, v161\n"
  "v_cvt_pk_bf16_f32 v175, v162, v163\n"
  "v_cvt_pk_bf16_f32 v178, v164, v165\n"
  "v_cvt_pk_bf16_f32 v179, v166, v167\n"
  "v_mfma_f32_16x16x32_f16 a[96:99], v[0:3], v[104:107], a[96:99]\n"
  "ds_read_b128 v[96:99], v232 offset:6144\n"
  "v_pk_add_f16 v120, v104, v108\n"
  "v_pk_add_f16 v121, v105, v109\n"
  "v_pk_add_f16 v122, v106, v110\n"
  "v_pk_add_f16 v123, v107, v111\n"
  "v_mfma_f32_16x16x32_f16 a[108:111], v[4:7], v[104:107], a[108:111]\n"
  "ds_read_b128 v[100:103], v232 offset:6656\n"
  "s_mov_b64 vcc, s[66:67]\n"
  "v_cndmask_b32_dpp v234, v176, v172, vcc quad_perm:[1,0,3,2] row_mask:0xf bank_mask:0xf\n"
  "v_cndmask_b32_dpp v235, v177, v173, vcc quad_perm:[1,0,3,2] row_mask:0xf bank_mask:0xf\n"
  "v_cndmask_b32_dpp v236, v178, v174, vcc quad_perm:[1,0,3,2] row_mask:0xf bank_mask:0xf\n"
  "v_cndmask_b32_dpp v237, v179, v175, vcc quad_perm:[1,0,3,2] row_mask:0xf bank_mask:0xf\n"
  "v_mfma_f32_16x16x32_f16 a[120:123], v[8:11], v[104:107], a[120:123]\n"
  "s_add_i32 s64, s80, 4096\n"
  "buffer_store_dwordx4 v[234:237], v250, s[72:75], s64 offen sc1\n"
  "s_not_b64 vcc, vcc\n"
  "v_cndmask_b32_dpp v238, v172, v176, vcc quad_perm:[1,0,3,2] row_mask:0xf bank_mask:0xf\n"
  "v_cndmask_b32_dpp v239, v173, v177, vcc quad_perm:[1,0,3,2] row_mask:0xf bank_mask:0xf\n"
  "v_cndmask_b32_dpp v240, v174, v178, vcc quad_perm:[1,0,3,2] row_mask:0xf bank_mask:0xf\n"
  "v_cndmask_b32_dpp v241, v175, v179, vcc quad_perm:[1,0,3,2] row_mask:0xf bank_mask:0xf\n"
  "v_mfma_f32_16x16x32_f16 a[132:135], v[12:15], v[104:107], a[132:135]\n"
  "s_add_i32 s65, s80, 69632\n"
  "buffer_store_dwordx4 v[238:241], v250, s[72:75], s65 offen sc1\n"
  "v_fmac_f32 v180, s76, v188\n"
  "v_fmac_f32 v184, s76, v188\n"
  "v_fmac_f32 v181, s76, v189\n"
  "v_fmac_f32 v185, s76, v189\n"
  "v_mfma_f32_16x16x32_f16 a[100:103], v[16:19], v[108:111], a[100:103]\n"
  "v_fmac_f32 v182, s76, v190\n"
  "v_fmac_f32 v186, s76, v190\n"
  "v_fmac_f32 v183, s76, v191\n"
  "v_fmac_f32 v187, s76, v191\n"
  "v_cvt_pk_bf16_f32 v172, v180, v181\n"
  "v_mfma_f32_16x16x32_f16 a[112:115], v[20:23], v[108:111], a[112:115]\n"
  "v_cvt_pk_bf16_f32 v173, v182, v183\n"
  "v_cvt_pk_bf16_f32 v176, v184, v185\n"
  "v_cvt_pk_bf16_f32 v177, v186, v187\n"
  "v_fmac_f32 v192, s76, v200\n"
  "v_fmac_f32 v196, s76, v200\n"
  "v_mfma_f32_16x16x32_f16 a[124:127], v[24:27], v[108:111], a[124:127]\n"
  "v_fmac_f32 v193, s76, v201\n"
  "v_fmac_f32 v197, s76, v201\n"
  "v_fmac_f32 v194, s76, v202\n"
  "v_fmac_f32 v198, s76, v202\n"
  "v_fmac_f32 v195, s76, v203\n"
  "v_mfma_f32_16x16x32_f16 a[136:139], v[28:31], v[108:111], a[136:139]\n"
  "v_fmac_f32 v199, s76, v203\n"
  "v_cvt_pk_bf16_f32 v174, v192, v193\n"
  "v_cvt_pk_bf16_f32 v175, v194, v195\n"
  "v_cvt_pk_bf16_f32 v178, v196, v197\n"
  "v_cvt_pk_bf16_f32 v179, v198, v199\n"
  "v_mfma_f32_16x16x32_f16 a[104:107], v[32:35], v[120:123], a[104:107]\n"
  "s_waitcnt lgkmcnt(2)\n"
  "s_mov_b64 vcc, s[66:67]\n"
  "v_cndmask_b32_dpp v234, v176, v172, vcc quad_perm:[1,0,3,2] row_mask:0xf bank_mask:0xf\n"
  "v_cndmask_b32_dpp v235, v177, v173, vcc quad_perm:[1,0,3,2] row_mask:0xf bank_mask:0xf\n"
  "v_cndmask_b32_dpp v236, v178, v174, vcc quad_perm:[1,0,3,2] row_mask:0xf bank_mask:0xf\n"
  "v_cndmask_b32_dpp v237, v179, v175, vcc quad_perm:[1,0,3,2] row_mask:0xf bank_mask:0xf\n"
  "v_fmac_f32 v204, s76, v212\n"
  "v_mfma_f32_16x16x32_f16 a[116:119], v[36:39], v[120:123], a[116:119]\n"
  "s_add_i32 s64, s81, 0\n"
  "buffer_store_dwordx4 v[234:237], v250, s[72:75], s64 offen sc1\n"
  "s_not_b64 vcc, vcc\n"
  "v_cndmask_b32_dpp v238, v172, v176, vcc quad_perm:[1,0,3,2] row_mask:0xf bank_mask:0xf\n"
  "v_cndmask_b32_dpp v239, v173, v177, vcc quad_perm:[1,0,3,2] row_mask:0xf bank_mask:0xf\n"
  "v_cndmask_b32_dpp v240, v174, v178, vcc quad_perm:[1,0,3,2] row_mask:0xf bank_mask:0xf\n"
  "v_cndmask_b32_dpp v241, v175, v179, vcc quad_perm:[1,0,3,2] row_mask:0xf bank_mask:0xf\n"
  "v_mfma_f32_16x16x32_f16 a[128:131], v[40:43], v[120:123], a[128:131]\n"
  "s_add_i32 s65, s81, 65536\n"
  "buffer_store_dwordx4 v[238:241], v250, s[72:75], s65 offen sc1\n"
  "v_fmac_f32 v208, s76, v212\n"
  "v_fmac_f32 v205, s76, v213\n"
  "v_fmac_f32 v209, s76, v213\n"
  "v_fmac_f32 v206, s76, v214\n"
  "v_mfma_f32_16x16x32_f16 a[140:143], v[44:47], v[120:123], a[140:143]\n"
  "v_fmac_f32 v210, s76, v214\n"
  "v_fmac_f32 v207, s76, v215\n"
  "v_fmac_f32 v211, s76, v215\n"
  "v_cvt_pk_bf16_f32 v172, v204, v205\n"
  "v_cvt_pk_bf16_f32 v173, v206, v207\n"
  "v_mfma_f32_16x16x32_f16 a[96:99], v[48:51], v[112:115], a[96:99]\n"
  "ds_read_b128 v[104:107], v242 offset:6144\n"
  "v_pk_add_f16 v120, v112, v116\n"
  "v_pk_add_f16 v121, v113, v117\n"
  "v_pk_add_f16 v122, v114, v118\n"
  "v_pk_add_f16 v123, v115, v119\n"
  "v_mfma_f32_16x16x32_f16 a[108:111], v[52:55], v[112:115], a[108:111]\n"
  "ds_read_b128 v[108:111], v242 offset:6656\n"
  "v_cvt_pk_bf16_f32 v176, v208, v209\n"
  "v_cvt_pk_bf16_f32 v177, v210, v211\n"
  "v_fmac_f32 v216, s76, v224\n"
  "v_fmac_f32 v220, s76, v224\n"
  "v_mfma_f32_16x16x32_f16 a[100:103], v[64:67], v[116:119], a[100:103]\n"
  "v_fmac_f32 v217, s76, v225\n"
  "v_fmac_f32 v221, s76, v225\n"
  "v_fmac_f32 v218, s76, v226\n"
  "v_fmac_f32 v222, s76, v226\n"
  "v_fmac_f32 v219, s76, v227\n"
  "v_mfma_f32_16x16x32_f16 a[112:115], v[68:71], v[116:119], a[112:115]\n"
  "v_fmac_f32 v223, s76, v227\n"
  "v_cvt_pk_bf16_f32 v174, v216, v217\n"
  "v_cvt_pk_bf16_f32 v175, v218, v219\n"
  "v_cvt_pk_bf16_f32 v178, v220, v221\n"
  "v_cvt_pk_bf16_f32 v179, v222, v223\n"
  "v_mfma_f32_16x16x32_f16 a[104:107], v[80:83], v[120:123], a[104:107]\n"
  "s_mov_b64 vcc, s[66:67]\n"
  "v_cndmask_b32_dpp v234, v176, v172, vcc quad_perm:[1,0,3,2] row_mask:0xf bank_mask:0xf\n"
  "v_cndmask_b32_dpp v235, v177, v173, vcc quad_perm:[1,0,3,2] row_mask:0xf bank_mask:0xf\n"
  "v_cndmask_b32_dpp v236, v178, v174, vcc quad_perm:[1,0,3,2] row_mask:0xf bank_mask:0xf\n"
  "v_cndmask_b32_dpp v237, v179, v175, vcc quad_perm:[1,0,3,2] row_mask:0xf bank_mask:0xf\n"
  "v_accvgpr_read_b32 v124, a96\n"
  "v_mfma_f32_16x16x32_f16 a[116:119], v[84:87], v[120:123], a[116:119]\n"
  "s_add_i32 s64, s81, 4096\n"
  "buffer_store_dwordx4 v[234:237], v250, s[72:75], s64 offen sc1\n"
  "s_not_b64 vcc, vcc\n"
  "v_cndmask_b32_dpp v238, v172, v176, vcc quad_perm:[1,0,3,2] row_mask:0xf bank_mask:0xf\n"
  "v_cndmask_b32_dpp v239, v173, v177, vcc quad_perm:[1,0,3,2] row_mask:0xf bank_mask:0xf\n"
  "v_cndmask_b32_dpp v240, v174, v178, vcc quad_perm:[1,0,3,2] row_mask:0xf bank_mask:0xf\n"
  "v_cndmask_b32_dpp v241, v175, v179, vcc quad_perm:[1,0,3,2] row_mask:0xf bank_mask:0xf\n"
  "v_mfma_f32_16x16x32_f16 a[120:123], v[56:59], v[112:115], a[120:123]\n"
  "s_add_i32 s65, s81, 69632\n"
  "buffer_store_dwordx4 v[238:241], v250, s[72:75], s65 offen sc1\n"
  "v_accvgpr_read_b32 v125, a97\n"
  "v_accvgpr_read_b32 v126, a98\n"
  "v_accvgpr_read_b32 v127, a99\n"
  "v_accvgpr_read_b32 v128, a100\n"
  "v_mfma_f32_16x16x32_f16 a[132:135], v[60:63], v[112:115], a[132:135]\n"
  "v_accvgpr_read_b32 v129, a101\n"
  "v_accvgpr_read_b32 v130, a102\n"
  "v_accvgpr_read_b32 v131, a103\n"
  "v_accvgpr_read_b32 v132, a104\n"
  "v_accvgpr_read_b32 v133, a105\n"
  "v_mfma_f32_16x16x32_f16 a[124:127], v[72:75], v[116:119], a[124:127]\n"
  "s_waitcnt lgkmcnt(2)\n"
  "v_accvgpr_read_b32 v134, a106\n"
  "v_accvgpr_read_b32 v135, a107\n"
  "v_fmac_f32 v124, s76, v132\n"
  "v_fmac_f32 v128, s76, v132\n"
  "v_fmac_f32 v125, s76, v133\n"
  "v_mfma_f32_16x16x32_f16 a[136:139], v[76:79], v[116:119], a[136:139]\n"
  "v_fmac_f32 v129, s76, v133\n"
  "v_fmac_f32 v126, s76, v134\n"
  "v_fmac_f32 v130, s76, v134\n"
  "v_fmac_f32 v127, s76, v135\n"
  "v_fmac_f32 v131, s76, v135\n"
  "v_mfma_f32_16x16x32_f16 a[128:131], v[88:91], v[120:123], a[128:131]\n"
  "v_cvt_pk_bf16_f32 v172, v124, v125\n"
  "v_cvt_pk_bf16_f32 v173, v126, v127\n"
  "v_cvt_pk_bf16_f32 v176, v128, v129\n"
  "v_cvt_pk_bf16_f32 v177, v130, v131\n"
  "v_accvgpr_read_b32 v136, a108\n"
  "v_mfma_f32_16x16x32_f16 a[140:143], v[92:95], v[120:123], a[140:143]\n"
  "v_accvgpr_read_b32 v137, a109\n"
  "v_accvgpr_read_b32 v138, a110\n"
  "v_accvgpr_read_b32 v139, a111\n"
  "v_accvgpr_read_b32 v140, a112\n"
  "v_accvgpr_read_b32 v141, a113\n"
  "v_mfma_f32_16x16x32_f16 a[144:147], v[0:3], v[96:99], a[144:147]\n"
  "ds_read_b128 v[112:115], v232 offset:8192\n"
  "v_pk_add_f16 v120, v96, v100\n"
  "v_pk_add_f16 v121, v97, v101\n"
  "v_pk_add_f16 v122, v98, v102\n"
  "v_pk_add_f16 v123, v99, v103\n"
  "v_mfma_f32_16x16x32_f16 a[156:159], v[4:7], v[96:99], a[156:159]\n"
  "ds_read_b128 v[116:119], v232 offset:8704\n"
  "v_accvgpr_read_b32 v142, a114\n"
  "v_accvgpr_read_b32 v143, a115\n"
  "v_accvgpr_read_b32 v144, a116\n"
  "v_accvgpr_read_b32 v145, a117\n"
  "v_mfma_f32_16x16x32_f16 a[168:171], v[8:11], v[96:99], a[168:171]\n"
  "v_accvgpr_read_b32 v146, a118\n"
  "v_accvgpr_read_b32 v147, a119\n"
  "v_fmac_f32 v136, s76, v144\n"
  "v_fmac_f32 v140, s76, v144\n"
  "v_fmac_f32 v137, s76, v145\n"
  "v_mfma_f32_16x16x32_f16 a[180:183], v[12:15], v[96:99], a[180:183]\n"
  "v_fmac_f32 v141, s76, v145\n"
  "v_fmac_f32 v138, s76, v146\n"
  "v_fmac_f32 v142, s76, v146\n"
  "v_fmac_f32 v139, s76, v147\n"
  "v_fmac_f32 v143, s76, v147\n"
  "v_mfma_f32_16x16x32_f16 a[148:151], v[16:19], v[100:103], a[148:151]\n"
  "v_cvt_pk_bf16_f32 v174, v136, v137\n"
  "v_cvt_pk_bf16_f32 v175, v138, v139\n"
  "v_cvt_pk_bf16_f32 v178, v140, v141\n"
  "v_cvt_pk_bf16_f32 v179, v142, v143\n"
  "v_accvgpr_read_b32 v148, a120\n"
  "v_mfma_f32_16x16x32_f16 a[160:163], v[20:23], v[100:103], a[160:163]\n"
  "s_mov_b64 vcc, s[66:67]\n"
  "v_cndmask_b32_dpp v234, v176, v172, vcc quad_perm:[1,0,3,2] row_mask:0xf bank_mask:0xf\n"
  "v_cndmask_b32_dpp v235, v177, v173, vcc quad_perm:[1,0,3,2] row_mask:0xf bank_mask:0xf\n"
  "v_cndmask_b32_dpp v236, v178, v174, vcc quad_perm:[1,0,3,2] row_mask:0xf bank_mask:0xf\n"
  "v_cndmask_b32_dpp v237, v179, v175, vcc quad_perm:[1,0,3,2] row_mask:0xf bank_mask:0xf\n"
  "v_accvgpr_read_b32 v149, a121\n"
  "v_mfma_f32_16x16x32_f16 a[172:175], v[24:27], v[100:103], a[172:175]\n"
  "s_add_i32 s64, s82, 0\n"
  "buffer_store_dwordx4 v[234:237], v250, s[72:75], s64 offen sc1\n"
  "s_not_b64 vcc, vcc\n"
  "v_cndmask_b32_dpp v238, v172, v176, vcc quad_perm:[1,0,3,2] row_mask:0xf bank_mask:0xf\n"
  "v_cndmask_b32_dpp v239, v173, v177, vcc quad_perm:[1,0,3,2] row_mask:0xf bank_mask:0xf\n"
  "v_cndmask_b32_dpp v240, v174, v178, vcc quad_perm:[1,0,3,2] row_mask:0xf bank_mask:0xf\n"
  "v_cndmask_b32_dpp v241, v175, v179, vcc quad_perm:[1,0,3,2] row_mask:0xf bank_mask:0xf\n"
  "v_mfma_f32_16x16x32_f16 a[184:187], v[28:31], v[100:103], a[184:187]\n"
  "s_add_i32 s65, s82, 65536\n"
  "buffer_store_dwordx4 v[238:241], v250, s[72:75], s65 offen sc1\n"
  "v_accvgpr_read_b32 v150, a122\n"
  "v_accvgpr_read_b32 v151, a123\n"
  "v_accvgpr_read_b32 v152, a124\n"
  "v_accvgpr_read_b32 v153, a125\n"
  "v_mfma_f32_16x16x32_f16 a[152:155], v[32:35], v[120:123], a[152:155]\n"
  "s_waitcnt lgkmcnt(2)\n"
  "v_accvgpr_read_b32 v154, a126\n"
  "v_accvgpr_read_b32 v155, a127\n"
  "v_accvgpr_read_b32 v156, a128\n"
  "v_accvgpr_read_b32 v157, a129\n"
  "v_accvgpr_read_b32 v158, a130\n"
  "v_mfma_f32_16x16x32_f16 a[164:167], v[36:39], v[120:123], a[164:167]\n"
  "v_accvgpr_read_b32 v159, a131\n"
  "v_fmac_f32 v148, s76, v156\n"
  "v_fmac_f32 v152, s76, v156\n"
  "v_fmac_f32 v149, s76, v157\n"
  "v_fmac_f32 v153, s76, v157\n"
  "v_mfma_f32_16x16x32_f16 a[176:179], v[40:43], v[120:123], a[176:179]\n"
  "v_fmac_f32 v150, s76, v158\n"
  "v_fmac_f32 v154, s76, v158\n"
  "v_fmac_f32 v151, s76, v159\n"
  "v_fmac_f32 v155, s76, v159\n"
  "v_cvt_pk_bf16_f32 v172, v148, v149\n"
  "v_mfma_f32_16x16x32_f16 a[188:191], v[44:47], v[120:123], a[188:191]\n"
  "v_cvt_pk_bf16_f32 v173, v150, v151\n"
  "v_cvt_pk_bf16_f32 v176, v152, v153\n"
  "v_cvt_pk_bf16_f32 v177, v154, v155\n"
  "v_accvgpr_read_b32 v160, a132\n"
  "v_accvgpr_read_b32 v161, a133\n"
  "v_mfma_f32_16x16x32_f16 a[144:147], v[48:51], v[104:107], a[144:147]\n"
  "ds_read_b128 v[96:99], v242 offset:8192\n"
  "v_pk_add_f16 v120, v104, v108\n"
  "v_pk_add_f16 v121, v105, v109\n"
  "v_pk_add_f16 v122, v106, v110\n"
  "v_pk_add_f16 v123, v107, v111\n"
  "v_mfma_f32_16x16x32_f16 a[156:159], v[52:55], v[104:107], a[156:159]\n"
  "ds_read_b128 v[100:103], v242 offset:8704\n"
  "v_accvgpr_read_b32 v162, a134\n"
  "v_accvgpr_read_b32 v163, a135\n"
  "v_accvgpr_read_b32 v164, a136\n"
  "v_accvgpr_read_b32 v165, a137\n"
  "v_mfma_f32_16x16x32_f16 a[148:151], v[64:67], v[108:111], a[148:151]\n"
  "v_accvgpr_read_b32 v166, a138\n"
  "v_accvgpr_read_b32 v167, a139\n"
  "v_accvgpr_read_b32 v168, a140\n"
  "v_accvgpr_read_b32 v169, a141\n"
  "v_accvgpr_read_b32 v170, a142\n"
  "v_mfma_f32_16x16x32_f16 a[160:163], v[68:71], v[108:111], a[160:163]\n"
  "v_accvgpr_read_b32 v171, a143\n"
  "v_fmac_f32 v160, s76, v168\n"
  "v_fmac_f32 v164, s76, v168\n"
  "v_fmac_f32 v161, s76, v169\n"
  "v_fmac_f32 v165, s76, v169\n"
  "v_mfma_f32_16x16x32_f16 a[152:155], v[80:83], v[120:123], a[152:155]\n"
  "v_fmac_f32 v162, s76, v170\n"
  "v_fmac_f32 v166, s76, v170\n"
  "v_fmac_f32 v163, s76, v171\n"
  "v_fmac_f32 v167, s76, v171\n"
  "v_cvt_pk_bf16_f32 v174, v160, v161\n"
  "v_mfma_f32_16x16x32_f16 a[164:167], v[84:87], v[120:123], a[164:167]\n"
  "v_cvt_pk_bf16_f32 v175, v162, v163\n"
  "v_cvt_pk_bf16_f32 v178, v164, v165\n"
  "v_cvt_pk_bf16_f32 v179, v166, v167\n"
  "v_accvgpr_read_b32 v180, a144\n"
  "v_accvgpr_read_b32 v181, a145\n"
  "v_mfma_f32_16x16x32_f16 a[168:171], v[56:59], v[104:107], a[168:171]\n"
  "s_mov_b64 vcc, s[66:67]\n"
  "v_cndmask_b32_dpp v234, v176, v172, vcc quad_perm:[1,0,3,2] row_mask:0xf bank_mask:0xf\n"
  "v_cndmask_b32_dpp v235, v177, v173, vcc quad_perm:[1,0,3,2] row_mask:0xf bank_mask:0xf\n"
  "v_cndmask_b32_dpp v236, v178, v174, vcc quad_perm:[1,0,3,2] row_mask:0xf bank_mask:0xf\n"
  "v_cndmask_b32_dpp v237, v179, v175, vcc quad_perm:[1,0,3,2] row_mask:0xf bank_mask:0xf\n"
  "v_accvgpr_read_b32 v182, a146\n"
  "v_mfma_f32_16x16x32_f16 a[180:183], v[60:63], v[104:107], a[180:183]\n"
  "s_add_i32 s64, s82, 4096\n"
  "buffer_store_dwordx4 v[234:237], v250, s[72:75], s64 offen sc1\n"
  "s_not_b64 vcc, vcc\n"
  "v_cndmask_b32_dpp v238, v172, v176, vcc quad_perm:[1,0,3,2] row_mask:0xf bank_mask:0xf\n"
  "v_cndmask_b32_dpp v239, v173, v177, vcc quad_perm:[1,0,3,2] row_mask:0xf bank_mask:0xf\n"
  "v_cndmask_b32_dpp v240, v174, v178, vcc quad_perm:[1,0,3,2] row_mask:0xf bank_mask:0xf\n"
  "v_cndmask_b32_dpp v241, v175, v179, vcc quad_perm:[1,0,3,2] row_mask:0xf bank_mask:0xf\n"
  "v_mfma_f32_16x16x32_f16 a[172:175], v[72:75], v[108:111], a[172:175]\n"
  "s_waitcnt lgkmcnt(2)\n"
  "s_add_i32 s65, s82, 69632\n"
  "buffer_store_dwordx4 v[238:241], v250, s[72:75], s65 offen sc1\n"
  "v_accvgpr_read_b32 v183, a147\n"
  "v_accvgpr_read_b32 v184, a148\n"
  "v_accvgpr_read_b32 v185, a149\n"
  "v_accvgpr_read_b32 v186, a150\n"
  "v_mfma_f32_16x16x32_f16 a[184:187], v[76:79], v[108:111], a[184:187]\n"
  "v_accvgpr_read_b32 v187, a151\n"
  "v_accvgpr_read_b32 v188, a152\n"
  "v_accvgpr_read_b32 v189, a153\n"
  "v_accvgpr_read_b32 v190, a154\n"
  "v_accvgpr_read_b32 v191, a155\n"
  "v_mfma_f32_16x16x32_f16 a[176:179], v[88:91], v[120:123], a[176:179]\n"
  "v_fmac_f32 v180, s76, v188\n"
  "v_fmac_f32 v184, s76, v188\n"
  "v_fmac_f32 v181, s76, v189\n"
  "v_fmac_f32 v185, s76, v189\n"
  "v_fmac_f32 v182, s76, v190\n"
  "v_mfma_f32_16x16x32_f16 a[188:191], v[92:95], v[120:123], a[188:191]\n"
  "v_fmac_f32 v186, s76, v190\n"
  "v_fmac_f32 v183, s76, v191\n"
  "v_fmac_f32 v187, s76, v191\n"
  "v_cvt_pk_bf16_f32 v172, v180, v181\n"
  "v_cvt_pk_bf16_f32 v173, v182, v183\n"
  "v_mfma_f32_16x16x32_f16 a[192:195], v[0:3], v[112:115], a[192:195]\n"
  "v_pk_add_f16 v120, v112, v116\n"
  "v_pk_add_f16 v121, v113, v117\n"
  "v_pk_add_f16 v122, v114, v118\n"
  "v_pk_add_f16 v123, v115, v119\n"
  "v_cvt_pk_bf16_f32 v176, v184, v185\n"
  "v_mfma_f32_16x16x32_f16 a[204:207], v[4:7], v[112:115], a[204:207]\n"
  "v_cvt_pk_bf16_f32 v177, v186, v187\n"
  "v_accvgpr_read_b32 v192, a156\n"
  "v_accvgpr_read_b32 v193, a157\n"
  "v_accvgpr_read_b32 v194, a158\n"
  "v_accvgpr_read_b32 v195, a159\n"
  "v_mfma_f32_16x16x32_f16 a[216:219], v[8:11], v[112:115], a[216:219]\n"
  "v_accvgpr_read_b32 v196, a160\n"
  "v_accvgpr_read_b32 v197, a161\n"
  "v_accvgpr_read_b32 v198, a162\n"
  "v_accvgpr_read_b32 v199, a163\n"
  "v_accvgpr_read_b32 v200, a164\n"
  "v_mfma_f32_16x16x32_f16 a[228:231], v[12:15], v[112:115], a[228:231]\n"
  "v_accvgpr_read_b32 v201, a165\n"
  "v_accvgpr_read_b32 v202, a166\n"
  "v_accvgpr_read_b32 v203, a167\n"
  "v_fmac_f32 v192, s76, v200\n"
  "v_fmac_f32 v196, s76, v200\n"
  "v_mfma_f32_16x16x32_f16 a[196:199], v[16:19], v[116:119], a[196:199]\n"
  "v_fmac_f32 v193, s76, v201\n"
  "v_fmac_f32 v197, s76, v201\n"
  "v_fmac_f32 v194, s76, v202\n"
  "v_fmac_f32 v198, s76, v202\n"
  "v_fmac_f32 v195, s76, v203\n"
  "v_mfma_f32_16x16x32_f16 a[208:211], v[20:23], v[116:119], a[208:211]\n"
  "v_fmac_f32 v199, s76, v203\n"
  "v_cvt_pk_bf16_f32 v174, v192, v193\n"
  "v_cvt_pk_bf16_f32 v175, v194, v195\n"
  "v_cvt_pk_bf16_f32 v178, v196, v197\n"
  "v_cvt_pk_bf16_f32 v179, v198, v199\n"
  "v_mfma_f32_16x16x32_f16 a[220:223], v[24:27], v[116:119], a[220:223]\n"
  "s_mov_b64 vcc, s[66:67]\n"
  "v_cndmask_b32_dpp v234, v176, v172, vcc quad_perm:[1,0,3,2] row_mask:0xf bank_mask:0xf\n"
  "v_cndmask_b32_dpp v235, v177, v173, vcc quad_perm:[1,0,3,2] row_mask:0xf bank_mask:0xf\n"
  "v_cndmask_b32_dpp v236, v178, v174, vcc quad_perm:[1,0,3,2] row_mask:0xf bank_mask:0xf\n"
  "v_cndmask_b32_dpp v237, v179, v175, vcc quad_perm:[1,0,3,2] row_mask:0xf bank_mask:0xf\n"
  "v_accvgpr_read_b32 v204, a168\n"
  "v_mfma_f32_16x16x32_f16 a[232:235], v[28:31], v[116:119], a[232:235]\n"
  "s_add_i32 s64, s83, 0\n"
  "buffer_store_dwordx4 v[234:237], v250, s[72:75], s64 offen sc1\n"
  "s_not_b64 vcc, vcc\n"
  "v_cndmask_b32_dpp v238, v172, v176, vcc quad_perm:[1,0,3,2] row_mask:0xf bank_mask:0xf\n"
  "v_cndmask_b32_dpp v239, v173, v177, vcc quad_perm:[1,0,3,2] row_mask:0xf bank_mask:0xf\n"
  "v_cndmask_b32_dpp v240, v174, v178, vcc quad_perm:[1,0,3,2] row_mask:0xf bank_mask:0xf\n"
  "v_cndmask_b32_dpp v241, v175, v179, vcc quad_perm:[1,0,3,2] row_mask:0xf bank_mask:0xf\n"
  "v_mfma_f32_16x16x32_f16 a[200:203], v[32:35], v[120:123], a[200:203]\n"
  "s_waitcnt lgkmcnt(0)\n"
  "s_add_i32 s65, s83, 65536\n"
  "buffer_store_dwordx4 v[238:241], v250, s[72:75], s65 offen sc1\n"
  "v_accvgpr_read_b32 v205, a169\n"
  "v_accvgpr_read_b32 v206, a170\n"
  "v_accvgpr_read_b32 v207, a171\n"
  "v_accvgpr_read_b32 v208, a172\n"
  "v_mfma_f32_16x16x32_f16 a[212:215], v[36:39], v[120:123], a[212:215]\n"
  "v_accvgpr_read_b32 v209, a173\n"
  "v_accvgpr_read_b32 v210, a174\n"
  "v_accvgpr_read_b32 v211, a175\n"
  "v_accvgpr_read_b32 v212, a176\n"
  "v_accvgpr_read_b32 v213, a177\n"
  "v_mfma_f32_16x16x32_f16 a[224:227], v[40:43], v[120:123], a[224:227]\n"
  "v_accvgpr_read_b32 v214, a178\n"
  "v_accvgpr_read_b32 v215, a179\n"
  "v_fmac_f32 v204, s76, v212\n"
  "v_fmac_f32 v208, s76, v212\n"
  "v_fmac_f32 v205, s76, v213\n"
  "v_mfma_f32_16x16x32_f16 a[236:239], v[44:47], v[120:123], a[236:239]\n"
  "v_fmac_f32 v209, s76, v213\n"
  "v_fmac_f32 v206, s76, v214\n"
  "v_fmac_f32 v210, s76, v214\n"
  "v_fmac_f32 v207, s76, v215\n"
  "v_fmac_f32 v211, s76, v215\n"
  "v_mfma_f32_16x16x32_f16 a[192:195], v[48:51], v[96:99], a[192:195]\n"
  "v_pk_add_f16 v120, v96, v100\n"
  "v_pk_add_f16 v121, v97, v101\n"
  "v_pk_add_f16 v122, v98, v102\n"
  "v_pk_add_f16 v123, v99, v103\n"
  "v_cvt_pk_bf16_f32 v172, v204, v205\n"
  "v_mfma_f32_16x16x32_f16 a[204:207], v[52:55], v[96:99], a[204:207]\n"
  "v_cvt_pk_bf16_f32 v173, v206, v207\n"
  "v_cvt_pk_bf16_f32 v176, v208, v209\n"
  "v_cvt_pk_bf16_f32 v177, v210, v211\n"
  "v_accvgpr_read_b32 v216, a180\n"
  "v_accvgpr_read_b32 v217, a181\n"
  "v_mfma_f32_16x16x32_f16 a[196:199], v[64:67], v[100:103], a[196:199]\n"
  "v_accvgpr_read_b32 v218, a182\n"
  "v_accvgpr_read_b32 v219, a183\n"
  "v_accvgpr_read_b32 v220, a184\n"
  "v_accvgpr_read_b32 v221, a185\n"
  "v_accvgpr_read_b32 v222, a186\n"
  "v_mfma_f32_16x16x32_f16 a[208:211], v[68:71], v[100:103], a[208:211]\n"
  "v_accvgpr_read_b32 v223, a187\n"
  "v_accvgpr_read_b32 v224, a188\n"
  "v_accvgpr_read_b32 v225, a189\n"
  "v_accvgpr_read_b32 v226, a190\n"
  "v_accvgpr_read_b32 v227, a191\n"
  "v_mfma_f32_16x16x32_f16 a[200:203], v[80:83], v[120:123], a[200:203]\n"
  "v_fmac_f32 v216, s76, v224\n"
  "v_fmac_f32 v220, s76, v224\n"
  "v_fmac_f32 v217, s76, v225\n"
  "v_fmac_f32 v221, s76, v225\n"
  "v_fmac_f32 v218, s76, v226\n"
  "v_mfma_f32_16x16x32_f16 a[212:215], v[84:87], v[120:123], a[212:215]\n"
  "v_fmac_f32 v222, s76, v226\n"
  "v_fmac_f32 v219, s76, v227\n"
  "v_fmac_f32 v223, s76, v227\n"
  "v_cvt_pk_bf16_f32 v174, v216, v217\n"
  "v_cvt_pk_bf16_f32 v175, v218, v219\n"
  "v_mfma_f32_16x16x32_f16 a[216:219], v[56:59], v[96:99], a[216:219]\n"
  "v_cvt_pk_bf16_f32 v178, v220, v221\n"
  "v_cvt_pk_bf16_f32 v179, v222, v223\n"
  "v_accvgpr_read_b32 v124, a192\n"
  "v_accvgpr_read_b32 v125, a193\n"
  "v_accvgpr_read_b32 v126, a194\n"
  "v_mfma_f32_16x16x32_f16 a[228:231], v[60:63], v[96:99], a[228:231]\n"
  "s_mov_b64 vcc, s[66:67]\n"
  "v_cndmask_b32_dpp v234, v176, v172, vcc quad_perm:[1,0,3,2] row_mask:0xf bank_mask:0xf\n"
  "v_cndmask_b32_dpp v235, v177, v173, vcc quad_perm:[1,0,3,2] row_mask:0xf bank_mask:0xf\n"
  "v_cndmask_b32_dpp v236, v178, v174, vcc quad_perm:[1,0,3,2] row_mask:0xf bank_mask:0xf\n"
  "v_cndmask_b32_dpp v237, v179, v175, vcc quad_perm:[1,0,3,2] row_mask:0xf bank_mask:0xf\n"
  "v_accvgpr_read_b32 v127, a195\n"
  "v_mfma_f32_16x16x32_f16 a[220:223], v[72:75], v[100:103], a[220:223]\n"
  "s_add_i32 s64, s83, 4096\n"
  "buffer_store_dwordx4 v[234:237], v250, s[72:75], s64 offen sc1\n"
  "s_not_b64 vcc, vcc\n"
  "v_cndmask_b32_dpp v238, v172, v176, vcc quad_perm:[1,0,3,2] row_mask:0xf bank_mask:0xf\n"
  "v_cndmask_b32_dpp v239, v173, v177, vcc quad_perm:[1,0,3,2] row_mask:0xf bank_mask:0xf\n"
  "v_cndmask_b32_dpp v240, v174, v178, vcc quad_perm:[1,0,3,2] row_mask:0xf bank_mask:0xf\n"
  "v_cndmask_b32_dpp v241, v175, v179, vcc quad_perm:[1,0,3,2] row_mask:0xf bank_mask:0xf\n"
  "v_mfma_f32_16x16x32_f16 a[232:235], v[76:79], v[100:103], a[232:235]\n"
  "s_add_i32 s65, s83, 69632\n"
  "buffer_store_dwordx4 v[238:241], v250, s[72:75], s65 offen sc1\n"
  "v_accvgpr_read_b32 v128, a196\n"
  "v_accvgpr_read_b32 v129, a197\n"
  "v_accvgpr_read_b32 v130, a198\n"
  "v_accvgpr_read_b32 v131, a199\n"
  "v_mfma_f32_16x16x32_f16 a[224:227], v[88:91], v[120:123], a[224:227]\n"
  "v_accvgpr_read_b32 v132, a200\n"
  "v_accvgpr_read_b32 v133, a201\n"
  "v_accvgpr_read_b32 v134, a202\n"
  "v_accvgpr_read_b32 v135, a203\n"
  "v_pk_fma_f32 v[124:125], v[132:133], s[76:77], v[124:125]\n"
  "v_mfma_f32_16x16x32_f16 a[236:239], v[92:95], v[120:123], a[236:239]\n"
  "v_pk_fma_f32 v[128:129], v[132:133], s[76:77], v[128:129]\n"
  "v_pk_fma_f32 v[126:127], v[134:135], s[76:77], v[126:127]\n"
  "v_pk_fma_f32 v[130:131], v[134:135], s[76:77], v[130:131]\n"
  "v_cvt_pk_bf16_f32 v172, v124, v125\n"
  "v_cvt_pk_bf16_f32 v173, v126, v127\n"
  "v_cvt_pk_bf16_f32 v176, v128, v129\n"
  "v_cvt_pk_bf16_f32 v177, v130, v131\n"
  "v_accvgpr_read_b32 v136, a204\n"
  "v_accvgpr_read_b32 v137, a205\n"
  "v_accvgpr_read_b32 v138, a206\n"
  "v_accvgpr_read_b32 v139, a207\n"
  "v_accvgpr_read_b32 v140, a208\n"
  "v_accvgpr_read_b32 v141, a209\n"
  "v_accvgpr_read_b32 v142, a210\n"
  "v_accvgpr_read_b32 v143, a211\n"
  "v_accvgpr_read_b32 v144, a212\n"
  "v_accvgpr_read_b32 v145, a213\n"
  "v_accvgpr_read_b32 v146, a214\n"
  "v_accvgpr_read_b32 v147, a215\n"
  "v_pk_fma_f32 v[136:137], v[144:145], s[76:77], v[136:137]\n"
  "v_pk_fma_f32 v[140:141], v[144:145], s[76:77], v[140:141]\n"
  "v_pk_fma_f32 v[138:139], v[146:147], s[76:77], v[138:139]\n"
  "v_pk_fma_f32 v[142:143], v[146:147], s[76:77], v[142:143]\n"
  "v_cvt_pk_bf16_f32 v174, v136, v137\n"
  "v_cvt_pk_bf16_f32 v175, v138, v139\n"
  "v_cvt_pk_bf16_f32 v178, v140, v141\n"
  "v_cvt_pk_bf16_f32 v179, v142, v143\n"
  "v_accvgpr_read_b32 v148, a216\n"
  "v_accvgpr_read_b32 v149, a217\n"
  "v_accvgpr_read_b32 v150, a218\n"
  "s_mov_b64 vcc, s[66:67]\n"
  "v_cndmask_b32_dpp v234, v176, v172, vcc quad_perm:[1,0,3,2] row_mask:0xf bank_mask:0xf\n"
  "v_cndmask_b32_dpp v235, v177, v173, vcc quad_perm:[1,0,3,2] row_mask:0xf bank_mask:0xf\n"
  "v_cndmask_b32_dpp v236, v178, v174, vcc quad_perm:[1,0,3,2] row_mask:0xf bank_mask:0xf\n"
  "v_cndmask_b32_dpp v237, v179, v175, vcc quad_perm:[1,0,3,2] row_mask:0xf bank_mask:0xf\n"
  "v_accvgpr_read_b32 v151, a219\n"
  "s_add_i32 s64, s84, 0\n"
  "buffer_store_dwordx4 v[234:237], v250, s[72:75], s64 offen sc1\n"
  "s_not_b64 vcc, vcc\n"
  "v_cndmask_b32_dpp v238, v172, v176, vcc quad_perm:[1,0,3,2] row_mask:0xf bank_mask:0xf\n"
  "v_cndmask_b32_dpp v239, v173, v177, vcc quad_perm:[1,0,3,2] row_mask:0xf bank_mask:0xf\n"
  "v_cndmask_b32_dpp v240, v174, v178, vcc quad_perm:[1,0,3,2] row_mask:0xf bank_mask:0xf\n"
  "v_cndmask_b32_dpp v241, v175, v179, vcc quad_perm:[1,0,3,2] row_mask:0xf bank_mask:0xf\n"
  "s_add_i32 s65, s84, 65536\n"
  "buffer_store_dwordx4 v[238:241], v250, s[72:75], s65 offen sc1\n"
  "v_accvgpr_read_b32 v152, a220\n"
  "v_accvgpr_read_b32 v153, a221\n"
  "v_accvgpr_read_b32 v154, a222\n"
  "v_accvgpr_read_b32 v155, a223\n"
  "v_accvgpr_read_b32 v156, a224\n"
  "v_accvgpr_read_b32 v157, a225\n"
  "v_accvgpr_read_b32 v158, a226\n"
  "v_accvgpr_read_b32 v159, a227\n"
  "v_pk_fma_f32 v[148:149], v[156:157], s[76:77], v[148:149]\n"
  "v_pk_fma_f32 v[152:153], v[156:157], s[76:77], v[152:153]\n"
  "v_pk_fma_f32 v[150:151], v[158:159], s[76:77], v[150:151]\n"
  "v_pk_fma_f32 v[154:155], v[158:159], s[76:77], v[154:155]\n"
  "v_cvt_pk_bf16_f32 v172, v148, v149\n"
  "v_cvt_pk_bf16_f32 v173, v150, v151\n"
  "v_cvt_pk_bf16_f32 v176, v152, v153\n"
  "v_cvt_pk_bf16_f32 v177, v154, v155\n"
  "v_accvgpr_read_b32 v160, a228\n"
  "v_accvgpr_read_b32 v161, a229\n"
  "v_accvgpr_read_b32 v162, a230\n"
  "v_accvgpr_read_b32 v163, a231\n"
  "v_accvgpr_read_b32 v164, a232\n"
  "v_accvgpr_read_b32 v165, a233\n"
  "v_accvgpr_read_b32 v166, a234\n"
  "v_accvgpr_read_b32 v167, a235\n"
  "v_accvgpr_read_b32 v168, a236\n"
  "v_accvgpr_read_b32 v169, a237\n"
  "v_accvgpr_read_b32 v170, a238\n"
  "v_accvgpr_read_b32 v171, a239\n"
  "v_pk_fma_f32 v[160:161], v[168:169], s[76:77], v[160:161]\n"
  "v_pk_fma_f32 v[164:165], v[168:169], s[76:77], v[164:165]\n"
  "v_pk_fma_f32 v[162:163], v[170:171], s[76:77], v[162:163]\n"
  "v_pk_fma_f32 v[166:167], v[170:171], s[76:77], v[166:167]\n"
  "v_cvt_pk_bf16_f32 v174, v160, v161\n"
  "v_cvt_pk_bf16_f32 v175, v162, v163\n"
  "v_cvt_pk_bf16_f32 v178, v164, v165\n"
  "v_cvt_pk_bf16_f32 v179, v166, v167\n"
  "s_mov_b64 vcc, s[66:67]\n"
  "v_cndmask_b32_dpp v234, v176, v172, vcc quad_perm:[1,0,3,2] row_mask:0xf bank_mask:0xf\n"
  "v_cndmask_b32_dpp v235, v177, v173, vcc quad_perm:[1,0,3,2] row_mask:0xf bank_mask:0xf\n"
  "v_cndmask_b32_dpp v236, v178, v174, vcc quad_perm:[1,0,3,2] row_mask:0xf bank_mask:0xf\n"
  "v_cndmask_b32_dpp v237, v179, v175, vcc quad_perm:[1,0,3,2] row_mask:0xf bank_mask:0xf\n"
  "s_add_i32 s64, s84, 4096\n"
  "buffer_store_dwordx4 v[234:237], v250, s[72:75], s64 offen sc1\n"
  "s_not_b64 vcc, vcc\n"
  "v_cndmask_b32_dpp v238, v172, v176, vcc quad_perm:[1,0,3,2] row_mask:0xf bank_mask:0xf\n"
  "v_cndmask_b32_dpp v239, v173, v177, vcc quad_perm:[1,0,3,2] row_mask:0xf bank_mask:0xf\n"
  "v_cndmask_b32_dpp v240, v174, v178, vcc quad_perm:[1,0,3,2] row_mask:0xf bank_mask:0xf\n"
  "v_cndmask_b32_dpp v241, v175, v179, vcc quad_perm:[1,0,3,2] row_mask:0xf bank_mask:0xf\n"
  "s_add_i32 s65, s84, 69632\n"
  "buffer_store_dwordx4 v[238:241], v250, s[72:75], s65 offen sc1\n"
  "s_waitcnt vmcnt(20)\n"
  "s_branch .Lend_%=\n"
  ".Lpace_s0_None_%=:\n"
  "s_memrealtime s[70:71]\n"
  "s_waitcnt lgkmcnt(0)\n"
  "s_sub_i32 s64, s70, s61\n"
  "s_cmp_lt_i32 s64, 0\n"
  "s_cbranch_scc0 .Lpace2_s0_None_%=\n"
  "s_sleep 1\n"
  "s_branch .Lpace_s0_None_%=\n"
  ".Lpace2_s0_None_%=:\n"
  "s_add_i32 s61, s70, s62\n"
  "s_add_i32 s61, s61, 8\n"
  "s_sub_i32 s62, s62, 14\n"
  "s_branch .Lpaced_s0_None_%=\n"
  ".Lpace_s1_None_%=:\n"
  "s_memrealtime s[70:71]\n"
  "s_waitcnt lgkmcnt(0)\n"
  "s_sub_i32 s64, s70, s61\n"
  "s_cmp_lt_i32 s64, 0\n"
  "s_cbranch_scc0 .Lpace2_s1_None_%=\n"
  "s_sleep 1\n"
  "s_branch .Lpace_s1_None_%=\n"
  ".Lpace2_s1_None_%=:\n"
  "s_add_i32 s61, s70, s62\n"
  "s_add_i32 s61, s61, 0\n"
  "s_sub_i32 s62, s62, 14\n"
  "s_branch .Lpaced_s1_None_%=\n"
  ".Lspin_pe_None_%=:\n"
  "s_mov_b32 s64, 0\n"
  ".Lspin2_pe_None_%=:\n"
  "s_sleep 3\n"
  "buffer_load_dword v247, v246, s[40:43], s54 offen sc1\n"
  "s_add_u32 s64, s64, 1\n"
  "s_waitcnt vmcnt(0)\n"
  "s_cmp_gt_u32 s64, 0x200000\n"
  "s_cbranch_scc1 .Lspun_pe_None_%=\n"
  "v_cmp_eq_u32 vcc, 0, v247\n"
  "s_cbranch_vccnz .Lspin2_pe_None_%=\n"
  "s_branch .Lspun_pe_None_%=\n"
  ".Lpace_pe_None_%=:\n"
  "s_memrealtime s[70:71]\n"
  "s_waitcnt lgkmcnt(0)\n"
  "s_sub_i32 s64, s70, s61\n"
  "s_cmp_lt_i32 s64, 0\n"
  "s_cbranch_scc0 .Lpace2_pe_None_%=\n"
  "s_sleep 1\n"
  "s_branch .Lpace_pe_None_%=\n"
  ".Lpace2_pe_None_%=:\n"
  "s_add_i32 s61, s70, s62\n"
  "s_add_i32 s61, s61, 8\n"
  "s_sub_i32 s62, s62, 14\n"
  "s_branch .Lpaced_pe_None_%=\n"
  ".Lpace_po_None_%=:\n"
  "s_memrealtime s[70:71]\n"
  "s_waitcnt lgkmcnt(0)\n"
  "s_sub_i32 s64, s70, s61\n"
  "s_cmp_lt_i32 s64, 0\n"
  "s_cbranch_scc0 .Lpace2_po_None_%=\n"
  "s_sleep 1\n"
  "s_branch .Lpace_po_None_%=\n"
  ".Lpace2_po_None_%=:\n"
  "s_add_i32 s61, s70, s62\n"
  "s_add_i32 s61, s61, 0\n"
  "s_sub_i32 s62, s62, 14\n"
  "s_branch .Lpaced_po_None_%=\n"
  ".Lspin_s22_None_%=:\n"
  "s_mov_b32 s64, 0\n"
  ".Lspin2_s22_None_%=:\n"
  "s_sleep 3\n"
  "buffer_load_dword v247, v246, s[40:43], s54 offen sc1\n"
  "s_add_u32 s64, s64, 1\n"
  "s_waitcnt vmcnt(0)\n"
  "s_cmp_gt_u32 s64, 0x200000\n"
  "s_cbranch_scc1 .Lspun_s22_None_%=\n"
  "v_cmp_eq_u32 vcc, 0, v247\n"
  "s_cbranch_vccnz .Lspin2_s22_None_%=\n"
  "s_branch .Lspun_s22_None_%=\n"
  ".Lspin_ne_None_%=:\n"
  "s_mov_b32 s64, 0\n"
  ".Lspin2_ne_None_%=:\n"
  "s_sleep 3\n"
  "buffer_load_dword v247, v246, s[40:43], s54 offen sc1\n"
  "s_add_u32 s64, s64, 1\n"
  "s_waitcnt vmcnt(0)\n"
  "s_cmp_gt_u32 s64, 0x200000\n"
  "s_cbranch_scc1 .Lspun_ne_None_%=\n"
  "v_cmp_eq_u32 vcc, 0, v247\n"
  "s_cbranch_vccnz .Lspin2_ne_None_%=\n"
  "s_branch .Lspun_ne_None_%=\n"
  ".Lspin_s28_None_%=:\n"
  "s_mov_b32 s64, 0\n"
  ".Lspin2_s28_None_%=:\n"
  "s_sleep 3\n"
  "buffer_load_dword v247, v246, s[40:43], s54 offen sc1\n"
  "s_add_u32 s64, s64, 1\n"
  "s_waitcnt vmcnt(0)\n"
  "s_cmp_gt_u32 s64, 0x200000\n"
  "s_cbranch_scc1 .Lspun_s28_None_%=\n"
  "v_cmp_eq_u32 vcc, 0, v247\n"
  "s_cbranch_vccnz .Lspin2_s28_None_%=\n"
  "s_branch .Lspun_s28_None_%=\n"
  ".Lend_%=:\n"
    : "+{s[40:55]}"(sA), "+{s[56:71]}"(sB), "+{s[72:87]}"(sC), "+{s[88:89]}"(sD),
      "={v[228:243]}"(o0), "={v[244:251]}"(o1), "={v[252:253]}"(o2),
      "={v[180:211]}"(o3), "={v[212:227]}"(o4), "={v[176:179]}"(o5),
      "={a[144:175]}"(o6), "={a[176:183]}"(o7), "={a[192:207]}"(o8), "={a[208:215]}"(o9)
    : "{v[228:243]}"(vA), "{v[244:251]}"(vB), "{v[252:253]}"(vC),
      "{v[180:211]}"(h0), "{v[212:227]}"(h1), "{v[176:179]}"(hd.vb),
      "{a[144:175]}"(raw0), "{a[176:183]}"(raw1), "{a[192:207]}"(st0), "{a[208:215]}"(st1)
    : "v0", "v1", "v2", "v3", "v4", "v5", "v6", "v7", "v8", "v9", "v10", "v11", "v12", "v13", "v14", "v15", "v16", "v17", "v18", "v19", "v20", "v21", "v22", "v23", "v24", "v25", "v26", "v27", "v28", "v29", "v30", "v31", "v32", "v33", "v34", "v35", "v36", "v37", "v38", "v39", "v40", "v41", "v42", "v43", "v44", "v45", "v46", "v47", "v48", "v49", "v50", "v51", "v52", "v53", "v54", "v55", "v56", "v57", "v58", "v59", "v60", "v61", "v62", "v63", "v64", "v65", "v66", "v67", "v68", "v69", "v70", "v71", "v72", "v73", "v74", "v75", "v76", "v77", "v78", "v79", "v80", "v81", "v82", "v83", "v84", "v85", "v86", "v87", "v88", "v89", "v90", "v91", "v92", "v93", "v94", "v95", "v96", "v97", "v98", "v99", "v100", "v101", "v102", "v103", "v104", "v105", "v106", "v107", "v108", "v109", "v110", "v111", "v112", "v113", "v114", "v115", "v116", "v117", "v118", "v119", "v120", "v121", "v122", "v123", "v124", "v125", "v126", "v127", "v128", "v129", "v130", "v131", "v132", "v133", "v134", "v135", "v136", "v137", "v138", "v139", "v140", "v141", "v142", "v143", "v144", "v145", "v146", "v147", "v148", "v149", "v150", "v151", "v152", "v153", "v154", "v155", "v156", "v157", "v158", "v159", "v160", "v161", "v162", "v163", "v164", "v165", "v166", "v167", "v168", "v169", "v170", "v171", "v172", "v173", "v174", "v175", "a0", "a1", "a2", "a3", "a4", "a5", "a6", "a7", "a8", "a9", "a10", "a11", "a12", "a13", "a14", "a15", "a16", "a17", "a18", "a19", "a20", "a21", "a22", "a23", "a24", "a25", "a26", "a27", "a28", "a29", "a30", "a31", "a32", "a33", "a34", "a35", "a36", "a37", "a38", "a39", "a40", "a41", "a42", "a43", "a44", "a45", "a46", "a47", "a48", "a49", "a50", "a51", "a52", "a53", "a54", "a55", "a56", "a57", "a58", "a59", "a60", "a61", "a62", "a63", "a64", "a65", "a66", "a67", "a68", "a69", "a70", "a71", "a72", "a73", "a74", "a75", "a76", "a77", "a78", "a79", "a80", "a81", "a82", "a83", "a84", "a85", "a86", "a87", "a88", "a89", "a90", "a91", "a92", "a93", "a94", "a95", "a96", "a97", "a98", "a99", "a100", "a101", "a102", "a103", "a104", "a105", "a106", "a107", "a108", "a109", "a110", "a111", "a112", "a113", "a114", "a115", "a116", "a117", "a118", "a119", "a120", "a121", "a122", "a123", "a124", "a125", "a126", "a127", "a128", "a129", "a130", "a131", "a132", "a133", "a134", "a135", "a136", "a137", "a138", "a139", "a140", "a141", "a142", "a143", "a184", "a185", "a186", "a187", "a188", "a189", "a190", "a191", "a216", "a217", "a218", "a219", "a220", "a221", "a222", "a223", "a224", "a225", "a226", "a227", "a228", "a229", "a230", "a231", "a232", "a233", "a234", "a235", "a236", "a237", "a238", "a239", "vcc", "scc", "m0", "memory");
}

__device__ __forceinline__ void imfma0(f32x4& d, const bf16x8& a, const bf16x8& b, const f32x4& c) {
  asm volatile("v_mfma_f32_16x16x32_bf16 %0, %1, %2, %3" : "=&v"(d) : "v"(a), "a"(b), "v"(c));
}
__device__ __forceinline__ void imfma1(f32x4& d, const bf16x8& a, const bf16x8& b) {
  asm volatile("v_mfma_f32_16x16x32_bf16 %0, %1, %2, %0" : "+v"(d) : "v"(a), "a"(b));
}
#define DPPADD(d, s, CTRL, BANKS) asm volatile("v_add_f32_dpp %0, %1, %1 " CTRL " row_mask:0xf bank_mask:" BANKS : "+v"(d) : "v"(s))
template <int CTRL>
__device__ __forceinline__ float dpp_add(float v) {
  asm("" : "+v"(v));
  return v + __builtin_bit_cast(float, __builtin_amdgcn_update_dpp(0, __builtin_bit_cast(int, v), CTRL, 0xf, 0xf, true));
}

extern "C" __global__ __launch_bounds__(256, 1) void conv_fft(char* ws, const unsigned short* __restrict__ x,
                                                   const unsigned short* __restrict__ w,
                                                   const unsigned short* __restrict__ gamma,
                                                   const unsigned short* __restrict__ beta,
                                                   unsigned short* __restrict__ out) {
  __shared__ __attribute__((aligned(16))) char smem[160 * 1024];
  const unsigned lds0 = (unsigned)(uintptr_t)(lds_void*)smem;
  const int wv = __builtin_amdgcn_readfirstlane(threadIdx.x >> 6);
  Ctx cx;

  {
    unsigned len;
    asm volatile("s_getpc_b64 %0\n.Lcf_a:\n\ts_mov_b32 %1, .Lcf_b-.Lcf_a" : "=s"(cx.pc), "=s"(len));
    cx.wv = wv;
    cx.pfo = len;
    unsigned xcc;
    asm volatile("s_getreg_b32 %0, hwreg(HW_REG_XCC_ID, 0, 3)" : "=s"(xcc));
    unsigned id = blockIdx.x;
    asm volatile("" : "+s"(id));
    cx.id = id;
    cx.vb = (int)((id & 248u) | (id & 256u ? (id >> 10) & 7u : xcc));
    const char* pb = ws + WS_PUB;
    u32x4 sx, sw;
    if ((id | wv) == 0) {
      const unsigned l = threadIdx.x;
      const u32x4 z = {0u, 0u, 0u, 0u};
      {
#pragma unroll
        for (int k = 0; k < 2; ++k)
          asm volatile("global_store_dwordx4 %0, %1, %2 sc1" :: "v"(32768 + (l & 7) * 16 + (l & 8) * 512 + (l & 16) * 1024 + (l & 32) * 32 + k * 8192), "v"(z), "s"(pb) : "memory");
      }
      {
        const unsigned so = l * (31u * 1024u + 16u) + (4u << 20);
        asm volatile("global_store_dwordx4 %2, %3, %4 sc1\n\tglobal_load_dwordx4 %0, %5, %6\n\tglobal_load_dwordx4 %1, %7, %8"
                     : "=&v"(sx), "=&v"(sw) : "v"(l * 16 + 1024), "v"(z), "s"(pb), "v"(so + (l & 3u) * (8u << 20)), "s"(x), "v"(so + (l & 1u) * (6u << 20)), "s"(w) : "memory");
      }
    }
    if (id) code_touch(cx);
    SB0;
    issue_x(0, wv, x_addr(cx.vb, 0, wv), x_rsrc(x, cx.vb), lds0);
    SB0;
    if ((id | wv) == 0) {
      wait_vm<17>();
      asm volatile("" : "+v"(sx), "+v"(sw));
      unsigned m = expo(amax(amax(amax(amax(0, sx[0]), sx[1]), sx[2]), sx[3])) | expo(amax(amax(amax(amax(0, sw[0]), sw[1]), sw[2]), sw[3])) << 16;
      m = amax(m, (unsigned)__builtin_amdgcn_update_dpp(0, (int)m, 0x128, 0xf, 0xf, false));
      m = amax(m, (unsigned)__builtin_amdgcn_update_dpp(0, (int)m, 0x124, 0xf, 0xf, false));
      m = amax(m, (unsigned)__builtin_amdgcn_update_dpp(0, (int)m, 0x122, 0xf, 0xf, false));
      m = amax(m, (unsigned)__builtin_amdgcn_update_dpp(0, (int)m, 0x121, 0xf, 0xf, false));
      unsigned ex = 28, ew = 28;
#pragma unroll
      for (int l = 0; l < 64; l += 16) {
        const unsigned v = (unsigned)__builtin_amdgcn_readlane((int)m, l);
        ex = max(ex + 2, v & 255u) - 2;
        ew = max(ew + 3, v >> 16) - 3;
      }
      const unsigned long long tg = __builtin_amdgcn_s_memrealtime();
      const unsigned lo = (unsigned)tg << 4 | xcc << 1, hi = ((unsigned)(tg >> 28) & 0xffffu) | min(ex, 220u) << 16 | min(ew, 220u) << 24;
      asm volatile("global_store_dwordx4 %0, %1, %2 sc1\n\ts_nop 1" :: "v"(threadIdx.x * 16), "v"(u32x4{lo, hi, ~lo, ~hi}), "s"(pb) : "memory");
      cx.tag[0] = lo;
      cx.tag[1] = hi;
    }
  }
  cx.smem = smem;
  cx.ws = ws;
  cx.gamma = gamma;
  cx.beta = beta;
  run12(cx, x, w);
  ws = cx.ws;
  const int bid = cx.vb, idx = bid >> 3;
  const int slot = (bid & 7) * 4 + (idx >> 3), b = (idx >> 2) & 1, nt = idx & 3;
  const int lane = (int)__builtin_amdgcn_mbcnt_hi(~0u, __builtin_amdgcn_mbcnt_lo(~0u, 0u));
  const int c64i = nt * 2 + ((bid >> 2) & 1), rg = (bid & 1) * 8 + (slot & 1) * 4 + (bid & 2) + (idx >> 4);
  const int bs = 10 * rg, hw = wv & 1, par = wv >> 1;
  const int g = lane >> 4, qq = (lane >> 2) & 3, pp = lane & 3, fr = lane & 15;
  const int lend = min(HOP * (bs + 10), L_);
  const u32x4 rsf = make_rsrc(ws + WS_GRP + nt * 256, 16 * 1024);
  unsigned fw = (unsigned)((lane + (lane & 4)) * 1024 + (b * 32 + slot) * 4);
  unsigned fp = (unsigned)(((bid & 7) + (bid & 4)) * 1024 + (b * 32 + ((lane & 31) ^ ((lane & 31) == slot))) * 4);
  f32x4 y[30], ye[2], cin[2], npv[2];
  {
    const int ee = fr & 1 ? cx.es : -2000;
#pragma unroll
    for (int s = 0; s < 2; ++s) {
      cin[s] = *reinterpret_cast<const f32x4*>(smem + CBUF + (par + (fr & 14)) * 256 + hw * 128 + g * 32 + s * 16);
#pragma unroll
      for (int r = 0; r < 4; ++r) cin[s][r] = __builtin_amdgcn_ldexpf(cin[s][r], ee);
    }
  }
  char* my = smem + wv * 20480;
  unsigned src = (unsigned)(uintptr_t)(lds_void*)my + (unsigned)((4 * g + qq) * 64 + ((g & 1) << 5) + pp * 8), src1 = src ^ 32u;
  unsigned vo = (unsigned)((lane >> 2) * 64 + (((lane & 3) ^ ((lane >> 3) & 2)) << 4)), vp = (unsigned)(g * 8);
  const u32x4 rsy = make_rsrc(ws + WS_Y, ROWS * 65536);
  unsigned so[6];
  {
    const unsigned t0 = (unsigned)(b * NBLK * 65536 + (c64i * 2 + hw) * 4096);
#pragma unroll
    for (int i = 0; i < 6; ++i) {
      const unsigned t = __builtin_amdgcn_readfirstlane(i < 5 ? t0 + (unsigned)(bs + par + 2 * i) * 65536 : t0);
      so[i] = t ^ ((t >> 8) & 8192u);
      asm volatile("" : "+s"(so[i]));
    }
  }
  asm volatile("" : "+v"(cin[0]), "+v"(cin[1]), "+v"(src), "+v"(src1), "+v"(vo), "+v"(vp), "+v"(fw), "+v"(fp));
  SB0;
  wait_vm<0>();
  __builtin_amdgcn_s_barrier();
  unsigned fv = 1u;
  if (wv == 1 && lane < 8) asm volatile("buffer_store_dword %0, %1, %2, 0 offen sc1" :: "v"(fv), "v"(fw), "s"(rsf) : "memory");
  if (wv == 0) asm volatile("buffer_load_dword %0, %1, %2, 0 offen sc1" : "=v"(fv) : "v"(fp), "s"(rsf) : "memory");
  bf16x8 gb[16];
  {
    const unsigned l16 = (unsigned)lane * 16u;
    const char* gt = (const char*)&TAB.G[0][0][0][0][0];
#pragma unroll
    for (int i = 0; i < 16; ++i)
      asm volatile("global_load_dwordx4 %0, %1, %2 offset:%3" : "=a"(gb[i]) : "v"(l16), "s"(gt + (i >> 2) * 4096), "i"((i & 3) * 1024) : "memory");
  }
  SB0;
  float gam = bf_lo(*reinterpret_cast<const unsigned short*>(smem + GAM + wv * 32 + fr * 2));
  float bet = bf_lo(*reinterpret_cast<const unsigned short*>(smem + GAM + 128 + wv * 32 + fr * 2));
  float eps = fmaxf(__builtin_amdgcn_ldexpf(1e-5f, 2 * cx.es), 1e-30f);
  const int l0 = HOP * (bs + par) + fr;
  const int le = fr < 10 ? HOP * (bs + par + 2 * (fr >> 1)) + 48 + (fr & 1) : 1 << 20;
  unsigned oo = (unsigned)((l0 * D_ + (c64i * 2 + hw) * 32 + 8 * g) * 2), oe = (unsigned)((le * D_ + (c64i * 2 + hw) * 32 + 8 * g) * 2);
  float v4[4];
#pragma unroll
  for (int tt = 0; tt < 4; ++tt) v4[tt] = (tt < 3 ? l0 + 8 * HOP + 16 * tt : le) < lend ? 1.f : 0.f;
  unsigned long long* stats = reinterpret_cast<unsigned long long*>(ws + WS_STATS);
  const u32x4 rss = make_rsrc(stats + (b * 32 + (c64i & 1) * 4) * 256 + nt * 64, 65536);
  unsigned sp = (unsigned)((g * 256 + 16 * wv + fr) * 8);
  const unsigned red = lds0 + 8 * 12288, aff = red + 4096;
  unsigned rw_ = red + (unsigned)((par * 64 + hw * 32 + 8 * g + (fr & 7)) * 8 + (fr >> 3) * 4), aw_ = aff + (unsigned)(((g & 1) * 64 + 16 * wv + fr) * 4);
  unsigned ar_ = aff + (unsigned)((hw * 32 + 8 * g) * 4);
  asm volatile("" : "+v"(gam), "+v"(bet), "+v"(eps), "+v"(oo), "+v"(oe), "+v"(sp), "+v"(rw_), "+v"(aw_), "+v"(ar_), "+v"(v4[0]), "+v"(v4[1]), "+v"(v4[2]), "+v"(v4[3]));
  SB0;
  if (wv == 0) {
    wait_vm<16>();
    asm volatile("" : "+v"(fv));
    for (unsigned spins = 0; !__all(fv != 0u) && spins < (1u << 21); ++spins)
      asm volatile("s_sleep 1\n\tbuffer_load_dword %0, %1, %2, 0 offen sc1\n\ts_waitcnt vmcnt(0)" : "=v"(fv) : "v"(fp), "s"(rsf) : "memory");
  }
  __builtin_amdgcn_s_barrier();
  u32x2 pv[2];
  asm volatile("buffer_load_dwordx2 %0, %2, %3, %4 offen sc1\n\tbuffer_load_dwordx2 %1, %2, %3, %4 offen offset:32 sc1"
               : "=&v"(pv[0]), "=&v"(pv[1]) : "v"(vp), "s"(rsy), "s"(so[5]) : "memory");
#pragma unroll
  for (int i = 0; i < 5; ++i)
    asm volatile("s_mov_b32 m0, %0\n\ts_nop 0\n\tbuffer_load_dwordx4 %1, %2, %3 offen sc1 lds\n\tbuffer_load_dwordx4 %1, %2, %3 offen offset:1024 sc1 lds\n\t"
                 "buffer_load_dwordx4 %1, %2, %3 offen offset:2048 sc1 lds\n\tbuffer_load_dwordx4 %1, %2, %3 offen offset:3072 sc1 lds"
                 :: "s"(lds0 + wv * 20480 + i * 4096), "v"(vo), "s"(rsy), "s"(so[i]) : "memory");
  float s1[8], s2[8];
  {
    bf16x8 a[2][4];
    auto rd = [&](int i, int q) __attribute__((always_inline)) {
      const lds_s16x4* p = (const lds_s16x4*)(uintptr_t)((q & 2 ? src1 : src) + i * 4096 + (q & 1) * 2048);
      a[i & 1][q] = cat(__builtin_amdgcn_ds_read_tr16_b64_v4i16((lds_s16x4*)p), __builtin_amdgcn_ds_read_tr16_b64_v4i16((lds_s16x4*)p + 128));
    };
    f32x4 t3[2];
    auto sums = [&](int cp, int q) __attribute__((always_inline)) {
      const int i = cp >> 3, s = (cp >> 2) & 1, tt = cp & 3, e = 4 * s + q;
      if (tt == 3) {
        if (i) ye[s][q] = (fr >> 1) == i ? t3[s][q] : ye[s][q];
        asm volatile("" : "+v"(ye[s]));
        return;
      }
      float d = y[6 * i + 3 * s + tt][q];
      if (i == 4) d *= v4[tt];
      if (i == 0 && tt == 0) { s1[e] = d; s2[e] = d * d; }
      else { s1[e] += d; s2[e] = __builtin_fmaf(d, d, s2[e]); }
      asm volatile("" : "+v"(s1[e]), "+v"(s2[e]));
    };
    wait_vm<16>();
    SB0;
#pragma unroll
    for (int q = 0; q < 4; ++q) { rd(0, q); SB0; }
    asm volatile("" : "+v"(pv[0]), "+v"(pv[1]));
#pragma unroll
    for (int s = 0; s < 2; ++s) {
      npv[s] = f32x4{bf_lo(pv[s][0]), bf_hi(pv[s][0]), bf_lo(pv[s][1]), bf_hi(pv[s][1])} * (-1.0f / 64.0f);
      cin[s] += npv[s];
    }
    asm volatile("" : "+v"(npv[0]), "+v"(npv[1]), "+v"(cin[0]), "+v"(cin[1]));
    asm volatile("s_nop 1");
    SB0;
    sfor<0, 42>([&](auto cc) __attribute__((always_inline)) {
      constexpr int c = cc.v, i = c >> 3, is = (c >> 2) & 1, tt = c & 3;
      if constexpr ((c & 7) == 4 && i < 4) wait_vm<12 - 4 * i>();
      sfor<0, 4>([&](auto qq_) __attribute__((always_inline)) {
        constexpr int q = qq_.v;
        if constexpr (c < 40) {
          f32x4& d = tt < 3 ? y[6 * i + 3 * is + (tt < 3 ? tt : 0)] : i ? t3[is] : ye[is];
          if constexpr (q == 0) imfma0(d, a[i & 1][2 * is], gb[4 * tt + 1], tt == 3 ? cin[is] : npv[is]);
          else imfma1(d, a[i & 1][2 * is + (q >> 1)], gb[4 * tt + (q ^ 1)]);
        }
        SB0;
        if constexpr (((c & 7) == 4 || (c & 7) == 5) && i < 4) { if (q & 1) rd(i + 1, (c & 1) * 2 + (q >> 1)); }
        if constexpr (c >= 2) sums(c - 2, q);
        SB0;
      });
    });
    asm volatile("" :: "v"(npv[0]), "v"(npv[1]), "v"(cin[0]), "v"(cin[1]));
#pragma unroll
    for (int e = 0; e < 8; ++e) {
      const float d = v4[3] != 0.f ? ye[e >> 2][e & 3] : 0.f;
      s1[e] += d;
      s2[e] = __builtin_fmaf(d, d, s2[e]);
    }
  }
  {
    float v[8];
#pragma unroll
    for (int j = 0; j < 8; ++j) { v[j] = s1[j]; }
#pragma unroll
    for (int j = 0; j < 8; ++j) DPPADD(v[j], s1[j], "row_ror:8", "0x3");
#pragma unroll
    for (int j = 0; j < 8; ++j) DPPADD(v[j], s2[j], "row_ror:8", "0xc");
    asm volatile("s_nop 0");
#pragma unroll
    for (int j = 0; j < 4; ++j) DPPADD(v[j], v[j], "row_half_mirror", "0x5");
#pragma unroll
    for (int j = 0; j < 4; ++j) DPPADD(v[j], v[j + 4], "row_half_mirror", "0xa");
    asm volatile("s_nop 0");
#pragma unroll
    for (int j = 0; j < 4; ++j) v[j] = dpp_add<0x4E>(v[j]);
    SB0;
#pragma unroll
    for (int j = 0; j < 4; ++j) v[j] = dpp_add<0xB1>(v[j]);
    const float w0 = fr & 1 ? v[1] : v[0], w1 = fr & 1 ? v[3] : v[2];
    *(__attribute__((address_space(3))) float*)(uintptr_t)rw_ = fr & 2 ? w1 : w0;
  }
  asm volatile("s_waitcnt lgkmcnt(0)" ::: "memory");
  __builtin_amdgcn_s_barrier();
  if (wv == 0) {
    typedef __attribute__((address_space(3))) f32x2 lds_f32x2;
    const f32x2 sm = *(lds_f32x2*)(uintptr_t)(red + lane * 8) + *(lds_f32x2*)(uintptr_t)(red + 512 + lane * 8);
    u32x2 pk = {__float_as_uint(sm[0]), __float_as_uint(sm[1])};
    if (pk[0] == ~0u) pk[0] = ~1u;
    asm volatile("buffer_store_dwordx2 %0, %1, %2, %3 offen sc1" :: "v"(pk), "v"(lane * 8), "s"(rss), "s"((rg + (rg & 12)) * 2048) : "memory");
  }
  {
    u32x2 pk[4];
    for (unsigned spins = 0;; ++spins) {
      asm volatile("buffer_load_dwordx2 %0, %4, %5, 0 offen sc1\n\tbuffer_load_dwordx2 %1, %4, %5, %6 offen sc1\n\tbuffer_load_dwordx2 %2, %4, %5, %7 offen sc1\n\t"
                   "buffer_load_dwordx2 %3, %4, %5, %8 offen sc1\n\ts_waitcnt vmcnt(0)"
                   : "=&v"(pk[0]), "=&v"(pk[1]), "=&v"(pk[2]), "=&v"(pk[3]) : "v"(sp), "s"(rss), "s"(16384), "s"(32768), "s"(49152) : "memory");
      if (__all(max(max(pk[0][0], pk[1][0]), max(pk[2][0], pk[3][0])) != ~0u) || spins > (1u << 20)) break;
      __builtin_amdgcn_s_sleep(1);
    }
    float a = 0.f, q = 0.f;
#pragma unroll
    for (int k = 0; k < 4; ++k) { a += __uint_as_float(pk[k][0]); q += __uint_as_float(pk[k][1]); }
    auto r0 = __builtin_amdgcn_permlane16_swap(__float_as_uint(a), __float_as_uint(q), false, false);
    const unsigned c0 = __float_as_uint(__uint_as_float(r0[0]) + __uint_as_float(r0[1]));
    auto r1 = __builtin_amdgcn_permlane32_swap(c0, c0, false, false);
    const unsigned c1 = __float_as_uint(__uint_as_float(r1[0]) + __uint_as_float(r1[1]));
    auto r2 = __builtin_amdgcn_permlane16_swap(c1, c1, false, false);
    const float mean = __uint_as_float(r2[0]) * (1.0f / (float)L_);
    const float var = fmaxf(__builtin_fmaf(-mean, mean, __uint_as_float(r2[1]) * (1.0f / (float)L_)), 0.f);
    const float gg = gam * __builtin_amdgcn_rsqf(var + eps);
    *(__attribute__((address_space(3))) float*)(uintptr_t)aw_ = g & 1 ? __builtin_fmaf(-mean, gg, bet) : gg;
    asm volatile("s_waitcnt lgkmcnt(0)" ::: "memory");
    __builtin_amdgcn_s_barrier();
  }
  {
    const u32x4 ro = make_rsrc(out + (size_t)b * L_ * D_, lend * (D_ * 2));
    f32x4 ga[2], be[2];
#pragma unroll
    for (int s = 0; s < 2; ++s) {
      typedef __attribute__((address_space(3))) f32x4 lds_f32x4;
      ga[s] = *(lds_f32x4*)(uintptr_t)(ar_ + 16 * s);
      be[s] = *(lds_f32x4*)(uintptr_t)(ar_ + 256 + 16 * s);
    }
    const float k1 = -2.0f * 0.7978845608028654f * 1.4426950408889634f, k2 = k1 * 0.044715f;
    f32x4 kk1 = {k1, k1, k1, k1}, kk2 = {k2, k2, k2, k2};
    asm volatile("" : "+v"(kk1), "+v"(kk2));
#pragma unroll
    for (int n = 0; n < 16; ++n) {
      const int i = n / 3, tt = n % 3;
      f32x4 o[2];
#pragma unroll
      for (int s = 0; s < 2; ++s) {
        const f32x4 z = (n < 15 ? y[(6 * i + 3 * s + tt) % 30] : ye[s]) * ga[s] + be[s];
        const f32x4 a = z * (z * z * kk2 + kk1);
        f32x4 e;
#pragma unroll
        for (int r = 0; r < 4; ++r) e[r] = __builtin_amdgcn_exp2f(a[r]);
        e += 1.0f;
#pragma unroll
        for (int r = 0; r < 4; ++r) e[r] = __builtin_amdgcn_rcpf(e[r]);
        o[s] = z * e;
      }
      st16(pack8(o[0], o[1]), ro, n < 15 ? oo : oe, n < 15 ? (2 * HOP * i + 16 * tt) * (D_ * 2) : 0);
      SB0;
    }
  }

  asm volatile(".Lcf_b:\n\ts_nop 0" ::: "memory");
}

extern "C" void kernel_launch(void* const* d_in, const int* in_sizes, int n_in,
                              void* d_out, int out_size, void* d_ws, size_t ws_size,
                              hipStream_t stream) {
  const unsigned short* x = (const unsigned short*)d_in[0];
  const unsigned short* w = (const unsigned short*)d_in[1];
  const unsigned short* gamma = (const unsigned short*)d_in[2];
  const unsigned short* beta = (const unsigned short*)d_in[3];
  char* ws = (char*)(((uintptr_t)d_ws + (2u << 20) - 1) & ~(uintptr_t)((2u << 20) - 1));
  conv_fft<<<256, 256, 0, stream>>>(ws, x, w, gamma, beta, (unsigned short*)d_out);
}
```

```cpp
#include <hip/hip_runtime.h>
#include <stdint.h>

typedef __attribute__((ext_vector_type(8))) short bf16x8;
typedef __attribute__((ext_vector_type(4))) short s16x4;
typedef __attribute__((ext_vector_type(4))) float f32x4;
typedef __attribute__((ext_vector_type(2))) float f32x2;
typedef __attribute__((ext_vector_type(2))) __bf16 bf16x2v;
typedef __attribute__((ext_vector_type(4))) unsigned int u32x4;
typedef __attribute__((ext_vector_type(2))) unsigned int u32x2;
constexpr int NH = 6, NST = 3;
typedef __attribute__((ext_vector_type(2 * NH))) unsigned int u32xh;
typedef __attribute__((ext_vector_type(8))) _Float16 f16x8;
typedef __attribute__((ext_vector_type(2))) _Float16 f16x2;
typedef __attribute__((ext_vector_type(2))) unsigned short u16x2;
typedef __attribute__((address_space(1))) unsigned long long gu64;
typedef __attribute__((address_space(1))) unsigned gu32;
typedef __attribute__((address_space(3))) void lds_void;
typedef __attribute__((address_space(3))) s16x4 lds_s16x4;

constexpr int T_ = 16000, D_ = 512, L_ = 7985;
constexpr int HOP = 50, NBLK = 160, ROWS = 2 * NBLK;

constexpr unsigned WS_S = 0;
constexpr unsigned SBLK = 4u << 20;
constexpr unsigned sbase(unsigned sl) { return sl * 131072 + (sl >> 4) * 8192; }
constexpr unsigned FLG = 32u * SBLK;
constexpr unsigned WS_GRP = WS_S + FLG + 16 * 8192;
constexpr unsigned WS_D = WS_S + FLG + 20 * 8192;
constexpr unsigned WS_L = WS_D + ROWS * 1024;
constexpr unsigned WS_Y = 40u << 20;
constexpr unsigned WS_STATS = 129u << 20;
constexpr unsigned WS_FA = WS_STATS + (1u << 17);
constexpr unsigned WS_PUB = WS_STATS + (1u << 19);
static_assert(WS_L + D_ * D_ * 2 <= WS_STATS && FLG % (16 * 8192) == 0, "workspace layout");

#define RLX_AGENT __ATOMIC_RELAXED, __HIP_MEMORY_SCOPE_AGENT

constexpr double COS64[17] = {1.0, 0.99518472667219688624, 0.98078528040323044913, 0.95694033573220886494,
    0.92387953251128675613, 0.88192126434835502971, 0.83146961230254523708, 0.77301045336273696081,
    0.70710678118654752440, 0.63439328416364549822, 0.55557023301960222474, 0.47139673682599764856,
    0.38268343236508977173, 0.29028467725446236764, 0.19509032201612826785, 0.09801714032956060199, 0.0};
constexpr double c64(int k) { k &= 63; if (k > 32) k = 64 - k; return k <= 16 ? COS64[k] : -COS64[32 - k]; }
constexpr double s64(int k) { return c64(k + 48); }
constexpr unsigned short bf_rne(double v) {
  const unsigned u = __builtin_bit_cast(unsigned, (float)v);
  return (unsigned short)((u + 0x7fffu + ((u >> 16) & 1u)) >> 16);
}
constexpr double bf_val(unsigned short h) { return (double)__builtin_bit_cast(float, (unsigned)h << 16); }
constexpr unsigned short hilo(double v, int lo) { return lo ? bf_rne(v - bf_val(bf_rne(v))) : bf_rne(v); }
constexpr unsigned short nz(unsigned short h) { return h & 0x7fff ? h : (unsigned short)(97 << 7); }
constexpr double R3 = 0.86602540378443864676;
constexpr double dft(int c, int t, double sgn) {
  return (c >> 1) == 0 ? ((c & t) & 1 ? -1.0 : 1.0)
                       : ((c & 1) ? -0.5 * c64((c >> 1) * t) - R3 * sgn * s64((c >> 1) * t) : c64((c >> 1) * t));
}
constexpr double idft(int c, int t) {
  return (c >> 1) == 0 ? ((c & t) & 1 ? -1.0 : 1.0) / 64.0
                       : ((c & 1) ? -s64((c >> 1) * t) / R3 - c64((c >> 1) * t) * (2.0 / 3.0) : c64((c >> 1) * t) * (4.0 / 3.0)) / 32.0;
}
constexpr double UP = 1.0 + 0.72134752 / 256.0;
constexpr int kidx(int g, int j) { return j < 4 ? 4 * g + j : 12 + 4 * g + j; }
struct Tables {
  unsigned short F[4][2][2][64][8];
  unsigned short V[2][4][64][8];
  unsigned short G[4][2][2][64][8];
};
constexpr Tables make_tables() {
  Tables tb{};
  for (int l = 0; l < 64; ++l)
    for (int j = 0; j < 8; ++j) {
      const int g = l >> 4, n = l & 15;
      for (int ct = 0; ct < 4; ++ct) {
        for (int ks = 0; ks < 2; ++ks)
          for (int h = 0; h < 2; ++h) tb.F[ct][ks][h][l][j] = nz(hilo(UP * dft(16 * ct + n, 32 * ks + kidx(g, j), -1.0), h));
        for (int p = 0; p < 2; ++p) {
          const int tap = 4 * g + (j & 3);
          tb.V[p][ct][l][j] = (p == 1 && tap == 15) ? (unsigned short)0 : nz(hilo(UP * dft(16 * ct + n, tap, 1.0), j >> 2));
        }
      }
      for (int tt = 0; tt < 4; ++tt)
        for (int ks = 0; ks < 2; ++ks)
          for (int h = 0; h < 2; ++h) {
            const int c = 32 * ks + kidx(g, j);
            tb.G[tt][ks][h][l][j] = hilo(idft(c, 16 * tt + (tt < 3 ? n : n & 1)), h);
          }
    }
  return tb;
}
__device__ const __attribute__((aligned(16))) Tables TAB = make_tables();

__device__ __forceinline__ unsigned pk_bf16(float lo, float hi) {
  return __builtin_bit_cast(unsigned, __builtin_convertvector(f32x2{lo, hi}, bf16x2v));
}
__device__ __forceinline__ float bf_lo(unsigned v) { return __uint_as_float(v << 16); }
__device__ __forceinline__ float bf_hi(unsigned v) { return __uint_as_float(v & 0xffff0000u); }
__device__ __forceinline__ u32x4 pack8(f32x4 a, f32x4 b) {
  return u32x4{pk_bf16(a[0], a[1]), pk_bf16(a[2], a[3]), pk_bf16(b[0], b[1]), pk_bf16(b[2], b[3])};
}
__device__ __forceinline__ unsigned pk_f16(float lo, float hi) {
  return (__builtin_bit_cast(unsigned, __builtin_amdgcn_cvt_pkrtz(lo, hi)) & 0xfff8fff8u);
}
__device__ __forceinline__ u32x4 pack8h(f32x4 a, f32x4 b) {
  return u32x4{pk_f16(a[0], a[1]), pk_f16(a[2], a[3]), pk_f16(b[0], b[1]), pk_f16(b[2], b[3])};
}
__device__ __forceinline__ unsigned add_h(const bf16x8& a, const bf16x8& b, int d) {
  const unsigned x = __builtin_bit_cast(u32x4, a)[d], y = __builtin_bit_cast(u32x4, b)[d];
  unsigned r = __builtin_bit_cast(unsigned, __builtin_bit_cast(f16x2, x) + __builtin_bit_cast(f16x2, y));
  return r;
}
__device__ __forceinline__ unsigned amax(unsigned m, unsigned v) {
  return __builtin_bit_cast(unsigned, __builtin_elementwise_max(__builtin_bit_cast(u16x2, m), __builtin_bit_cast(u16x2, v & 0x7fff7fffu)));
}
struct Rng { f16x2 hi = {0, 0}, lo = {0, 0}; };
__device__ __forceinline__ void widen(Rng& r, unsigned a, unsigned b) {
  const f16x2 x = __builtin_bit_cast(f16x2, a), y = __builtin_bit_cast(f16x2, b);
  r.hi = __builtin_elementwise_maximum(__builtin_elementwise_maximum(r.hi, x), y);
  r.lo = __builtin_elementwise_minimum(__builtin_elementwise_minimum(r.lo, x), y);
}
__device__ __forceinline__ unsigned amax(const Rng& r) { return amax(amax(0, __builtin_bit_cast(unsigned, r.hi)), __builtin_bit_cast(unsigned, r.lo)); }
__device__ __forceinline__ unsigned expo(unsigned m) { return max(m & 0xffffu, m >> 16) >> 7; }
__device__ __forceinline__ unsigned wave_max(unsigned v) {
  unsigned m = 0;
#pragma unroll 1
  for (int l = 0; l < 64; ++l) m = max(m, (unsigned)__builtin_amdgcn_readlane((int)v, l));
  return m;
}
__device__ __forceinline__ void scale8(bf16x8& v, int e) {
  u32x4 u = __builtin_bit_cast(u32x4, v);
#pragma unroll
  for (int d = 0; d < 4; ++d)
    u[d] = __builtin_amdgcn_perm(__float_as_uint(__builtin_amdgcn_ldexpf(bf_hi(u[d]), e)), __float_as_uint(__builtin_amdgcn_ldexpf(bf_lo(u[d]), e)), 0x07060302u);
  v = __builtin_bit_cast(bf16x8, u);
}
__device__ __forceinline__ void restart(unsigned id) {
  asm volatile("s_waitcnt vmcnt(0)\n\ts_barrier\n\ts_branch conv_fft" :: "{s[0:1]}"(__builtin_amdgcn_kernarg_segment_ptr()), "{s2}"(id), "{v0}"(threadIdx.x) : "memory");
}
__device__ __forceinline__ s16x4 tr_read(const char* p) {
  return __builtin_amdgcn_ds_read_tr16_b64_v4i16((lds_s16x4*)p);
}
__device__ __forceinline__ bf16x8 cat(s16x4 a, s16x4 b) {
  const u32x2 x = __builtin_bit_cast(u32x2, a), y = __builtin_bit_cast(u32x2, b);
  return __builtin_bit_cast(bf16x8, u32x4{x[0], x[1], y[0], y[1]});
}
#define MFMA(a, b, c) __builtin_amdgcn_mfma_f32_16x16x32_bf16(a, b, c, 0, 0, 0)
#define MFMH(a, b, c) __builtin_amdgcn_mfma_f32_16x16x32_f16(__builtin_bit_cast(f16x8, a), __builtin_bit_cast(f16x8, b), c, 0, 0, 0)
#define SB0 __builtin_amdgcn_sched_barrier(0)
#define ZERO4 (f32x4{0.f, 0.f, 0.f, 0.f})
#define DMA16(POL, rs, lds, voff, soff) \
  asm volatile("s_mov_b32 m0, %0\n\ts_nop 0\n\tbuffer_load_dwordx4 %1, %2, %3 offen " POL " lds" \
               :: "s"(lds), "v"(voff), "s"(rs), "s"(soff) : "memory")
__device__ __forceinline__ void st16(const u32x4& v, const u32x4& rs, unsigned voff, unsigned soff) {
  asm volatile("buffer_store_dwordx4 %0, %1, %2, %3 offen sc1\n\ts_nop 1" :: "v"(v), "v"(voff), "s"(rs), "s"(soff) : "memory");
}
__device__ __forceinline__ void ld16(bf16x8& v, const void* p) {
  asm volatile("global_load_dwordx4 %0, %1, off" : "=v"(v) : "v"(p) : "memory");
}
template <int N> __device__ __forceinline__ void wait_vm() { asm volatile("s_waitcnt vmcnt(%0)" :: "i"(N) : "memory"); }
__device__ __forceinline__ u32x4 make_rsrc(const void* p, unsigned bytes) {
  const unsigned long long a = (unsigned long long)p;
  return u32x4{(unsigned)a, (unsigned)(a >> 32) & 0xffffu, bytes, 0x00020000u};
}
__device__ __forceinline__ void spin_flags(const char* p) {
  for (unsigned spins = 0; !__all(__hip_atomic_load((const gu32*)p, RLX_AGENT) != 0u) && spins < (1u << 21); ++spins)
    __builtin_amdgcn_s_sleep(3);
}

typedef __attribute__((ext_vector_type(8))) unsigned int u32x8;
typedef __attribute__((ext_vector_type(16))) unsigned int u32x16;
typedef __attribute__((ext_vector_type(32))) unsigned int u32x32;
constexpr int GW = 10;
constexpr int NQ1 = GW / 2, NQW = 16 - NQ1;
constexpr int STG = 36 * 1024;
constexpr int RAWB = 67 * 1024, XT1 = RAWB + NQ1 * 4096, SINK = XT1 + RAWB;
constexpr int RAWL = 3 * STG;
constexpr int RAW0 = RAWL + 12 * 4096;
constexpr int GAM = RAWL + 36864, CBUF = RAWL + 40960;

struct Ctx {

  char* smem;
  char* ws;
  const unsigned short *gamma, *beta;
  u32x4 rs;
  unsigned lds0;
  unsigned voffw, voffx;
  unsigned poff, xo;
  unsigned id;
  unsigned tag[2];
  int es;
  int wv, vb;
  unsigned long long pc;
  unsigned pfo;
  u32x4 sx, sw;
  unsigned xcc;
};
__device__ __forceinline__ void code_touch(const Ctx& cx) {
  if ((cx.wv ^ (cx.vb >> 3)) & 3) return;
  const unsigned l = __builtin_amdgcn_mbcnt_hi(~0u, __builtin_amdgcn_mbcnt_lo(~0u, cx.wv * 64u));
#pragma unroll
  for (int k = 0; k < 2; ++k)
    asm volatile("s_mov_b32 m0, %0\n\ts_nop 0\n\tglobal_load_lds_dword %1, %2" :: "s"(SINK + cx.wv * 512 + k * 256), "v"(min((l + k * 256) * 128u, cx.pfo)), "s"(cx.pc) : "memory");
}
__device__ __forceinline__ void stage_issue(const Ctx& cx, int kc) {
  const unsigned slot = (unsigned)((kc + 1) % 3) * STG;
#pragma unroll
  for (int i = 0; i < 9; ++i) {
    const int j = i < 4 ? i : i - 4;
    const bool ahead = i < 4 || j == 0 || j == 3;
    const unsigned lo = i < 4 ? j * 4096 : j < 3 ? 16384 + j * 4096 : j == 3 ? 16384 + 10240 : 16384 + 14336;
    const unsigned go = i < 4 ? j * 4096 : (j >> 1) * 16384 + (j & 1) * 4096;
    if (kc >= 0 || ahead)
      DMA16("sc1", cx.rs, cx.lds0 + slot + lo + (i == 6 ? (cx.wv & 2) * 4096 : 0), i < 4 ? cx.voffw : cx.voffx, (unsigned)(kc + ahead) * SBLK + go);
  }
}
__device__ __forceinline__ unsigned x_place(unsigned kb, unsigned blk) { return kb + (blk + (blk & 192u)) * 128u; }
template <int V> struct IC { static constexpr int v = V; constexpr operator int() const { return V; } };
template <int A, int B, class F> __device__ __forceinline__ void sfor(F f) {
  if constexpr (A < B) { f(IC<A>{}); sfor<A + 1, B>(f); }
}
__device__ __forceinline__ unsigned dpp_swap1(unsigned v) {
  return (unsigned)__builtin_amdgcn_update_dpp(0, (int)v, 0xB1, 0xf, 0xf, false);
}

__device__ __forceinline__ int run_bs(int vb, int t) { return 5 * ((vb >> 4) + 16 * ((vb ^ t) & 1)); }
__device__ __forceinline__ int run_q(int vb, int t) { return (((vb >> 1) & 1) + ((vb >> 2) & 2) + 2 * t) & 3; }
__device__ __forceinline__ unsigned x_addr(int vb, int t, int wv) {
  const int lane = threadIdx.x & 63, q = run_q(vb, t);
  const unsigned c = (unsigned)((lane & 15) ^ (((4 * wv + (lane >> 4)) & 7) << 1));
  return (unsigned)(2 * (HOP * run_bs(vb, t) + 4 * wv + (lane >> 4)) + ((t ^ q ^ (q >> 1)) & 1)) * 1024u + c * 16 + q * 256;
}
__device__ __forceinline__ u32x4 x_rsrc(const unsigned short* x, int vb) { return make_rsrc(x + (size_t)((vb >> 2) & 1) * T_ * D_, T_ * D_ * 2); }
__device__ __forceinline__ void issue_x(int t, int wv, unsigned a, const u32x4& rx, unsigned lds0, int j0 = 0, int j1 = 17) {
#pragma unroll
  for (int jj = 0; jj < 17; ++jj)
    if (jj >= j0 && jj < j1 && (jj < 16 || wv < 3)) DMA16("nt", rx, lds0 + t * XT1 + wv * 1024 + jj * 4096, a, jj * 32768u);
}
#define CKP(i) CK(i)
#define CKQ(p)
#define CKI(i)
__device__ __forceinline__ void mfma0(f32x4& d, const bf16x8& a, const bf16x8& b) {
  asm volatile("v_mfma_f32_16x16x32_bf16 %0, %1, %2, 0" : "=&v"(d) : "v"(a), "v"(b));
}
__device__ __forceinline__ void mfma1(f32x4& d, const bf16x8& a, const bf16x8& b) {
  asm volatile("v_mfma_f32_16x16x32_bf16 %0, %1, %2, %0" : "+v"(d) : "v"(a), "v"(b));
}
__device__ __forceinline__ bf16x8 dup(s16x4 a) {
  u32x2 lo = __builtin_bit_cast(u32x2, a), hi;
  asm volatile("v_mov_b64 %0, %1" : "=v"(hi) : "v"(lo));
  return __builtin_bit_cast(bf16x8, u32x4{lo[0], lo[1], hi[0], hi[1]});
}
__device__ __forceinline__ void scale8p(bf16x8& v, int e) {
  typedef __attribute__((ext_vector_type(8))) unsigned short u16x8;
  const unsigned short h = (unsigned short)(e << 7);
  v = __builtin_bit_cast(bf16x8, (u16x8)(__builtin_bit_cast(u16x8, v) + h));
}
__device__ __forceinline__ void pin(u32x4& v) { asm volatile("" : "+v"(v)); }
struct XR { s16x4 t[8]; };
#define P0_ 16
#define SLP_ 3
#define GPO_ 113u
#define GPE_ 137u
#define CT_ 0
#define PRK_ 16
#define TE_ 6
#define LDN_ 4
#define NA_ 14
#define SX0_ 9
#define SCN_ 8
#define PG_ 42
#define PS_ 42
#define NFA_ 2
constexpr int P0 = P0_, P1 = 46;
constexpr int PG = PG_, NFA = NFA_;
constexpr int TE = TE_;
constexpr int NLD = 17 - TE + 1 + NQW + 1;
constexpr int ld_pos(int l) { return 2 * l / LDN_; }
constexpr int ld_slot(int l) { int o = 0; for (int k = 0; k < l; ++k) o += ld_pos(k) == ld_pos(l); return 1 + 2 * o; }
constexpr int NIT = 46, NS = 40, NA = NA_, NB = 26;
static_assert(NH == 6 && NST == 3, "x blocks stored in the loop");
constexpr int ord(int p) { return p == 0 ? 0 : p < 6 ? p + 2 : p < 8 ? p - 5 : p; }
constexpr int st_w(int j) { return j < 2 ? j : j < 7 ? -1 : j < 15 ? j - 5 : -1; }
constexpr int st_x(int j) { return j < 7 ? j - 2 : j - 10; }
constexpr int st_pos(int j) { return st_w(j) >= 0 ? (j < 9 ? 0 : j < 13 ? 6 : 7) : j < 7 ? j - 1 : j - 7; }
#define NBP_ 4
constexpr int NBP = NBP_;
constexpr int st_first(int h) {
  int J = h <= P0 ? 0 : h >= P1 ? NS : h <= P0 + NBP ? (h - P0) * (NA + NFA) / NBP : NA + NFA + (h - P0 - NBP) * (NS - NA - NFA) / (P1 - P0 - NBP);
  for (int k = 0; k < J; ++k)
    if (2 * st_pos(k >> 1) + (k & 1) + 2 > h - 1) return k;
  return J;
}
constexpr int sc_pos(int k) { return k < 17 ? k * SX0_ / 17 : SX0_ + (k - 17) * (P0 - SX0_) / 17; }
constexpr int sc_raw(int k) { return k < NQ1 ? k : P0 + (k - NQ1) * (NBP - 1) / NQW; }
constexpr int sc_cnt(int h) { int c = 0; for (int k = 0; k < 34; ++k) c += sc_pos(k) == h; return c; }
constexpr bool sc_ok() { for (int h = 0; h < NIT; ++h) if (sc_cnt(h) > 3) return false; return true; }
constexpr int sc_wait(int h) {
  int j = -1;
  for (int k = 17; k < 34; ++k) if (sc_pos(k) == h) j = k - 17;
  if (j < 0) return -1;
  int c = j < TE ? TE - 1 - j : 0;
  for (int l = 0; l < NLD; ++l)
    if (ld_pos(l) < h && l > j - TE) c += l != 16 - TE;
  return c;
}
constexpr int XBAR = st_first(25) > NA + NFA - 1 ? -1 : 24;
static_assert(ld_pos(NLD - 1) < P0 && P0 < 24 && LDN_ <= 8, "loads before the first store");
static_assert(sc_ok() && st_first(P0 + NBP) == NA + NFA && st_first(P0 + NBP - 1) <= NA + NFA - 1 && SX0_ < P0, "range check before the first hand-off");
struct Hand {
  u32x4 held[2 * NH], st[2 * NST];
  bf16x8 vb;
};
constexpr int A_RAW = 144, A_WL = 184, A_VEC = 188, A_SCA = 214, A_XA = 240;
template <int I> __device__ __forceinline__ void stash(unsigned v) { asm volatile("v_accvgpr_write_b32 a%c0, %1" :: "i"(I), "v"(v)); }
template <int I> __device__ __forceinline__ unsigned unstash() {
  unsigned v;
  asm volatile("v_accvgpr_read_b32 %0, a%c1" : "=v"(v) : "i"(I));
  return v;
}
__device__ __forceinline__ void stash4(int a, const u32x4& v) {
  asm volatile("v_accvgpr_write_b32 a%c0, %4\n\tv_accvgpr_write_b32 a%c1, %5\n\tv_accvgpr_write_b32 a%c2, %6\n\tv_accvgpr_write_b32 a%c3, %7"
               :: "i"(a), "i"(a + 1), "i"(a + 2), "i"(a + 3), "v"(v[0]), "v"(v[1]), "v"(v[2]), "v"(v[3]));
}
constexpr int hw_reg(int mg, int q, int half) { return ((mg * 5 + q) * 2 + half) * 4; }
template <int A> __device__ __forceinline__ void load_acc(unsigned vo, const u32x4& rs, unsigned so) {
  asm volatile("buffer_load_dwordx4 a[%c3:%c4], %0, %1, %2 offen nt" :: "v"(vo), "s"(rs), "s"(so), "i"(A), "i"(A + 3) : "memory");
}
__device__ __forceinline__ void ld8(u32x2& v, const char* p, unsigned o) {
  asm volatile("global_load_dwordx2 %0, %1, %2 sc1" : "=v"(v) : "v"(o), "s"(p) : "memory");
}
__device__ __forceinline__ void loop_setup(const Ctx& cx, unsigned pwr, unsigned prd, unsigned hst) {
  const int bid = cx.vb, idx = bid >> 3, wv = cx.wv, lane = threadIdx.x & 63;
  const int slot = (bid & 7) * 4 + (idx >> 3), b = (idx >> 2) & 1, nt = idx & 3, xb = (bid >> 2) & 1;
  const int mg = wv >> 1, nh = wv & 1, g = lane >> 4, qq = (lane >> 2) & 3, pp = lane & 3, rt = 4 * g + qq;
  const int rg = (bid & 1) * 8 + (slot & 1) * 4 + (bid & 2) + (idx >> 4);
  sfor<0, 4>([&](auto c) __attribute__((always_inline)) {
    stash<A_VEC + c.v>((unsigned)(rt * 512 + (((nh * 4 + c.v) ^ (rt & 7)) << 5) + pp * 8));
    stash<A_VEC + 5 + c.v>((RAW0 + prd) ^ (unsigned)(c.v << 5));
  });
  { const int r = lane & 15; stash<A_VEC + 4>((unsigned)(16384 + mg * 5 * 2048 + (r >> 3) * 1024 + (r & 7) * 64 + ((g ^ ((r >> 3) << 1)) << 4))); }
  stash<A_VEC + 9>(cx.voffw);
  stash<A_VEC + 10>(cx.voffx);
  {
    const int kr8 = lane >> 3;
    stash<A_VEC + 11>((unsigned)(WS_L - WS_S + (8 * wv + kr8) * 1024 + (nt * 2 + xb) * 128 + (((lane & 7) ^ ((kr8 & 2) | (wv & 1) << 2)) << 4)));
  }
  stash<A_VEC + 12>((unsigned)(WS_D - WS_S + (b * NBLK + 10 * rg + min(lane & 15, 9)) * 1024 + g * 16));
  stash<A_VEC + 13>((unsigned)(RAWL + (8 * g + qq) * 128 + ((wv ^ ((qq >> 1) | (g & 1) << 1)) << 5) + pp * 8));
  stash<A_VEC + 14>(cx.poff);
  stash<A_VEC + 15>(cx.xo);
  stash<A_VEC + 16>(hst);
  stash<A_VEC + 17>(wv == 0 && lane < 8 ? (unsigned)(lane * 1024 + (bid >> 2) * 4) : 0x80000000u);
  stash<A_VEC + 18>((unsigned)((bid & 7) * 1024 + wv * 256 + lane * 4));
  {
    const char* gp = (const char*)(lane < 32 ? cx.gamma : cx.beta) + (nt * 2 + xb) * 128 + (lane & 31) * 4;
    stash<A_VEC + 20>((unsigned)(uintptr_t)gp);
    stash<A_VEC + 21>((unsigned)((uintptr_t)gp >> 32));
  }
  stash<A_VEC + 22>((unsigned)((b * NBLK + mg * 80 + (lane & 14)) * 65536 + nt * 16384 + g * 16 + (2 * slot + (lane & 1)) * 64));
  {
    unsigned len;
    asm volatile("s_mov_b32 %0, .Lcf_b-.Lcf_t-64" : "=s"(len));
    stash<A_VEC + 23>(min((unsigned)(wv * 64 + lane) * 128u, len));
  }
  stash<A_VEC + 24>(RAWL + pwr);
  stash<A_VEC + 25>((unsigned)(CBUF + (lane & 15) * 256 + wv * 64 + g * 16));
  const unsigned long long gt = (unsigned long long)&TAB.G[0][0][0][0][0];
  unsigned sc[15] = {cx.lds0, (unsigned)(wv & 2) * 4096u, FLG + 2 * 8192 + (bid & 3) * 256, wv == nt ? 0x3ffcu : 0x444u,
                     NQ1 * 2 * SBLK + ((bid + NQ1) & 3) * 16384, wv == 3 ? (bid & 1 ? GPO_ : GPE_) - 14u : 0u, slot ? 0xbf800000u : 0u,
                     0, 0, 0, 0, 0, ((wv ^ idx) & 3) == 0 ? RAWL + 32768 + wv * 256u : 0u, (unsigned)gt, (unsigned)(gt >> 32)};
#pragma unroll
  for (int m = 0; m < 5; ++m) {
    const int xt = mg ? (m + 1) % 5 : m;
    sc[7 + m] = (unsigned)(xt * 16 * 65536 + ((b * 10 + mg * 5 + 2 * nh + xt) & 2) * 4096);
  }
  unsigned sv = 0;
#pragma unroll
  for (int i = 0; i < 15; ++i)
    asm volatile("v_writelane_b32 %0, %1, %2" : "+v"(sv) : "s"(__builtin_amdgcn_readfirstlane((int)sc[i])), "i"(i));
  stash<A_SCA>(sv);
}
__device__ __forceinline__ void phase1(Ctx& cx, const unsigned short* __restrict__ x, const unsigned short* __restrict__ w, Hand& hd) {
  u32x4 rq0;
  static_assert(NQ1 == 5, "phase-1 wait counts assume five quanta");
  char* smem = cx.smem;
  const unsigned lds0 = (unsigned)(uintptr_t)(lds_void*)smem;
  const int idx = cx.vb >> 3, wv = cx.wv, lane = threadIdx.x & 63;
  const u32x4 rw = make_rsrc(w, 31 * D_ * D_ * 2);
  const int tap = threadIdx.x >> 4, pc = threadIdx.x & 15;
  const unsigned roff = (unsigned)(tap * (2 * D_ * D_ * 2) + pc * 16);
  const int ng = wv;
  const int g = lane >> 4, qq = (lane >> 2) & 3, pp = lane & 3;
  u32x4 r1[NQ1];
  bf16x8 fb[2][2];
  unsigned long long tag;
  const char* rec = cx.ws + WS_PUB + ((cx.id & 7) * 8 + wv) * 16;
  u32x4 pr;
  unsigned pwr, prd, pvo, pww, xb0[4], pq[4], xa[2];
  const int kr = (cx.vb >> 7) * 32 + (cx.vb & 4) * 4 + (idx & 15);
  auto raw_so = [&](int q) {
    return (unsigned)(kr + 64 * (2 * (q & 3) + ((q >> 2) & 1)) + 512 * (((q >> 3) ^ q ^ (q >> 1)) & 1)) * 1024u + (unsigned)((cx.vb + q) & 3) * 256u;
  };
  auto raw_load = [&](u32x4& r, int q) {
    asm volatile("buffer_load_dwordx4 %0, %1, %2, %3 offen nt" : "=v"(r) : "v"(roff), "s"(rw), "s"(raw_so(q)) : "memory");
  };
  const unsigned xa1 = x_addr(cx.vb, 1, wv);
  {
    const int vb = cx.vb;
    auto setup = [&]() __attribute__((always_inline)) {
    {
      const int bid = vb, xb = (bid >> 2) & 1, krow = kr;
      const int slot = (bid & 7) * 4 + (idx >> 3), b = (idx >> 2) & 1, nt = idx & 3;
      const int rt = 4 * g + qq;
      pvo = (unsigned)((bid & 7) * 1024 + wv * 256 + lane * 4);
      pwr = (unsigned)((pc >> 3) * 2048 + tap * 128 + ((((pc >> 1) ^ (tap >> 1)) & 3) << 5) + (pc & 1) * 16);
      prd = (unsigned)(rt * 128 + (((rt >> 1) & 3) << 5) + pp * 8);
      cx.poff = (unsigned)(sbase(8 * ng + ((lane >> 1) & 7)) + (krow >> 5) * SBLK + (krow & 31) * 512 + (lane & 1) * 64 + g * 16);
      cx.xo = (unsigned)(sbase(8 * ng + ((lane >> 1) & 7)) + 65536 + xb * 8192 + (lane & 1) * 64 + g * 16);
      cx.lds0 = lds0 + wv * 1024;
#pragma unroll
      for (int k = 0; k < 4; ++k) {
        xb0[k] = (unsigned)((4 * g + qq) * 256 + (((4 * g + qq + 2 * k) & 7) << 5) + pp * 8);
        pq[k] = (prd + RAWB) ^ (unsigned)(k << 5);
      }
      pww = pwr + RAWB;
      asm volatile("" : "+v"(xb0[0]), "+v"(xb0[1]), "+v"(xb0[2]), "+v"(xb0[3]), "+v"(pq[0]), "+v"(pq[1]), "+v"(pq[2]), "+v"(pq[3]), "+v"(pww));
      {
        const int row = 2 * wv + (lane >> 5), p = ((lane & 31) >> 1) ^ (row & 7);
        const int chunk = ((p >> 2) & 1) * 8 + ((p >> 1) & 1) * 4 + ((p >> 3) & 1) * 2 + (p & 1);
        cx.voffw = (unsigned)(sbase(slot) + (nt * 32 + row) * 512 + chunk * 32 + (lane & 1) * 16);
        cx.voffx = (unsigned)(sbase(slot) + 65536 + b * 8192 + (8 * wv + ((lane >> 2) & 7)) * 128 + (lane >> 5) * 64 + (((lane & 3) ^ ((wv & 1) << 1)) << 4));
      }
    }
    asm volatile("" : "+v"(pvo), "+v"(cx.poff), "+v"(cx.xo), "+v"(cx.voffw), "+v"(cx.voffx));
    {
      const unsigned so1 = (unsigned)(16 + 2 * run_q(vb, 1)) * SBLK;
      const int e = lane & 31, n = (lane < 32 ? 20 - NH : 10) + (e >> 1);
      unsigned hst = e < (lane < 32 ? 2 * NH : 8) ? x_place(so1 + (n >= 15 ? 8 * SBLK : 0) + (e & 1) * SBLK, (unsigned)(run_bs(vb, 1) + n % 5)) : 0xf0000000u;
      if ((lane >> 1) == 20 && run_q(vb, 0)) hst = x_place((unsigned)(8 + 2 * run_q(vb, 0) + (e & 1)) * SBLK, (unsigned)(run_bs(vb, 0) + 4));
      loop_setup(cx, pwr, prd, hst);
    }
      };
    if (cx.id | wv) issue_x(0, wv, x_addr(vb, 0, wv), x_rsrc(x, vb), lds0);
    if (cx.id == 0 && wv) {
#pragma unroll
      for (int k = 0; k < 6; ++k)
        if (k < 5 || wv < 3) DMA16("nt", x_rsrc(x, vb), lds0 + (3 * k + wv - 1) * 4096, x_addr(vb, 0, 0), (3 * k + wv - 1) * 32768u);
    }
    SB0;
#pragma unroll
    for (int k = 0; k < NQ1; ++k) raw_load(r1[k], k);
#pragma unroll
    for (int i = 0; i < 4; ++i) ld16(fb[i >> 1][i & 1], TAB.F[ng][i >> 1][i & 1][lane]);
    ld16(hd.vb, TAB.V[0][ng][lane]);
    if (cx.id | wv) asm volatile("global_load_dwordx4 %0, %1, %2 sc1" : "=v"(pr) : "v"(0u), "s"(rec) : "memory");
    SB0;
    setup();
    SB0;
    if ((cx.id | wv) == 0) {
      wait_vm<3 + NQ1 + 5>();
      u32x4 &sx = cx.sx, &sw = cx.sw;
      asm volatile("" : "+v"(sx), "+v"(sw));
      unsigned m = expo(amax(amax(amax(amax(0, sx[0]), sx[1]), sx[2]), sx[3])) | expo(amax(amax(amax(amax(0, sw[0]), sw[1]), sw[2]), sw[3])) << 16;
      m = amax(m, (unsigned)__builtin_amdgcn_update_dpp(0, (int)m, 0x128, 0xf, 0xf, false));
      m = amax(m, (unsigned)__builtin_amdgcn_update_dpp(0, (int)m, 0x124, 0xf, 0xf, false));
      m = amax(m, (unsigned)__builtin_amdgcn_update_dpp(0, (int)m, 0x122, 0xf, 0xf, false));
      m = amax(m, (unsigned)__builtin_amdgcn_update_dpp(0, (int)m, 0x121, 0xf, 0xf, false));
      unsigned ex = 28, ew = 28;
#pragma unroll
      for (int l = 0; l < 64; l += 16) {
        const unsigned v = (unsigned)__builtin_amdgcn_readlane((int)m, l);
        ex = max(ex + 2, v & 255u) - 2;
        ew = max(ew + 3, v >> 16) - 3;
      }
      const unsigned long long tg = __builtin_amdgcn_s_memrealtime();
      const unsigned lo = (unsigned)tg << 4 | cx.xcc << 1, hi = ((unsigned)(tg >> 28) & 0xffffu) | min(ex, 220u) << 16 | min(ew, 220u) << 24;
      asm volatile("global_store_dwordx4 %0, %1, %2 sc1\n\ts_nop 1" :: "v"(threadIdx.x * 16), "v"(u32x4{lo, hi, ~lo, ~hi}), "s"(cx.ws + WS_PUB) : "memory");
      pr = u32x4{lo, hi, ~lo, ~hi};
      wait_vm<NQ1 + 5 + 1>();
      unsigned g[2] = {~0u, ~0u};
#pragma unroll 1
      for (int k = -1; k < PRK_ && ((k & 1) || min(g[0], g[1]) > 440u); ++k) {
        unsigned long long t0, t1;
        unsigned d;
        asm volatile("s_memtime %0\n\ts_waitcnt lgkmcnt(0)\n\ts_load_dword %2, %3, 0x0\n\ts_waitcnt lgkmcnt(0)\n\ts_memtime %1\n\ts_waitcnt lgkmcnt(0)"
                     : "=&s"(t0), "=&s"(t1), "=&s"(d) : "s"(rec + 32768 + (k < 0 ? 128 : (k & 1) * 8192 + (k & 2) * 2048 + (k & 4) * 4096 + (k & 8) * 128)) : "memory");
        if (k >= 0) { if (k & 1) g[1] = min(g[1], (unsigned)(t1 - t0)); else g[0] = min(g[0], (unsigned)(t1 - t0)); }
      }
      asm volatile("global_store_dwordx4 %0, %1, %2 sc1\n\ts_nop 1" :: "v"(lane * 16 + 2048), "v"(u32x4{lo | (((g[0] < g[1] ? 0u : 1u) ^ (cx.vb >> 2)) & 1u), hi, lo | (((g[0] < g[1] ? 0u : 1u) ^ (cx.vb >> 2)) & 1u), hi}), "s"(rec) : "memory");
    }
    issue_x(1, wv, xa1, x_rsrc(x, vb), lds0, 0, TE);
  }
  Rng gx, gw;
  wait_vm<6 + TE>();
#pragma unroll
  for (int q = 0; q < NQ1; ++q) {
    asm volatile("" : "+v"(r1[q]));
    *reinterpret_cast<u32x4*>(smem + (pww + q * 4096)) = r1[q];
  }
  SB0;
  unsigned near;
  {
    wait_vm<TE>();
    unsigned d0, d1;
    for (unsigned spins = 0;; ++spins) {
      asm volatile("" : "+v"(pr));
      d0 = __builtin_amdgcn_readfirstlane(pr[0]);
      d1 = __builtin_amdgcn_readfirstlane(pr[1]);
      const unsigned d2 = __builtin_amdgcn_readfirstlane(pr[2]), d3 = __builtin_amdgcn_readfirstlane(pr[3]);
      if (__builtin_expect((d0 == ~d2 && d1 == ~d3) || spins > (1u << 21), 1)) break;
      __builtin_amdgcn_s_sleep(2);
      asm volatile("global_load_dwordx4 %0, %1, %2 sc1\n\ts_waitcnt vmcnt(0)" : "=v"(pr) : "v"(0u), "s"(rec) : "memory");
    }
    tag = d0 | (unsigned long long)d1 << 32;
    const unsigned vn = (cx.id + (d0 >> 1)) & 7u;
    if (__builtin_expect(vn != (cx.vb & 7u), 0)) restart(cx.id | 256u | vn << 10);
  }
  const unsigned ee = (cx.id & 512u ? cx.id : (unsigned)(tag >> 32)) >> 16, ex = ee & 255u, ew = ee >> 8;
  cx.es = 256 - (int)(ex + ew);
  char* ws;
  const int bid = cx.vb;
  bf16x8& vb = hd.vb;
  u32x4 f0, f1;
  const u32x4* fa;
  u32x2 ov;
  unsigned pollv;
  const int rb[2] = {run_bs(bid, 0), run_bs(bid, 1)};
  asm volatile("" : "+v"(fb[0][0]), "+v"(fb[0][1]), "+v"(fb[1][0]), "+v"(fb[1][1]), "+v"(vb));
  scale8p(vb, 128 - (int)ew);
#pragma unroll
  for (int i = 0; i < 4; ++i) scale8p(fb[i >> 1][i & 1], 128 - (int)ex);
  asm volatile("" : "+v"(fb[0][0]), "+v"(fb[0][1]), "+v"(fb[1][0]), "+v"(fb[1][1]), "+v"(vb));
  asm volatile("s_waitcnt lgkmcnt(0)" ::: "memory");
  __builtin_amdgcn_s_barrier();
  SB0;
  u32x4 hx[2][20], hwl;
  f32x4 pend[2];
  auto rd = [&](int h, int i) __attribute__((always_inline)) {
    const int it = ord(h >> 1), mg = h & 1;
    if (it < 3) return tr_read(smem + (pq[i & 3] + mg * 2048 + (2 * it + (i >> 2)) * 4096));
    const int n = it - 3;
    const int c = 4 * ((n / 5) & 1) + 2 * mg + (i & 1);
    return tr_read(smem + ((c ? xa[i & 1] : xb0[n % 5 & 3]) + n % 5 * (HOP * 256) + (i >> 1) * (16 * 256)));
  };
  auto xaddr = [&](int h) __attribute__((always_inline)) {
    const int n = ord(h >> 1) - 3, mg = h & 1;
    if (h >= NIT || n < 0) return;
    if (h == 26) {
#pragma unroll
      for (int k = 0; k < 4; ++k) xb0[k] += XT1;
      asm volatile("" : "+v"(xb0[0]), "+v"(xb0[1]), "+v"(xb0[2]), "+v"(xb0[3]));
    }
#pragma unroll
    for (int i = 0; i < 2; ++i) {
      const int c = 4 * ((n / 5) & 1) + 2 * mg + i;
      if (c) { xa[i] = xb0[n % 5 & 3] ^ (unsigned)(c << 5); asm volatile("" : "+v"(xa[i])); }
    }
  };
  auto dst = [&](int h) -> u32x4& { const int it = ord(h >> 1); return it < 3 || it == 12 || it == 16 ? hwl : hx[h & 1][it - 3]; };
  auto item = [&](int h, XR& xr, auto side) __attribute__((always_inline)) {
    const XR c = xr;
    const int it = ord(h >> 1), mg = h & 1;
    const int nm = it == 2 ? 4 : 8, nr = h + 1 < NIT && ord((h + 1) >> 1) == 2 ? 4 : 8;
    const int pn = h > 0 && ord((h - 1) >> 1) == 2 ? 4 : 8;
    f32x4 r[8];
    bf16x8 dq[3];
    if (it < 3) { dq[0] = dup(c.t[0]); dq[1] = dup(c.t[1]); asm volatile("s_nop 0"); }
#pragma unroll
    for (int i = 0; i < 8; ++i) {
      if (i >= nm) break;
      const int s2 = i & 1, ks = (i >> 1) & 1;
      if (it < 3) mfma0(r[i], dq[i % 3], vb);
      else if (i < 2) mfma0(r[6 + i], cat(c.t[4 * ks + s2], c.t[4 * ks + 2 + s2]), fb[ks][1]);
      else mfma1(r[6 + s2], cat(c.t[4 * ks + s2], c.t[4 * ks + 2 + s2]), fb[ks][i < 4]);
      SB0;
      if (it < 3 && i + 2 < nm) dq[(i + 2) % 3] = dup(c.t[i + 2]);
      if (h + 1 < NIT && i < 4) {
        const bool nq = ord((h + 1) >> 1) < 3;
#pragma unroll
        for (int u = 0; u < 2; ++u) {
          const int k = 2 * i + u, j = nq ? k : (k & 4) + ((k >> 1) & 1) + (k & 1) * 2;
          if (j < nr) xr.t[j] = rd(h + 1, j);
        }
      }
      if (i == (nm == 4 ? 3 : 4)) xaddr(h + 2);
      if (h > 0 && (nm == 4 ? i >= 2 : i >= 4)) {
        u32x4& d = dst(h - 1);
#pragma unroll
        for (int e = 0; e < 4; ++e)
          if ((nm == 4 ? 2 + (e >> 1) : 4 + e) == i) d[e] = pk_f16(pend[e >> 1][(e & 1) * 2], pend[e >> 1][(e & 1) * 2 + 1]);
        if (i == nm - 1) {
          const int pi = ord((h - 1) >> 1);
          if (pi < 3) stash4(hw_reg((h - 1) & 1, pi == 2 ? 4 : 2 * pi + 1, 1), d);
          else if (pi == 12 || pi == 16) stash4(A_XA + (pi == 12 ? 8 : 0) + ((h - 1) & 1) * 4, d);
          else asm volatile("" : "+v"(d));
        }
      }
      if (it < 3 && i >= 3 && (i & 1)) {
        stash4(hw_reg(mg, 2 * it + ((i - 3) >> 2), ((i - 3) >> 1) & 1), pack8h(r[i - 3], r[i - 2]));
      }
      side(i * (8 / nm));
      if (nm == 4) side(2 * i + 1);
      SB0;
    }
    (void)pn;
    pend[0] = r[it < 3 ? nm - 2 : 6];
    pend[1] = r[it < 3 ? nm - 1 : 7];
  };
  const unsigned so0 = (unsigned)(2 * run_q(bid, 0)) * SBLK, so1 = (unsigned)(16 + 2 * run_q(bid, 1)) * SBLK;
  auto store = [&](int J) __attribute__((always_inline)) {
    const int j = J >> 1, mg = J & 1;
    if (J == 0) {
      wait_vm<0>();
      for (unsigned spins = 0;; ++spins) {
        asm volatile("" : "+v"(ov));
        near = __builtin_amdgcn_readfirstlane(ov[0]) ^ (unsigned)tag;
        if (__builtin_expect((near < 2u && __builtin_amdgcn_readfirstlane(ov[1]) == (unsigned)(tag >> 32)) || spins > (1u << 21), 1)) break;
        __builtin_amdgcn_s_sleep(2);
        asm volatile("global_load_dwordx2 %0, %1, %2 sc1\n\ts_waitcnt vmcnt(0)" : "=v"(ov) : "v"(2048u), "s"(rec) : "memory");
      }
      ws = cx.ws += (near & 1u) * 8192;
      cx.rs = make_rsrc(ws + WS_S, WS_L - WS_S + D_ * D_ * 2);
    }
    if (J == 1) {
      if (wv == 1) {
        const unsigned o = (lane < 32 ? WS_STATS : WS_S + FLG + 16384 - 512) + bid * 512 + lane * 16;
        const unsigned v = lane < 32 ? ~0u : 0u;
        asm volatile("global_store_dwordx4 %0, %1, %2 sc1\n\ts_nop 1" :: "v"(o), "v"(u32x4{v, v, v, v}), "s"(ws) : "memory");
        if (lane < 8) __hip_atomic_store((gu32*)(ws + WS_S + FLG + lane * 1024 + ((bid + 2) & 3) * 256 + (bid >> 2) * 4), 0u, RLX_AGENT);
      }
    }
    if (J == NB + NFA) {
      if (((bid >> 1) ^ (bid >> 3) ^ 1) & 1 && threadIdx.x < 160) {
        const int t = threadIdx.x >= 80, blk = (threadIdx.x >> 4) - 5 * t;
        const unsigned o = (unsigned)(t * XT1 + blk * (HOP * 256) + (((lane & 15) ^ ((blk & 3) << 2)) << 4));
        const u32x4 lo = *reinterpret_cast<const u32x4*>(smem + o), hi = *reinterpret_cast<const u32x4*>(smem + o + 64 * 256);
        u32x4 d;
#pragma unroll
        for (int e = 0; e < 4; ++e) d[e] = pk_bf16(bf_lo(hi[e]) - bf_lo(lo[e]), bf_hi(hi[e]) - bf_hi(lo[e]));
        st16(d, cx.rs, (unsigned)((((bid >> 2) & 1) * NBLK + (t ? rb[1] : rb[0]) + blk) * 1024 + run_q(bid, t) * 256 + (lane & 15) * 16), WS_D - WS_S);
      }
      asm volatile("buffer_store_dwordx4 a[%c3:%c4], %0, %1, %2 offen sc1" :: "v"(wv >= 2 ? (threadIdx.x & 127) * 16 : 0x80000000u), "s"(cx.rs), "s"(WS_L - WS_S + bid * 2048), "i"(A_WL), "i"(A_WL + 3) : "memory");
    }
    const int wq = st_w(j), xn = st_x(j);
    if (wq >= 0)
      asm volatile("buffer_store_dwordx4 a[%c3:%c4], %0, %1, %2 offen sc1" :: "v"(cx.poff), "s"(cx.rs),
                   "s"((unsigned)(wq >> 1) * (2 * SBLK) + (unsigned)((bid + (wq >> 1)) & 3) * 16384u + (wq & 1) * 128u + mg * 256u),
                   "i"(hw_reg(mg, wq >> 1, wq & 1)), "i"(hw_reg(mg, wq >> 1, wq & 1) + 3) : "memory");
    else if (xn == 9)
      asm volatile("buffer_store_dwordx4 a[%c3:%c4], %0, %1, %2 offen sc1" :: "v"(cx.xo), "s"(cx.rs),
                   "s"(run_q(bid, 0) ? 0xf0000000u : x_place(so0 + 8 * SBLK + mg * SBLK, (unsigned)(rb[0] + 4))), "i"(A_XA + 8 + mg * 4), "i"(A_XA + 11 + mg * 4) : "memory");
    else st16(hx[mg][xn], cx.rs, cx.xo, x_place((xn < 10 ? so0 : so1) + ((xn / 5) & 1) * (8 * SBLK) + mg * SBLK, (unsigned)(rb[xn / 10] + xn % 5)));
    if (J == NA + NFA - 1) {
      const unsigned mx = amax(gx), mw = amax(gw);
      if (__builtin_expect(__any(expo(mx) > ex + 7 || expo(mw) > ew + 9) && !(cx.id & 512u), 0)) {
        const u32x2 m = {wave_max(expo(mx)), wave_max(expo(mw))};
        asm volatile("global_atomic_umax %0, %1, %3 sc1\n\tglobal_atomic_umax %0, %2, %3 offset:4 sc1\n\ts_waitcnt vmcnt(0)"
                     :: "v"(lane * 16 + 1024), "v"(m[0]), "v"(m[1]), "s"(rec - ((cx.id & 7) * 8 + wv) * 16) : "memory");
      }
    }
    if (J == NA + NFA - 1 || J == NB + NFA - 1) {
      if (J < NB) {
      } else {
      }
      wait_vm<NFA>();
      if (J < NB) {
      } else {
      }
      __builtin_amdgcn_s_barrier();
      if (J < NB) {
      } else {
      }
      if (wv == 0 && lane < 8) {
        if (J < NB) __hip_atomic_store((gu64*)(ws + WS_FA + (cx.id & 512u) * 32 + lane * 2048) + bid, tag, RLX_AGENT);
        else __hip_atomic_store((gu32*)(ws + WS_S + FLG + lane * 1024 + ((bid + 2) & 3) * 256 + (bid >> 2) * 4), 1u, RLX_AGENT);
      }
    }
    SB0;
  };
  u32x4 sv[3];
  auto check = [&](auto hc, int s) __attribute__((always_inline)) {
    constexpr int h = hc.v;
    if constexpr (sc_wait(h) >= 0) { if (s == 0) wait_vm<sc_wait(h) < 0 ? 0 : sc_wait(h)>(); }
#pragma unroll
    for (int k = 0; k < 34; ++k) {
      if (sc_pos(k) != h) continue;
      int o = 0;
#pragma unroll
      for (int k2 = 0; k2 < k; ++k2) o += sc_pos(k2) == h;
      const int t = k / 17, j = k % 17;
      if (s == o) sv[o] = *reinterpret_cast<const u32x4*>(smem + (t * XT1 + (j < 16 || wv < 3 ? j : 15) * 4096 + threadIdx.x * 16));
      if (s == 5 + o) {
        widen(gx, sv[o][0], sv[o][1]);
        widen(gx, sv[o][2], sv[o][3]);
      }
    }
    if (s == 4)
      sfor<0, 16>([&](auto k) __attribute__((always_inline)) {
        if (sc_raw(k.v) != h) return;
        u32x4 r;
        if constexpr (k.v <= NQ1) {
          u32x4& q = k.v < NQ1 ? r1[k.v < NQ1 ? k.v : 0] : rq0;
          pin(q);
          r = q;
        } else {
          constexpr int a = A_RAW + 4 * (k.v - NQ1 - 1);
          r = u32x4{unstash<a>(), unstash<a + 1>(), unstash<a + 2>(), unstash<a + 3>()};
        }
        widen(gw, r[0], r[1]);
        widen(gw, r[2], r[3]);
      });
    if (s == 1 && h <= P0 + NBP) asm volatile("" : "+v"(gx.hi), "+v"(gx.lo), "+v"(gw.hi), "+v"(gw.lo));
  };
  auto loads = [&](auto p, int s) __attribute__((always_inline)) {
    sfor<0, NLD>([&](auto l) __attribute__((always_inline)) {
      if (ld_pos(l.v) != p.v || ld_slot(l.v) != s) return;
      constexpr int n = l.v - (17 - TE);
      if constexpr (n < 0) {
        issue_x(1, wv, xa1, x_rsrc(x, bid), lds0, TE + l.v, TE + l.v + 1);
      } else if constexpr (n == 0) {
        load_acc<A_WL>(wv >= 2 ? (threadIdx.x & 127) * 16 : 0x80000000u, rw, 30 * D_ * D_ * 2 + bid * 2048);
      } else if constexpr (n == 1) {
        raw_load(rq0, NQ1);
      } else if constexpr (n <= NQW) {
        load_acc<A_RAW + 4 * (n - 2)>(roff, rw, raw_so(NQ1 + n - 1));
      } else {
        ld8(ov, rec, 2048u);
      }
    });
  };
  XR xr;
#pragma unroll
  for (int j = 0; j < 8; ++j) xr.t[j] = rd(0, j);
  static_assert(ord(0) < 3 && ord(1) >= 3 && ord(0) != 2, "positions 0, 1: weights");
  SB0;
  static_assert(st_first(NIT) == NS && PG >= 28 && st_first(PG - 4) > NB + NFA && NA + NFA <= NB && NFA >= 1 && NH <= 7, "order of the hand-off steps");
  sfor<0, NIT>([&](auto p) __attribute__((always_inline)) {
    constexpr int j0 = st_first(p.v), j1 = st_first(p.v + 1);
    if (SLP_ && p >= 24 && p < PG && !(p & 1)) { if (!(bid & 1) && cx.id) __builtin_amdgcn_s_sleep(SLP_); }
    if (p == XBAR) __builtin_amdgcn_s_barrier();
    if (p == PG - 4) {
      code_touch(cx);
      fa = reinterpret_cast<const u32x4*>(ws + WS_FA + (cx.id & 512u) * 32 + (bid & 7) * 2048) + lane;
      if (wv == 0)
        asm volatile("global_load_dwordx4 %0, %2, off sc1\n\tglobal_load_dwordx4 %1, %2, off offset:1024 sc1" : "=&v"(f0), "=&v"(f1) : "v"(fa) : "memory");
    }
    if (p == PG) {
      if (wv == 0) {
        wait_vm<st_first(PG) - st_first(PG - 4)>();
        for (unsigned spins = 0; spins < (1u << 21); ++spins) {
          asm volatile("" : "+v"(f0), "+v"(f1));
          const unsigned t0 = (unsigned)tag, t1 = (unsigned)(tag >> 32);
          if (__all(f0[0] == t0 && f0[1] == t1 && f0[2] == t0 && f0[3] == t1 && f1[0] == t0 && f1[1] == t1 && f1[2] == t0 && f1[3] == t1)) break;
          __builtin_amdgcn_s_sleep(2);
          asm volatile("global_load_dwordx4 %0, %2, off sc1\n\tglobal_load_dwordx4 %1, %2, off offset:1024 sc1\n\ts_waitcnt vmcnt(0)"
                       : "=&v"(f0), "=&v"(f1) : "v"(fa) : "memory");
        }
      }
      __builtin_amdgcn_s_barrier();
      stage_issue(cx, -1);
      asm volatile("global_load_dwordx2 %0, %1, %2 sc1" : "=v"(ov) : "v"(1024u), "s"(rec) : "memory");
      stage_issue(cx, 0);
      SB0;
    }
    if (p == NIT - 2)
      asm volatile("buffer_load_dword %0, %1, %2, %3 offen sc1" : "=v"(pollv) : "v"(pvo), "s"(cx.rs), "s"(FLG) : "memory");
    item(p, xr, [&](int i) __attribute__((always_inline)) {
      if ((i * (j1 - j0) + 7) / 8 < ((i + 1) * (j1 - j0) + 7) / 8) store(j0 + (i * (j1 - j0) + 7) / 8);
      check(p, i);
      loads(p, i);
    });
  });
  hx[1][19] = pack8h(pend[0], pend[1]);
#pragma unroll
  for (int e = 0; e < 2 * NH; ++e) hd.held[e] = hx[e & 1][20 - NH + (e >> 1)];
#pragma unroll
  for (int e = 0; e < 2 * NST; ++e) hd.st[e] = hx[e & 1][10 + (e >> 1)];
  *reinterpret_cast<u32x4*>(smem + RAW0 + pwr) = rq0;
  wait_vm<NS - st_first(NIT - 2)>();
  {
    asm volatile("" : "+v"(ov));
    const unsigned ox = __builtin_amdgcn_readfirstlane(ov[0]), ow = __builtin_amdgcn_readfirstlane(ov[1]);
    if (__builtin_expect((ox | ow) && !(cx.id & 512u), 0))
      restart((cx.id & 255u) | 768u | (bid & 7u) << 10 | min(max(ex + 7, ox) - 7, 220u) << 16 | min(max(ew + 9, ow) - 9, 220u) << 24);
    if ((cx.id & 255u) == 0 && wv == 0)
      asm volatile("global_store_dwordx4 %0, %1, %2 sc1\n\ts_nop 1" :: "v"(lane * 16), "v"(u32x4{0u, 0u, 0u, 0u}), "s"(rec) : "memory");
  }
  asm volatile("" : "+v"(pollv));
  if (!__all(pollv != 0u)) spin_flags(ws + WS_S + FLG + pvo);
  asm volatile("s_waitcnt lgkmcnt(0)" ::: "memory");
  __builtin_amdgcn_s_barrier();
  SB0;
}

__device__ __forceinline__ void run12(Ctx& cx, const unsigned short* __restrict__ x, const unsigned short* __restrict__ w) {
  Hand hd;
  phase1(cx, x, w, hd);
  u32x16 h1, st0;
  u32x8 st1;
  u32x32 h0;
#pragma unroll
  for (int i = 0; i < 32; ++i) {
    h0[i] = hd.held[i >> 2][i & 3];
    if (i < 16) { h1[i] = hd.held[8 + (i >> 2)][i & 3]; st0[i] = hd.st[i >> 2][i & 3]; }
    if (i < 8) st1[i] = hd.st[4 + (i >> 2)][i & 3];
  }
  u32x16 o0, o4;
  u32x8 o1;
  u32x4 o5;
  u32x32 o3;
  asm volatile(
  "v_accvgpr_read_b32 v228, a188\n"
  "v_accvgpr_read_b32 v229, a189\n"
  "v_accvgpr_read_b32 v230, a190\n"
  "v_accvgpr_read_b32 v231, a191\n"
  "v_accvgpr_read_b32 v232, a192\n"
  "ds_read_b64_tr_b16 v[0:1], v228 offset:0\n"
  "ds_read_b64_tr_b16 v[2:3], v228 offset:8192\n"
  "ds_read_b64_tr_b16 v[16:17], v228 offset:256\n"
  "ds_read_b64_tr_b16 v[18:19], v228 offset:8448\n"
  "ds_read_b64_tr_b16 v[4:5], v229 offset:0\n"
  "ds_read_b64_tr_b16 v[6:7], v229 offset:8192\n"
  "ds_read_b64_tr_b16 v[20:21], v229 offset:256\n"
  "ds_read_b64_tr_b16 v[22:23], v229 offset:8448\n"
  "ds_read_b64_tr_b16 v[8:9], v230 offset:0\n"
  "ds_read_b64_tr_b16 v[10:11], v230 offset:8192\n"
  "ds_read_b64_tr_b16 v[24:25], v230 offset:256\n"
  "ds_read_b64_tr_b16 v[26:27], v230 offset:8448\n"
  "ds_read_b64_tr_b16 v[12:13], v231 offset:0\n"
  "ds_read_b64_tr_b16 v[14:15], v231 offset:8192\n"
  "ds_read_b64_tr_b16 v[28:29], v231 offset:256\n"
  "ds_read_b64_tr_b16 v[30:31], v231 offset:8448\n"
  "ds_read_b128 v[96:99], v232 offset:0\n"
  "ds_read_b128 v[100:103], v232 offset:512\n"
  "ds_read_b128 v[104:107], v232 offset:2048\n"
  "ds_read_b128 v[108:111], v232 offset:2560\n"
  "v_accvgpr_read_b32 v255, a214\n"
  "v_accvgpr_read_b32 v237, a197\n"
  "v_accvgpr_read_b32 v238, a198\n"
  "v_readlane_b32 s44, v255, 0\n"
  "v_readlane_b32 s45, v255, 1\n"
  "v_readlane_b32 s62, v255, 5\n"
  "v_accvgpr_read_b32 v233, a193\n"
  "v_accvgpr_read_b32 v234, a194\n"
  "v_accvgpr_read_b32 v235, a195\n"
  "v_accvgpr_read_b32 v236, a196\n"
  "v_accvgpr_read_b32 v239, a199\n"
  "v_accvgpr_read_b32 v240, a200\n"
  "v_accvgpr_read_b32 v241, a201\n"
  "v_accvgpr_read_b32 v242, a202\n"
  "v_accvgpr_read_b32 v243, a203\n"
  "v_accvgpr_read_b32 v244, a204\n"
  "v_accvgpr_read_b32 v245, a205\n"
  "v_accvgpr_read_b32 v246, a206\n"
  "v_accvgpr_read_b32 v248, a208\n"
  "v_accvgpr_read_b32 v249, a209\n"
  "v_accvgpr_read_b32 v250, a210\n"
  "v_accvgpr_read_b32 v251, a211\n"
  "v_accvgpr_read_b32 v252, a212\n"
  "v_accvgpr_read_b32 v253, a213\n"
  "s_add_i32 s67, s44, s45\n"
  "s_add_i32 m0, s44, 73728\n"
  "s_mov_b32 s64, 8388608\n"
  "buffer_load_dwordx4 v237, s[40:43], s64 offen sc1 lds\n"
  "s_add_i32 m0, s44, 77824\n"
  "s_mov_b32 s64, 8392704\n"
  "buffer_load_dwordx4 v237, s[40:43], s64 offen sc1 lds\n"
  "s_add_i32 m0, s44, 81920\n"
  "s_mov_b32 s64, 8396800\n"
  "buffer_load_dwordx4 v237, s[40:43], s64 offen sc1 lds\n"
  "s_add_i32 m0, s44, 86016\n"
  "s_mov_b32 s64, 8400896\n"
  "buffer_load_dwordx4 v237, s[40:43], s64 offen sc1 lds\n"
  "s_add_i32 m0, s44, 90112\n"
  "s_mov_b32 s64, 8388608\n"
  "buffer_load_dwordx4 v238, s[40:43], s64 offen sc1 lds\n"
  "s_add_i32 m0, s44, 94208\n"
  "s_mov_b32 s64, 4198400\n"
  "buffer_load_dwordx4 v238, s[40:43], s64 offen sc1 lds\n"
  "s_add_i32 m0, s67, 98304\n"
  "s_mov_b32 s64, 4210688\n"
  "buffer_load_dwordx4 v238, s[40:43], s64 offen sc1 lds\n"
  "s_add_i32 m0, s44, 100352\n"
  "s_mov_b32 s64, 8409088\n"
  "buffer_load_dwordx4 v238, s[40:43], s64 offen sc1 lds\n"
  "s_add_i32 m0, s44, 104448\n"
  "s_mov_b32 s64, 4227072\n"
  "buffer_load_dwordx4 v238, s[40:43], s64 offen sc1 lds\n"
  "s_memrealtime s[70:71]\n"
  "v_readlane_b32 s51, v255, 2\n"
  "v_readlane_b32 s52, v255, 3\n"
  "v_readlane_b32 s55, v255, 4\n"
  "v_readlane_b32 s56, v255, 4\n"
  "v_readlane_b32 s76, v255, 6\n"
  "v_readlane_b32 s77, v255, 6\n"
  "v_readlane_b32 s80, v255, 7\n"
  "v_readlane_b32 s81, v255, 8\n"
  "v_readlane_b32 s82, v255, 9\n"
  "v_readlane_b32 s83, v255, 10\n"
  "v_readlane_b32 s84, v255, 11\n"
  "v_readlane_b32 s85, v255, 12\n"
  "v_readlane_b32 s88, v255, 13\n"
  "v_readlane_b32 s89, v255, 14\n"
  "v_mov_b32 v172, 0\n"
  "v_mov_b32 v173, 0\n"
  "v_mov_b32 v174, 0\n"
  "v_mov_b32 v175, 0\n"
  "v_mov_b32 v247, 1\n"
  "s_mov_b32 s46, 36864\n"
  "s_mov_b32 s47, s44\n"
  "s_mov_b32 s48, s67\n"
  "s_mov_b32 s49, 36864\n"
  "s_mov_b32 s50, 8388608\n"
  "s_mov_b32 s53, 134217728\n"
  "s_mov_b32 s54, 134217728\n"
  "s_mov_b32 s57, -131072\n"
  "s_mov_b32 s58, 0xf0000000\n"
  "s_mov_b32 s59, 0xf0000000\n"
  "s_mov_b32 s60, -1\n"
  "s_mov_b32 s68, -73728\n"
  "s_mov_b32 s69, 36864\n"
  "s_add_u32 s72, s40, 41943040\n"
  "s_addc_u32 s73, s41, 0\n"
  "s_mov_b32 s74, 20971520\n"
  "s_mov_b32 s75, 0x20000\n"
  "s_waitcnt lgkmcnt(0)\n"
  "s_add_i32 s61, s70, s62\n"
  "s_add_i32 s61, s61, 14\n"
  "v_pk_add_f16 v32, v0, v16\n"
  "v_pk_add_f16 v33, v1, v17\n"
  "v_pk_add_f16 v34, v2, v18\n"
  "v_pk_add_f16 v35, v3, v19\n"
  "v_pk_add_f16 v36, v4, v20\n"
  "v_pk_add_f16 v37, v5, v21\n"
  "v_pk_add_f16 v38, v6, v22\n"
  "v_pk_add_f16 v39, v7, v23\n"
  "v_pk_add_f16 v40, v8, v24\n"
  "v_pk_add_f16 v41, v9, v25\n"
  "v_pk_add_f16 v42, v10, v26\n"
  "v_pk_add_f16 v43, v11, v27\n"
  "v_pk_add_f16 v44, v12, v28\n"
  "v_pk_add_f16 v45, v13, v29\n"
  "v_pk_add_f16 v46, v14, v30\n"
  "v_pk_add_f16 v47, v15, v31\n"
  "v_add_u32 v228, 36864, v228\n"
  "v_add_u32 v229, 36864, v229\n"
  "v_add_u32 v230, 36864, v230\n"
  "v_add_u32 v231, 36864, v231\n"
  "v_add_u32 v232, 36864, v232\n"
  "s_barrier\n"
  "v_mfma_f32_16x16x32_f16 a[0:3], v[0:3], v[96:99], 0\n"
  "v_readlane_b32 s65, v244, 32\n"
  "v_pk_add_f16 v120, v96, v100\n"
  "v_mfma_f32_16x16x32_f16 a[12:15], v[4:7], v[96:99], 0\n"
  "v_pk_add_f16 v121, v97, v101\n"
  "v_pk_add_f16 v122, v98, v102\n"
  "v_mfma_f32_16x16x32_f16 a[24:27], v[8:11], v[96:99], 0\n"
  "s_add_i32 m0, s47, 0\n"
  "ds_read_b64_tr_b16 v[48:49], v228 offset:0\n"
  "v_pk_add_f16 v123, v99, v103\n"
  "v_mfma_f32_16x16x32_f16 a[36:39], v[12:15], v[96:99], 0\n"
  "s_add_i32 s64, s50, 4194304\n"
  "buffer_load_dwordx4 v237, s[40:43], s64 offen sc1 lds\n"
  "v_readlane_b32 s66, v244, 33\n"
  "v_mfma_f32_16x16x32_f16 a[4:7], v[16:19], v[100:103], 0\n"
  "s_cmp_eq_u32 s46, 73728\n"
  "s_cselect_b32 s49, s68, s69\n"
  "ds_read_b64_tr_b16 v[124:125], v233 offset:0\n"
  "v_mfma_f32_16x16x32_f16 a[16:19], v[20:23], v[100:103], 0\n"
  "ds_read_b64_tr_b16 v[128:129], v234 offset:0\n"
  "ds_read_b64_tr_b16 v[50:51], v228 offset:8192\n"
  "v_mfma_f32_16x16x32_f16 a[28:31], v[24:27], v[100:103], 0\n"
  "buffer_store_dwordx4 v[80:83], v243, s[40:43], s65 offen sc1\n"
  "ds_read_b128 v[96:99], v232 offset:4096\n"
  "v_mfma_f32_16x16x32_f16 a[40:43], v[28:31], v[100:103], 0\n"
  "ds_read_b64_tr_b16 v[132:133], v235 offset:0\n"
  "v_readlane_b32 s65, v244, 34\n"
  "v_mfma_f32_16x16x32_f16 a[8:11], v[32:35], v[120:123], 0\n"
  "s_add_i32 m0, s47, 4096\n"
  "ds_read_b64_tr_b16 v[64:65], v228 offset:256\n"
  "ds_read_b128 v[100:103], v232 offset:4608\n"
  "v_mfma_f32_16x16x32_f16 a[20:23], v[36:39], v[120:123], 0\n"
  "s_add_i32 s64, s50, 4198400\n"
  "buffer_load_dwordx4 v237, s[40:43], s64 offen sc1 lds\n"
  "ds_read_b64_tr_b16 v[136:137], v236 offset:0\n"
  "v_mfma_f32_16x16x32_f16 a[32:35], v[40:43], v[120:123], 0\n"
  "ds_read_b64_tr_b16 v[66:67], v228 offset:8448\n"
  "v_add_u32 v228, s49, v228\n"
  "v_mfma_f32_16x16x32_f16 a[44:47], v[44:47], v[120:123], 0\n"
  "ds_read_b64_tr_b16 v[52:53], v229 offset:0\n"
  "ds_read_b64_tr_b16 v[54:55], v229 offset:8192\n"
  "v_mfma_f32_16x16x32_f16 a[48:51], v[0:3], v[104:107], 0\n"
  "buffer_store_dwordx4 v[84:87], v243, s[40:43], s66 offen sc1\n"
  "v_pk_add_f16 v120, v104, v108\n"
  "v_mfma_f32_16x16x32_f16 a[60:63], v[4:7], v[104:107], 0\n"
  "v_pk_add_f16 v121, v105, v109\n"
  "v_pk_add_f16 v122, v106, v110\n"
  "v_mfma_f32_16x16x32_f16 a[72:75], v[8:11], v[104:107], 0\n"
  "s_add_i32 m0, s47, 8192\n"
  "ds_read_b64_tr_b16 v[68:69], v229 offset:256\n"
  "v_pk_add_f16 v123, v107, v111\n"
  "v_mfma_f32_16x16x32_f16 a[84:87], v[12:15], v[104:107], 0\n"
  "s_add_i32 s64, s50, 4202496\n"
  "buffer_load_dwordx4 v237, s[40:43], s64 offen sc1 lds\n"
  "v_readlane_b32 s66, v244, 35\n"
  "v_mfma_f32_16x16x32_f16 a[52:55], v[16:19], v[108:111], 0\n"
  "s_waitcnt lgkmcnt(4)\n"
  "ds_read_b64_tr_b16 v[70:71], v229 offset:8448\n"
  "v_mov_b64 v[126:127], v[124:125]\n"
  "v_mfma_f32_16x16x32_f16 a[64:67], v[20:23], v[108:111], 0\n"
  "s_waitcnt lgkmcnt(4)\n"
  "ds_read_b128 v[104:107], v232 offset:6144\n"
  "v_mov_b64 v[130:131], v[128:129]\n"
  "v_mfma_f32_16x16x32_f16 a[76:79], v[24:27], v[108:111], 0\n"
  "buffer_store_dwordx4 v[88:91], v243, s[40:43], s65 offen sc1\n"
  "v_mov_b64 v[134:135], v[132:133]\n"
  "v_mfma_f32_16x16x32_f16 a[88:91], v[28:31], v[108:111], 0\n"
  "ds_read_b64_tr_b16 v[56:57], v230 offset:0\n"
  "v_readlane_b32 s65, v244, 36\n"
  "v_mfma_f32_16x16x32_f16 a[56:59], v[32:35], v[120:123], 0\n"
  "s_add_i32 m0, s47, 12288\n"
  "ds_read_b128 v[108:111], v232 offset:6656\n"
  "v_mov_b64 v[138:139], v[136:137]\n"
  "v_mfma_f32_16x16x32_f16 a[68:71], v[36:39], v[120:123], 0\n"
  "s_add_i32 s64, s50, 4206592\n"
  "buffer_load_dwordx4 v237, s[40:43], s64 offen sc1 lds\n"
  "v_mfma_f32_16x16x32_f16 a[80:83], v[40:43], v[120:123], 0\n"
  "ds_read_b64_tr_b16 v[58:59], v230 offset:8192\n"
  "v_pk_add_f16 v80, v48, v64\n"
  "v_mfma_f32_16x16x32_f16 a[92:95], v[44:47], v[120:123], 0\n"
  "s_waitcnt lgkmcnt(4)\n"
  "ds_read_b64_tr_b16 v[72:73], v230 offset:256\n"
  "v_pk_add_f16 v81, v49, v65\n"
  "v_mfma_f32_16x16x32_f16 a[96:99], v[0:3], v[96:99], 0\n"
  "buffer_store_dwordx4 v[92:95], v243, s[40:43], s66 offen sc1\n"
  "v_pk_add_f16 v120, v96, v100\n"
  "v_mfma_f32_16x16x32_bf16 v[140:143], v[124:127], v[176:179], 0\n"
  "ds_read_b64_tr_b16 v[74:75], v230 offset:8448\n"
  "v_pk_add_f16 v121, v97, v101\n"
  "v_mfma_f32_16x16x32_f16 a[108:111], v[4:7], v[96:99], 0\n"
  "s_add_i32 m0, s47, 16384\n"
  "v_pk_add_f16 v122, v98, v102\n"
  "v_pk_add_f16 v123, v99, v103\n"
  "v_mfma_f32_16x16x32_f16 a[120:123], v[8:11], v[96:99], 0\n"
  "s_add_i32 s64, s50, 4194304\n"
  "buffer_load_dwordx4 v238, s[40:43], s64 offen sc1 lds\n"
  "v_readlane_b32 s66, v244, 37\n"
  "v_mfma_f32_16x16x32_f16 a[132:135], v[12:15], v[96:99], 0\n"
  "ds_read_b64_tr_b16 v[60:61], v231 offset:0\n"
  "v_cvt_pkrtz_f16_f32 v124, v140, v141\n"
  "v_mfma_f32_16x16x32_bf16 v[144:147], v[128:131], v[176:179], 0\n"
  "ds_read_b64_tr_b16 v[62:63], v231 offset:8192\n"
  "v_cvt_pkrtz_f16_f32 v125, v142, v143\n"
  "v_mfma_f32_16x16x32_f16 a[100:103], v[16:19], v[100:103], 0\n"
  "buffer_store_dwordx4 v[156:159], v243, s[40:43], s65 offen sc1\n"
  "v_and_b32 v124, 0xfff8fff8, v124\n"
  "v_mfma_f32_16x16x32_f16 a[112:115], v[20:23], v[100:103], 0\n"
  "ds_read_b128 v[96:99], v232 offset:0\n"
  "v_and_b32 v125, 0xfff8fff8, v125\n"
  "v_mfma_f32_16x16x32_f16 a[124:127], v[24:27], v[100:103], 0\n"
  "s_add_i32 m0, s47, 20480\n"
  "ds_read_b128 v[112:115], v232 offset:8192\n"
  "v_cvt_pkrtz_f16_f32 v126, v144, v145\n"
  "v_mfma_f32_16x16x32_bf16 v[148:151], v[132:135], v[176:179], 0\n"
  "s_add_i32 s64, s50, 4096\n"
  "buffer_load_dwordx4 v238, s[40:43], s64 offen sc1 lds\n"
  "v_cvt_pkrtz_f16_f32 v127, v146, v147\n"
  "v_mfma_f32_16x16x32_f16 a[136:139], v[28:31], v[100:103], 0\n"
  "s_waitcnt lgkmcnt(4)\n"
  "ds_write_b128 v252, a[144:147] offset:4096\n"
  "v_and_b32 v126, 0xfff8fff8, v126\n"
  "v_mfma_f32_16x16x32_f16 a[104:107], v[32:35], v[120:123], 0\n"
  "ds_read_b64_tr_b16 v[76:77], v231 offset:256\n"
  "v_and_b32 v127, 0xfff8fff8, v127\n"
  "v_mfma_f32_16x16x32_f16 a[116:119], v[36:39], v[120:123], 0\n"
  "buffer_store_dwordx4 v[160:163], v243, s[40:43], s66 offen sc1\n"
  "v_cvt_pkrtz_f16_f32 v128, v148, v149\n"
  "v_mfma_f32_16x16x32_bf16 v[152:155], v[136:139], v[176:179], 0\n"
  "ds_write_b128 v252, a[156:159] offset:16384\n"
  "v_cvt_pkrtz_f16_f32 v129, v150, v151\n"
  "v_mfma_f32_16x16x32_f16 a[128:131], v[40:43], v[120:123], 0\n"
  "s_add_i32 m0, s48, 24576\n"
  "ds_read_b128 v[100:103], v232 offset:512\n"
  "v_and_b32 v128, 0xfff8fff8, v128\n"
  "v_mfma_f32_16x16x32_f16 a[140:143], v[44:47], v[120:123], 0\n"
  "s_add_i32 s64, s50, 16384\n"
  "buffer_load_dwordx4 v238, s[40:43], s64 offen sc1 lds\n"
  "v_and_b32 v129, 0xfff8fff8, v129\n"
  "v_mfma_f32_16x16x32_f16 a[144:147], v[0:3], v[104:107], 0\n"
  "ds_read_b128 v[116:119], v232 offset:8704\n"
  "v_pk_add_f16 v120, v104, v108\n"
  "v_mfma_f32_16x16x32_f16 a[156:159], v[4:7], v[104:107], 0\n"
  "ds_write_b128 v252, a[168:171] offset:28672\n"
  "v_pk_add_f16 v121, v105, v109\n"
  "v_mfma_f32_16x16x32_f16 a[168:171], v[8:11], v[104:107], 0\n"
  "buffer_store_dwordx4 v[124:127], v242, s[40:43], s56 offen offset:0 sc1\n"
  "ds_read_b64_tr_b16 v[78:79], v231 offset:8448\n"
  "ds_write_b128 v252, a[180:183] offset:40960\n"
  "v_mfma_f32_16x16x32_f16 a[180:183], v[12:15], v[104:107], 0\n"
  "ds_write_b128 v252, a[148:151] offset:8192\n"
  "v_pk_add_f16 v122, v106, v110\n"
  "v_mfma_f32_16x16x32_f16 a[148:151], v[16:19], v[108:111], 0\n"
  "s_add_i32 m0, s47, 26624\n"
  "ds_write_b128 v252, a[160:163] offset:20480\n"
  "v_pk_add_f16 v123, v107, v111\n"
  "v_mfma_f32_16x16x32_f16 a[160:163], v[20:23], v[108:111], 0\n"
  "s_add_i32 s64, s50, 4214784\n"
  "buffer_load_dwordx4 v238, s[40:43], s64 offen sc1 lds\n"
  "ds_write_b128 v252, a[172:175] offset:32768\n"
  "v_mfma_f32_16x16x32_f16 a[172:175], v[24:27], v[108:111], 0\n"
  "ds_write_b128 v252, a[152:155] offset:12288\n"
  "v_cvt_pkrtz_f16_f32 v130, v152, v153\n"
  "v_mfma_f32_16x16x32_f16 a[184:187], v[28:31], v[108:111], 0\n"
  "s_add_i32 m0, s47, 30720\n"
  "ds_write_b128 v252, a[164:167] offset:24576\n"
  "v_cvt_pkrtz_f16_f32 v131, v154, v155\n"
  "v_mfma_f32_16x16x32_f16 a[152:155], v[32:35], v[120:123], 0\n"
  "s_add_i32 s64, s50, 32768\n"
  "buffer_load_dwordx4 v238, s[40:43], s64 offen sc1 lds\n"
  "ds_write_b128 v252, a[176:179] offset:36864\n"
  "v_mfma_f32_16x16x32_f16 a[164:167], v[36:39], v[120:123], 0\n"
  "s_waitcnt lgkmcnt(9)\n"
  "v_and_b32 v130, 0xfff8fff8, v130\n"
  "v_and_b32 v131, 0xfff8fff8, v131\n"
  "v_mfma_f32_16x16x32_f16 a[176:179], v[40:43], v[120:123], 0\n"
  "s_add_i32 s47, s44, s46\n"
  "s_waitcnt lgkmcnt(7)\n"
  "ds_read_b128 v[104:107], v232 offset:2048\n"
  "v_mfma_f32_16x16x32_f16 a[188:191], v[44:47], v[120:123], 0\n"
  "s_add_i32 s48, s47, s45\n"
  "buffer_store_dwordx4 v[128:131], v242, s[40:43], s56 offen offset:128 sc1\n"
  "v_pk_add_f16 v82, v50, v66\n"
  "v_mfma_f32_16x16x32_f16 a[192:195], v[0:3], v[112:115], 0\n"
  "s_add_i32 s46, s46, s49\n"
  "ds_read_b128 v[108:111], v232 offset:2560\n"
  "v_pk_add_f16 v120, v112, v116\n"
  "v_mfma_f32_16x16x32_f16 a[204:207], v[4:7], v[112:115], 0\n"
  "s_add_i32 s50, s50, 4194304\n"
  "v_pk_add_f16 v121, v113, v117\n"
  "v_pk_add_f16 v122, v114, v118\n"
  "v_mfma_f32_16x16x32_f16 a[216:219], v[8:11], v[112:115], 0\n"
  "v_pk_add_f16 v123, v115, v119\n"
  "v_pk_add_f16 v83, v51, v67\n"
  "v_mfma_f32_16x16x32_f16 a[228:231], v[12:15], v[112:115], 0\n"
  "v_pk_add_f16 v84, v52, v68\n"
  "v_pk_add_f16 v85, v53, v69\n"
  "v_mfma_f32_16x16x32_f16 a[196:199], v[16:19], v[116:119], 0\n"
  "v_pk_add_f16 v86, v54, v70\n"
  "v_pk_add_f16 v87, v55, v71\n"
  "v_mfma_f32_16x16x32_f16 a[208:211], v[20:23], v[116:119], 0\n"
  "v_pk_add_f16 v88, v56, v72\n"
  "v_pk_add_f16 v89, v57, v73\n"
  "v_mfma_f32_16x16x32_f16 a[220:223], v[24:27], v[116:119], 0\n"
  "v_pk_add_f16 v90, v58, v74\n"
  "v_pk_add_f16 v91, v59, v75\n"
  "v_mfma_f32_16x16x32_f16 a[232:235], v[28:31], v[116:119], 0\n"
  "v_pk_add_f16 v92, v60, v76\n"
  "v_pk_add_f16 v93, v61, v77\n"
  "v_mfma_f32_16x16x32_f16 a[200:203], v[32:35], v[120:123], 0\n"
  "v_pk_add_f16 v94, v62, v78\n"
  "v_pk_add_f16 v95, v63, v79\n"
  "s_waitcnt vmcnt(17) lgkmcnt(0)\n"
  "v_mfma_f32_16x16x32_f16 a[212:215], v[36:39], v[120:123], 0\n"
  "s_cmp_gt_u32 s62, 88\n"
  "s_cbranch_scc1 .Lpace_s0_None_%=\n"
  ".Lpaced_s0_None_%=:\n"
  "v_mfma_f32_16x16x32_f16 a[224:227], v[40:43], v[120:123], 0\n"
  "v_add_u32 v229, s49, v229\n"
  "v_add_u32 v230, s49, v230\n"
  "s_barrier\n"
  "v_mfma_f32_16x16x32_f16 a[236:239], v[44:47], v[120:123], 0\n"
  "v_add_u32 v231, s49, v231\n"
  "v_add_u32 v232, s49, v232\n"
  "v_mfma_f32_16x16x32_f16 a[0:3], v[48:51], v[96:99], a[0:3]\n"
  "v_pk_add_f16 v120, v96, v100\n"
  "v_pk_add_f16 v121, v97, v101\n"
  "v_mfma_f32_16x16x32_f16 a[12:15], v[52:55], v[96:99], a[12:15]\n"
  "v_pk_add_f16 v122, v98, v102\n"
  "v_pk_add_f16 v123, v99, v103\n"
  "v_mfma_f32_16x16x32_f16 a[24:27], v[56:59], v[96:99], a[24:27]\n"
  "s_and_b32 s65, s51, 0xffffe300\n"
  "ds_read_b64_tr_b16 v[0:1], v228 offset:0\n"
  "v_readlane_b32 s66, v244, 38\n"
  "v_mfma_f32_16x16x32_f16 a[36:39], v[60:63], v[96:99], a[36:39]\n"
  "buffer_load_dwordx4 v[164:167], v240, s[40:43], s59 offen sc1\n"
  "v_readlane_b32 s67, v244, 39\n"
  "v_mfma_f32_16x16x32_f16 a[4:7], v[64:67], v[100:103], a[4:7]\n"
  "s_cmp_eq_u32 s46, 73728\n"
  "s_cselect_b32 s49, s68, s69\n"
  "ds_read_b64_tr_b16 v[124:125], v233 offset:2048\n"
  "v_mfma_f32_16x16x32_f16 a[16:19], v[68:71], v[100:103], a[16:19]\n"
  "s_add_i32 m0, s44, 155648\n"
  "ds_read_b64_tr_b16 v[128:129], v234 offset:2048\n"
  "v_add_u32 v233, -45056, v233\n"
  "v_mfma_f32_16x16x32_f16 a[28:31], v[72:75], v[100:103], a[28:31]\n"
  "buffer_load_dwordx4 v239, s[40:43], s58 offen sc1 lds\n"
  "v_add_u32 v234, -45056, v234\n"
  "v_mfma_f32_16x16x32_f16 a[40:43], v[76:79], v[100:103], a[40:43]\n"
  "s_add_i32 s57, s57, 32768\n"
  "ds_read_b64_tr_b16 v[2:3], v228 offset:8192\n"
  "ds_read_b128 v[96:99], v232 offset:4096\n"
  "v_mfma_f32_16x16x32_f16 a[8:11], v[80:83], v[120:123], a[8:11]\n"
  "s_add_i32 m0, s47, 0\n"
  "s_lshr_b32 s59, s57, 9\n"
  "ds_read_b64_tr_b16 v[132:133], v235 offset:2048\n"
  "v_mfma_f32_16x16x32_f16 a[20:23], v[84:87], v[120:123], a[20:23]\n"
  "s_add_i32 s64, s50, 4194304\n"
  "buffer_load_dwordx4 v237, s[40:43], s64 offen sc1 lds\n"
  "v_add_u32 v235, -45056, v235\n"
  "v_mfma_f32_16x16x32_f16 a[32:35], v[88:91], v[120:123], a[32:35]\n"
  "s_add_i32 s53, s53, 8192\n"
  "s_lshr_b32 s52, s52, 1\n"
  "ds_read_b64_tr_b16 v[16:17], v228 offset:256\n"
  "v_mfma_f32_16x16x32_f16 a[44:47], v[92:95], v[120:123], a[44:47]\n"
  "s_bitcmp1_b32 s52, 0\n"
  "s_cselect_b32 s54, s53, 134217728\n"
  "ds_read_b128 v[100:103], v232 offset:4608\n"
  "v_mfma_f32_16x16x32_f16 a[48:51], v[48:51], v[104:107], a[48:51]\n"
  "s_add_i32 s51, s51, 8448\n"
  "buffer_store_dword v232, v245, s[40:43], s65 offen sc1\n"
  "v_pk_add_f16 v120, v104, v108\n"
  "v_mfma_f32_16x16x32_f16 a[60:63], v[52:55], v[104:107], a[60:63]\n"
  "ds_read_b64_tr_b16 v[136:137], v236 offset:2048\n"
  "v_pk_add_f16 v121, v105, v109\n"
  "v_mfma_f32_16x16x32_f16 a[72:75], v[56:59], v[104:107], a[72:75]\n"
  "ds_read_b64_tr_b16 v[18:19], v228 offset:8448\n"
  "v_pk_add_f16 v122, v106, v110\n"
  "v_mfma_f32_16x16x32_f16 a[84:87], v[60:63], v[104:107], a[84:87]\n"
  "buffer_store_dwordx4 a[240:243], v243, s[40:43], s66 offen sc1\n"
  "v_pk_add_f16 v123, v107, v111\n"
  "v_mfma_f32_16x16x32_f16 a[52:55], v[64:67], v[108:111], a[52:55]\n"
  "ds_read_b64_tr_b16 v[4:5], v229 offset:0\n"
  "v_readlane_b32 s66, v244, 40\n"
  "v_mfma_f32_16x16x32_f16 a[64:67], v[68:71], v[108:111], a[64:67]\n"
  "s_add_i32 m0, s47, 4096\n"
  "ds_read_b64_tr_b16 v[6:7], v229 offset:8192\n"
  "v_add_u32 v228, s49, v228\n"
  "v_mfma_f32_16x16x32_f16 a[76:79], v[72:75], v[108:111], a[76:79]\n"
  "s_add_i32 s64, s50, 4198400\n"
  "buffer_load_dwordx4 v237, s[40:43], s64 offen sc1 lds\n"
  "v_add_u32 v236, -45056, v236\n"
  "v_mfma_f32_16x16x32_f16 a[88:91], v[76:79], v[108:111], a[88:91]\n"
  "ds_read_b64_tr_b16 v[20:21], v229 offset:256\n"
  "ds_read_b64_tr_b16 v[22:23], v229 offset:8448\n"
  "v_mfma_f32_16x16x32_f16 a[56:59], v[80:83], v[120:123], a[56:59]\n"
  "s_waitcnt lgkmcnt(5)\n"
  "ds_read_b128 v[104:107], v232 offset:6144\n"
  "v_mov_b64 v[126:127], v[124:125]\n"
  "v_mfma_f32_16x16x32_f16 a[68:71], v[84:87], v[120:123], a[68:71]\n"
  "buffer_store_dwordx4 a[244:247], v243, s[40:43], s67 offen sc1\n"
  "v_mov_b64 v[130:131], v[128:129]\n"
  "v_mfma_f32_16x16x32_f16 a[80:83], v[88:91], v[120:123], a[80:83]\n"
  "s_waitcnt lgkmcnt(5)\n"
  "ds_read_b64_tr_b16 v[8:9], v230 offset:0\n"
  "v_readlane_b32 s67, v244, 41\n"
  "v_mfma_f32_16x16x32_f16 a[92:95], v[92:95], v[120:123], a[92:95]\n"
  "s_add_i32 m0, s47, 8192\n"
  "ds_read_b128 v[108:111], v232 offset:6656\n"
  "v_mov_b64 v[134:135], v[132:133]\n"
  "v_mfma_f32_16x16x32_f16 a[96:99], v[48:51], v[96:99], a[96:99]\n"
  "s_add_i32 s64, s50, 4202496\n"
  "buffer_load_dwordx4 v237, s[40:43], s64 offen sc1 lds\n"
  "v_pk_add_f16 v120, v96, v100\n"
  "v_mfma_f32_16x16x32_bf16 v[140:143], v[124:127], v[176:179], 0\n"
  "ds_read_b64_tr_b16 v[10:11], v230 offset:8192\n"
  "v_pk_add_f16 v121, v97, v101\n"
  "v_mfma_f32_16x16x32_f16 a[108:111], v[52:55], v[96:99], a[108:111]\n"
  "s_waitcnt lgkmcnt(4)\n"
  "ds_read_b64_tr_b16 v[24:25], v230 offset:256\n"
  "v_pk_add_f16 v122, v98, v102\n"
  "v_mfma_f32_16x16x32_f16 a[120:123], v[56:59], v[96:99], a[120:123]\n"
  "buffer_store_dwordx4 a[248:251], v243, s[40:43], s66 offen sc1\n"
  "v_pk_add_f16 v123, v99, v103\n"
  "v_mfma_f32_16x16x32_f16 a[132:135], v[60:63], v[96:99], a[132:135]\n"
  "ds_read_b64_tr_b16 v[26:27], v230 offset:8448\n"
  "v_mov_b64 v[138:139], v[136:137]\n"
  "v_mfma_f32_16x16x32_bf16 v[144:147], v[128:131], v[176:179], 0\n"
  "s_add_i32 m0, s47, 12288\n"
  "ds_read_b64_tr_b16 v[12:13], v231 offset:0\n"
  "v_cvt_pkrtz_f16_f32 v124, v140, v141\n"
  "v_mfma_f32_16x16x32_f16 a[100:103], v[64:67], v[100:103], a[100:103]\n"
  "s_add_i32 s64, s50, 4206592\n"
  "buffer_load_dwordx4 v237, s[40:43], s64 offen sc1 lds\n"
  "v_cvt_pkrtz_f16_f32 v125, v142, v143\n"
  "v_mfma_f32_16x16x32_f16 a[112:115], v[68:71], v[100:103], a[112:115]\n"
  "ds_read_b64_tr_b16 v[14:15], v231 offset:8192\n"
  "v_and_b32 v124, 0xfff8fff8, v124\n"
  "v_mfma_f32_16x16x32_f16 a[124:127], v[72:75], v[100:103], a[124:127]\n"
  "ds_read_b128 v[96:99], v232 offset:0\n"
  "v_cvt_pkrtz_f16_f32 v126, v144, v145\n"
  "v_mfma_f32_16x16x32_bf16 v[148:151], v[132:135], v[176:179], 0\n"
  "buffer_store_dwordx4 a[252:255], v243, s[40:43], s67 offen sc1\n"
  "v_cvt_pkrtz_f16_f32 v127, v146, v147\n"
  "v_mfma_f32_16x16x32_f16 a[136:139], v[76:79], v[100:103], a[136:139]\n"
  "ds_read_b128 v[112:115], v232 offset:8192\n"
  "v_and_b32 v125, 0xfff8fff8, v125\n"
  "v_mfma_f32_16x16x32_f16 a[104:107], v[80:83], v[120:123], a[104:107]\n"
  "s_add_i32 m0, s47, 16384\n"
  "ds_read_b64_tr_b16 v[28:29], v231 offset:256\n"
  "v_and_b32 v126, 0xfff8fff8, v126\n"
  "v_mfma_f32_16x16x32_f16 a[116:119], v[84:87], v[120:123], a[116:119]\n"
  "s_waitcnt lgkmcnt(8)\n"
  "s_add_i32 s64, s50, 4194304\n"
  "buffer_load_dwordx4 v238, s[40:43], s64 offen sc1 lds\n"
  "v_mfma_f32_16x16x32_bf16 v[152:155], v[136:139], v[176:179], 0\n"
  "s_waitcnt lgkmcnt(5)\n"
  "ds_read_b128 v[100:103], v232 offset:512\n"
  "v_and_b32 v127, 0xfff8fff8, v127\n"
  "v_mfma_f32_16x16x32_f16 a[128:131], v[88:91], v[120:123], a[128:131]\n"
  "s_add_i32 m0, s47, 20480\n"
  "ds_read_b128 v[116:119], v232 offset:8704\n"
  "v_cvt_pkrtz_f16_f32 v128, v148, v149\n"
  "v_mfma_f32_16x16x32_f16 a[140:143], v[92:95], v[120:123], a[140:143]\n"
  "s_add_i32 s64, s50, 4096\n"
  "buffer_load_dwordx4 v238, s[40:43], s64 offen sc1 lds\n"
  "v_cvt_pkrtz_f16_f32 v129, v150, v151\n"
  "v_mfma_f32_16x16x32_f16 a[144:147], v[48:51], v[104:107], a[144:147]\n"
  "ds_read_b64_tr_b16 v[30:31], v231 offset:8448\n"
  "v_pk_add_f16 v120, v104, v108\n"
  "v_mfma_f32_16x16x32_f16 a[156:159], v[52:55], v[104:107], a[156:159]\n"
  "s_add_i32 m0, s48, 24576\n"
  "v_pk_add_f16 v121, v105, v109\n"
  "v_pk_add_f16 v122, v106, v110\n"
  "v_mfma_f32_16x16x32_f16 a[168:171], v[56:59], v[104:107], a[168:171]\n"
  "s_add_i32 s64, s50, 16384\n"
  "buffer_load_dwordx4 v238, s[40:43], s64 offen sc1 lds\n"
  "v_pk_add_f16 v123, v107, v111\n"
  "v_mfma_f32_16x16x32_f16 a[180:183], v[60:63], v[104:107], a[180:183]\n"
  "ds_read_b128 v[104:107], v232 offset:2048\n"
  "v_cvt_pkrtz_f16_f32 v130, v152, v153\n"
  "v_mfma_f32_16x16x32_f16 a[148:151], v[64:67], v[108:111], a[148:151]\n"
  "v_cvt_pkrtz_f16_f32 v131, v154, v155\n"
  "v_and_b32 v128, 0xfff8fff8, v128\n"
  "v_mfma_f32_16x16x32_f16 a[160:163], v[68:71], v[108:111], a[160:163]\n"
  "buffer_store_dwordx4 v[124:127], v242, s[40:43], s56 offen offset:256 sc1\n"
  "v_and_b32 v129, 0xfff8fff8, v129\n"
  "v_mfma_f32_16x16x32_f16 a[172:175], v[72:75], v[108:111], a[172:175]\n"
  "v_and_b32 v130, 0xfff8fff8, v130\n"
  "v_and_b32 v131, 0xfff8fff8, v131\n"
  "v_mfma_f32_16x16x32_f16 a[184:187], v[76:79], v[108:111], a[184:187]\n"
  "s_add_i32 m0, s47, 26624\n"
  "s_waitcnt lgkmcnt(1)\n"
  "ds_read_b128 v[108:111], v232 offset:2560\n"
  "v_mfma_f32_16x16x32_f16 a[152:155], v[80:83], v[120:123], a[152:155]\n"
  "s_add_i32 s64, s50, 4214784\n"
  "buffer_load_dwordx4 v238, s[40:43], s64 offen sc1 lds\n"
  "v_pk_add_f16 v32, v0, v16\n"
  "v_mfma_f32_16x16x32_f16 a[164:167], v[84:87], v[120:123], a[164:167]\n"
  "v_pk_add_f16 v33, v1, v17\n"
  "v_pk_add_f16 v34, v2, v18\n"
  "v_mfma_f32_16x16x32_f16 a[176:179], v[88:91], v[120:123], a[176:179]\n"
  "s_add_i32 m0, s47, 30720\n"
  "v_pk_add_f16 v35, v3, v19\n"
  "v_pk_add_f16 v36, v4, v20\n"
  "v_mfma_f32_16x16x32_f16 a[188:191], v[92:95], v[120:123], a[188:191]\n"
  "s_add_i32 s64, s50, 32768\n"
  "buffer_load_dwordx4 v238, s[40:43], s64 offen sc1 lds\n"
  "v_pk_add_f16 v37, v5, v21\n"
  "v_mfma_f32_16x16x32_f16 a[192:195], v[48:51], v[112:115], a[192:195]\n"
  "s_add_i32 s47, s44, s46\n"
  "v_pk_add_f16 v120, v112, v116\n"
  "v_pk_add_f16 v121, v113, v117\n"
  "v_mfma_f32_16x16x32_f16 a[204:207], v[52:55], v[112:115], a[204:207]\n"
  "s_add_i32 s48, s47, s45\n"
  "v_pk_add_f16 v122, v114, v118\n"
  "v_pk_add_f16 v123, v115, v119\n"
  "v_mfma_f32_16x16x32_f16 a[216:219], v[56:59], v[112:115], a[216:219]\n"
  "s_add_i32 s46, s46, s49\n"
  "buffer_store_dwordx4 v[128:131], v242, s[40:43], s56 offen offset:384 sc1\n"
  "v_pk_add_f16 v38, v6, v22\n"
  "v_mfma_f32_16x16x32_f16 a[228:231], v[60:63], v[112:115], a[228:231]\n"
  "s_cmp_lt_u32 s57, 0x80000\n"
  "s_cselect_b32 s58, s57, 0xf0000000\n"
  "s_cselect_b32 s59, s59, 0xf0000000\n"
  "v_mfma_f32_16x16x32_f16 a[196:199], v[64:67], v[116:119], a[196:199]\n"
  "s_add_i32 s55, s55, 8404992\n"
  "v_pk_add_f16 v39, v7, v23\n"
  "v_pk_add_f16 v40, v8, v24\n"
  "v_mfma_f32_16x16x32_f16 a[208:211], v[68:71], v[116:119], a[208:211]\n"
  "s_add_i32 s50, s50, 4194304\n"
  "v_pk_add_f16 v41, v9, v25\n"
  "v_pk_add_f16 v42, v10, v26\n"
  "v_mfma_f32_16x16x32_f16 a[220:223], v[72:75], v[116:119], a[220:223]\n"
  "s_and_b32 s56, s55, 0xff80c000\n"
  "v_pk_add_f16 v43, v11, v27\n"
  "v_pk_add_f16 v44, v12, v28\n"
  "v_mfma_f32_16x16x32_f16 a[232:235], v[76:79], v[116:119], a[232:235]\n"
  "v_pk_add_f16 v45, v13, v29\n"
  "v_pk_add_f16 v46, v14, v30\n"
  "v_mfma_f32_16x16x32_f16 a[200:203], v[80:83], v[120:123], a[200:203]\n"
  "v_pk_add_f16 v47, v15, v31\n"
  "v_add_u32 v229, s49, v229\n"
  "s_waitcnt vmcnt(16) lgkmcnt(0)\n"
  "v_mfma_f32_16x16x32_f16 a[212:215], v[84:87], v[120:123], a[212:215]\n"
  "s_cmp_gt_u32 s62, 88\n"
  "s_cbranch_scc1 .Lpace_s1_None_%=\n"
  ".Lpaced_s1_None_%=:\n"
  "v_mfma_f32_16x16x32_f16 a[224:227], v[88:91], v[120:123], a[224:227]\n"
  "v_add_u32 v230, s49, v230\n"
  "v_add_u32 v231, s49, v231\n"
  "s_barrier\n"
  "v_mfma_f32_16x16x32_f16 a[236:239], v[92:95], v[120:123], a[236:239]\n"
  "v_add_u32 v232, s49, v232\n"
  "s_mov_b32 s63, 10\n"
  ".p2align 6\n"
  ".LloopP_%=:\n"
  "v_mfma_f32_16x16x32_f16 a[0:3], v[0:3], v[96:99], a[0:3]\n"
  "buffer_load_dword v247, v246, s[40:43], s54 offen sc1\n"
  "v_pk_add_f16 v120, v96, v100\n"
  "v_mfma_f32_16x16x32_f16 a[12:15], v[4:7], v[96:99], a[12:15]\n"
  "v_pk_add_f16 v121, v97, v101\n"
  "v_pk_add_f16 v122, v98, v102\n"
  "v_mfma_f32_16x16x32_f16 a[24:27], v[8:11], v[96:99], a[24:27]\n"
  "s_min_u32 s67, s60, 11\n"
  "s_lshl_b32 s67, s67, 2\n"
  "v_pk_add_f16 v123, v99, v103\n"
  "v_mfma_f32_16x16x32_f16 a[36:39], v[12:15], v[96:99], a[36:39]\n"
  "ds_read_b64_tr_b16 v[48:49], v228 offset:0\n"
  "v_readlane_b32 s66, v244, s60\n"
  "v_mfma_f32_16x16x32_f16 a[4:7], v[16:19], v[100:103], a[4:7]\n"
  "s_cmp_eq_u32 s46, 73728\n"
  "s_cselect_b32 s49, s68, s69\n"
  "buffer_load_dwordx4 v[168:171], v240, s[40:43], s59 offen sc1\n"
  "v_mfma_f32_16x16x32_f16 a[16:19], v[20:23], v[100:103], a[16:19]\n"
  "ds_read_b64_tr_b16 v[124:125], v233 offset:0\n"
  "ds_read_b64_tr_b16 v[128:129], v234 offset:0\n"
  "v_mfma_f32_16x16x32_f16 a[28:31], v[24:27], v[100:103], a[28:31]\n"
  "ds_read_b64_tr_b16 v[50:51], v228 offset:8192\n"
  "ds_read_b128 v[96:99], v232 offset:4096\n"
  "v_mfma_f32_16x16x32_f16 a[40:43], v[28:31], v[100:103], a[40:43]\n"
  "s_add_i32 m0, s44, 110592\n"
  "ds_read_b64_tr_b16 v[160:161], v241 offset:45056\n"
  "ds_read_b64_tr_b16 v[162:163], v241 offset:45568\n"
  "v_mfma_f32_16x16x32_f16 a[8:11], v[32:35], v[120:123], a[8:11]\n"
  "buffer_load_dwordx4 v239, s[40:43], s58 offen sc1 lds\n"
  "ds_read_b64_tr_b16 v[132:133], v235 offset:0\n"
  "v_mfma_f32_16x16x32_f16 a[20:23], v[36:39], v[120:123], a[20:23]\n"
  "s_add_i32 s57, s57, 32768\n"
  "ds_read_b64_tr_b16 v[64:65], v228 offset:256\n"
  "ds_read_b128 v[100:103], v232 offset:4608\n"
  "v_mfma_f32_16x16x32_f16 a[32:35], v[40:43], v[120:123], a[32:35]\n"
  "s_lshr_b32 s59, s57, 9\n"
  "s_add_i32 s60, s60, 1\n"
  "ds_read_b64_tr_b16 v[136:137], v236 offset:0\n"
  "v_mfma_f32_16x16x32_f16 a[44:47], v[44:47], v[120:123], a[44:47]\n"
  "s_add_i32 m0, s47, 0\n"
  "ds_read_b64_tr_b16 v[66:67], v228 offset:8448\n"
  "v_add_u32 v228, s49, v228\n"
  "v_mfma_f32_16x16x32_f16 a[48:51], v[0:3], v[104:107], a[48:51]\n"
  "s_add_i32 s64, s50, 4194304\n"
  "buffer_load_dwordx4 v237, s[40:43], s64 offen sc1 lds\n"
  "v_pk_add_f16 v120, v104, v108\n"
  "v_mfma_f32_16x16x32_f16 a[60:63], v[4:7], v[104:107], a[60:63]\n"
  "s_set_gpr_idx_on s67, gpr_idx(SRC0)\n"
  "v_mov_b64 v[156:157], v[180:181]\n"
  "v_mov_b64 v[158:159], v[182:183]\n"
  "s_set_gpr_idx_off\n"
  "v_mfma_f32_16x16x32_f16 a[72:75], v[8:11], v[104:107], a[72:75]\n"
  "s_waitcnt lgkmcnt(5)\n"
  "ds_read_b64_tr_b16 v[52:53], v229 offset:0\n"
  "v_pk_add_f16 v121, v105, v109\n"
  "v_mfma_f32_16x16x32_f16 a[84:87], v[12:15], v[104:107], a[84:87]\n"
  "v_pk_add_f16 v122, v106, v110\n"
  "v_pk_add_f16 v123, v107, v111\n"
  "v_mfma_f32_16x16x32_f16 a[52:55], v[16:19], v[108:111], a[52:55]\n"
  "buffer_store_dwordx4 v[156:159], v243, s[40:43], s66 offen sc1\n"
  "ds_read_b64_tr_b16 v[54:55], v229 offset:8192\n"
  "v_mfma_f32_16x16x32_f16 a[64:67], v[20:23], v[108:111], a[64:67]\n"
  "s_waitcnt lgkmcnt(3)\n"
  "ds_read_b64_tr_b16 v[68:69], v229 offset:256\n"
  "v_mov_b64 v[126:127], v[124:125]\n"
  "v_mfma_f32_16x16x32_f16 a[76:79], v[24:27], v[108:111], a[76:79]\n"
  "s_waitcnt lgkmcnt(3)\n"
  "ds_read_b64_tr_b16 v[70:71], v229 offset:8448\n"
  "v_mov_b64 v[130:131], v[128:129]\n"
  "v_mfma_f32_16x16x32_bf16 v[172:175], v[160:163], v[164:167], v[172:175]\n"
  "s_add_i32 m0, s47, 4096\n"
  "ds_read_b128 v[104:107], v232 offset:6144\n"
  "v_mov_b64 v[134:135], v[132:133]\n"
  "v_mfma_f32_16x16x32_f16 a[88:91], v[28:31], v[108:111], a[88:91]\n"
  "s_add_i32 s64, s50, 4198400\n"
  "buffer_load_dwordx4 v237, s[40:43], s64 offen sc1 lds\n"
  "v_mov_b64 v[138:139], v[136:137]\n"
  "v_mfma_f32_16x16x32_f16 a[56:59], v[32:35], v[120:123], a[56:59]\n"
  "ds_read_b64_tr_b16 v[56:57], v230 offset:0\n"
  "v_pk_add_f16 v80, v48, v64\n"
  "v_mfma_f32_16x16x32_f16 a[68:71], v[36:39], v[120:123], a[68:71]\n"
  "ds_read_b128 v[108:111], v232 offset:6656\n"
  "v_pk_add_f16 v81, v49, v65\n"
  "v_mfma_f32_16x16x32_f16 a[80:83], v[40:43], v[120:123], a[80:83]\n"
  "s_add_i32 m0, s47, 8192\n"
  "ds_read_b64_tr_b16 v[58:59], v230 offset:8192\n"
  "v_pk_add_f16 v82, v50, v66\n"
  "v_mfma_f32_16x16x32_f16 a[92:95], v[44:47], v[120:123], a[92:95]\n"
  "s_add_i32 s64, s50, 4202496\n"
  "buffer_load_dwordx4 v237, s[40:43], s64 offen sc1 lds\n"
  "v_pk_add_f16 v83, v51, v67\n"
  "v_mfma_f32_16x16x32_f16 a[96:99], v[0:3], v[96:99], a[96:99]\n"
  "s_waitcnt lgkmcnt(4)\n"
  "v_pk_add_f16 v120, v96, v100\n"
  "v_pk_add_f16 v121, v97, v101\n"
  "v_mfma_f32_16x16x32_bf16 v[140:143], v[124:127], v[176:179], 0\n"
  "ds_read_b64_tr_b16 v[72:73], v230 offset:256\n"
  "v_pk_add_f16 v84, v52, v68\n"
  "v_mfma_f32_16x16x32_f16 a[108:111], v[4:7], v[96:99], a[108:111]\n"
  "s_add_i32 m0, s47, 12288\n"
  "v_pk_add_f16 v122, v98, v102\n"
  "v_pk_add_f16 v123, v99, v103\n"
  "v_mfma_f32_16x16x32_f16 a[120:123], v[8:11], v[96:99], a[120:123]\n"
  "s_add_i32 s64, s50, 4206592\n"
  "buffer_load_dwordx4 v237, s[40:43], s64 offen sc1 lds\n"
  "v_pk_add_f16 v85, v53, v69\n"
  "v_mfma_f32_16x16x32_f16 a[132:135], v[12:15], v[96:99], a[132:135]\n"
  "ds_read_b64_tr_b16 v[74:75], v230 offset:8448\n"
  "v_cvt_pkrtz_f16_f32 v124, v140, v141\n"
  "v_mfma_f32_16x16x32_bf16 v[144:147], v[128:131], v[176:179], 0\n"
  "ds_read_b64_tr_b16 v[60:61], v231 offset:0\n"
  "v_cvt_pkrtz_f16_f32 v125, v142, v143\n"
  "v_mfma_f32_16x16x32_f16 a[100:103], v[16:19], v[100:103], a[100:103]\n"
  "s_add_i32 m0, s47, 16384\n"
  "ds_read_b64_tr_b16 v[62:63], v231 offset:8192\n"
  "v_and_b32 v124, 0xfff8fff8, v124\n"
  "v_mfma_f32_16x16x32_f16 a[112:115], v[20:23], v[100:103], a[112:115]\n"
  "s_add_i32 s64, s50, 4194304\n"
  "buffer_load_dwordx4 v238, s[40:43], s64 offen sc1 lds\n"
  "v_and_b32 v125, 0xfff8fff8, v125\n"
  "v_mfma_f32_16x16x32_f16 a[124:127], v[24:27], v[100:103], a[124:127]\n"
  "ds_read_b128 v[96:99], v232 offset:0\n"
  "v_cvt_pkrtz_f16_f32 v126, v144, v145\n"
  "v_mfma_f32_16x16x32_bf16 v[148:151], v[132:135], v[176:179], 0\n"
  "ds_read_b128 v[112:115], v232 offset:8192\n"
  "v_cvt_pkrtz_f16_f32 v127, v146, v147\n"
  "v_mfma_f32_16x16x32_f16 a[136:139], v[28:31], v[100:103], a[136:139]\n"
  "s_add_i32 m0, s47, 20480\n"
  "ds_read_b64_tr_b16 v[76:77], v231 offset:256\n"
  "v_and_b32 v126, 0xfff8fff8, v126\n"
  "v_mfma_f32_16x16x32_f16 a[104:107], v[32:35], v[120:123], a[104:107]\n"
  "s_add_i32 s64, s50, 4096\n"
  "buffer_load_dwordx4 v238, s[40:43], s64 offen sc1 lds\n"
  "v_and_b32 v127, 0xfff8fff8, v127\n"
  "v_mfma_f32_16x16x32_f16 a[116:119], v[36:39], v[120:123], a[116:119]\n"
  "s_waitcnt lgkmcnt(8)\n"
  "ds_read_b128 v[100:103], v232 offset:512\n"
  "v_cvt_pkrtz_f16_f32 v128, v148, v149\n"
  "v_mfma_f32_16x16x32_bf16 v[152:155], v[136:139], v[176:179], 0\n"
  "s_waitcnt lgkmcnt(6)\n"
  "ds_read_b128 v[116:119], v232 offset:8704\n"
  "v_cvt_pkrtz_f16_f32 v129, v150, v151\n"
  "v_mfma_f32_16x16x32_f16 a[128:131], v[40:43], v[120:123], a[128:131]\n"
  "s_add_i32 m0, s48, 24576\n"
  "ds_read_b64_tr_b16 v[78:79], v231 offset:8448\n"
  "v_and_b32 v128, 0xfff8fff8, v128\n"
  "v_mfma_f32_16x16x32_f16 a[140:143], v[44:47], v[120:123], a[140:143]\n"
  "s_add_i32 s64, s50, 16384\n"
  "buffer_load_dwordx4 v238, s[40:43], s64 offen sc1 lds\n"
  "v_and_b32 v129, 0xfff8fff8, v129\n"
  "v_mfma_f32_16x16x32_f16 a[144:147], v[0:3], v[104:107], a[144:147]\n"
  "v_pk_add_f16 v120, v104, v108\n"
  "v_pk_add_f16 v121, v105, v109\n"
  "v_mfma_f32_16x16x32_f16 a[156:159], v[4:7], v[104:107], a[156:159]\n"
  "v_pk_add_f16 v122, v106, v110\n"
  "v_pk_add_f16 v123, v107, v111\n"
  "v_mfma_f32_16x16x32_f16 a[168:171], v[8:11], v[104:107], a[168:171]\n"
  "v_cvt_pkrtz_f16_f32 v130, v152, v153\n"
  "v_cvt_pkrtz_f16_f32 v131, v154, v155\n"
  "v_mfma_f32_16x16x32_f16 a[180:183], v[12:15], v[104:107], a[180:183]\n"
  "buffer_store_dwordx4 v[124:127], v242, s[40:43], s56 offen offset:0 sc1\n"
  "v_and_b32 v130, 0xfff8fff8, v130\n"
  "v_mfma_f32_16x16x32_f16 a[148:151], v[16:19], v[108:111], a[148:151]\n"
  "ds_read_b128 v[104:107], v232 offset:2048\n"
  "v_and_b32 v131, 0xfff8fff8, v131\n"
  "v_mfma_f32_16x16x32_f16 a[160:163], v[20:23], v[108:111], a[160:163]\n"
  "s_cmp_lt_u32 s57, 0x80000\n"
  "s_cselect_b32 s58, s57, 0xf0000000\n"
  "s_cselect_b32 s59, s59, 0xf0000000\n"
  "v_mfma_f32_16x16x32_f16 a[172:175], v[24:27], v[108:111], a[172:175]\n"
  "s_add_i32 m0, s47, 26624\n"
  "s_waitcnt lgkmcnt(1)\n"
  "v_pk_add_f16 v86, v54, v70\n"
  "v_mfma_f32_16x16x32_f16 a[184:187], v[28:31], v[108:111], a[184:187]\n"
  "s_add_i32 s64, s50, 4214784\n"
  "buffer_load_dwordx4 v238, s[40:43], s64 offen sc1 lds\n"
  "v_pk_add_f16 v87, v55, v71\n"
  "v_mfma_f32_16x16x32_f16 a[152:155], v[32:35], v[120:123], a[152:155]\n"
  "ds_read_b128 v[108:111], v232 offset:2560\n"
  "v_pk_add_f16 v88, v56, v72\n"
  "v_mfma_f32_16x16x32_f16 a[164:167], v[36:39], v[120:123], a[164:167]\n"
  "v_pk_add_f16 v89, v57, v73\n"
  "v_pk_add_f16 v90, v58, v74\n"
  "v_mfma_f32_16x16x32_f16 a[176:179], v[40:43], v[120:123], a[176:179]\n"
  "s_add_i32 m0, s47, 30720\n"
  "v_pk_add_f16 v91, v59, v75\n"
  "v_pk_add_f16 v92, v60, v76\n"
  "v_mfma_f32_16x16x32_f16 a[188:191], v[44:47], v[120:123], a[188:191]\n"
  "s_add_i32 s64, s50, 32768\n"
  "buffer_load_dwordx4 v238, s[40:43], s64 offen sc1 lds\n"
  "v_pk_add_f16 v93, v61, v77\n"
  "v_mfma_f32_16x16x32_f16 a[192:195], v[0:3], v[112:115], a[192:195]\n"
  "s_add_i32 s47, s44, s46\n"
  "v_pk_add_f16 v120, v112, v116\n"
  "v_pk_add_f16 v121, v113, v117\n"
  "v_mfma_f32_16x16x32_f16 a[204:207], v[4:7], v[112:115], a[204:207]\n"
  "s_add_i32 s48, s47, s45\n"
  "v_pk_add_f16 v122, v114, v118\n"
  "v_pk_add_f16 v123, v115, v119\n"
  "v_mfma_f32_16x16x32_f16 a[216:219], v[8:11], v[112:115], a[216:219]\n"
  "s_add_i32 s46, s46, s49\n"
  "v_pk_add_f16 v94, v62, v78\n"
  "v_pk_add_f16 v95, v63, v79\n"
  "v_mfma_f32_16x16x32_f16 a[228:231], v[12:15], v[112:115], a[228:231]\n"
  "s_add_i32 s50, s50, 4194304\n"
  "buffer_store_dwordx4 v[128:131], v242, s[40:43], s56 offen offset:128 sc1\n"
  "v_add_u32 v229, s49, v229\n"
  "v_mfma_f32_16x16x32_f16 a[196:199], v[16:19], v[116:119], a[196:199]\n"
  "v_add_u32 v230, s49, v230\n"
  "v_add_u32 v231, s49, v231\n"
  "v_mfma_f32_16x16x32_f16 a[208:211], v[20:23], v[116:119], a[208:211]\n"
  "v_add_u32 v232, s49, v232\n"
  "v_mfma_f32_16x16x32_f16 a[220:223], v[24:27], v[116:119], a[220:223]\n"
  "v_mfma_f32_16x16x32_f16 a[232:235], v[28:31], v[116:119], a[232:235]\n"
  "v_mfma_f32_16x16x32_f16 a[200:203], v[32:35], v[120:123], a[200:203]\n"
  "s_waitcnt vmcnt(12) lgkmcnt(0)\n"
  "v_mfma_f32_16x16x32_f16 a[212:215], v[36:39], v[120:123], a[212:215]\n"
  "v_cmp_eq_u32 vcc, 0, v247\n"
  "s_cbranch_vccnz .Lspin_pe_None_%=\n"
  ".Lspun_pe_None_%=:\n"
  "s_cmp_gt_u32 s62, 88\n"
  "s_cbranch_scc1 .Lpace_pe_None_%=\n"
  ".Lpaced_pe_None_%=:\n"
  "v_mfma_f32_16x16x32_f16 a[224:227], v[40:43], v[120:123], a[224:227]\n"
  "s_barrier\n"
  "v_mfma_f32_16x16x32_f16 a[236:239], v[44:47], v[120:123], a[236:239]\n"
  "v_mfma_f32_16x16x32_f16 a[0:3], v[48:51], v[96:99], a[0:3]\n"
  "buffer_load_dwordx4 v[164:167], v240, s[40:43], s59 offen sc1\n"
  "v_pk_add_f16 v120, v96, v100\n"
  "v_mfma_f32_16x16x32_f16 a[12:15], v[52:55], v[96:99], a[12:15]\n"
  "v_pk_add_f16 v121, v97, v101\n"
  "v_pk_add_f16 v122, v98, v102\n"
  "v_mfma_f32_16x16x32_f16 a[24:27], v[56:59], v[96:99], a[24:27]\n"
  "s_min_u32 s67, s60, 11\n"
  "s_lshl_b32 s67, s67, 2\n"
  "v_pk_add_f16 v123, v99, v103\n"
  "v_mfma_f32_16x16x32_f16 a[36:39], v[60:63], v[96:99], a[36:39]\n"
  "s_add_i32 m0, s44, 155648\n"
  "s_and_b32 s65, s51, 0xffffe300\n"
  "ds_read_b64_tr_b16 v[0:1], v228 offset:0\n"
  "v_mfma_f32_16x16x32_f16 a[4:7], v[64:67], v[100:103], a[4:7]\n"
  "buffer_load_dwordx4 v239, s[40:43], s58 offen sc1 lds\n"
  "v_readlane_b32 s66, v244, s60\n"
  "v_mfma_f32_16x16x32_f16 a[16:19], v[68:71], v[100:103], a[16:19]\n"
  "s_cmp_eq_u32 s46, 73728\n"
  "s_cselect_b32 s49, s68, s69\n"
  "ds_read_b64_tr_b16 v[124:125], v233 offset:2048\n"
  "v_mfma_f32_16x16x32_f16 a[28:31], v[72:75], v[100:103], a[28:31]\n"
  "s_add_i32 s57, s57, 32768\n"
  "ds_read_b64_tr_b16 v[128:129], v234 offset:2048\n"
  "v_add_u32 v233, 4096, v233\n"
  "v_mfma_f32_16x16x32_f16 a[40:43], v[76:79], v[100:103], a[40:43]\n"
  "s_add_i32 m0, s47, 0\n"
  "s_lshr_b32 s59, s57, 9\n"
  "ds_read_b64_tr_b16 v[2:3], v228 offset:8192\n"
  "v_mfma_f32_16x16x32_f16 a[8:11], v[80:83], v[120:123], a[8:11]\n"
  "s_add_i32 s64, s50, 4194304\n"
  "buffer_load_dwordx4 v237, s[40:43], s64 offen sc1 lds\n"
  "v_add_u32 v234, 4096, v234\n"
  "v_mfma_f32_16x16x32_f16 a[20:23], v[84:87], v[120:123], a[20:23]\n"
  "ds_read_b128 v[96:99], v232 offset:4096\n"
  "ds_read_b64_tr_b16 v[160:161], v241 offset:0\n"
  "v_mfma_f32_16x16x32_f16 a[32:35], v[88:91], v[120:123], a[32:35]\n"
  "s_add_i32 s53, s53, 8192\n"
  "s_lshr_b32 s52, s52, 1\n"
  "ds_read_b64_tr_b16 v[162:163], v241 offset:512\n"
  "v_mfma_f32_16x16x32_f16 a[44:47], v[92:95], v[120:123], a[44:47]\n"
  "s_add_i32 s60, s60, 1\n"
  "ds_read_b64_tr_b16 v[132:133], v235 offset:2048\n"
  "v_add_u32 v235, 4096, v235\n"
  "v_mfma_f32_16x16x32_f16 a[48:51], v[48:51], v[104:107], a[48:51]\n"
  "s_add_i32 s51, s51, 8448\n"
  "buffer_store_dword v232, v245, s[40:43], s65 offen sc1\n"
  "ds_read_b64_tr_b16 v[16:17], v228 offset:256\n"
  "v_mfma_f32_16x16x32_f16 a[60:63], v[52:55], v[104:107], a[60:63]\n"
  "ds_read_b128 v[100:103], v232 offset:4608\n"
  "v_pk_add_f16 v120, v104, v108\n"
  "v_mfma_f32_16x16x32_f16 a[72:75], v[56:59], v[104:107], a[72:75]\n"
  "ds_read_b64_tr_b16 v[136:137], v236 offset:2048\n"
  "ds_read_b64_tr_b16 v[18:19], v228 offset:8448\n"
  "s_set_gpr_idx_on s67, gpr_idx(SRC0)\n"
  "v_mov_b64 v[156:157], v[180:181]\n"
  "v_mov_b64 v[158:159], v[182:183]\n"
  "s_set_gpr_idx_off\n"
  "v_mfma_f32_16x16x32_f16 a[84:87], v[60:63], v[104:107], a[84:87]\n"
  "ds_read_b64_tr_b16 v[4:5], v229 offset:0\n"
  "v_pk_add_f16 v121, v105, v109\n"
  "v_mfma_f32_16x16x32_f16 a[52:55], v[64:67], v[108:111], a[52:55]\n"
  "buffer_store_dwordx4 v[156:159], v243, s[40:43], s66 offen sc1\n"
  "v_pk_add_f16 v122, v106, v110\n"
  "v_mfma_f32_16x16x32_f16 a[64:67], v[68:71], v[108:111], a[64:67]\n"
  "s_waitcnt lgkmcnt(6)\n"
  "ds_read_b64_tr_b16 v[6:7], v229 offset:8192\n"
  "v_pk_add_f16 v123, v107, v111\n"
  "v_mfma_f32_16x16x32_f16 a[76:79], v[72:75], v[108:111], a[76:79]\n"
  "s_bitcmp1_b32 s52, 0\n"
  "s_cselect_b32 s54, s53, 134217728\n"
  "ds_read_b64_tr_b16 v[20:21], v229 offset:256\n"
  "v_mfma_f32_16x16x32_bf16 v[172:175], v[160:163], v[168:171], v[172:175]\n"
  "s_add_i32 m0, s47, 4096\n"
  "ds_read_b64_tr_b16 v[22:23], v229 offset:8448\n"
  "v_add_u32 v228, s49, v228\n"
  "v_mfma_f32_16x16x32_f16 a[88:91], v[76:79], v[108:111], a[88:91]\n"
  "s_add_i32 s64, s50, 4198400\n"
  "buffer_load_dwordx4 v237, s[40:43], s64 offen sc1 lds\n"
  "v_add_u32 v229, s49, v229\n"
  "v_mfma_f32_16x16x32_f16 a[56:59], v[80:83], v[120:123], a[56:59]\n"
  "s_waitcnt lgkmcnt(5)\n"
  "ds_read_b128 v[104:107], v232 offset:6144\n"
  "v_mov_b64 v[126:127], v[124:125]\n"
  "v_mfma_f32_16x16x32_f16 a[68:71], v[84:87], v[120:123], a[68:71]\n"
  "ds_read_b64_tr_b16 v[8:9], v230 offset:0\n"
  "v_mov_b64 v[130:131], v[128:129]\n"
  "v_mfma_f32_16x16x32_f16 a[80:83], v[88:91], v[120:123], a[80:83]\n"
  "s_add_i32 m0, s47, 8192\n"
  "ds_read_b128 v[108:111], v232 offset:6656\n"
  "v_mov_b64 v[134:135], v[132:133]\n"
  "v_mfma_f32_16x16x32_f16 a[92:95], v[92:95], v[120:123], a[92:95]\n"
  "s_add_i32 s64, s50, 4202496\n"
  "buffer_load_dwordx4 v237, s[40:43], s64 offen sc1 lds\n"
  "v_mov_b64 v[138:139], v[136:137]\n"
  "v_mfma_f32_16x16x32_f16 a[96:99], v[48:51], v[96:99], a[96:99]\n"
  "s_waitcnt lgkmcnt(7)\n"
  "ds_read_b64_tr_b16 v[10:11], v230 offset:8192\n"
  "v_pk_add_f16 v120, v96, v100\n"
  "v_mfma_f32_16x16x32_bf16 v[140:143], v[124:127], v[176:179], 0\n"
  "s_waitcnt lgkmcnt(4)\n"
  "ds_read_b64_tr_b16 v[24:25], v230 offset:256\n"
  "v_pk_add_f16 v121, v97, v101\n"
  "v_mfma_f32_16x16x32_f16 a[108:111], v[52:55], v[96:99], a[108:111]\n"
  "s_add_i32 m0, s47, 12288\n"
  "v_pk_add_f16 v122, v98, v102\n"
  "v_pk_add_f16 v123, v99, v103\n"
  "v_mfma_f32_16x16x32_f16 a[120:123], v[56:59], v[96:99], a[120:123]\n"
  "s_add_i32 s64, s50, 4206592\n"
  "buffer_load_dwordx4 v237, s[40:43], s64 offen sc1 lds\n"
  "v_pk_add_f16 v32, v0, v16\n"
  "v_mfma_f32_16x16x32_f16 a[132:135], v[60:63], v[96:99], a[132:135]\n"
  "ds_read_b64_tr_b16 v[26:27], v230 offset:8448\n"
  "v_cvt_pkrtz_f16_f32 v124, v140, v141\n"
  "v_mfma_f32_16x16x32_bf16 v[144:147], v[128:131], v[176:179], 0\n"
  "ds_read_b64_tr_b16 v[12:13], v231 offset:0\n"
  "v_cvt_pkrtz_f16_f32 v125, v142, v143\n"
  "v_mfma_f32_16x16x32_f16 a[100:103], v[64:67], v[100:103], a[100:103]\n"
  "s_add_i32 m0, s47, 16384\n"
  "ds_read_b64_tr_b16 v[14:15], v231 offset:8192\n"
  "v_and_b32 v124, 0xfff8fff8, v124\n"
  "v_mfma_f32_16x16x32_f16 a[112:115], v[68:71], v[100:103], a[112:115]\n"
  "s_add_i32 s64, s50, 4194304\n"
  "buffer_load_dwordx4 v238, s[40:43], s64 offen sc1 lds\n"
  "v_and_b32 v125, 0xfff8fff8, v125\n"
  "v_mfma_f32_16x16x32_f16 a[124:127], v[72:75], v[100:103], a[124:127]\n"
  "ds_read_b128 v[96:99], v232 offset:0\n"
  "v_cvt_pkrtz_f16_f32 v126, v144, v145\n"
  "v_mfma_f32_16x16x32_bf16 v[148:151], v[132:135], v[176:179], 0\n"
  "ds_read_b128 v[112:115], v232 offset:8192\n"
  "v_cvt_pkrtz_f16_f32 v127, v146, v147\n"
  "v_mfma_f32_16x16x32_f16 a[136:139], v[76:79], v[100:103], a[136:139]\n"
  "s_add_i32 m0, s47, 20480\n"
  "ds_read_b64_tr_b16 v[28:29], v231 offset:256\n"
  "v_and_b32 v126, 0xfff8fff8, v126\n"
  "v_mfma_f32_16x16x32_f16 a[104:107], v[80:83], v[120:123], a[104:107]\n"
  "s_add_i32 s64, s50, 4096\n"
  "buffer_load_dwordx4 v238, s[40:43], s64 offen sc1 lds\n"
  "v_and_b32 v127, 0xfff8fff8, v127\n"
  "v_mfma_f32_16x16x32_f16 a[116:119], v[84:87], v[120:123], a[116:119]\n"
  "s_waitcnt lgkmcnt(8)\n"
  "ds_read_b128 v[100:103], v232 offset:512\n"
  "v_cvt_pkrtz_f16_f32 v128, v148, v149\n"
  "v_mfma_f32_16x16x32_bf16 v[152:155], v[136:139], v[176:179], 0\n"
  "s_waitcnt lgkmcnt(6)\n"
  "ds_read_b128 v[116:119], v232 offset:8704\n"
  "v_cvt_pkrtz_f16_f32 v129, v150, v151\n"
  "v_mfma_f32_16x16x32_f16 a[128:131], v[88:91], v[120:123], a[128:131]\n"
  "s_add_i32 m0, s48, 24576\n"
  "ds_read_b64_tr_b16 v[30:31], v231 offset:8448\n"
  "v_and_b32 v128, 0xfff8fff8, v128\n"
  "v_mfma_f32_16x16x32_f16 a[140:143], v[92:95], v[120:123], a[140:143]\n"
  "s_add_i32 s64, s50, 16384\n"
  "buffer_load_dwordx4 v238, s[40:43], s64 offen sc1 lds\n"
  "v_and_b32 v129, 0xfff8fff8, v129\n"
  "v_mfma_f32_16x16x32_f16 a[144:147], v[48:51], v[104:107], a[144:147]\n"
  "v_pk_add_f16 v120, v104, v108\n"
  "v_pk_add_f16 v121, v105, v109\n"
  "v_mfma_f32_16x16x32_f16 a[156:159], v[52:55], v[104:107], a[156:159]\n"
  "v_pk_add_f16 v122, v106, v110\n"
  "v_pk_add_f16 v123, v107, v111\n"
  "v_mfma_f32_16x16x32_f16 a[168:171], v[56:59], v[104:107], a[168:171]\n"
  "v_cvt_pkrtz_f16_f32 v130, v152, v153\n"
  "v_cvt_pkrtz_f16_f32 v131, v154, v155\n"
  "v_mfma_f32_16x16x32_f16 a[180:183], v[60:63], v[104:107], a[180:183]\n"
  "buffer_store_dwordx4 v[124:127], v242, s[40:43], s56 offen offset:256 sc1\n"
  "v_and_b32 v130, 0xfff8fff8, v130\n"
  "v_mfma_f32_16x16x32_f16 a[148:151], v[64:67], v[108:111], a[148:151]\n"
  "ds_read_b128 v[104:107], v232 offset:2048\n"
  "v_and_b32 v131, 0xfff8fff8, v131\n"
  "v_mfma_f32_16x16x32_f16 a[160:163], v[68:71], v[108:111], a[160:163]\n"
  "s_cmp_lt_u32 s57, 0x80000\n"
  "s_cselect_b32 s58, s57, 0xf0000000\n"
  "s_cselect_b32 s59, s59, 0xf0000000\n"
  "v_mfma_f32_16x16x32_f16 a[172:175], v[72:75], v[108:111], a[172:175]\n"
  "s_add_i32 m0, s47, 26624\n"
  "s_waitcnt lgkmcnt(1)\n"
  "v_pk_add_f16 v33, v1, v17\n"
  "v_mfma_f32_16x16x32_f16 a[184:187], v[76:79], v[108:111], a[184:187]\n"
  "s_add_i32 s64, s50, 4214784\n"
  "buffer_load_dwordx4 v238, s[40:43], s64 offen sc1 lds\n"
  "v_pk_add_f16 v34, v2, v18\n"
  "v_mfma_f32_16x16x32_f16 a[152:155], v[80:83], v[120:123], a[152:155]\n"
  "ds_read_b128 v[108:111], v232 offset:2560\n"
  "v_pk_add_f16 v35, v3, v19\n"
  "v_mfma_f32_16x16x32_f16 a[164:167], v[84:87], v[120:123], a[164:167]\n"
  "v_pk_add_f16 v36, v4, v20\n"
  "v_pk_add_f16 v37, v5, v21\n"
  "v_mfma_f32_16x16x32_f16 a[176:179], v[88:91], v[120:123], a[176:179]\n"
  "s_add_i32 m0, s47, 30720\n"
  "v_pk_add_f16 v38, v6, v22\n"
  "v_pk_add_f16 v39, v7, v23\n"
  "v_mfma_f32_16x16x32_f16 a[188:191], v[92:95], v[120:123], a[188:191]\n"
  "s_add_i32 s64, s50, 32768\n"
  "buffer_load_dwordx4 v238, s[40:43], s64 offen sc1 lds\n"
  "v_pk_add_f16 v40, v8, v24\n"
  "v_mfma_f32_16x16x32_f16 a[192:195], v[48:51], v[112:115], a[192:195]\n"
  "s_add_i32 s47, s44, s46\n"
  "v_pk_add_f16 v120, v112, v116\n"
  "v_pk_add_f16 v121, v113, v117\n"
  "v_mfma_f32_16x16x32_f16 a[204:207], v[52:55], v[112:115], a[204:207]\n"
  "s_add_i32 s48, s47, s45\n"
  "v_pk_add_f16 v122, v114, v118\n"
  "v_pk_add_f16 v123, v115, v119\n"
  "v_mfma_f32_16x16x32_f16 a[216:219], v[56:59], v[112:115], a[216:219]\n"
  "s_add_i32 s46, s46, s49\n"
  "v_pk_add_f16 v41, v9, v25\n"
  "v_pk_add_f16 v42, v10, v26\n"
  "v_mfma_f32_16x16x32_f16 a[228:231], v[60:63], v[112:115], a[228:231]\n"
  "s_add_i32 s50, s50, 4194304\n"
  "buffer_store_dwordx4 v[128:131], v242, s[40:43], s56 offen offset:384 sc1\n"
  "v_pk_add_f16 v43, v11, v27\n"
  "v_mfma_f32_16x16x32_f16 a[196:199], v[64:67], v[116:119], a[196:199]\n"
  "s_add_i32 s55, s55, 8404992\n"
  "v_pk_add_f16 v44, v12, v28\n"
  "v_pk_add_f16 v45, v13, v29\n"
  "v_mfma_f32_16x16x32_f16 a[208:211], v[68:71], v[116:119], a[208:211]\n"
  "s_and_b32 s56, s55, 0xff80c000\n"
  "v_pk_add_f16 v46, v14, v30\n"
  "v_pk_add_f16 v47, v15, v31\n"
  "v_mfma_f32_16x16x32_f16 a[220:223], v[72:75], v[116:119], a[220:223]\n"
  "v_add_u32 v230, s49, v230\n"
  "v_add_u32 v231, s49, v231\n"
  "v_mfma_f32_16x16x32_f16 a[232:235], v[76:79], v[116:119], a[232:235]\n"
  "v_add_u32 v232, s49, v232\n"
  "v_add_u32 v236, 4096, v236\n"
  "v_mfma_f32_16x16x32_f16 a[200:203], v[80:83], v[120:123], a[200:203]\n"
  "s_waitcnt vmcnt(13) lgkmcnt(0)\n"
  "v_mfma_f32_16x16x32_f16 a[212:215], v[84:87], v[120:123], a[212:215]\n"
  "s_cmp_gt_u32 s62, 88\n"
  "s_cbranch_scc1 .Lpace_po_None_%=\n"
  ".Lpaced_po_None_%=:\n"
  "v_mfma_f32_16x16x32_f16 a[224:227], v[88:91], v[120:123], a[224:227]\n"
  "s_barrier\n"
  "v_mfma_f32_16x16x32_f16 a[236:239], v[92:95], v[120:123], a[236:239]\n"
  "s_sub_i32 s63, s63, 1\n"
  "s_cmp_lg_u32 s63, 0\n"
  "s_cbranch_scc1 .LloopP_%=\n"
  ".Lcf_t:\n"
  "s_cmp_eq_u32 s85, 0\n"
  "s_cbranch_scc1 .Lnt_%=\n"
  "s_getpc_b64 s[86:87]\n"
  "s_mov_b32 m0, s85\n"
  "s_nop 0\n"
  "global_load_lds_dword v251, s[86:87]\n"
  ".Lnt_%=:\n"
  "v_mfma_f32_16x16x32_f16 a[0:3], v[0:3], v[96:99], a[0:3]\n"
  "buffer_load_dword v247, v246, s[40:43], s54 offen sc1\n"
  "v_pk_add_f16 v120, v96, v100\n"
  "v_mfma_f32_16x16x32_f16 a[12:15], v[4:7], v[96:99], a[12:15]\n"
  "v_pk_add_f16 v121, v97, v101\n"
  "v_pk_add_f16 v122, v98, v102\n"
  "v_mfma_f32_16x16x32_f16 a[24:27], v[8:11], v[96:99], a[24:27]\n"
  "ds_read_b64_tr_b16 v[48:49], v228 offset:0\n"
  "v_pk_add_f16 v123, v99, v103\n"
  "v_mfma_f32_16x16x32_f16 a[36:39], v[12:15], v[96:99], a[36:39]\n"
  "s_add_i32 m0, s47, 0\n"
  "ds_read_b64_tr_b16 v[50:51], v228 offset:8192\n"
  "v_accvgpr_read_b32 v124, a0\n"
  "v_mfma_f32_16x16x32_f16 a[4:7], v[16:19], v[100:103], a[4:7]\n"
  "s_add_i32 s64, s50, 4194304\n"
  "buffer_load_dwordx4 v237, s[40:43], s64 offen sc1 lds\n"
  "v_accvgpr_read_b32 v125, a1\n"
  "v_mfma_f32_16x16x32_f16 a[16:19], v[20:23], v[100:103], a[16:19]\n"
  "s_cmp_eq_u32 s46, 73728\n"
  "s_cselect_b32 s49, s68, s69\n"
  "ds_read_b128 v[96:99], v232 offset:4096\n"
  "v_mfma_f32_16x16x32_f16 a[28:31], v[24:27], v[100:103], a[28:31]\n"
  "ds_read_b64_tr_b16 v[64:65], v228 offset:256\n"
  "v_accvgpr_read_b32 v126, a2\n"
  "v_mfma_f32_16x16x32_f16 a[40:43], v[28:31], v[100:103], a[40:43]\n"
  "s_add_i32 m0, s47, 4096\n"
  "ds_read_b128 v[100:103], v232 offset:4608\n"
  "v_accvgpr_read_b32 v127, a3\n"
  "v_mfma_f32_16x16x32_f16 a[8:11], v[32:35], v[120:123], a[8:11]\n"
  "s_add_i32 s64, s50, 4198400\n"
  "buffer_load_dwordx4 v237, s[40:43], s64 offen sc1 lds\n"
  "v_accvgpr_read_b32 v128, a4\n"
  "v_mfma_f32_16x16x32_f16 a[20:23], v[36:39], v[120:123], a[20:23]\n"
  "ds_read_b64_tr_b16 v[66:67], v228 offset:8448\n"
  "v_accvgpr_read_b32 v129, a5\n"
  "v_mfma_f32_16x16x32_f16 a[32:35], v[40:43], v[120:123], a[32:35]\n"
  "ds_read_b64_tr_b16 v[52:53], v229 offset:0\n"
  "v_accvgpr_read_b32 v130, a6\n"
  "v_mfma_f32_16x16x32_f16 a[44:47], v[44:47], v[120:123], a[44:47]\n"
  "s_add_i32 m0, s47, 8192\n"
  "ds_read_b64_tr_b16 v[54:55], v229 offset:8192\n"
  "v_accvgpr_read_b32 v131, a7\n"
  "v_mfma_f32_16x16x32_f16 a[48:51], v[0:3], v[104:107], a[48:51]\n"
  "s_add_i32 s64, s50, 4202496\n"
  "buffer_load_dwordx4 v237, s[40:43], s64 offen sc1 lds\n"
  "v_pk_add_f16 v120, v104, v108\n"
  "v_mfma_f32_16x16x32_f16 a[60:63], v[4:7], v[104:107], a[60:63]\n"
  "v_pk_add_f16 v121, v105, v109\n"
  "v_pk_add_f16 v122, v106, v110\n"
  "v_mfma_f32_16x16x32_f16 a[72:75], v[8:11], v[104:107], a[72:75]\n"
  "ds_read_b64_tr_b16 v[68:69], v229 offset:256\n"
  "v_pk_add_f16 v123, v107, v111\n"
  "v_mfma_f32_16x16x32_f16 a[84:87], v[12:15], v[104:107], a[84:87]\n"
  "s_add_i32 m0, s47, 12288\n"
  "ds_read_b64_tr_b16 v[70:71], v229 offset:8448\n"
  "v_accvgpr_read_b32 v132, a8\n"
  "v_mfma_f32_16x16x32_f16 a[52:55], v[16:19], v[108:111], a[52:55]\n"
  "s_add_i32 s64, s50, 4206592\n"
  "buffer_load_dwordx4 v237, s[40:43], s64 offen sc1 lds\n"
  "v_accvgpr_read_b32 v133, a9\n"
  "v_mfma_f32_16x16x32_f16 a[64:67], v[20:23], v[108:111], a[64:67]\n"
  "s_waitcnt lgkmcnt(4)\n"
  "ds_read_b128 v[104:107], v232 offset:6144\n"
  "v_accvgpr_read_b32 v134, a10\n"
  "v_mfma_f32_16x16x32_f16 a[76:79], v[24:27], v[108:111], a[76:79]\n"
  "ds_read_b64_tr_b16 v[56:57], v230 offset:0\n"
  "v_accvgpr_read_b32 v135, a11\n"
  "v_mfma_f32_16x16x32_f16 a[88:91], v[28:31], v[108:111], a[88:91]\n"
  "s_add_i32 m0, s47, 16384\n"
  "ds_read_b128 v[108:111], v232 offset:6656\n"
  "v_accvgpr_read_b32 v136, a12\n"
  "v_mfma_f32_16x16x32_f16 a[56:59], v[32:35], v[120:123], a[56:59]\n"
  "s_add_i32 s64, s50, 4194304\n"
  "buffer_load_dwordx4 v238, s[40:43], s64 offen sc1 lds\n"
  "v_accvgpr_read_b32 v137, a13\n"
  "v_mfma_f32_16x16x32_f16 a[68:71], v[36:39], v[120:123], a[68:71]\n"
  "ds_read_b64_tr_b16 v[58:59], v230 offset:8192\n"
  "v_accvgpr_read_b32 v138, a14\n"
  "v_mfma_f32_16x16x32_f16 a[80:83], v[40:43], v[120:123], a[80:83]\n"
  "s_waitcnt lgkmcnt(4)\n"
  "ds_read_b64_tr_b16 v[72:73], v230 offset:256\n"
  "v_accvgpr_read_b32 v139, a15\n"
  "v_mfma_f32_16x16x32_f16 a[92:95], v[44:47], v[120:123], a[92:95]\n"
  "s_add_i32 m0, s47, 20480\n"
  "ds_read_b64_tr_b16 v[74:75], v230 offset:8448\n"
  "v_accvgpr_read_b32 v140, a16\n"
  "v_mfma_f32_16x16x32_f16 a[96:99], v[0:3], v[96:99], a[96:99]\n"
  "s_add_i32 s64, s50, 4096\n"
  "buffer_load_dwordx4 v238, s[40:43], s64 offen sc1 lds\n"
  "v_pk_add_f16 v120, v96, v100\n"
  "v_mfma_f32_16x16x32_f16 a[108:111], v[4:7], v[96:99], a[108:111]\n"
  "v_pk_add_f16 v121, v97, v101\n"
  "v_pk_add_f16 v122, v98, v102\n"
  "v_mfma_f32_16x16x32_f16 a[120:123], v[8:11], v[96:99], a[120:123]\n"
  "ds_read_b64_tr_b16 v[60:61], v231 offset:0\n"
  "v_pk_add_f16 v123, v99, v103\n"
  "v_mfma_f32_16x16x32_f16 a[132:135], v[12:15], v[96:99], a[132:135]\n"
  "s_add_i32 m0, s48, 24576\n"
  "ds_read_b64_tr_b16 v[62:63], v231 offset:8192\n"
  "v_accvgpr_read_b32 v141, a17\n"
  "v_mfma_f32_16x16x32_f16 a[100:103], v[16:19], v[100:103], a[100:103]\n"
  "s_add_i32 s64, s50, 16384\n"
  "buffer_load_dwordx4 v238, s[40:43], s64 offen sc1 lds\n"
  "v_accvgpr_read_b32 v142, a18\n"
  "v_mfma_f32_16x16x32_f16 a[112:115], v[20:23], v[100:103], a[112:115]\n"
  "ds_read_b128 v[96:99], v232 offset:0\n"
  "v_accvgpr_read_b32 v143, a19\n"
  "v_mfma_f32_16x16x32_f16 a[124:127], v[24:27], v[100:103], a[124:127]\n"
  "s_waitcnt lgkmcnt(3)\n"
  "ds_read_b128 v[112:115], v232 offset:8192\n"
  "v_accvgpr_read_b32 v144, a20\n"
  "v_mfma_f32_16x16x32_f16 a[136:139], v[28:31], v[100:103], a[136:139]\n"
  "s_add_i32 m0, s47, 26624\n"
  "ds_read_b64_tr_b16 v[76:77], v231 offset:256\n"
  "v_accvgpr_read_b32 v145, a21\n"
  "v_mfma_f32_16x16x32_f16 a[104:107], v[32:35], v[120:123], a[104:107]\n"
  "s_add_i32 s64, s50, 4214784\n"
  "buffer_load_dwordx4 v238, s[40:43], s64 offen sc1 lds\n"
  "v_accvgpr_read_b32 v146, a22\n"
  "v_mfma_f32_16x16x32_f16 a[116:119], v[36:39], v[120:123], a[116:119]\n"
  "ds_read_b128 v[100:103], v232 offset:512\n"
  "v_accvgpr_read_b32 v147, a23\n"
  "v_mfma_f32_16x16x32_f16 a[128:131], v[40:43], v[120:123], a[128:131]\n"
  "ds_read_b128 v[116:119], v232 offset:8704\n"
  "v_accvgpr_read_b32 v148, a24\n"
  "v_mfma_f32_16x16x32_f16 a[140:143], v[44:47], v[120:123], a[140:143]\n"
  "s_add_i32 m0, s47, 30720\n"
  "ds_read_b64_tr_b16 v[78:79], v231 offset:8448\n"
  "v_accvgpr_read_b32 v149, a25\n"
  "v_mfma_f32_16x16x32_f16 a[144:147], v[0:3], v[104:107], a[144:147]\n"
  "s_add_i32 s64, s50, 32768\n"
  "buffer_load_dwordx4 v238, s[40:43], s64 offen sc1 lds\n"
  "v_pk_add_f16 v120, v104, v108\n"
  "v_mfma_f32_16x16x32_f16 a[156:159], v[4:7], v[104:107], a[156:159]\n"
  "v_pk_add_f16 v121, v105, v109\n"
  "v_pk_add_f16 v122, v106, v110\n"
  "v_mfma_f32_16x16x32_f16 a[168:171], v[8:11], v[104:107], a[168:171]\n"
  "v_pk_add_f16 v123, v107, v111\n"
  "v_accvgpr_read_b32 v150, a26\n"
  "v_mfma_f32_16x16x32_f16 a[180:183], v[12:15], v[104:107], a[180:183]\n"
  "ds_read_b128 v[104:107], v232 offset:2048\n"
  "v_accvgpr_read_b32 v151, a27\n"
  "v_mfma_f32_16x16x32_f16 a[148:151], v[16:19], v[108:111], a[148:151]\n"
  "s_add_i32 s47, s44, s46\n"
  "v_accvgpr_read_b32 v152, a28\n"
  "v_accvgpr_read_b32 v153, a29\n"
  "v_mfma_f32_16x16x32_f16 a[160:163], v[20:23], v[108:111], a[160:163]\n"
  "s_add_i32 s48, s47, s45\n"
  "v_accvgpr_read_b32 v154, a30\n"
  "v_accvgpr_read_b32 v155, a31\n"
  "v_mfma_f32_16x16x32_f16 a[172:175], v[24:27], v[108:111], a[172:175]\n"
  "s_waitcnt lgkmcnt(1)\n"
  "v_accvgpr_read_b32 v156, a32\n"
  "v_accvgpr_read_b32 v157, a33\n"
  "v_mfma_f32_16x16x32_f16 a[184:187], v[28:31], v[108:111], a[184:187]\n"
  "s_add_i32 s46, s46, s49\n"
  "ds_read_b128 v[108:111], v232 offset:2560\n"
  "v_accvgpr_read_b32 v158, a34\n"
  "v_mfma_f32_16x16x32_f16 a[152:155], v[32:35], v[120:123], a[152:155]\n"
  "s_add_i32 s50, s50, 4194304\n"
  "v_accvgpr_read_b32 v159, a35\n"
  "v_accvgpr_read_b32 v160, a36\n"
  "v_mfma_f32_16x16x32_f16 a[164:167], v[36:39], v[120:123], a[164:167]\n"
  "v_accvgpr_read_b32 v161, a37\n"
  "v_accvgpr_read_b32 v162, a38\n"
  "v_mfma_f32_16x16x32_f16 a[176:179], v[40:43], v[120:123], a[176:179]\n"
  "v_accvgpr_read_b32 v163, a39\n"
  "v_accvgpr_read_b32 v164, a40\n"
  "v_mfma_f32_16x16x32_f16 a[188:191], v[44:47], v[120:123], a[188:191]\n"
  "v_accvgpr_read_b32 v165, a41\n"
  "v_accvgpr_read_b32 v166, a42\n"
  "v_mfma_f32_16x16x32_f16 a[192:195], v[0:3], v[112:115], a[192:195]\n"
  "v_pk_add_f16 v120, v112, v116\n"
  "v_pk_add_f16 v121, v113, v117\n"
  "v_mfma_f32_16x16x32_f16 a[204:207], v[4:7], v[112:115], a[204:207]\n"
  "v_pk_add_f16 v122, v114, v118\n"
  "v_pk_add_f16 v123, v115, v119\n"
  "v_mfma_f32_16x16x32_f16 a[216:219], v[8:11], v[112:115], a[216:219]\n"
  "v_accvgpr_read_b32 v167, a43\n"
  "v_accvgpr_read_b32 v168, a44\n"
  "v_mfma_f32_16x16x32_f16 a[228:231], v[12:15], v[112:115], a[228:231]\n"
  "v_accvgpr_read_b32 v169, a45\n"
  "v_accvgpr_read_b32 v170, a46\n"
  "v_mfma_f32_16x16x32_f16 a[196:199], v[16:19], v[116:119], a[196:199]\n"
  "v_accvgpr_read_b32 v171, a47\n"
  "v_pk_add_f16 v80, v48, v64\n"
  "v_mfma_f32_16x16x32_f16 a[208:211], v[20:23], v[116:119], a[208:211]\n"
  "v_pk_add_f16 v81, v49, v65\n"
  "v_pk_add_f16 v82, v50, v66\n"
  "v_mfma_f32_16x16x32_f16 a[220:223], v[24:27], v[116:119], a[220:223]\n"
  "v_pk_add_f16 v83, v51, v67\n"
  "v_pk_add_f16 v84, v52, v68\n"
  "v_mfma_f32_16x16x32_f16 a[232:235], v[28:31], v[116:119], a[232:235]\n"
  "v_pk_add_f16 v85, v53, v69\n"
  "v_pk_add_f16 v86, v54, v70\n"
  "v_mfma_f32_16x16x32_f16 a[200:203], v[32:35], v[120:123], a[200:203]\n"
  "v_pk_add_f16 v87, v55, v71\n"
  "v_pk_add_f16 v88, v56, v72\n"
  "s_waitcnt vmcnt(9) lgkmcnt(0)\n"
  "v_mfma_f32_16x16x32_f16 a[212:215], v[36:39], v[120:123], a[212:215]\n"
  "v_cmp_eq_u32 vcc, 0, v247\n"
  "s_cbranch_vccnz .Lspin_s22_None_%=\n"
  ".Lspun_s22_None_%=:\n"
  "v_mfma_f32_16x16x32_f16 a[224:227], v[40:43], v[120:123], a[224:227]\n"
  "v_pk_add_f16 v89, v57, v73\n"
  "v_pk_add_f16 v90, v58, v74\n"
  "s_barrier\n"
  "v_mfma_f32_16x16x32_f16 a[236:239], v[44:47], v[120:123], a[236:239]\n"
  "v_pk_add_f16 v91, v59, v75\n"
  "v_pk_add_f16 v92, v60, v76\n"
  "v_pk_add_f16 v93, v61, v77\n"
  "v_pk_add_f16 v94, v62, v78\n"
  "v_pk_add_f16 v95, v63, v79\n"
  "v_add_u32 v228, s49, v228\n"
  "v_add_u32 v229, s49, v229\n"
  "v_add_u32 v230, s49, v230\n"
  "v_add_u32 v231, s49, v231\n"
  "v_add_u32 v232, s49, v232\n"
  "v_mfma_f32_16x16x32_f16 v[124:127], v[48:51], v[96:99], v[124:127]\n"
  "v_pk_add_f16 v120, v96, v100\n"
  "v_pk_add_f16 v121, v97, v101\n"
  "v_mfma_f32_16x16x32_f16 v[136:139], v[52:55], v[96:99], v[136:139]\n"
  "v_pk_add_f16 v122, v98, v102\n"
  "v_pk_add_f16 v123, v99, v103\n"
  "v_mfma_f32_16x16x32_f16 v[148:151], v[56:59], v[96:99], v[148:151]\n"
  "s_and_b32 s65, s51, 0xffffe300\n"
  "ds_read_b64_tr_b16 v[0:1], v228 offset:0\n"
  "ds_read_b64_tr_b16 v[2:3], v228 offset:8192\n"
  "v_mfma_f32_16x16x32_f16 v[160:163], v[60:63], v[96:99], v[160:163]\n"
  "s_add_i32 m0, s47, 0\n"
  "ds_read_b128 v[96:99], v232 offset:4096\n"
  "ds_read_b64_tr_b16 v[16:17], v228 offset:256\n"
  "v_mfma_f32_16x16x32_f16 v[128:131], v[64:67], v[100:103], v[128:131]\n"
  "s_add_i32 s64, s50, 4194304\n"
  "buffer_load_dwordx4 v237, s[40:43], s64 offen sc1 lds\n"
  "ds_read_b64_tr_b16 v[18:19], v228 offset:8448\n"
  "v_mfma_f32_16x16x32_f16 v[140:143], v[68:71], v[100:103], v[140:143]\n"
  "s_cmp_eq_u32 s46, 73728\n"
  "s_cselect_b32 s49, s68, s69\n"
  "ds_read_b64_tr_b16 v[4:5], v229 offset:0\n"
  "v_mfma_f32_16x16x32_f16 v[152:155], v[72:75], v[100:103], v[152:155]\n"
  "ds_read_b64_tr_b16 v[6:7], v229 offset:8192\n"
  "v_add_u32 v228, s49, v228\n"
  "v_mfma_f32_16x16x32_f16 v[164:167], v[76:79], v[100:103], v[164:167]\n"
  "ds_read_b128 v[100:103], v232 offset:4608\n"
  "ds_read_b64_tr_b16 v[20:21], v229 offset:256\n"
  "v_mfma_f32_16x16x32_f16 v[132:135], v[80:83], v[120:123], v[132:135]\n"
  "buffer_store_dword v232, v245, s[40:43], s65 offen sc1\n"
  "ds_read_b64_tr_b16 v[22:23], v229 offset:8448\n"
  "v_mfma_f32_16x16x32_f16 v[144:147], v[84:87], v[120:123], v[144:147]\n"
  "ds_read_b64_tr_b16 v[8:9], v230 offset:0\n"
  "v_add_u32 v229, s49, v229\n"
  "v_mfma_f32_16x16x32_f16 v[156:159], v[88:91], v[120:123], v[156:159]\n"
  "s_add_i32 s53, s53, 8192\n"
  "s_lshr_b32 s52, s52, 1\n"
  "ds_read_b64_tr_b16 v[10:11], v230 offset:8192\n"
  "v_mfma_f32_16x16x32_f16 v[168:171], v[92:95], v[120:123], v[168:171]\n"
  "s_add_i32 m0, s47, 4096\n"
  "s_waitcnt lgkmcnt(7)\n"
  "ds_read_b64_tr_b16 v[24:25], v230 offset:256\n"
  "v_mfma_f32_16x16x32_f16 a[48:51], v[48:51], v[104:107], a[48:51]\n"
  "s_add_i32 s64, s50, 4198400\n"
  "buffer_load_dwordx4 v237, s[40:43], s64 offen sc1 lds\n"
  "v_pk_add_f16 v120, v104, v108\n"
  "v_mfma_f32_16x16x32_f16 a[60:63], v[52:55], v[104:107], a[60:63]\n"
  "s_add_i32 s51, s51, 8448\n"
  "v_pk_add_f16 v121, v105, v109\n"
  "v_pk_add_f16 v122, v106, v110\n"
  "v_mfma_f32_16x16x32_f16 a[72:75], v[56:59], v[104:107], a[72:75]\n"
  "ds_read_b64_tr_b16 v[26:27], v230 offset:8448\n"
  "v_pk_add_f16 v123, v107, v111\n"
  "v_mfma_f32_16x16x32_f16 a[84:87], v[60:63], v[104:107], a[84:87]\n"
  "s_add_i32 m0, s47, 8192\n"
  "ds_read_b128 v[104:107], v232 offset:6144\n"
  "v_accvgpr_read_b32 v180, a48\n"
  "v_mfma_f32_16x16x32_f16 a[52:55], v[64:67], v[108:111], a[52:55]\n"
  "s_add_i32 s64, s50, 4202496\n"
  "buffer_load_dwordx4 v237, s[40:43], s64 offen sc1 lds\n"
  "v_accvgpr_read_b32 v181, a49\n"
  "v_mfma_f32_16x16x32_f16 a[64:67], v[68:71], v[108:111], a[64:67]\n"
  "s_bitcmp1_b32 s52, 0\n"
  "s_cselect_b32 s54, s53, 134217728\n"
  "ds_read_b64_tr_b16 v[12:13], v231 offset:0\n"
  "v_mfma_f32_16x16x32_f16 a[76:79], v[72:75], v[108:111], a[76:79]\n"
  "s_waitcnt lgkmcnt(6)\n"
  "ds_read_b64_tr_b16 v[14:15], v231 offset:8192\n"
  "v_accvgpr_read_b32 v182, a50\n"
  "v_mfma_f32_16x16x32_f16 a[88:91], v[76:79], v[108:111], a[88:91]\n"
  "s_add_i32 m0, s47, 12288\n"
  "ds_read_b128 v[108:111], v232 offset:6656\n"
  "v_accvgpr_read_b32 v183, a51\n"
  "v_mfma_f32_16x16x32_f16 a[56:59], v[80:83], v[120:123], a[56:59]\n"
  "s_add_i32 s64, s50, 4206592\n"
  "buffer_load_dwordx4 v237, s[40:43], s64 offen sc1 lds\n"
  "v_accvgpr_read_b32 v184, a52\n"
  "v_mfma_f32_16x16x32_f16 a[68:71], v[84:87], v[120:123], a[68:71]\n"
  "ds_read_b128 v[112:115], v232 offset:8192\n"
  "v_accvgpr_read_b32 v185, a53\n"
  "v_mfma_f32_16x16x32_f16 a[80:83], v[88:91], v[120:123], a[80:83]\n"
  "s_waitcnt lgkmcnt(5)\n"
  "ds_read_b64_tr_b16 v[28:29], v231 offset:256\n"
  "v_accvgpr_read_b32 v186, a54\n"
  "v_mfma_f32_16x16x32_f16 a[92:95], v[92:95], v[120:123], a[92:95]\n"
  "s_add_i32 m0, s47, 16384\n"
  "ds_read_b128 v[116:119], v232 offset:8704\n"
  "v_accvgpr_read_b32 v187, a55\n"
  "v_mfma_f32_16x16x32_f16 a[96:99], v[48:51], v[96:99], a[96:99]\n"
  "s_add_i32 s64, s50, 4194304\n"
  "buffer_load_dwordx4 v238, s[40:43], s64 offen sc1 lds\n"
  "v_pk_add_f16 v120, v96, v100\n"
  "v_mfma_f32_16x16x32_f16 a[108:111], v[52:55], v[96:99], a[108:111]\n"
  "v_pk_add_f16 v121, v97, v101\n"
  "v_pk_add_f16 v122, v98, v102\n"
  "v_mfma_f32_16x16x32_f16 a[120:123], v[56:59], v[96:99], a[120:123]\n"
  "ds_read_b64_tr_b16 v[30:31], v231 offset:8448\n"
  "v_pk_add_f16 v123, v99, v103\n"
  "v_mfma_f32_16x16x32_f16 a[132:135], v[60:63], v[96:99], a[132:135]\n"
  "s_add_i32 m0, s47, 20480\n"
  "ds_read_b128 v[96:99], v232 offset:0\n"
  "v_accvgpr_read_b32 v188, a56\n"
  "v_mfma_f32_16x16x32_f16 a[100:103], v[64:67], v[100:103], a[100:103]\n"
  "s_add_i32 s64, s50, 4096\n"
  "buffer_load_dwordx4 v238, s[40:43], s64 offen sc1 lds\n"
  "v_accvgpr_read_b32 v189, a57\n"
  "v_mfma_f32_16x16x32_f16 a[112:115], v[68:71], v[100:103], a[112:115]\n"
  "v_accvgpr_read_b32 v190, a58\n"
  "v_accvgpr_read_b32 v191, a59\n"
  "v_mfma_f32_16x16x32_f16 a[124:127], v[72:75], v[100:103], a[124:127]\n"
  "v_accvgpr_read_b32 v192, a60\n"
  "v_accvgpr_read_b32 v193, a61\n"
  "v_mfma_f32_16x16x32_f16 a[136:139], v[76:79], v[100:103], a[136:139]\n"
  "s_add_i32 m0, s48, 24576\n"
  "ds_read_b128 v[100:103], v232 offset:512\n"
  "v_accvgpr_read_b32 v194, a62\n"
  "v_mfma_f32_16x16x32_f16 a[104:107], v[80:83], v[120:123], a[104:107]\n"
  "s_add_i32 s64, s50, 16384\n"
  "buffer_load_dwordx4 v238, s[40:43], s64 offen sc1 lds\n"
  "v_accvgpr_read_b32 v195, a63\n"
  "v_mfma_f32_16x16x32_f16 a[116:119], v[84:87], v[120:123], a[116:119]\n"
  "s_waitcnt lgkmcnt(6)\n"
  "v_accvgpr_read_b32 v196, a64\n"
  "v_accvgpr_read_b32 v197, a65\n"
  "v_mfma_f32_16x16x32_f16 a[128:131], v[88:91], v[120:123], a[128:131]\n"
  "s_waitcnt lgkmcnt(2)\n"
  "v_accvgpr_read_b32 v198, a66\n"
  "v_accvgpr_read_b32 v199, a67\n"
  "v_mfma_f32_16x16x32_f16 a[140:143], v[92:95], v[120:123], a[140:143]\n"
  "s_add_i32 m0, s47, 26624\n"
  "v_accvgpr_read_b32 v200, a68\n"
  "v_accvgpr_read_b32 v201, a69\n"
  "v_mfma_f32_16x16x32_f16 a[144:147], v[48:51], v[104:107], a[144:147]\n"
  "s_add_i32 s64, s50, 4214784\n"
  "buffer_load_dwordx4 v238, s[40:43], s64 offen sc1 lds\n"
  "v_pk_add_f16 v120, v104, v108\n"
  "v_mfma_f32_16x16x32_f16 a[156:159], v[52:55], v[104:107], a[156:159]\n"
  "v_pk_add_f16 v121, v105, v109\n"
  "v_pk_add_f16 v122, v106, v110\n"
  "v_mfma_f32_16x16x32_f16 a[168:171], v[56:59], v[104:107], a[168:171]\n"
  "v_pk_add_f16 v123, v107, v111\n"
  "v_accvgpr_read_b32 v202, a70\n"
  "v_mfma_f32_16x16x32_f16 a[180:183], v[60:63], v[104:107], a[180:183]\n"
  "s_add_i32 m0, s47, 30720\n"
  "ds_read_b128 v[104:107], v232 offset:2048\n"
  "v_accvgpr_read_b32 v203, a71\n"
  "v_mfma_f32_16x16x32_f16 a[148:151], v[64:67], v[108:111], a[148:151]\n"
  "s_add_i32 s64, s50, 32768\n"
  "buffer_load_dwordx4 v238, s[40:43], s64 offen sc1 lds\n"
  "v_accvgpr_read_b32 v204, a72\n"
  "v_mfma_f32_16x16x32_f16 a[160:163], v[68:71], v[108:111], a[160:163]\n"
  "s_add_i32 s47, s44, s46\n"
  "v_accvgpr_read_b32 v205, a73\n"
  "v_accvgpr_read_b32 v206, a74\n"
  "v_mfma_f32_16x16x32_f16 a[172:175], v[72:75], v[108:111], a[172:175]\n"
  "s_add_i32 s48, s47, s45\n"
  "v_accvgpr_read_b32 v207, a75\n"
  "v_accvgpr_read_b32 v208, a76\n"
  "v_mfma_f32_16x16x32_f16 a[184:187], v[76:79], v[108:111], a[184:187]\n"
  "s_add_i32 s46, s46, s49\n"
  "ds_read_b128 v[108:111], v232 offset:2560\n"
  "v_accvgpr_read_b32 v209, a77\n"
  "v_mfma_f32_16x16x32_f16 a[152:155], v[80:83], v[120:123], a[152:155]\n"
  "s_add_i32 s50, s50, 4194304\n"
  "v_accvgpr_read_b32 v210, a78\n"
  "v_accvgpr_read_b32 v211, a79\n"
  "v_mfma_f32_16x16x32_f16 a[164:167], v[84:87], v[120:123], a[164:167]\n"
  "v_accvgpr_read_b32 v212, a80\n"
  "v_accvgpr_read_b32 v213, a81\n"
  "v_mfma_f32_16x16x32_f16 a[176:179], v[88:91], v[120:123], a[176:179]\n"
  "v_accvgpr_read_b32 v214, a82\n"
  "v_accvgpr_read_b32 v215, a83\n"
  "v_mfma_f32_16x16x32_f16 a[188:191], v[92:95], v[120:123], a[188:191]\n"
  "v_accvgpr_read_b32 v216, a84\n"
  "v_accvgpr_read_b32 v217, a85\n"
  "v_mfma_f32_16x16x32_f16 a[192:195], v[48:51], v[112:115], a[192:195]\n"
  "v_pk_add_f16 v120, v112, v116\n"
  "v_pk_add_f16 v121, v113, v117\n"
  "v_mfma_f32_16x16x32_f16 a[204:207], v[52:55], v[112:115], a[204:207]\n"
  "v_pk_add_f16 v122, v114, v118\n"
  "v_pk_add_f16 v123, v115, v119\n"
  "v_mfma_f32_16x16x32_f16 a[216:219], v[56:59], v[112:115], a[216:219]\n"
  "v_accvgpr_read_b32 v218, a86\n"
  "v_accvgpr_read_b32 v219, a87\n"
  "v_mfma_f32_16x16x32_f16 a[228:231], v[60:63], v[112:115], a[228:231]\n"
  "v_accvgpr_read_b32 v220, a88\n"
  "v_accvgpr_read_b32 v221, a89\n"
  "v_mfma_f32_16x16x32_f16 a[196:199], v[64:67], v[116:119], a[196:199]\n"
  "v_accvgpr_read_b32 v222, a90\n"
  "v_accvgpr_read_b32 v223, a91\n"
  "v_mfma_f32_16x16x32_f16 a[208:211], v[68:71], v[116:119], a[208:211]\n"
  "v_accvgpr_read_b32 v224, a92\n"
  "v_accvgpr_read_b32 v225, a93\n"
  "v_mfma_f32_16x16x32_f16 a[220:223], v[72:75], v[116:119], a[220:223]\n"
  "v_accvgpr_read_b32 v226, a94\n"
  "v_accvgpr_read_b32 v227, a95\n"
  "v_mfma_f32_16x16x32_f16 a[232:235], v[76:79], v[116:119], a[232:235]\n"
  "v_pk_add_f16 v32, v0, v16\n"
  "v_pk_add_f16 v33, v1, v17\n"
  "v_mfma_f32_16x16x32_f16 a[200:203], v[80:83], v[120:123], a[200:203]\n"
  "v_pk_add_f16 v34, v2, v18\n"
  "v_pk_add_f16 v35, v3, v19\n"
  "s_waitcnt vmcnt(10) lgkmcnt(0)\n"
  "v_mfma_f32_16x16x32_f16 a[212:215], v[84:87], v[120:123], a[212:215]\n"
  "v_pk_add_f16 v36, v4, v20\n"
  "v_pk_add_f16 v37, v5, v21\n"
  "v_mfma_f32_16x16x32_f16 a[224:227], v[88:91], v[120:123], a[224:227]\n"
  "v_pk_add_f16 v38, v6, v22\n"
  "v_pk_add_f16 v39, v7, v23\n"
  "s_barrier\n"
  "v_mfma_f32_16x16x32_f16 a[236:239], v[92:95], v[120:123], a[236:239]\n"
  "v_pk_add_f16 v40, v8, v24\n"
  "v_pk_add_f16 v41, v9, v25\n"
  "v_pk_add_f16 v42, v10, v26\n"
  "v_pk_add_f16 v43, v11, v27\n"
  "v_pk_add_f16 v44, v12, v28\n"
  "v_pk_add_f16 v45, v13, v29\n"
  "v_pk_add_f16 v46, v14, v30\n"
  "v_pk_add_f16 v47, v15, v31\n"
  "v_add_u32 v230, s49, v230\n"
  "v_add_u32 v231, s49, v231\n"
  "v_add_u32 v232, s49, v232\n"
  "s_mov_b32 s63, 2\n"
  ".p2align 6\n"
  ".LloopN_%=:\n"
  "v_mfma_f32_16x16x32_f16 v[124:127], v[0:3], v[96:99], v[124:127]\n"
  "buffer_load_dword v247, v246, s[40:43], s54 offen sc1\n"
  "v_pk_add_f16 v120, v96, v100\n"
  "v_mfma_f32_16x16x32_f16 v[136:139], v[4:7], v[96:99], v[136:139]\n"
  "v_pk_add_f16 v121, v97, v101\n"
  "v_pk_add_f16 v122, v98, v102\n"
  "v_mfma_f32_16x16x32_f16 v[148:151], v[8:11], v[96:99], v[148:151]\n"
  "ds_read_b64_tr_b16 v[48:49], v228 offset:0\n"
  "v_pk_add_f16 v123, v99, v103\n"
  "v_mfma_f32_16x16x32_f16 v[160:163], v[12:15], v[96:99], v[160:163]\n"
  "s_add_i32 m0, s47, 0\n"
  "ds_read_b64_tr_b16 v[50:51], v228 offset:8192\n"
  "ds_read_b128 v[96:99], v232 offset:4096\n"
  "v_mfma_f32_16x16x32_f16 v[128:131], v[16:19], v[100:103], v[128:131]\n"
  "s_add_i32 s64, s50, 4194304\n"
  "buffer_load_dwordx4 v237, s[40:43], s64 offen sc1 lds\n"
  "ds_read_b64_tr_b16 v[64:65], v228 offset:256\n"
  "v_mfma_f32_16x16x32_f16 v[140:143], v[20:23], v[100:103], v[140:143]\n"
  "s_cmp_eq_u32 s46, 73728\n"
  "s_cselect_b32 s49, s68, s69\n"
  "ds_read_b64_tr_b16 v[66:67], v228 offset:8448\n"
  "v_mfma_f32_16x16x32_f16 v[152:155], v[24:27], v[100:103], v[152:155]\n"
  "ds_read_b64_tr_b16 v[52:53], v229 offset:0\n"
  "v_add_u32 v228, s49, v228\n"
  "v_mfma_f32_16x16x32_f16 v[164:167], v[28:31], v[100:103], v[164:167]\n"
  "s_add_i32 m0, s47, 4096\n"
  "ds_read_b128 v[100:103], v232 offset:4608\n"
  "ds_read_b64_tr_b16 v[54:55], v229 offset:8192\n"
  "v_mfma_f32_16x16x32_f16 v[132:135], v[32:35], v[120:123], v[132:135]\n"
  "s_add_i32 s64, s50, 4198400\n"
  "buffer_load_dwordx4 v237, s[40:43], s64 offen sc1 lds\n"
  "ds_read_b64_tr_b16 v[68:69], v229 offset:256\n"
  "v_mfma_f32_16x16x32_f16 v[144:147], v[36:39], v[120:123], v[144:147]\n"
  "ds_read_b64_tr_b16 v[70:71], v229 offset:8448\n"
  "v_add_u32 v229, s49, v229\n"
  "v_mfma_f32_16x16x32_f16 v[156:159], v[40:43], v[120:123], v[156:159]\n"
  "ds_read_b64_tr_b16 v[56:57], v230 offset:0\n"
  "ds_read_b64_tr_b16 v[58:59], v230 offset:8192\n"
  "v_mfma_f32_16x16x32_f16 v[168:171], v[44:47], v[120:123], v[168:171]\n"
  "s_add_i32 m0, s47, 8192\n"
  "ds_read_b64_tr_b16 v[72:73], v230 offset:256\n"
  "ds_read_b64_tr_b16 v[74:75], v230 offset:8448\n"
  "v_mfma_f32_16x16x32_f16 v[180:183], v[0:3], v[104:107], v[180:183]\n"
  "s_add_i32 s64, s50, 4202496\n"
  "buffer_load_dwordx4 v237, s[40:43], s64 offen sc1 lds\n"
  "v_pk_add_f16 v120, v104, v108\n"
  "v_mfma_f32_16x16x32_f16 v[192:195], v[4:7], v[104:107], v[192:195]\n"
  "s_waitcnt lgkmcnt(9)\n"
  "v_pk_add_f16 v121, v105, v109\n"
  "v_pk_add_f16 v122, v106, v110\n"
  "v_mfma_f32_16x16x32_f16 v[204:207], v[8:11], v[104:107], v[204:207]\n"
  "ds_read_b64_tr_b16 v[60:61], v231 offset:0\n"
  "v_pk_add_f16 v123, v107, v111\n"
  "v_mfma_f32_16x16x32_f16 v[216:219], v[12:15], v[104:107], v[216:219]\n"
  "s_add_i32 m0, s47, 12288\n"
  "ds_read_b128 v[104:107], v232 offset:6144\n"
  "v_pk_add_f16 v80, v48, v64\n"
  "v_mfma_f32_16x16x32_f16 v[184:187], v[16:19], v[108:111], v[184:187]\n"
  "s_waitcnt lgkmcnt(6)\n"
  "s_add_i32 s64, s50, 4206592\n"
  "buffer_load_dwordx4 v237, s[40:43], s64 offen sc1 lds\n"
  "v_mfma_f32_16x16x32_f16 v[196:199], v[20:23], v[108:111], v[196:199]\n"
  "ds_read_b64_tr_b16 v[62:63], v231 offset:8192\n"
  "v_pk_add_f16 v81, v49, v65\n"
  "v_mfma_f32_16x16x32_f16 v[208:211], v[24:27], v[108:111], v[208:211]\n"
  "s_waitcnt lgkmcnt(3)\n"
  "ds_read_b128 v[112:115], v232 offset:8192\n"
  "v_pk_add_f16 v82, v50, v66\n"
  "v_mfma_f32_16x16x32_f16 v[220:223], v[28:31], v[108:111], v[220:223]\n"
  "s_add_i32 m0, s47, 16384\n"
  "ds_read_b128 v[108:111], v232 offset:6656\n"
  "v_pk_add_f16 v83, v51, v67\n"
  "v_mfma_f32_16x16x32_f16 v[188:191], v[32:35], v[120:123], v[188:191]\n"
  "s_add_i32 s64, s50, 4194304\n"
  "buffer_load_dwordx4 v238, s[40:43], s64 offen sc1 lds\n"
  "v_pk_add_f16 v84, v52, v68\n"
  "v_mfma_f32_16x16x32_f16 v[200:203], v[36:39], v[120:123], v[200:203]\n"
  "ds_read_b64_tr_b16 v[76:77], v231 offset:256\n"
  "v_pk_add_f16 v85, v53, v69\n"
  "v_mfma_f32_16x16x32_f16 v[212:215], v[40:43], v[120:123], v[212:215]\n"
  "ds_read_b128 v[116:119], v232 offset:8704\n"
  "v_pk_add_f16 v86, v54, v70\n"
  "v_mfma_f32_16x16x32_f16 v[224:227], v[44:47], v[120:123], v[224:227]\n"
  "s_add_i32 m0, s47, 20480\n"
  "ds_read_b64_tr_b16 v[78:79], v231 offset:8448\n"
  "v_pk_add_f16 v87, v55, v71\n"
  "v_mfma_f32_16x16x32_f16 a[96:99], v[0:3], v[96:99], a[96:99]\n"
  "s_add_i32 s64, s50, 4096\n"
  "buffer_load_dwordx4 v238, s[40:43], s64 offen sc1 lds\n"
  "v_pk_add_f16 v120, v96, v100\n"
  "v_mfma_f32_16x16x32_f16 a[108:111], v[4:7], v[96:99], a[108:111]\n"
  "v_pk_add_f16 v121, v97, v101\n"
  "v_pk_add_f16 v122, v98, v102\n"
  "v_mfma_f32_16x16x32_f16 a[120:123], v[8:11], v[96:99], a[120:123]\n"
  "v_pk_add_f16 v123, v99, v103\n"
  "v_pk_add_f16 v88, v56, v72\n"
  "v_mfma_f32_16x16x32_f16 a[132:135], v[12:15], v[96:99], a[132:135]\n"
  "s_add_i32 m0, s48, 24576\n"
  "ds_read_b128 v[96:99], v232 offset:0\n"
  "v_pk_add_f16 v89, v57, v73\n"
  "v_mfma_f32_16x16x32_f16 a[100:103], v[16:19], v[100:103], a[100:103]\n"
  "s_add_i32 s64, s50, 16384\n"
  "buffer_load_dwordx4 v238, s[40:43], s64 offen sc1 lds\n"
  "v_pk_add_f16 v90, v58, v74\n"
  "v_mfma_f32_16x16x32_f16 a[112:115], v[20:23], v[100:103], a[112:115]\n"
  "v_pk_add_f16 v91, v59, v75\n"
  "v_add_u32 v230, s49, v230\n"
  "v_mfma_f32_16x16x32_f16 a[124:127], v[24:27], v[100:103], a[124:127]\n"
  "s_waitcnt lgkmcnt(1)\n"
  "v_pk_add_f16 v92, v60, v76\n"
  "v_pk_add_f16 v93, v61, v77\n"
  "v_mfma_f32_16x16x32_f16 a[136:139], v[28:31], v[100:103], a[136:139]\n"
  "s_add_i32 m0, s47, 26624\n"
  "ds_read_b128 v[100:103], v232 offset:512\n"
  "v_pk_add_f16 v94, v62, v78\n"
  "v_mfma_f32_16x16x32_f16 a[104:107], v[32:35], v[120:123], a[104:107]\n"
  "s_add_i32 s64, s50, 4214784\n"
  "buffer_load_dwordx4 v238, s[40:43], s64 offen sc1 lds\n"
  "v_pk_add_f16 v95, v63, v79\n"
  "v_mfma_f32_16x16x32_f16 a[116:119], v[36:39], v[120:123], a[116:119]\n"
  "v_add_u32 v231, s49, v231\n"
  "v_mfma_f32_16x16x32_f16 a[128:131], v[40:43], v[120:123], a[128:131]\n"
  "v_mfma_f32_16x16x32_f16 a[140:143], v[44:47], v[120:123], a[140:143]\n"
  "s_add_i32 m0, s47, 30720\n"
  "v_mfma_f32_16x16x32_f16 a[144:147], v[0:3], v[104:107], a[144:147]\n"
  "s_add_i32 s64, s50, 32768\n"
  "buffer_load_dwordx4 v238, s[40:43], s64 offen sc1 lds\n"
  "v_pk_add_f16 v120, v104, v108\n"
  "v_mfma_f32_16x16x32_f16 a[156:159], v[4:7], v[104:107], a[156:159]\n"
  "v_pk_add_f16 v121, v105, v109\n"
  "v_pk_add_f16 v122, v106, v110\n"
  "v_mfma_f32_16x16x32_f16 a[168:171], v[8:11], v[104:107], a[168:171]\n"
  "v_pk_add_f16 v123, v107, v111\n"
  "v_mfma_f32_16x16x32_f16 a[180:183], v[12:15], v[104:107], a[180:183]\n"
  "ds_read_b128 v[104:107], v232 offset:2048\n"
  "v_mfma_f32_16x16x32_f16 a[148:151], v[16:19], v[108:111], a[148:151]\n"
  "s_add_i32 s47, s44, s46\n"
  "s_add_i32 s50, s50, 4194304\n"
  "v_mfma_f32_16x16x32_f16 a[160:163], v[20:23], v[108:111], a[160:163]\n"
  "s_add_i32 s48, s47, s45\n"
  "s_add_i32 s46, s46, s49\n"
  "v_mfma_f32_16x16x32_f16 a[172:175], v[24:27], v[108:111], a[172:175]\n"
  "v_mfma_f32_16x16x32_f16 a[184:187], v[28:31], v[108:111], a[184:187]\n"
  "ds_read_b128 v[108:111], v232 offset:2560\n"
  "v_add_u32 v232, s49, v232\n"
  "v_mfma_f32_16x16x32_f16 a[152:155], v[32:35], v[120:123], a[152:155]\n"
  "v_mfma_f32_16x16x32_f16 a[164:167], v[36:39], v[120:123], a[164:167]\n"
  "v_mfma_f32_16x16x32_f16 a[176:179], v[40:43], v[120:123], a[176:179]\n"
  "v_mfma_f32_16x16x32_f16 a[188:191], v[44:47], v[120:123], a[188:191]\n"
  "v_mfma_f32_16x16x32_f16 a[192:195], v[0:3], v[112:115], a[192:195]\n"
  "v_pk_add_f16 v120, v112, v116\n"
  "v_pk_add_f16 v121, v113, v117\n"
  "v_mfma_f32_16x16x32_f16 a[204:207], v[4:7], v[112:115], a[204:207]\n"
  "v_pk_add_f16 v122, v114, v118\n"
  "v_pk_add_f16 v123, v115, v119\n"
  "v_mfma_f32_16x16x32_f16 a[216:219], v[8:11], v[112:115], a[216:219]\n"
  "v_mfma_f32_16x16x32_f16 a[228:231], v[12:15], v[112:115], a[228:231]\n"
  "v_mfma_f32_16x16x32_f16 a[196:199], v[16:19], v[116:119], a[196:199]\n"
  "v_mfma_f32_16x16x32_f16 a[208:211], v[20:23], v[116:119], a[208:211]\n"
  "v_mfma_f32_16x16x32_f16 a[220:223], v[24:27], v[116:119], a[220:223]\n"
  "v_mfma_f32_16x16x32_f16 a[232:235], v[28:31], v[116:119], a[232:235]\n"
  "v_mfma_f32_16x16x32_f16 a[200:203], v[32:35], v[120:123], a[200:203]\n"
  "s_waitcnt vmcnt(9) lgkmcnt(0)\n"
  "v_mfma_f32_16x16x32_f16 a[212:215], v[36:39], v[120:123], a[212:215]\n"
  "v_cmp_eq_u32 vcc, 0, v247\n"
  "s_cbranch_vccnz .Lspin_ne_None_%=\n"
  ".Lspun_ne_None_%=:\n"
  "v_mfma_f32_16x16x32_f16 a[224:227], v[40:43], v[120:123], a[224:227]\n"
  "s_barrier\n"
  "v_mfma_f32_16x16x32_f16 a[236:239], v[44:47], v[120:123], a[236:239]\n"
  "v_mfma_f32_16x16x32_f16 v[124:127], v[48:51], v[96:99], v[124:127]\n"
  "v_pk_add_f16 v120, v96, v100\n"
  "v_pk_add_f16 v121, v97, v101\n"
  "v_mfma_f32_16x16x32_f16 v[136:139], v[52:55], v[96:99], v[136:139]\n"
  "v_pk_add_f16 v122, v98, v102\n"
  "v_pk_add_f16 v123, v99, v103\n"
  "v_mfma_f32_16x16x32_f16 v[148:151], v[56:59], v[96:99], v[148:151]\n"
  "s_and_b32 s65, s51, 0xffffe300\n"
  "ds_read_b64_tr_b16 v[0:1], v228 offset:0\n"
  "ds_read_b64_tr_b16 v[2:3], v228 offset:8192\n"
  "v_mfma_f32_16x16x32_f16 v[160:163], v[60:63], v[96:99], v[160:163]\n"
  "s_add_i32 m0, s47, 0\n"
  "ds_read_b128 v[96:99], v232 offset:4096\n"
  "ds_read_b64_tr_b16 v[16:17], v228 offset:256\n"
  "v_mfma_f32_16x16x32_f16 v[128:131], v[64:67], v[100:103], v[128:131]\n"
  "s_add_i32 s64, s50, 4194304\n"
  "buffer_load_dwordx4 v237, s[40:43], s64 offen sc1 lds\n"
  "ds_read_b64_tr_b16 v[18:19], v228 offset:8448\n"
  "v_mfma_f32_16x16x32_f16 v[140:143], v[68:71], v[100:103], v[140:143]\n"
  "s_cmp_eq_u32 s46, 73728\n"
  "s_cselect_b32 s49, s68, s69\n"
  "ds_read_b64_tr_b16 v[4:5], v229 offset:0\n"
  "v_mfma_f32_16x16x32_f16 v[152:155], v[72:75], v[100:103], v[152:155]\n"
  "ds_read_b64_tr_b16 v[6:7], v229 offset:8192\n"
  "v_add_u32 v228, s49, v228\n"
  "v_mfma_f32_16x16x32_f16 v[164:167], v[76:79], v[100:103], v[164:167]\n"
  "ds_read_b128 v[100:103], v232 offset:4608\n"
  "ds_read_b64_tr_b16 v[20:21], v229 offset:256\n"
  "v_mfma_f32_16x16x32_f16 v[132:135], v[80:83], v[120:123], v[132:135]\n"
  "buffer_store_dword v232, v245, s[40:43], s65 offen sc1\n"
  "ds_read_b64_tr_b16 v[22:23], v229 offset:8448\n"
  "v_mfma_f32_16x16x32_f16 v[144:147], v[84:87], v[120:123], v[144:147]\n"
  "ds_read_b64_tr_b16 v[8:9], v230 offset:0\n"
  "v_add_u32 v229, s49, v229\n"
  "v_mfma_f32_16x16x32_f16 v[156:159], v[88:91], v[120:123], v[156:159]\n"
  "s_add_i32 s53, s53, 8192\n"
  "s_lshr_b32 s52, s52, 1\n"
  "ds_read_b64_tr_b16 v[10:11], v230 offset:8192\n"
  "v_mfma_f32_16x16x32_f16 v[168:171], v[92:95], v[120:123], v[168:171]\n"
  "s_add_i32 m0, s47, 4096\n"
  "s_waitcnt lgkmcnt(7)\n"
  "ds_read_b64_tr_b16 v[24:25], v230 offset:256\n"
  "v_mfma_f32_16x16x32_f16 v[180:183], v[48:51], v[104:107], v[180:183]\n"
  "s_add_i32 s64, s50, 4198400\n"
  "buffer_load_dwordx4 v237, s[40:43], s64 offen sc1 lds\n"
  "v_pk_add_f16 v120, v104, v108\n"
  "v_mfma_f32_16x16x32_f16 v[192:195], v[52:55], v[104:107], v[192:195]\n"
  "s_add_i32 s51, s51, 8448\n"
  "v_pk_add_f16 v121, v105, v109\n"
  "v_pk_add_f16 v122, v106, v110\n"
  "v_mfma_f32_16x16x32_f16 v[204:207], v[56:59], v[104:107], v[204:207]\n"
  "ds_read_b64_tr_b16 v[26:27], v230 offset:8448\n"
  "v_pk_add_f16 v123, v107, v111\n"
  "v_mfma_f32_16x16x32_f16 v[216:219], v[60:63], v[104:107], v[216:219]\n"
  "s_add_i32 m0, s47, 8192\n"
  "s_waitcnt lgkmcnt(4)\n"
  "ds_read_b128 v[104:107], v232 offset:6144\n"
  "v_mfma_f32_16x16x32_f16 v[184:187], v[64:67], v[108:111], v[184:187]\n"
  "s_add_i32 s64, s50, 4202496\n"
  "buffer_load_dwordx4 v237, s[40:43], s64 offen sc1 lds\n"
  "v_pk_add_f16 v32, v0, v16\n"
  "v_mfma_f32_16x16x32_f16 v[196:199], v[68:71], v[108:111], v[196:199]\n"
  "s_bitcmp1_b32 s52, 0\n"
  "s_cselect_b32 s54, s53, 134217728\n"
  "ds_read_b64_tr_b16 v[12:13], v231 offset:0\n"
  "v_mfma_f32_16x16x32_f16 v[208:211], v[72:75], v[108:111], v[208:211]\n"
  "ds_read_b64_tr_b16 v[14:15], v231 offset:8192\n"
  "v_pk_add_f16 v33, v1, v17\n"
  "v_mfma_f32_16x16x32_f16 v[220:223], v[76:79], v[108:111], v[220:223]\n"
  "s_add_i32 m0, s47, 12288\n"
  "ds_read_b128 v[108:111], v232 offset:6656\n"
  "v_pk_add_f16 v34, v2, v18\n"
  "v_mfma_f32_16x16x32_f16 v[188:191], v[80:83], v[120:123], v[188:191]\n"
  "s_add_i32 s64, s50, 4206592\n"
  "buffer_load_dwordx4 v237, s[40:43], s64 offen sc1 lds\n"
  "v_pk_add_f16 v35, v3, v19\n"
  "v_mfma_f32_16x16x32_f16 v[200:203], v[84:87], v[120:123], v[200:203]\n"
  "s_waitcnt lgkmcnt(4)\n"
  "ds_read_b128 v[112:115], v232 offset:8192\n"
  "v_mfma_f32_16x16x32_f16 v[212:215], v[88:91], v[120:123], v[212:215]\n"
  "ds_read_b64_tr_b16 v[28:29], v231 offset:256\n"
  "v_pk_add_f16 v36, v4, v20\n"
  "v_mfma_f32_16x16x32_f16 v[224:227], v[92:95], v[120:123], v[224:227]\n"
  "s_add_i32 m0, s47, 16384\n"
  "ds_read_b128 v[116:119], v232 offset:8704\n"
  "v_pk_add_f16 v37, v5, v21\n"
  "v_mfma_f32_16x16x32_f16 a[96:99], v[48:51], v[96:99], a[96:99]\n"
  "s_add_i32 s64, s50, 4194304\n"
  "buffer_load_dwordx4 v238, s[40:43], s64 offen sc1 lds\n"
  "v_pk_add_f16 v120, v96, v100\n"
  "v_mfma_f32_16x16x32_f16 a[108:111], v[52:55], v[96:99], a[108:111]\n"
  "v_pk_add_f16 v121, v97, v101\n"
  "v_pk_add_f16 v122, v98, v102\n"
  "v_mfma_f32_16x16x32_f16 a[120:123], v[56:59], v[96:99], a[120:123]\n"
  "ds_read_b64_tr_b16 v[30:31], v231 offset:8448\n"
  "v_pk_add_f16 v123, v99, v103\n"
  "v_mfma_f32_16x16x32_f16 a[132:135], v[60:63], v[96:99], a[132:135]\n"
  "s_add_i32 m0, s47, 20480\n"
  "ds_read_b128 v[96:99], v232 offset:0\n"
  "v_pk_add_f16 v38, v6, v22\n"
  "v_mfma_f32_16x16x32_f16 a[100:103], v[64:67], v[100:103], a[100:103]\n"
  "s_add_i32 s64, s50, 4096\n"
  "buffer_load_dwordx4 v238, s[40:43], s64 offen sc1 lds\n"
  "v_pk_add_f16 v39, v7, v23\n"
  "v_mfma_f32_16x16x32_f16 a[112:115], v[68:71], v[100:103], a[112:115]\n"
  "v_pk_add_f16 v40, v8, v24\n"
  "v_pk_add_f16 v41, v9, v25\n"
  "v_mfma_f32_16x16x32_f16 a[124:127], v[72:75], v[100:103], a[124:127]\n"
  "v_pk_add_f16 v42, v10, v26\n"
  "v_pk_add_f16 v43, v11, v27\n"
  "v_mfma_f32_16x16x32_f16 a[136:139], v[76:79], v[100:103], a[136:139]\n"
  "s_add_i32 m0, s48, 24576\n"
  "ds_read_b128 v[100:103], v232 offset:512\n"
  "v_add_u32 v230, s49, v230\n"
  "v_mfma_f32_16x16x32_f16 a[104:107], v[80:83], v[120:123], a[104:107]\n"
  "s_add_i32 s64, s50, 16384\n"
  "buffer_load_dwordx4 v238, s[40:43], s64 offen sc1 lds\n"
  "v_add_u32 v231, s49, v231\n"
  "v_mfma_f32_16x16x32_f16 a[116:119], v[84:87], v[120:123], a[116:119]\n"
  "s_waitcnt lgkmcnt(6)\n"
  "s_waitcnt lgkmcnt(2)\n"
  "v_pk_add_f16 v44, v12, v28\n"
  "v_mfma_f32_16x16x32_f16 a[128:131], v[88:91], v[120:123], a[128:131]\n"
  "v_pk_add_f16 v45, v13, v29\n"
  "v_pk_add_f16 v46, v14, v30\n"
  "v_mfma_f32_16x16x32_f16 a[140:143], v[92:95], v[120:123], a[140:143]\n"
  "s_add_i32 m0, s47, 26624\n"
  "v_pk_add_f16 v47, v15, v31\n"
  "v_mfma_f32_16x16x32_f16 a[144:147], v[48:51], v[104:107], a[144:147]\n"
  "s_add_i32 s64, s50, 4214784\n"
  "buffer_load_dwordx4 v238, s[40:43], s64 offen sc1 lds\n"
  "v_pk_add_f16 v120, v104, v108\n"
  "v_mfma_f32_16x16x32_f16 a[156:159], v[52:55], v[104:107], a[156:159]\n"
  "v_pk_add_f16 v121, v105, v109\n"
  "v_pk_add_f16 v122, v106, v110\n"
  "v_mfma_f32_16x16x32_f16 a[168:171], v[56:59], v[104:107], a[168:171]\n"
  "v_pk_add_f16 v123, v107, v111\n"
  "v_mfma_f32_16x16x32_f16 a[180:183], v[60:63], v[104:107], a[180:183]\n"
  "s_add_i32 m0, s47, 30720\n"
  "ds_read_b128 v[104:107], v232 offset:2048\n"
  "v_mfma_f32_16x16x32_f16 a[148:151], v[64:67], v[108:111], a[148:151]\n"
  "s_add_i32 s64, s50, 32768\n"
  "buffer_load_dwordx4 v238, s[40:43], s64 offen sc1 lds\n"
  "v_mfma_f32_16x16x32_f16 a[160:163], v[68:71], v[108:111], a[160:163]\n"
  "s_add_i32 s47, s44, s46\n"
  "s_add_i32 s50, s50, 4194304\n"
  "v_mfma_f32_16x16x32_f16 a[172:175], v[72:75], v[108:111], a[172:175]\n"
  "s_add_i32 s48, s47, s45\n"
  "s_add_i32 s46, s46, s49\n"
  "v_mfma_f32_16x16x32_f16 a[184:187], v[76:79], v[108:111], a[184:187]\n"
  "ds_read_b128 v[108:111], v232 offset:2560\n"
  "v_add_u32 v232, s49, v232\n"
  "v_mfma_f32_16x16x32_f16 a[152:155], v[80:83], v[120:123], a[152:155]\n"
  "v_mfma_f32_16x16x32_f16 a[164:167], v[84:87], v[120:123], a[164:167]\n"
  "v_mfma_f32_16x16x32_f16 a[176:179], v[88:91], v[120:123], a[176:179]\n"
  "v_mfma_f32_16x16x32_f16 a[188:191], v[92:95], v[120:123], a[188:191]\n"
  "v_mfma_f32_16x16x32_f16 a[192:195], v[48:51], v[112:115], a[192:195]\n"
  "v_pk_add_f16 v120, v112, v116\n"
  "v_pk_add_f16 v121, v113, v117\n"
  "v_mfma_f32_16x16x32_f16 a[204:207], v[52:55], v[112:115], a[204:207]\n"
  "v_pk_add_f16 v122, v114, v118\n"
  "v_pk_add_f16 v123, v115, v119\n"
  "v_mfma_f32_16x16x32_f16 a[216:219], v[56:59], v[112:115], a[216:219]\n"
  "v_mfma_f32_16x16x32_f16 a[228:231], v[60:63], v[112:115], a[228:231]\n"
  "v_mfma_f32_16x16x32_f16 a[196:199], v[64:67], v[116:119], a[196:199]\n"
  "v_mfma_f32_16x16x32_f16 a[208:211], v[68:71], v[116:119], a[208:211]\n"
  "v_mfma_f32_16x16x32_f16 a[220:223], v[72:75], v[116:119], a[220:223]\n"
  "v_mfma_f32_16x16x32_f16 a[232:235], v[76:79], v[116:119], a[232:235]\n"
  "v_mfma_f32_16x16x32_f16 a[200:203], v[80:83], v[120:123], a[200:203]\n"
  "s_waitcnt vmcnt(10) lgkmcnt(0)\n"
  "v_mfma_f32_16x16x32_f16 a[212:215], v[84:87], v[120:123], a[212:215]\n"
  "v_mfma_f32_16x16x32_f16 a[224:227], v[88:91], v[120:123], a[224:227]\n"
  "s_barrier\n"
  "v_mfma_f32_16x16x32_f16 a[236:239], v[92:95], v[120:123], a[236:239]\n"
  "s_sub_i32 s63, s63, 1\n"
  "s_cmp_lg_u32 s63, 0\n"
  "s_cbranch_scc1 .LloopN_%=\n"
  "v_mfma_f32_16x16x32_f16 v[124:127], v[0:3], v[96:99], v[124:127]\n"
  "s_add_i32 s67, s44, s45\n"
  "buffer_load_dword v247, v246, s[40:43], s54 offen sc1\n"
  "v_pk_add_f16 v120, v96, v100\n"
  "v_mfma_f32_16x16x32_f16 v[136:139], v[4:7], v[96:99], v[136:139]\n"
  "v_pk_add_f16 v121, v97, v101\n"
  "v_pk_add_f16 v122, v98, v102\n"
  "v_mfma_f32_16x16x32_f16 v[148:151], v[8:11], v[96:99], v[148:151]\n"
  "ds_read_b64_tr_b16 v[48:49], v228 offset:0\n"
  "v_pk_add_f16 v123, v99, v103\n"
  "v_mfma_f32_16x16x32_f16 v[160:163], v[12:15], v[96:99], v[160:163]\n"
  "s_add_i32 m0, s47, 0\n"
  "ds_read_b64_tr_b16 v[50:51], v228 offset:8192\n"
  "ds_read_b128 v[96:99], v232 offset:4096\n"
  "v_mfma_f32_16x16x32_f16 v[128:131], v[16:19], v[100:103], v[128:131]\n"
  "s_add_i32 s64, s50, 4194304\n"
  "buffer_load_dwordx4 v237, s[40:43], s64 offen sc1 lds\n"
  "ds_read_b64_tr_b16 v[64:65], v228 offset:256\n"
  "v_mfma_f32_16x16x32_f16 v[140:143], v[20:23], v[100:103], v[140:143]\n"
  "s_cmp_eq_u32 s46, 73728\n"
  "s_cselect_b32 s49, s68, s69\n"
  "ds_read_b64_tr_b16 v[66:67], v228 offset:8448\n"
  "v_mfma_f32_16x16x32_f16 v[152:155], v[24:27], v[100:103], v[152:155]\n"
  "ds_read_b64_tr_b16 v[52:53], v229 offset:0\n"
  "v_add_u32 v228, s49, v228\n"
  "v_mfma_f32_16x16x32_f16 v[164:167], v[28:31], v[100:103], v[164:167]\n"
  "s_add_i32 m0, s47, 4096\n"
  "ds_read_b128 v[100:103], v232 offset:4608\n"
  "ds_read_b64_tr_b16 v[54:55], v229 offset:8192\n"
  "v_mfma_f32_16x16x32_f16 v[132:135], v[32:35], v[120:123], v[132:135]\n"
  "s_add_i32 s64, s50, 4198400\n"
  "buffer_load_dwordx4 v237, s[40:43], s64 offen sc1 lds\n"
  "ds_read_b64_tr_b16 v[68:69], v229 offset:256\n"
  "v_mfma_f32_16x16x32_f16 v[144:147], v[36:39], v[120:123], v[144:147]\n"
  "ds_read_b64_tr_b16 v[70:71], v229 offset:8448\n"
  "v_add_u32 v229, s49, v229\n"
  "v_mfma_f32_16x16x32_f16 v[156:159], v[40:43], v[120:123], v[156:159]\n"
  "ds_read_b64_tr_b16 v[56:57], v230 offset:0\n"
  "ds_read_b64_tr_b16 v[58:59], v230 offset:8192\n"
  "v_mfma_f32_16x16x32_f16 v[168:171], v[44:47], v[120:123], v[168:171]\n"
  "s_add_i32 m0, s47, 8192\n"
  "ds_read_b64_tr_b16 v[72:73], v230 offset:256\n"
  "ds_read_b64_tr_b16 v[74:75], v230 offset:8448\n"
  "v_mfma_f32_16x16x32_f16 v[180:183], v[0:3], v[104:107], v[180:183]\n"
  "s_add_i32 s64, s50, 4202496\n"
  "buffer_load_dwordx4 v237, s[40:43], s64 offen sc1 lds\n"
  "v_pk_add_f16 v120, v104, v108\n"
  "v_mfma_f32_16x16x32_f16 v[192:195], v[4:7], v[104:107], v[192:195]\n"
  "s_waitcnt lgkmcnt(9)\n"
  "v_pk_add_f16 v121, v105, v109\n"
  "v_pk_add_f16 v122, v106, v110\n"
  "v_mfma_f32_16x16x32_f16 v[204:207], v[8:11], v[104:107], v[204:207]\n"
  "ds_read_b64_tr_b16 v[60:61], v231 offset:0\n"
  "v_pk_add_f16 v123, v107, v111\n"
  "v_mfma_f32_16x16x32_f16 v[216:219], v[12:15], v[104:107], v[216:219]\n"
  "s_add_i32 m0, s47, 12288\n"
  "ds_read_b128 v[104:107], v232 offset:6144\n"
  "v_pk_add_f16 v80, v48, v64\n"
  "v_mfma_f32_16x16x32_f16 v[184:187], v[16:19], v[108:111], v[184:187]\n"
  "s_waitcnt lgkmcnt(6)\n"
  "s_add_i32 s64, s50, 4206592\n"
  "buffer_load_dwordx4 v237, s[40:43], s64 offen sc1 lds\n"
  "v_mfma_f32_16x16x32_f16 v[196:199], v[20:23], v[108:111], v[196:199]\n"
  "ds_read_b64_tr_b16 v[62:63], v231 offset:8192\n"
  "v_pk_add_f16 v81, v49, v65\n"
  "v_mfma_f32_16x16x32_f16 v[208:211], v[24:27], v[108:111], v[208:211]\n"
  "s_waitcnt lgkmcnt(3)\n"
  "ds_read_b128 v[112:115], v232 offset:8192\n"
  "v_pk_add_f16 v82, v50, v66\n"
  "v_mfma_f32_16x16x32_f16 v[220:223], v[28:31], v[108:111], v[220:223]\n"
  "s_add_i32 m0, s47, 16384\n"
  "ds_read_b128 v[108:111], v232 offset:6656\n"
  "v_pk_add_f16 v83, v51, v67\n"
  "v_mfma_f32_16x16x32_f16 v[188:191], v[32:35], v[120:123], v[188:191]\n"
  "s_add_i32 s64, s50, 4194304\n"
  "buffer_load_dwordx4 v238, s[40:43], s64 offen sc1 lds\n"
  "v_pk_add_f16 v84, v52, v68\n"
  "v_mfma_f32_16x16x32_f16 v[200:203], v[36:39], v[120:123], v[200:203]\n"
  "ds_read_b64_tr_b16 v[76:77], v231 offset:256\n"
  "v_pk_add_f16 v85, v53, v69\n"
  "v_mfma_f32_16x16x32_f16 v[212:215], v[40:43], v[120:123], v[212:215]\n"
  "ds_read_b128 v[116:119], v232 offset:8704\n"
  "v_pk_add_f16 v86, v54, v70\n"
  "v_mfma_f32_16x16x32_f16 v[224:227], v[44:47], v[120:123], v[224:227]\n"
  "s_add_i32 m0, s47, 20480\n"
  "ds_read_b64_tr_b16 v[78:79], v231 offset:8448\n"
  "v_pk_add_f16 v87, v55, v71\n"
  "v_mfma_f32_16x16x32_f16 a[96:99], v[0:3], v[96:99], a[96:99]\n"
  "s_add_i32 s64, s50, 4096\n"
  "buffer_load_dwordx4 v238, s[40:43], s64 offen sc1 lds\n"
  "v_pk_add_f16 v120, v96, v100\n"
  "v_mfma_f32_16x16x32_f16 a[108:111], v[4:7], v[96:99], a[108:111]\n"
  "v_pk_add_f16 v121, v97, v101\n"
  "v_pk_add_f16 v122, v98, v102\n"
  "v_mfma_f32_16x16x32_f16 a[120:123], v[8:11], v[96:99], a[120:123]\n"
  "ds_write_b128 v253, v[172:175]\n"
  "v_pk_add_f16 v123, v99, v103\n"
  "v_mfma_f32_16x16x32_f16 a[132:135], v[12:15], v[96:99], a[132:135]\n"
  "s_add_i32 m0, s48, 24576\n"
  "ds_read_b128 v[96:99], v232 offset:0\n"
  "v_pk_add_f16 v88, v56, v72\n"
  "v_mfma_f32_16x16x32_f16 a[100:103], v[16:19], v[100:103], a[100:103]\n"
  "s_add_i32 s64, s50, 16384\n"
  "buffer_load_dwordx4 v238, s[40:43], s64 offen sc1 lds\n"
  "v_pk_add_f16 v89, v57, v73\n"
  "v_mfma_f32_16x16x32_f16 a[112:115], v[20:23], v[100:103], a[112:115]\n"
  "v_pk_add_f16 v90, v58, v74\n"
  "v_pk_add_f16 v91, v59, v75\n"
  "v_mfma_f32_16x16x32_f16 a[124:127], v[24:27], v[100:103], a[124:127]\n"
  "s_waitcnt lgkmcnt(2)\n"
  "v_pk_add_f16 v92, v60, v76\n"
  "v_pk_add_f16 v93, v61, v77\n"
  "v_mfma_f32_16x16x32_f16 a[136:139], v[28:31], v[100:103], a[136:139]\n"
  "s_add_i32 m0, s47, 26624\n"
  "ds_read_b128 v[100:103], v232 offset:512\n"
  "v_pk_add_f16 v94, v62, v78\n"
  "v_mfma_f32_16x16x32_f16 a[104:107], v[32:35], v[120:123], a[104:107]\n"
  "s_add_i32 s64, s50, 4214784\n"
  "buffer_load_dwordx4 v238, s[40:43], s64 offen sc1 lds\n"
  "v_pk_add_f16 v95, v63, v79\n"
  "v_mfma_f32_16x16x32_f16 a[116:119], v[36:39], v[120:123], a[116:119]\n"
  "v_add_u32 v230, s49, v230\n"
  "v_add_u32 v231, s49, v231\n"
  "v_mfma_f32_16x16x32_f16 a[128:131], v[40:43], v[120:123], a[128:131]\n"
  "v_mfma_f32_16x16x32_f16 a[140:143], v[44:47], v[120:123], a[140:143]\n"
  "s_add_i32 m0, s47, 30720\n"
  "v_mfma_f32_16x16x32_f16 a[144:147], v[0:3], v[104:107], a[144:147]\n"
  "s_add_i32 s64, s50, 32768\n"
  "buffer_load_dwordx4 v238, s[40:43], s64 offen sc1 lds\n"
  "v_pk_add_f16 v120, v104, v108\n"
  "v_mfma_f32_16x16x32_f16 a[156:159], v[4:7], v[104:107], a[156:159]\n"
  "v_pk_add_f16 v121, v105, v109\n"
  "v_pk_add_f16 v122, v106, v110\n"
  "v_mfma_f32_16x16x32_f16 a[168:171], v[8:11], v[104:107], a[168:171]\n"
  "v_pk_add_f16 v123, v107, v111\n"
  "v_mfma_f32_16x16x32_f16 a[180:183], v[12:15], v[104:107], a[180:183]\n"
  "s_add_i32 m0, s44, 114688\n"
  "ds_read_b128 v[104:107], v232 offset:2048\n"
  "v_mfma_f32_16x16x32_f16 a[148:151], v[16:19], v[108:111], a[148:151]\n"
  "s_add_i32 s47, s44, s46\n"
  "s_add_i32 s64, s50, 4198400\n"
  "buffer_load_dwordx4 v238, s[40:43], s64 offen sc1 lds\n"
  "v_mfma_f32_16x16x32_f16 a[160:163], v[20:23], v[108:111], a[160:163]\n"
  "s_add_i32 s48, s47, s45\n"
  "s_add_i32 s46, s46, s49\n"
  "v_mfma_f32_16x16x32_f16 a[172:175], v[24:27], v[108:111], a[172:175]\n"
  "v_mfma_f32_16x16x32_f16 a[184:187], v[28:31], v[108:111], a[184:187]\n"
  "s_add_i32 m0, s67, 118784\n"
  "ds_read_b128 v[108:111], v232 offset:2560\n"
  "v_add_u32 v232, s49, v232\n"
  "v_mfma_f32_16x16x32_f16 a[152:155], v[32:35], v[120:123], a[152:155]\n"
  "s_add_i32 s64, s50, 4210688\n"
  "buffer_load_dwordx4 v238, s[40:43], s64 offen sc1 lds\n"
  "v_mfma_f32_16x16x32_f16 a[164:167], v[36:39], v[120:123], a[164:167]\n"
  "v_mfma_f32_16x16x32_f16 a[176:179], v[40:43], v[120:123], a[176:179]\n"
  "v_mfma_f32_16x16x32_f16 a[188:191], v[44:47], v[120:123], a[188:191]\n"
  "s_add_i32 m0, s44, 124928\n"
  "v_mfma_f32_16x16x32_f16 a[192:195], v[0:3], v[112:115], a[192:195]\n"
  "s_add_i32 s64, s50, 4227072\n"
  "buffer_load_dwordx4 v238, s[40:43], s64 offen sc1 lds\n"
  "v_pk_add_f16 v120, v112, v116\n"
  "v_mfma_f32_16x16x32_f16 a[204:207], v[4:7], v[112:115], a[204:207]\n"
  "s_add_i32 s50, s50, 4194304\n"
  "v_pk_add_f16 v121, v113, v117\n"
  "v_pk_add_f16 v122, v114, v118\n"
  "v_mfma_f32_16x16x32_f16 a[216:219], v[8:11], v[112:115], a[216:219]\n"
  "v_pk_add_f16 v123, v115, v119\n"
  "v_mfma_f32_16x16x32_f16 a[228:231], v[12:15], v[112:115], a[228:231]\n"
  "v_mfma_f32_16x16x32_f16 a[196:199], v[16:19], v[116:119], a[196:199]\n"
  "v_mfma_f32_16x16x32_f16 a[208:211], v[20:23], v[116:119], a[208:211]\n"
  "v_mfma_f32_16x16x32_f16 a[220:223], v[24:27], v[116:119], a[220:223]\n"
  "v_mfma_f32_16x16x32_f16 a[232:235], v[28:31], v[116:119], a[232:235]\n"
  "v_mfma_f32_16x16x32_f16 a[200:203], v[32:35], v[120:123], a[200:203]\n"
  "s_waitcnt vmcnt(12) lgkmcnt(0)\n"
  "v_mfma_f32_16x16x32_f16 a[212:215], v[36:39], v[120:123], a[212:215]\n"
  "v_cmp_eq_u32 vcc, 0, v247\n"
  "s_cbranch_vccnz .Lspin_s28_None_%=\n"
  ".Lspun_s28_None_%=:\n"
  "v_mfma_f32_16x16x32_f16 a[224:227], v[40:43], v[120:123], a[224:227]\n"
  "s_barrier\n"
  "v_mfma_f32_16x16x32_f16 a[236:239], v[44:47], v[120:123], a[236:239]\n"
  "v_mfma_f32_16x16x32_f16 v[124:127], v[48:51], v[96:99], v[124:127]\n"
  "v_mbcnt_lo_u32_b32 v252, -1, 0\n"
  "v_mbcnt_hi_u32_b32 v252, -1, v252\n"
  "v_mfma_f32_16x16x32_f16 v[136:139], v[52:55], v[96:99], v[136:139]\n"
  "v_pk_add_f16 v120, v96, v100\n"
  "v_pk_add_f16 v121, v97, v101\n"
  "v_mfma_f32_16x16x32_f16 v[148:151], v[56:59], v[96:99], v[148:151]\n"
  "v_lshlrev_b32 v252, 7, v252\n"
  "v_pk_add_f16 v122, v98, v102\n"
  "v_mfma_f32_16x16x32_f16 v[160:163], v[60:63], v[96:99], v[160:163]\n"
  "s_mov_b32 m0, 147456\n"
  "v_pk_add_f16 v123, v99, v103\n"
  "v_add_u32 v252, s45, v252\n"
  "v_mfma_f32_16x16x32_f16 v[128:131], v[64:67], v[100:103], v[128:131]\n"
  "s_cmp_eq_u32 s46, 73728\n"
  "s_cselect_b32 s49, s68, s69\n"
  "global_load_lds_dword v[248:249], off\n"
  "v_mfma_f32_16x16x32_f16 v[140:143], v[68:71], v[100:103], v[140:143]\n"
  "ds_read_b64_tr_b16 v[0:1], v228 offset:0\n"
  "ds_read_b64_tr_b16 v[2:3], v228 offset:8192\n"
  "v_mfma_f32_16x16x32_f16 v[152:155], v[72:75], v[100:103], v[152:155]\n"
  "ds_read_b128 v[96:99], v232 offset:4096\n"
  "ds_read_b64_tr_b16 v[16:17], v228 offset:256\n"
  "v_mfma_f32_16x16x32_f16 v[164:167], v[76:79], v[100:103], v[164:167]\n"
  "ds_read_b128 v[100:103], v232 offset:4608\n"
  "ds_read_b64_tr_b16 v[18:19], v228 offset:8448\n"
  "v_mfma_f32_16x16x32_f16 v[132:135], v[80:83], v[120:123], v[132:135]\n"
  "ds_read_b64_tr_b16 v[4:5], v229 offset:0\n"
  "v_add_u32 v228, s49, v228\n"
  "v_mfma_f32_16x16x32_f16 v[144:147], v[84:87], v[120:123], v[144:147]\n"
  "ds_read_b64_tr_b16 v[6:7], v229 offset:8192\n"
  "ds_read_b64_tr_b16 v[20:21], v229 offset:256\n"
  "v_mfma_f32_16x16x32_f16 v[156:159], v[88:91], v[120:123], v[156:159]\n"
  "ds_read_b64_tr_b16 v[22:23], v229 offset:8448\n"
  "v_add_u32 v229, s49, v229\n"
  "v_mfma_f32_16x16x32_f16 v[168:171], v[92:95], v[120:123], v[168:171]\n"
  "s_mov_b32 m0, 144384\n"
  "ds_read_b64_tr_b16 v[8:9], v230 offset:0\n"
  "ds_read_b64_tr_b16 v[10:11], v230 offset:8192\n"
  "v_mfma_f32_16x16x32_f16 v[180:183], v[48:51], v[104:107], v[180:183]\n"
  "global_load_lds_dword v252, s[88:89]\n"
  "v_pk_add_f16 v120, v104, v108\n"
  "v_mfma_f32_16x16x32_f16 v[192:195], v[52:55], v[104:107], v[192:195]\n"
  "v_pk_add_f16 v121, v105, v109\n"
  "v_pk_add_f16 v122, v106, v110\n"
  "v_mfma_f32_16x16x32_f16 v[204:207], v[56:59], v[104:107], v[204:207]\n"
  "s_waitcnt lgkmcnt(6)\n"
  "ds_read_b64_tr_b16 v[24:25], v230 offset:256\n"
  "v_pk_add_f16 v123, v107, v111\n"
  "v_mfma_f32_16x16x32_f16 v[216:219], v[60:63], v[104:107], v[216:219]\n"
  "ds_read_b128 v[104:107], v232 offset:6144\n"
  "v_pk_add_f16 v32, v0, v16\n"
  "v_mfma_f32_16x16x32_f16 v[184:187], v[64:67], v[108:111], v[184:187]\n"
  "ds_read_b64_tr_b16 v[26:27], v230 offset:8448\n"
  "v_pk_add_f16 v33, v1, v17\n"
  "v_mfma_f32_16x16x32_f16 v[196:199], v[68:71], v[108:111], v[196:199]\n"
  "s_waitcnt lgkmcnt(5)\n"
  "ds_read_b64_tr_b16 v[12:13], v231 offset:0\n"
  "v_pk_add_f16 v34, v2, v18\n"
  "v_mfma_f32_16x16x32_f16 v[208:211], v[72:75], v[108:111], v[208:211]\n"
  "ds_read_b64_tr_b16 v[14:15], v231 offset:8192\n"
  "v_pk_add_f16 v35, v3, v19\n"
  "v_mfma_f32_16x16x32_f16 v[220:223], v[76:79], v[108:111], v[220:223]\n"
  "ds_read_b128 v[108:111], v232 offset:6656\n"
  "v_pk_add_f16 v36, v4, v20\n"
  "v_mfma_f32_16x16x32_f16 v[188:191], v[80:83], v[120:123], v[188:191]\n"
  "ds_read_b128 v[112:115], v232 offset:8192\n"
  "v_pk_add_f16 v37, v5, v21\n"
  "v_mfma_f32_16x16x32_f16 v[200:203], v[84:87], v[120:123], v[200:203]\n"
  "ds_read_b64_tr_b16 v[28:29], v231 offset:256\n"
  "v_pk_add_f16 v38, v6, v22\n"
  "v_mfma_f32_16x16x32_f16 v[212:215], v[88:91], v[120:123], v[212:215]\n"
  "ds_read_b128 v[116:119], v232 offset:8704\n"
  "v_pk_add_f16 v39, v7, v23\n"
  "v_mfma_f32_16x16x32_f16 v[224:227], v[92:95], v[120:123], v[224:227]\n"
  "s_waitcnt lgkmcnt(6)\n"
  "ds_read_b64_tr_b16 v[30:31], v231 offset:8448\n"
  "v_pk_add_f16 v40, v8, v24\n"
  "v_mfma_f32_16x16x32_f16 a[96:99], v[48:51], v[96:99], a[96:99]\n"
  "v_pk_add_f16 v120, v96, v100\n"
  "v_pk_add_f16 v121, v97, v101\n"
  "v_mfma_f32_16x16x32_f16 a[108:111], v[52:55], v[96:99], a[108:111]\n"
  "v_pk_add_f16 v122, v98, v102\n"
  "v_pk_add_f16 v123, v99, v103\n"
  "v_mfma_f32_16x16x32_f16 a[120:123], v[56:59], v[96:99], a[120:123]\n"
  "v_pk_add_f16 v41, v9, v25\n"
  "v_pk_add_f16 v42, v10, v26\n"
  "v_mfma_f32_16x16x32_f16 a[132:135], v[60:63], v[96:99], a[132:135]\n"
  "ds_read_b128 v[96:99], v232 offset:0\n"
  "v_pk_add_f16 v43, v11, v27\n"
  "v_mfma_f32_16x16x32_f16 a[100:103], v[64:67], v[100:103], a[100:103]\n"
  "v_add_u32 v230, s49, v230\n"
  "v_add_u32 v231, s49, v231\n"
  "v_mfma_f32_16x16x32_f16 a[112:115], v[68:71], v[100:103], a[112:115]\n"
  "v_mfma_f32_16x16x32_f16 a[124:127], v[72:75], v[100:103], a[124:127]\n"
  "s_waitcnt lgkmcnt(1)\n"
  "v_pk_add_f16 v44, v12, v28\n"
  "v_pk_add_f16 v45, v13, v29\n"
  "v_mfma_f32_16x16x32_f16 a[136:139], v[76:79], v[100:103], a[136:139]\n"
  "ds_read_b128 v[100:103], v232 offset:512\n"
  "v_pk_add_f16 v46, v14, v30\n"
  "v_mfma_f32_16x16x32_f16 a[104:107], v[80:83], v[120:123], a[104:107]\n"
  "v_pk_add_f16 v47, v15, v31\n"
  "v_mfma_f32_16x16x32_f16 a[116:119], v[84:87], v[120:123], a[116:119]\n"
  "v_mfma_f32_16x16x32_f16 a[128:131], v[88:91], v[120:123], a[128:131]\n"
  "v_mfma_f32_16x16x32_f16 a[140:143], v[92:95], v[120:123], a[140:143]\n"
  "v_mfma_f32_16x16x32_f16 a[144:147], v[48:51], v[104:107], a[144:147]\n"
  "v_pk_add_f16 v120, v104, v108\n"
  "v_pk_add_f16 v121, v105, v109\n"
  "v_mfma_f32_16x16x32_f16 a[156:159], v[52:55], v[104:107], a[156:159]\n"
  "v_pk_add_f16 v122, v106, v110\n"
  "v_pk_add_f16 v123, v107, v111\n"
  "v_mfma_f32_16x16x32_f16 a[168:171], v[56:59], v[104:107], a[168:171]\n"
  "v_mfma_f32_16x16x32_f16 a[180:183], v[60:63], v[104:107], a[180:183]\n"
  "ds_read_b128 v[104:107], v232 offset:2048\n"
  "v_mfma_f32_16x16x32_f16 a[148:151], v[64:67], v[108:111], a[148:151]\n"
  "s_add_i32 s47, s44, s46\n"
  "s_add_i32 s50, s50, 4194304\n"
  "v_mfma_f32_16x16x32_f16 a[160:163], v[68:71], v[108:111], a[160:163]\n"
  "s_add_i32 s48, s47, s45\n"
  "s_add_i32 s46, s46, s49\n"
  "v_mfma_f32_16x16x32_f16 a[172:175], v[72:75], v[108:111], a[172:175]\n"
  "v_mfma_f32_16x16x32_f16 a[184:187], v[76:79], v[108:111], a[184:187]\n"
  "ds_read_b128 v[108:111], v232 offset:2560\n"
  "v_add_u32 v232, s49, v232\n"
  "v_mfma_f32_16x16x32_f16 a[152:155], v[80:83], v[120:123], a[152:155]\n"
  "v_mfma_f32_16x16x32_f16 a[164:167], v[84:87], v[120:123], a[164:167]\n"
  "v_mfma_f32_16x16x32_f16 a[176:179], v[88:91], v[120:123], a[176:179]\n"
  "v_mfma_f32_16x16x32_f16 a[188:191], v[92:95], v[120:123], a[188:191]\n"
  "v_mfma_f32_16x16x32_f16 a[192:195], v[48:51], v[112:115], a[192:195]\n"
  "v_pk_add_f16 v120, v112, v116\n"
  "v_pk_add_f16 v121, v113, v117\n"
  "v_mfma_f32_16x16x32_f16 a[204:207], v[52:55], v[112:115], a[204:207]\n"
  "v_pk_add_f16 v122, v114, v118\n"
  "v_pk_add_f16 v123, v115, v119\n"
  "v_mfma_f32_16x16x32_f16 a[216:219], v[56:59], v[112:115], a[216:219]\n"
  "v_mfma_f32_16x16x32_f16 a[228:231], v[60:63], v[112:115], a[228:231]\n"
  "v_mfma_f32_16x16x32_f16 a[196:199], v[64:67], v[116:119], a[196:199]\n"
  "v_mfma_f32_16x16x32_f16 a[208:211], v[68:71], v[116:119], a[208:211]\n"
  "v_mfma_f32_16x16x32_f16 a[220:223], v[72:75], v[116:119], a[220:223]\n"
  "v_mfma_f32_16x16x32_f16 a[232:235], v[76:79], v[116:119], a[232:235]\n"
  "v_mfma_f32_16x16x32_f16 a[200:203], v[80:83], v[120:123], a[200:203]\n"
  "s_waitcnt vmcnt(2) lgkmcnt(0)\n"
  "v_mfma_f32_16x16x32_f16 a[212:215], v[84:87], v[120:123], a[212:215]\n"
  "v_mfma_f32_16x16x32_f16 a[224:227], v[88:91], v[120:123], a[224:227]\n"
  "s_barrier\n"
  "v_mfma_f32_16x16x32_f16 a[236:239], v[92:95], v[120:123], a[236:239]\n"
  "v_mfma_f32_16x16x32_f16 v[124:127], v[0:3], v[96:99], v[124:127]\n"
  "v_pk_add_f16 v120, v96, v100\n"
  "v_pk_add_f16 v121, v97, v101\n"
  "v_mfma_f32_16x16x32_f16 v[136:139], v[4:7], v[96:99], v[136:139]\n"
  "v_pk_add_f16 v122, v98, v102\n"
  "v_pk_add_f16 v123, v99, v103\n"
  "v_mfma_f32_16x16x32_f16 v[148:151], v[8:11], v[96:99], v[148:151]\n"
  "ds_read_b128 v[112:115], v232 offset:0\n"
  "v_add_u32 v242, 57344, v232\n"
  "v_mfma_f32_16x16x32_f16 v[160:163], v[12:15], v[96:99], v[160:163]\n"
  "s_mov_b32 s66, 0x55555555\n"
  "s_mov_b32 s67, 0x55555555\n"
  "ds_read_b128 v[116:119], v232 offset:512\n"
  "v_mfma_f32_16x16x32_f16 v[128:131], v[16:19], v[100:103], v[128:131]\n"
  "ds_read_b64_tr_b16 v[48:49], v228 offset:0\n"
  "ds_read_b64_tr_b16 v[50:51], v228 offset:8192\n"
  "v_mfma_f32_16x16x32_f16 v[140:143], v[20:23], v[100:103], v[140:143]\n"
  "ds_read_b64_tr_b16 v[64:65], v228 offset:256\n"
  "ds_read_b64_tr_b16 v[66:67], v228 offset:8448\n"
  "v_mfma_f32_16x16x32_f16 v[152:155], v[24:27], v[100:103], v[152:155]\n"
  "ds_read_b64_tr_b16 v[52:53], v229 offset:0\n"
  "ds_read_b64_tr_b16 v[54:55], v229 offset:8192\n"
  "v_mfma_f32_16x16x32_f16 v[164:167], v[28:31], v[100:103], v[164:167]\n"
  "ds_read_b64_tr_b16 v[68:69], v229 offset:256\n"
  "ds_read_b64_tr_b16 v[70:71], v229 offset:8448\n"
  "v_mfma_f32_16x16x32_f16 v[132:135], v[32:35], v[120:123], v[132:135]\n"
  "ds_read_b64_tr_b16 v[56:57], v230 offset:0\n"
  "ds_read_b64_tr_b16 v[58:59], v230 offset:8192\n"
  "v_mfma_f32_16x16x32_f16 v[144:147], v[36:39], v[120:123], v[144:147]\n"
  "ds_read_b64_tr_b16 v[72:73], v230 offset:256\n"
  "ds_read_b64_tr_b16 v[74:75], v230 offset:8448\n"
  "v_mfma_f32_16x16x32_f16 v[156:159], v[40:43], v[120:123], v[156:159]\n"
  "ds_read_b64_tr_b16 v[60:61], v231 offset:0\n"
  "ds_read_b64_tr_b16 v[62:63], v231 offset:8192\n"
  "v_mfma_f32_16x16x32_f16 v[168:171], v[44:47], v[120:123], v[168:171]\n"
  "ds_read_b64_tr_b16 v[76:77], v231 offset:256\n"
  "ds_read_b64_tr_b16 v[78:79], v231 offset:8448\n"
  "v_mfma_f32_16x16x32_f16 v[180:183], v[0:3], v[104:107], v[180:183]\n"
  "s_waitcnt lgkmcnt(12)\n"
  "v_pk_add_f16 v120, v104, v108\n"
  "v_pk_add_f16 v121, v105, v109\n"
  "v_mfma_f32_16x16x32_f16 v[192:195], v[4:7], v[104:107], v[192:195]\n"
  "v_pk_add_f16 v122, v106, v110\n"
  "v_pk_add_f16 v123, v107, v111\n"
  "v_mfma_f32_16x16x32_f16 v[204:207], v[8:11], v[104:107], v[204:207]\n"
  "s_waitcnt lgkmcnt(8)\n"
  "v_pk_add_f16 v80, v48, v64\n"
  "v_pk_add_f16 v81, v49, v65\n"
  "v_mfma_f32_16x16x32_f16 v[216:219], v[12:15], v[104:107], v[216:219]\n"
  "v_pk_add_f16 v82, v50, v66\n"
  "v_pk_add_f16 v83, v51, v67\n"
  "v_mfma_f32_16x16x32_f16 v[184:187], v[16:19], v[108:111], v[184:187]\n"
  "s_waitcnt lgkmcnt(4)\n"
  "ds_read_b128 v[96:99], v232 offset:2048\n"
  "v_pk_add_f16 v84, v52, v68\n"
  "v_mfma_f32_16x16x32_f16 v[196:199], v[20:23], v[108:111], v[196:199]\n"
  "ds_read_b128 v[100:103], v232 offset:2560\n"
  "v_pk_add_f16 v85, v53, v69\n"
  "v_mfma_f32_16x16x32_f16 v[208:211], v[24:27], v[108:111], v[208:211]\n"
  "s_waitcnt lgkmcnt(2)\n"
  "v_pk_add_f16 v86, v54, v70\n"
  "v_pk_add_f16 v87, v55, v71\n"
  "v_mfma_f32_16x16x32_f16 v[220:223], v[28:31], v[108:111], v[220:223]\n"
  "v_pk_add_f16 v88, v56, v72\n"
  "v_pk_add_f16 v89, v57, v73\n"
  "v_mfma_f32_16x16x32_f16 v[188:191], v[32:35], v[120:123], v[188:191]\n"
  "v_pk_add_f16 v90, v58, v74\n"
  "v_pk_add_f16 v91, v59, v75\n"
  "v_mfma_f32_16x16x32_f16 v[200:203], v[36:39], v[120:123], v[200:203]\n"
  "v_pk_add_f16 v92, v60, v76\n"
  "v_pk_add_f16 v93, v61, v77\n"
  "v_mfma_f32_16x16x32_f16 v[212:215], v[40:43], v[120:123], v[212:215]\n"
  "v_pk_add_f16 v94, v62, v78\n"
  "v_pk_add_f16 v95, v63, v79\n"
  "v_mfma_f32_16x16x32_f16 v[224:227], v[44:47], v[120:123], v[224:227]\n"
  "v_mfma_f32_16x16x32_f16 v[124:127], v[48:51], v[112:115], v[124:127]\n"
  "v_pk_add_f16 v120, v112, v116\n"
  "v_pk_add_f16 v121, v113, v117\n"
  "v_mfma_f32_16x16x32_f16 v[136:139], v[52:55], v[112:115], v[136:139]\n"
  "v_pk_add_f16 v122, v114, v118\n"
  "v_pk_add_f16 v123, v115, v119\n"
  "v_mfma_f32_16x16x32_f16 v[128:131], v[64:67], v[116:119], v[128:131]\n"
  "ds_read_b128 v[104:107], v232 offset:4096\n"
  "ds_read_b128 v[108:111], v232 offset:4608\n"
  "v_mfma_f32_16x16x32_f16 v[140:143], v[68:71], v[116:119], v[140:143]\n"
  "v_mfma_f32_16x16x32_f16 v[132:135], v[80:83], v[120:123], v[132:135]\n"
  "v_mfma_f32_16x16x32_f16 v[144:147], v[84:87], v[120:123], v[144:147]\n"
  "v_mfma_f32_16x16x32_f16 v[148:151], v[56:59], v[112:115], v[148:151]\n"
  "v_mfma_f32_16x16x32_f16 v[160:163], v[60:63], v[112:115], v[160:163]\n"
  "v_fmac_f32 v124, s76, v132\n"
  "v_fmac_f32 v128, s76, v132\n"
  "v_mfma_f32_16x16x32_f16 v[152:155], v[72:75], v[116:119], v[152:155]\n"
  "s_waitcnt lgkmcnt(2)\n"
  "v_fmac_f32 v125, s76, v133\n"
  "v_fmac_f32 v129, s76, v133\n"
  "v_mfma_f32_16x16x32_f16 v[164:167], v[76:79], v[116:119], v[164:167]\n"
  "v_fmac_f32 v126, s76, v134\n"
  "v_fmac_f32 v130, s76, v134\n"
  "v_mfma_f32_16x16x32_f16 v[156:159], v[88:91], v[120:123], v[156:159]\n"
  "v_fmac_f32 v127, s76, v135\n"
  "v_fmac_f32 v131, s76, v135\n"
  "v_mfma_f32_16x16x32_f16 v[168:171], v[92:95], v[120:123], v[168:171]\n"
  "v_cvt_pk_bf16_f32 v172, v124, v125\n"
  "v_cvt_pk_bf16_f32 v173, v126, v127\n"
  "v_mfma_f32_16x16x32_f16 v[180:183], v[48:51], v[96:99], v[180:183]\n"
  "v_pk_add_f16 v120, v96, v100\n"
  "v_pk_add_f16 v121, v97, v101\n"
  "v_mfma_f32_16x16x32_f16 v[192:195], v[52:55], v[96:99], v[192:195]\n"
  "v_pk_add_f16 v122, v98, v102\n"
  "v_pk_add_f16 v123, v99, v103\n"
  "v_mfma_f32_16x16x32_f16 v[184:187], v[64:67], v[100:103], v[184:187]\n"
  "ds_read_b128 v[112:115], v242 offset:4096\n"
  "v_cvt_pk_bf16_f32 v176, v128, v129\n"
  "v_cvt_pk_bf16_f32 v177, v130, v131\n"
  "v_fmac_f32 v136, s76, v144\n"
  "v_fmac_f32 v140, s76, v144\n"
  "v_mfma_f32_16x16x32_f16 v[196:199], v[68:71], v[100:103], v[196:199]\n"
  "ds_read_b128 v[116:119], v242 offset:4608\n"
  "v_fmac_f32 v137, s76, v145\n"
  "v_fmac_f32 v141, s76, v145\n"
  "v_fmac_f32 v138, s76, v146\n"
  "v_fmac_f32 v142, s76, v146\n"
  "v_mfma_f32_16x16x32_f16 v[188:191], v[80:83], v[120:123], v[188:191]\n"
  "v_fmac_f32 v139, s76, v147\n"
  "v_fmac_f32 v143, s76, v147\n"
  "v_cvt_pk_bf16_f32 v174, v136, v137\n"
  "v_cvt_pk_bf16_f32 v175, v138, v139\n"
  "v_cvt_pk_bf16_f32 v178, v140, v141\n"
  "v_mfma_f32_16x16x32_f16 v[200:203], v[84:87], v[120:123], v[200:203]\n"
  "v_cvt_pk_bf16_f32 v179, v142, v143\n"
  "v_fmac_f32 v148, s76, v156\n"
  "v_fmac_f32 v152, s76, v156\n"
  "v_fmac_f32 v149, s76, v157\n"
  "v_fmac_f32 v153, s76, v157\n"
  "v_mfma_f32_16x16x32_f16 v[204:207], v[56:59], v[96:99], v[204:207]\n"
  "s_mov_b64 vcc, s[66:67]\n"
  "v_cndmask_b32_dpp v234, v176, v172, vcc quad_perm:[1,0,3,2] row_mask:0xf bank_mask:0xf\n"
  "v_cndmask_b32_dpp v235, v177, v173, vcc quad_perm:[1,0,3,2] row_mask:0xf bank_mask:0xf\n"
  "v_cndmask_b32_dpp v236, v178, v174, vcc quad_perm:[1,0,3,2] row_mask:0xf bank_mask:0xf\n"
  "v_cndmask_b32_dpp v237, v179, v175, vcc quad_perm:[1,0,3,2] row_mask:0xf bank_mask:0xf\n"
  "v_fmac_f32 v150, s76, v158\n"
  "v_mfma_f32_16x16x32_f16 v[216:219], v[60:63], v[96:99], v[216:219]\n"
  "s_add_i32 s64, s80, 0\n"
  "buffer_store_dwordx4 v[234:237], v250, s[72:75], s64 offen sc1\n"
  "s_not_b64 vcc, vcc\n"
  "v_cndmask_b32_dpp v238, v172, v176, vcc quad_perm:[1,0,3,2] row_mask:0xf bank_mask:0xf\n"
  "v_cndmask_b32_dpp v239, v173, v177, vcc quad_perm:[1,0,3,2] row_mask:0xf bank_mask:0xf\n"
  "v_cndmask_b32_dpp v240, v174, v178, vcc quad_perm:[1,0,3,2] row_mask:0xf bank_mask:0xf\n"
  "v_cndmask_b32_dpp v241, v175, v179, vcc quad_perm:[1,0,3,2] row_mask:0xf bank_mask:0xf\n"
  "v_mfma_f32_16x16x32_f16 v[208:211], v[72:75], v[100:103], v[208:211]\n"
  "s_waitcnt lgkmcnt(2)\n"
  "s_add_i32 s65, s80, 65536\n"
  "buffer_store_dwordx4 v[238:241], v250, s[72:75], s65 offen sc1\n"
  "v_fmac_f32 v154, s76, v158\n"
  "v_fmac_f32 v151, s76, v159\n"
  "v_fmac_f32 v155, s76, v159\n"
  "v_cvt_pk_bf16_f32 v172, v148, v149\n"
  "v_mfma_f32_16x16x32_f16 v[220:223], v[76:79], v[100:103], v[220:223]\n"
  "v_cvt_pk_bf16_f32 v173, v150, v151\n"
  "v_cvt_pk_bf16_f32 v176, v152, v153\n"
  "v_cvt_pk_bf16_f32 v177, v154, v155\n"
  "v_fmac_f32 v160, s76, v168\n"
  "v_fmac_f32 v164, s76, v168\n"
  "v_mfma_f32_16x16x32_f16 v[212:215], v[88:91], v[120:123], v[212:215]\n"
  "v_fmac_f32 v161, s76, v169\n"
  "v_fmac_f32 v165, s76, v169\n"
  "v_fmac_f32 v162, s76, v170\n"
  "v_fmac_f32 v166, s76, v170\n"
  "v_fmac_f32 v163, s76, v171\n"
  "v_mfma_f32_16x16x32_f16 v[224:227], v[92:95], v[120:123], v[224:227]\n"
  "v_fmac_f32 v167, s76, v171\n"
  "v_cvt_pk_bf16_f32 v174, v160, v161\n"
  "v_cvt_pk_bf16_f32 v175, v162, v163\n"
  "v_cvt_pk_bf16_f32 v178, v164, v165\n"
  "v_cvt_pk_bf16_f32 v179, v166, v167\n"
  "v_mfma_f32_16x16x32_f16 a[96:99], v[0:3], v[104:107], a[96:99]\n"
  "ds_read_b128 v[96:99], v232 offset:6144\n"
  "v_pk_add_f16 v120, v104, v108\n"
  "v_pk_add_f16 v121, v105, v109\n"
  "v_pk_add_f16 v122, v106, v110\n"
  "v_pk_add_f16 v123, v107, v111\n"
  "v_mfma_f32_16x16x32_f16 a[108:111], v[4:7], v[104:107], a[108:111]\n"
  "ds_read_b128 v[100:103], v232 offset:6656\n"
  "s_mov_b64 vcc, s[66:67]\n"
  "v_cndmask_b32_dpp v234, v176, v172, vcc quad_perm:[1,0,3,2] row_mask:0xf bank_mask:0xf\n"
  "v_cndmask_b32_dpp v235, v177, v173, vcc quad_perm:[1,0,3,2] row_mask:0xf bank_mask:0xf\n"
  "v_cndmask_b32_dpp v236, v178, v174, vcc quad_perm:[1,0,3,2] row_mask:0xf bank_mask:0xf\n"
  "v_cndmask_b32_dpp v237, v179, v175, vcc quad_perm:[1,0,3,2] row_mask:0xf bank_mask:0xf\n"
  "v_mfma_f32_16x16x32_f16 a[120:123], v[8:11], v[104:107], a[120:123]\n"
  "s_add_i32 s64, s80, 4096\n"
  "buffer_store_dwordx4 v[234:237], v250, s[72:75], s64 offen sc1\n"
  "s_not_b64 vcc, vcc\n"
  "v_cndmask_b32_dpp v238, v172, v176, vcc quad_perm:[1,0,3,2] row_mask:0xf bank_mask:0xf\n"
  "v_cndmask_b32_dpp v239, v173, v177, vcc quad_perm:[1,0,3,2] row_mask:0xf bank_mask:0xf\n"
  "v_cndmask_b32_dpp v240, v174, v178, vcc quad_perm:[1,0,3,2] row_mask:0xf bank_mask:0xf\n"
  "v_cndmask_b32_dpp v241, v175, v179, vcc quad_perm:[1,0,3,2] row_mask:0xf bank_mask:0xf\n"
  "v_mfma_f32_16x16x32_f16 a[132:135], v[12:15], v[104:107], a[132:135]\n"
  "s_add_i32 s65, s80, 69632\n"
  "buffer_store_dwordx4 v[238:241], v250, s[72:75], s65 offen sc1\n"
  "v_fmac_f32 v180, s76, v188\n"
  "v_fmac_f32 v184, s76, v188\n"
  "v_fmac_f32 v181, s76, v189\n"
  "v_fmac_f32 v185, s76, v189\n"
  "v_mfma_f32_16x16x32_f16 a[100:103], v[16:19], v[108:111], a[100:103]\n"
  "v_fmac_f32 v182, s76, v190\n"
  "v_fmac_f32 v186, s76, v190\n"
  "v_fmac_f32 v183, s76, v191\n"
  "v_fmac_f32 v187, s76, v191\n"
  "v_cvt_pk_bf16_f32 v172, v180, v181\n"
  "v_mfma_f32_16x16x32_f16 a[112:115], v[20:23], v[108:111], a[112:115]\n"
  "v_cvt_pk_bf16_f32 v173, v182, v183\n"
  "v_cvt_pk_bf16_f32 v176, v184, v185\n"
  "v_cvt_pk_bf16_f32 v177, v186, v187\n"
  "v_fmac_f32 v192, s76, v200\n"
  "v_fmac_f32 v196, s76, v200\n"
  "v_mfma_f32_16x16x32_f16 a[124:127], v[24:27], v[108:111], a[124:127]\n"
  "v_fmac_f32 v193, s76, v201\n"
  "v_fmac_f32 v197, s76, v201\n"
  "v_fmac_f32 v194, s76, v202\n"
  "v_fmac_f32 v198, s76, v202\n"
  "v_fmac_f32 v195, s76, v203\n"
  "v_mfma_f32_16x16x32_f16 a[136:139], v[28:31], v[108:111], a[136:139]\n"
  "v_fmac_f32 v199, s76, v203\n"
  "v_cvt_pk_bf16_f32 v174, v192, v193\n"
  "v_cvt_pk_bf16_f32 v175, v194, v195\n"
  "v_cvt_pk_bf16_f32 v178, v196, v197\n"
  "v_cvt_pk_bf16_f32 v179, v198, v199\n"
  "v_mfma_f32_16x16x32_f16 a[104:107], v[32:35], v[120:123], a[104:107]\n"
  "s_waitcnt lgkmcnt(2)\n"
  "s_mov_b64 vcc, s[66:67]\n"
  "v_cndmask_b32_dpp v234, v176, v172, vcc quad_perm:[1,0,3,2] row_mask:0xf bank_mask:0xf\n"
  "v_cndmask_b32_dpp v235, v177, v173, vcc quad_perm:[1,0,3,2] row_mask:0xf bank_mask:0xf\n"
  "v_cndmask_b32_dpp v236, v178, v174, vcc quad_perm:[1,0,3,2] row_mask:0xf bank_mask:0xf\n"
  "v_cndmask_b32_dpp v237, v179, v175, vcc quad_perm:[1,0,3,2] row_mask:0xf bank_mask:0xf\n"
  "v_fmac_f32 v204, s76, v212\n"
  "v_mfma_f32_16x16x32_f16 a[116:119], v[36:39], v[120:123], a[116:119]\n"
  "s_add_i32 s64, s81, 0\n"
  "buffer_store_dwordx4 v[234:237], v250, s[72:75], s64 offen sc1\n"
  "s_not_b64 vcc, vcc\n"
  "v_cndmask_b32_dpp v238, v172, v176, vcc quad_perm:[1,0,3,2] row_mask:0xf bank_mask:0xf\n"
  "v_cndmask_b32_dpp v239, v173, v177, vcc quad_perm:[1,0,3,2] row_mask:0xf bank_mask:0xf\n"
  "v_cndmask_b32_dpp v240, v174, v178, vcc quad_perm:[1,0,3,2] row_mask:0xf bank_mask:0xf\n"
  "v_cndmask_b32_dpp v241, v175, v179, vcc quad_perm:[1,0,3,2] row_mask:0xf bank_mask:0xf\n"
  "v_mfma_f32_16x16x32_f16 a[128:131], v[40:43], v[120:123], a[128:131]\n"
  "s_add_i32 s65, s81, 65536\n"
  "buffer_store_dwordx4 v[238:241], v250, s[72:75], s65 offen sc1\n"
  "v_fmac_f32 v208, s76, v212\n"
  "v_fmac_f32 v205, s76, v213\n"
  "v_fmac_f32 v209, s76, v213\n"
  "v_fmac_f32 v206, s76, v214\n"
  "v_mfma_f32_16x16x32_f16 a[140:143], v[44:47], v[120:123], a[140:143]\n"
  "v_fmac_f32 v210, s76, v214\n"
  "v_fmac_f32 v207, s76, v215\n"
  "v_fmac_f32 v211, s76, v215\n"
  "v_cvt_pk_bf16_f32 v172, v204, v205\n"
  "v_cvt_pk_bf16_f32 v173, v206, v207\n"
  "v_mfma_f32_16x16x32_f16 a[96:99], v[48:51], v[112:115], a[96:99]\n"
  "ds_read_b128 v[104:107], v242 offset:6144\n"
  "v_pk_add_f16 v120, v112, v116\n"
  "v_pk_add_f16 v121, v113, v117\n"
  "v_pk_add_f16 v122, v114, v118\n"
  "v_pk_add_f16 v123, v115, v119\n"
  "v_mfma_f32_16x16x32_f16 a[108:111], v[52:55], v[112:115], a[108:111]\n"
  "ds_read_b128 v[108:111], v242 offset:6656\n"
  "v_cvt_pk_bf16_f32 v176, v208, v209\n"
  "v_cvt_pk_bf16_f32 v177, v210, v211\n"
  "v_fmac_f32 v216, s76, v224\n"
  "v_fmac_f32 v220, s76, v224\n"
  "v_mfma_f32_16x16x32_f16 a[100:103], v[64:67], v[116:119], a[100:103]\n"
  "v_fmac_f32 v217, s76, v225\n"
  "v_fmac_f32 v221, s76, v225\n"
  "v_fmac_f32 v218, s76, v226\n"
  "v_fmac_f32 v222, s76, v226\n"
  "v_fmac_f32 v219, s76, v227\n"
  "v_mfma_f32_16x16x32_f16 a[112:115], v[68:71], v[116:119], a[112:115]\n"
  "v_fmac_f32 v223, s76, v227\n"
  "v_cvt_pk_bf16_f32 v174, v216, v217\n"
  "v_cvt_pk_bf16_f32 v175, v218, v219\n"
  "v_cvt_pk_bf16_f32 v178, v220, v221\n"
  "v_cvt_pk_bf16_f32 v179, v222, v223\n"
  "v_mfma_f32_16x16x32_f16 a[104:107], v[80:83], v[120:123], a[104:107]\n"
  "s_mov_b64 vcc, s[66:67]\n"
  "v_cndmask_b32_dpp v234, v176, v172, vcc quad_perm:[1,0,3,2] row_mask:0xf bank_mask:0xf\n"
  "v_cndmask_b32_dpp v235, v177, v173, vcc quad_perm:[1,0,3,2] row_mask:0xf bank_mask:0xf\n"
  "v_cndmask_b32_dpp v236, v178, v174, vcc quad_perm:[1,0,3,2] row_mask:0xf bank_mask:0xf\n"
  "v_cndmask_b32_dpp v237, v179, v175, vcc quad_perm:[1,0,3,2] row_mask:0xf bank_mask:0xf\n"
  "v_accvgpr_read_b32 v124, a96\n"
  "v_mfma_f32_16x16x32_f16 a[116:119], v[84:87], v[120:123], a[116:119]\n"
  "s_add_i32 s64, s81, 4096\n"
  "buffer_store_dwordx4 v[234:237], v250, s[72:75], s64 offen sc1\n"
  "s_not_b64 vcc, vcc\n"
  "v_cndmask_b32_dpp v238, v172, v176, vcc quad_perm:[1,0,3,2] row_mask:0xf bank_mask:0xf\n"
  "v_cndmask_b32_dpp v239, v173, v177, vcc quad_perm:[1,0,3,2] row_mask:0xf bank_mask:0xf\n"
  "v_cndmask_b32_dpp v240, v174, v178, vcc quad_perm:[1,0,3,2] row_mask:0xf bank_mask:0xf\n"
  "v_cndmask_b32_dpp v241, v175, v179, vcc quad_perm:[1,0,3,2] row_mask:0xf bank_mask:0xf\n"
  "v_mfma_f32_16x16x32_f16 a[120:123], v[56:59], v[112:115], a[120:123]\n"
  "s_add_i32 s65, s81, 69632\n"
  "buffer_store_dwordx4 v[238:241], v250, s[72:75], s65 offen sc1\n"
  "v_accvgpr_read_b32 v125, a97\n"
  "v_accvgpr_read_b32 v126, a98\n"
  "v_accvgpr_read_b32 v127, a99\n"
  "v_accvgpr_read_b32 v128, a100\n"
  "v_mfma_f32_16x16x32_f16 a[132:135], v[60:63], v[112:115], a[132:135]\n"
  "v_accvgpr_read_b32 v129, a101\n"
  "v_accvgpr_read_b32 v130, a102\n"
  "v_accvgpr_read_b32 v131, a103\n"
  "v_accvgpr_read_b32 v132, a104\n"
  "v_accvgpr_read_b32 v133, a105\n"
  "v_mfma_f32_16x16x32_f16 a[124:127], v[72:75], v[116:119], a[124:127]\n"
  "s_waitcnt lgkmcnt(2)\n"
  "v_accvgpr_read_b32 v134, a106\n"
  "v_accvgpr_read_b32 v135, a107\n"
  "v_fmac_f32 v124, s76, v132\n"
  "v_fmac_f32 v128, s76, v132\n"
  "v_fmac_f32 v125, s76, v133\n"
  "v_mfma_f32_16x16x32_f16 a[136:139], v[76:79], v[116:119], a[136:139]\n"
  "v_fmac_f32 v129, s76, v133\n"
  "v_fmac_f32 v126, s76, v134\n"
  "v_fmac_f32 v130, s76, v134\n"
  "v_fmac_f32 v127, s76, v135\n"
  "v_fmac_f32 v131, s76, v135\n"
  "v_mfma_f32_16x16x32_f16 a[128:131], v[88:91], v[120:123], a[128:131]\n"
  "v_cvt_pk_bf16_f32 v172, v124, v125\n"
  "v_cvt_pk_bf16_f32 v173, v126, v127\n"
  "v_cvt_pk_bf16_f32 v176, v128, v129\n"
  "v_cvt_pk_bf16_f32 v177, v130, v131\n"
  "v_accvgpr_read_b32 v136, a108\n"
  "v_mfma_f32_16x16x32_f16 a[140:143], v[92:95], v[120:123], a[140:143]\n"
  "v_accvgpr_read_b32 v137, a109\n"
  "v_accvgpr_read_b32 v138, a110\n"
  "v_accvgpr_read_b32 v139, a111\n"
  "v_accvgpr_read_b32 v140, a112\n"
  "v_accvgpr_read_b32 v141, a113\n"
  "v_mfma_f32_16x16x32_f16 a[144:147], v[0:3], v[96:99], a[144:147]\n"
  "ds_read_b128 v[112:115], v232 offset:8192\n"
  "v_pk_add_f16 v120, v96, v100\n"
  "v_pk_add_f16 v121, v97, v101\n"
  "v_pk_add_f16 v122, v98, v102\n"
  "v_pk_add_f16 v123, v99, v103\n"
  "v_mfma_f32_16x16x32_f16 a[156:159], v[4:7], v[96:99], a[156:159]\n"
  "ds_read_b128 v[116:119], v232 offset:8704\n"
  "v_accvgpr_read_b32 v142, a114\n"
  "v_accvgpr_read_b32 v143, a115\n"
  "v_accvgpr_read_b32 v144, a116\n"
  "v_accvgpr_read_b32 v145, a117\n"
  "v_mfma_f32_16x16x32_f16 a[168:171], v[8:11], v[96:99], a[168:171]\n"
  "v_accvgpr_read_b32 v146, a118\n"
  "v_accvgpr_read_b32 v147, a119\n"
  "v_fmac_f32 v136, s76, v144\n"
  "v_fmac_f32 v140, s76, v144\n"
  "v_fmac_f32 v137, s76, v145\n"
  "v_mfma_f32_16x16x32_f16 a[180:183], v[12:15], v[96:99], a[180:183]\n"
  "v_fmac_f32 v141, s76, v145\n"
  "v_fmac_f32 v138, s76, v146\n"
  "v_fmac_f32 v142, s76, v146\n"
  "v_fmac_f32 v139, s76, v147\n"
  "v_fmac_f32 v143, s76, v147\n"
  "v_mfma_f32_16x16x32_f16 a[148:151], v[16:19], v[100:103], a[148:151]\n"
  "v_cvt_pk_bf16_f32 v174, v136, v137\n"
  "v_cvt_pk_bf16_f32 v175, v138, v139\n"
  "v_cvt_pk_bf16_f32 v178, v140, v141\n"
  "v_cvt_pk_bf16_f32 v179, v142, v143\n"
  "v_accvgpr_read_b32 v148, a120\n"
  "v_mfma_f32_16x16x32_f16 a[160:163], v[20:23], v[100:103], a[160:163]\n"
  "s_mov_b64 vcc, s[66:67]\n"
  "v_cndmask_b32_dpp v234, v176, v172, vcc quad_perm:[1,0,3,2] row_mask:0xf bank_mask:0xf\n"
  "v_cndmask_b32_dpp v235, v177, v173, vcc quad_perm:[1,0,3,2] row_mask:0xf bank_mask:0xf\n"
  "v_cndmask_b32_dpp v236, v178, v174, vcc quad_perm:[1,0,3,2] row_mask:0xf bank_mask:0xf\n"
  "v_cndmask_b32_dpp v237, v179, v175, vcc quad_perm:[1,0,3,2] row_mask:0xf bank_mask:0xf\n"
  "v_accvgpr_read_b32 v149, a121\n"
  "v_mfma_f32_16x16x32_f16 a[172:175], v[24:27], v[100:103], a[172:175]\n"
  "s_add_i32 s64, s82, 0\n"
  "buffer_store_dwordx4 v[234:237], v250, s[72:75], s64 offen sc1\n"
  "s_not_b64 vcc, vcc\n"
  "v_cndmask_b32_dpp v238, v172, v176, vcc quad_perm:[1,0,3,2] row_mask:0xf bank_mask:0xf\n"
  "v_cndmask_b32_dpp v239, v173, v177, vcc quad_perm:[1,0,3,2] row_mask:0xf bank_mask:0xf\n"
  "v_cndmask_b32_dpp v240, v174, v178, vcc quad_perm:[1,0,3,2] row_mask:0xf bank_mask:0xf\n"
  "v_cndmask_b32_dpp v241, v175, v179, vcc quad_perm:[1,0,3,2] row_mask:0xf bank_mask:0xf\n"
  "v_mfma_f32_16x16x32_f16 a[184:187], v[28:31], v[100:103], a[184:187]\n"
  "s_add_i32 s65, s82, 65536\n"
  "buffer_store_dwordx4 v[238:241], v250, s[72:75], s65 offen sc1\n"
  "v_accvgpr_read_b32 v150, a122\n"
  "v_accvgpr_read_b32 v151, a123\n"
  "v_accvgpr_read_b32 v152, a124\n"
  "v_accvgpr_read_b32 v153, a125\n"
  "v_mfma_f32_16x16x32_f16 a[152:155], v[32:35], v[120:123], a[152:155]\n"
  "s_waitcnt lgkmcnt(2)\n"
  "v_accvgpr_read_b32 v154, a126\n"
  "v_accvgpr_read_b32 v155, a127\n"
  "v_accvgpr_read_b32 v156, a128\n"
  "v_accvgpr_read_b32 v157, a129\n"
  "v_accvgpr_read_b32 v158, a130\n"
  "v_mfma_f32_16x16x32_f16 a[164:167], v[36:39], v[120:123], a[164:167]\n"
  "v_accvgpr_read_b32 v159, a131\n"
  "v_fmac_f32 v148, s76, v156\n"
  "v_fmac_f32 v152, s76, v156\n"
  "v_fmac_f32 v149, s76, v157\n"
  "v_fmac_f32 v153, s76, v157\n"
  "v_mfma_f32_16x16x32_f16 a[176:179], v[40:43], v[120:123], a[176:179]\n"
  "v_fmac_f32 v150, s76, v158\n"
  "v_fmac_f32 v154, s76, v158\n"
  "v_fmac_f32 v151, s76, v159\n"
  "v_fmac_f32 v155, s76, v159\n"
  "v_cvt_pk_bf16_f32 v172, v148, v149\n"
  "v_mfma_f32_16x16x32_f16 a[188:191], v[44:47], v[120:123], a[188:191]\n"
  "v_cvt_pk_bf16_f32 v173, v150, v151\n"
  "v_cvt_pk_bf16_f32 v176, v152, v153\n"
  "v_cvt_pk_bf16_f32 v177, v154, v155\n"
  "v_accvgpr_read_b32 v160, a132\n"
  "v_accvgpr_read_b32 v161, a133\n"
  "v_mfma_f32_16x16x32_f16 a[144:147], v[48:51], v[104:107], a[144:147]\n"
  "ds_read_b128 v[96:99], v242 offset:8192\n"
  "v_pk_add_f16 v120, v104, v108\n"
  "v_pk_add_f16 v121, v105, v109\n"
  "v_pk_add_f16 v122, v106, v110\n"
  "v_pk_add_f16 v123, v107, v111\n"
  "v_mfma_f32_16x16x32_f16 a[156:159], v[52:55], v[104:107], a[156:159]\n"
  "ds_read_b128 v[100:103], v242 offset:8704\n"
  "v_accvgpr_read_b32 v162, a134\n"
  "v_accvgpr_read_b32 v163, a135\n"
  "v_accvgpr_read_b32 v164, a136\n"
  "v_accvgpr_read_b32 v165, a137\n"
  "v_mfma_f32_16x16x32_f16 a[148:151], v[64:67], v[108:111], a[148:151]\n"
  "v_accvgpr_read_b32 v166, a138\n"
  "v_accvgpr_read_b32 v167, a139\n"
  "v_accvgpr_read_b32 v168, a140\n"
  "v_accvgpr_read_b32 v169, a141\n"
  "v_accvgpr_read_b32 v170, a142\n"
  "v_mfma_f32_16x16x32_f16 a[160:163], v[68:71], v[108:111], a[160:163]\n"
  "v_accvgpr_read_b32 v171, a143\n"
  "v_fmac_f32 v160, s76, v168\n"
  "v_fmac_f32 v164, s76, v168\n"
  "v_fmac_f32 v161, s76, v169\n"
  "v_fmac_f32 v165, s76, v169\n"
  "v_mfma_f32_16x16x32_f16 a[152:155], v[80:83], v[120:123], a[152:155]\n"
  "v_fmac_f32 v162, s76, v170\n"
  "v_fmac_f32 v166, s76, v170\n"
  "v_fmac_f32 v163, s76, v171\n"
  "v_fmac_f32 v167, s76, v171\n"
  "v_cvt_pk_bf16_f32 v174, v160, v161\n"
  "v_mfma_f32_16x16x32_f16 a[164:167], v[84:87], v[120:123], a[164:167]\n"
  "v_cvt_pk_bf16_f32 v175, v162, v163\n"
  "v_cvt_pk_bf16_f32 v178, v164, v165\n"
  "v_cvt_pk_bf16_f32 v179, v166, v167\n"
  "v_accvgpr_read_b32 v180, a144\n"
  "v_accvgpr_read_b32 v181, a145\n"
  "v_mfma_f32_16x16x32_f16 a[168:171], v[56:59], v[104:107], a[168:171]\n"
  "s_mov_b64 vcc, s[66:67]\n"
  "v_cndmask_b32_dpp v234, v176, v172, vcc quad_perm:[1,0,3,2] row_mask:0xf bank_mask:0xf\n"
  "v_cndmask_b32_dpp v235, v177, v173, vcc quad_perm:[1,0,3,2] row_mask:0xf bank_mask:0xf\n"
  "v_cndmask_b32_dpp v236, v178, v174, vcc quad_perm:[1,0,3,2] row_mask:0xf bank_mask:0xf\n"
  "v_cndmask_b32_dpp v237, v179, v175, vcc quad_perm:[1,0,3,2] row_mask:0xf bank_mask:0xf\n"
  "v_accvgpr_read_b32 v182, a146\n"
  "v_mfma_f32_16x16x32_f16 a[180:183], v[60:63], v[104:107], a[180:183]\n"
  "s_add_i32 s64, s82, 4096\n"
  "buffer_store_dwordx4 v[234:237], v250, s[72:75], s64 offen sc1\n"
  "s_not_b64 vcc, vcc\n"
  "v_cndmask_b32_dpp v238, v172, v176, vcc quad_perm:[1,0,3,2] row_mask:0xf bank_mask:0xf\n"
  "v_cndmask_b32_dpp v239, v173, v177, vcc quad_perm:[1,0,3,2] row_mask:0xf bank_mask:0xf\n"
  "v_cndmask_b32_dpp v240, v174, v178, vcc quad_perm:[1,0,3,2] row_mask:0xf bank_mask:0xf\n"
  "v_cndmask_b32_dpp v241, v175, v179, vcc quad_perm:[1,0,3,2] row_mask:0xf bank_mask:0xf\n"
  "v_mfma_f32_16x16x32_f16 a[172:175], v[72:75], v[108:111], a[172:175]\n"
  "s_waitcnt lgkmcnt(2)\n"
  "s_add_i32 s65, s82, 69632\n"
  "buffer_store_dwordx4 v[238:241], v250, s[72:75], s65 offen sc1\n"
  "v_accvgpr_read_b32 v183, a147\n"
  "v_accvgpr_read_b32 v184, a148\n"
  "v_accvgpr_read_b32 v185, a149\n"
  "v_accvgpr_read_b32 v186, a150\n"
  "v_mfma_f32_16x16x32_f16 a[184:187], v[76:79], v[108:111], a[184:187]\n"
  "v_accvgpr_read_b32 v187, a151\n"
  "v_accvgpr_read_b32 v188, a152\n"
  "v_accvgpr_read_b32 v189, a153\n"
  "v_accvgpr_read_b32 v190, a154\n"
  "v_accvgpr_read_b32 v191, a155\n"
  "v_mfma_f32_16x16x32_f16 a[176:179], v[88:91], v[120:123], a[176:179]\n"
  "v_fmac_f32 v180, s76, v188\n"
  "v_fmac_f32 v184, s76, v188\n"
  "v_fmac_f32 v181, s76, v189\n"
  "v_fmac_f32 v185, s76, v189\n"
  "v_fmac_f32 v182, s76, v190\n"
  "v_mfma_f32_16x16x32_f16 a[188:191], v[92:95], v[120:123], a[188:191]\n"
  "v_fmac_f32 v186, s76, v190\n"
  "v_fmac_f32 v183, s76, v191\n"
  "v_fmac_f32 v187, s76, v191\n"
  "v_cvt_pk_bf16_f32 v172, v180, v181\n"
  "v_cvt_pk_bf16_f32 v173, v182, v183\n"
  "v_mfma_f32_16x16x32_f16 a[192:195], v[0:3], v[112:115], a[192:195]\n"
  "v_pk_add_f16 v120, v112, v116\n"
  "v_pk_add_f16 v121, v113, v117\n"
  "v_pk_add_f16 v122, v114, v118\n"
  "v_pk_add_f16 v123, v115, v119\n"
  "v_cvt_pk_bf16_f32 v176, v184, v185\n"
  "v_mfma_f32_16x16x32_f16 a[204:207], v[4:7], v[112:115], a[204:207]\n"
  "v_cvt_pk_bf16_f32 v177, v186, v187\n"
  "v_accvgpr_read_b32 v192, a156\n"
  "v_accvgpr_read_b32 v193, a157\n"
  "v_accvgpr_read_b32 v194, a158\n"
  "v_accvgpr_read_b32 v195, a159\n"
  "v_mfma_f32_16x16x32_f16 a[216:219], v[8:11], v[112:115], a[216:219]\n"
  "v_accvgpr_read_b32 v196, a160\n"
  "v_accvgpr_read_b32 v197, a161\n"
  "v_accvgpr_read_b32 v198, a162\n"
  "v_accvgpr_read_b32 v199, a163\n"
  "v_accvgpr_read_b32 v200, a164\n"
  "v_mfma_f32_16x16x32_f16 a[228:231], v[12:15], v[112:115], a[228:231]\n"
  "v_accvgpr_read_b32 v201, a165\n"
  "v_accvgpr_read_b32 v202, a166\n"
  "v_accvgpr_read_b32 v203, a167\n"
  "v_fmac_f32 v192, s76, v200\n"
  "v_fmac_f32 v196, s76, v200\n"
  "v_mfma_f32_16x16x32_f16 a[196:199], v[16:19], v[116:119], a[196:199]\n"
  "v_fmac_f32 v193, s76, v201\n"
  "v_fmac_f32 v197, s76, v201\n"
  "v_fmac_f32 v194, s76, v202\n"
  "v_fmac_f32 v198, s76, v202\n"
  "v_fmac_f32 v195, s76, v203\n"
  "v_mfma_f32_16x16x32_f16 a[208:211], v[20:23], v[116:119], a[208:211]\n"
  "v_fmac_f32 v199, s76, v203\n"
  "v_cvt_pk_bf16_f32 v174, v192, v193\n"
  "v_cvt_pk_bf16_f32 v175, v194, v195\n"
  "v_cvt_pk_bf16_f32 v178, v196, v197\n"
  "v_cvt_pk_bf16_f32 v179, v198, v199\n"
  "v_mfma_f32_16x16x32_f16 a[220:223], v[24:27], v[116:119], a[220:223]\n"
  "s_mov_b64 vcc, s[66:67]\n"
  "v_cndmask_b32_dpp v234, v176, v172, vcc quad_perm:[1,0,3,2] row_mask:0xf bank_mask:0xf\n"
  "v_cndmask_b32_dpp v235, v177, v173, vcc quad_perm:[1,0,3,2] row_mask:0xf bank_mask:0xf\n"
  "v_cndmask_b32_dpp v236, v178, v174, vcc quad_perm:[1,0,3,2] row_mask:0xf bank_mask:0xf\n"
  "v_cndmask_b32_dpp v237, v179, v175, vcc quad_perm:[1,0,3,2] row_mask:0xf bank_mask:0xf\n"
  "v_accvgpr_read_b32 v204, a168\n"
  "v_mfma_f32_16x16x32_f16 a[232:235], v[28:31], v[116:119], a[232:235]\n"
  "s_add_i32 s64, s83, 0\n"
  "buffer_store_dwordx4 v[234:237], v250, s[72:75], s64 offen sc1\n"
  "s_not_b64 vcc, vcc\n"
  "v_cndmask_b32_dpp v238, v172, v176, vcc quad_perm:[1,0,3,2] row_mask:0xf bank_mask:0xf\n"
  "v_cndmask_b32_dpp v239, v173, v177, vcc quad_perm:[1,0,3,2] row_mask:0xf bank_mask:0xf\n"
  "v_cndmask_b32_dpp v240, v174, v178, vcc quad_perm:[1,0,3,2] row_mask:0xf bank_mask:0xf\n"
  "v_cndmask_b32_dpp v241, v175, v179, vcc quad_perm:[1,0,3,2] row_mask:0xf bank_mask:0xf\n"
  "v_mfma_f32_16x16x32_f16 a[200:203], v[32:35], v[120:123], a[200:203]\n"
  "s_waitcnt lgkmcnt(0)\n"
  "s_add_i32 s65, s83, 65536\n"
  "buffer_store_dwordx4 v[238:241], v250, s[72:75], s65 offen sc1\n"
  "v_accvgpr_read_b32 v205, a169\n"
  "v_accvgpr_read_b32 v206, a170\n"
  "v_accvgpr_read_b32 v207, a171\n"
  "v_accvgpr_read_b32 v208, a172\n"
  "v_mfma_f32_16x16x32_f16 a[212:215], v[36:39], v[120:123], a[212:215]\n"
  "v_accvgpr_read_b32 v209, a173\n"
  "v_accvgpr_read_b32 v210, a174\n"
  "v_accvgpr_read_b32 v211, a175\n"
  "v_accvgpr_read_b32 v212, a176\n"
  "v_accvgpr_read_b32 v213, a177\n"
  "v_mfma_f32_16x16x32_f16 a[224:227], v[40:43], v[120:123], a[224:227]\n"
  "v_accvgpr_read_b32 v214, a178\n"
  "v_accvgpr_read_b32 v215, a179\n"
  "v_fmac_f32 v204, s76, v212\n"
  "v_fmac_f32 v208, s76, v212\n"
  "v_fmac_f32 v205, s76, v213\n"
  "v_mfma_f32_16x16x32_f16 a[236:239], v[44:47], v[120:123], a[236:239]\n"
  "v_fmac_f32 v209, s76, v213\n"
  "v_fmac_f32 v206, s76, v214\n"
  "v_fmac_f32 v210, s76, v214\n"
  "v_fmac_f32 v207, s76, v215\n"
  "v_fmac_f32 v211, s76, v215\n"
  "v_mfma_f32_16x16x32_f16 a[192:195], v[48:51], v[96:99], a[192:195]\n"
  "v_pk_add_f16 v120, v96, v100\n"
  "v_pk_add_f16 v121, v97, v101\n"
  "v_pk_add_f16 v122, v98, v102\n"
  "v_pk_add_f16 v123, v99, v103\n"
  "v_cvt_pk_bf16_f32 v172, v204, v205\n"
  "v_mfma_f32_16x16x32_f16 a[204:207], v[52:55], v[96:99], a[204:207]\n"
  "v_cvt_pk_bf16_f32 v173, v206, v207\n"
  "v_cvt_pk_bf16_f32 v176, v208, v209\n"
  "v_cvt_pk_bf16_f32 v177, v210, v211\n"
  "v_accvgpr_read_b32 v216, a180\n"
  "v_accvgpr_read_b32 v217, a181\n"
  "v_mfma_f32_16x16x32_f16 a[196:199], v[64:67], v[100:103], a[196:199]\n"
  "v_accvgpr_read_b32 v218, a182\n"
  "v_accvgpr_read_b32 v219, a183\n"
  "v_accvgpr_read_b32 v220, a184\n"
  "v_accvgpr_read_b32 v221, a185\n"
  "v_accvgpr_read_b32 v222, a186\n"
  "v_mfma_f32_16x16x32_f16 a[208:211], v[68:71], v[100:103], a[208:211]\n"
  "v_accvgpr_read_b32 v223, a187\n"
  "v_accvgpr_read_b32 v224, a188\n"
  "v_accvgpr_read_b32 v225, a189\n"
  "v_accvgpr_read_b32 v226, a190\n"
  "v_accvgpr_read_b32 v227, a191\n"
  "v_mfma_f32_16x16x32_f16 a[200:203], v[80:83], v[120:123], a[200:203]\n"
  "v_fmac_f32 v216, s76, v224\n"
  "v_fmac_f32 v220, s76, v224\n"
  "v_fmac_f32 v217, s76, v225\n"
  "v_fmac_f32 v221, s76, v225\n"
  "v_fmac_f32 v218, s76, v226\n"
  "v_mfma_f32_16x16x32_f16 a[212:215], v[84:87], v[120:123], a[212:215]\n"
  "v_fmac_f32 v222, s76, v226\n"
  "v_fmac_f32 v219, s76, v227\n"
  "v_fmac_f32 v223, s76, v227\n"
  "v_cvt_pk_bf16_f32 v174, v216, v217\n"
  "v_cvt_pk_bf16_f32 v175, v218, v219\n"
  "v_mfma_f32_16x16x32_f16 a[216:219], v[56:59], v[96:99], a[216:219]\n"
  "v_cvt_pk_bf16_f32 v178, v220, v221\n"
  "v_cvt_pk_bf16_f32 v179, v222, v223\n"
  "v_accvgpr_read_b32 v124, a192\n"
  "v_accvgpr_read_b32 v125, a193\n"
  "v_accvgpr_read_b32 v126, a194\n"
  "v_mfma_f32_16x16x32_f16 a[228:231], v[60:63], v[96:99], a[228:231]\n"
  "s_mov_b64 vcc, s[66:67]\n"
  "v_cndmask_b32_dpp v234, v176, v172, vcc quad_perm:[1,0,3,2] row_mask:0xf bank_mask:0xf\n"
  "v_cndmask_b32_dpp v235, v177, v173, vcc quad_perm:[1,0,3,2] row_mask:0xf bank_mask:0xf\n"
  "v_cndmask_b32_dpp v236, v178, v174, vcc quad_perm:[1,0,3,2] row_mask:0xf bank_mask:0xf\n"
  "v_cndmask_b32_dpp v237, v179, v175, vcc quad_perm:[1,0,3,2] row_mask:0xf bank_mask:0xf\n"
  "v_accvgpr_read_b32 v127, a195\n"
  "v_mfma_f32_16x16x32_f16 a[220:223], v[72:75], v[100:103], a[220:223]\n"
  "s_add_i32 s64, s83, 4096\n"
  "buffer_store_dwordx4 v[234:237], v250, s[72:75], s64 offen sc1\n"
  "s_not_b64 vcc, vcc\n"
  "v_cndmask_b32_dpp v238, v172, v176, vcc quad_perm:[1,0,3,2] row_mask:0xf bank_mask:0xf\n"
  "v_cndmask_b32_dpp v239, v173, v177, vcc quad_perm:[1,0,3,2] row_mask:0xf bank_mask:0xf\n"
  "v_cndmask_b32_dpp v240, v174, v178, vcc quad_perm:[1,0,3,2] row_mask:0xf bank_mask:0xf\n"
  "v_cndmask_b32_dpp v241, v175, v179, vcc quad_perm:[1,0,3,2] row_mask:0xf bank_mask:0xf\n"
  "v_mfma_f32_16x16x32_f16 a[232:235], v[76:79], v[100:103], a[232:235]\n"
  "s_add_i32 s65, s83, 69632\n"
  "buffer_store_dwordx4 v[238:241], v250, s[72:75], s65 offen sc1\n"
  "v_accvgpr_read_b32 v128, a196\n"
  "v_accvgpr_read_b32 v129, a197\n"
  "v_accvgpr_read_b32 v130, a198\n"
  "v_accvgpr_read_b32 v131, a199\n"
  "v_mfma_f32_16x16x32_f16 a[224:227], v[88:91], v[120:123], a[224:227]\n"
  "v_accvgpr_read_b32 v132, a200\n"
  "v_accvgpr_read_b32 v133, a201\n"
  "v_accvgpr_read_b32 v134, a202\n"
  "v_accvgpr_read_b32 v135, a203\n"
  "v_pk_fma_f32 v[124:125], v[132:133], s[76:77], v[124:125]\n"
  "v_mfma_f32_16x16x32_f16 a[236:239], v[92:95], v[120:123], a[236:239]\n"
  "v_pk_fma_f32 v[128:129], v[132:133], s[76:77], v[128:129]\n"
  "v_pk_fma_f32 v[126:127], v[134:135], s[76:77], v[126:127]\n"
  "v_pk_fma_f32 v[130:131], v[134:135], s[76:77], v[130:131]\n"
  "v_cvt_pk_bf16_f32 v172, v124, v125\n"
  "v_cvt_pk_bf16_f32 v173, v126, v127\n"
  "v_cvt_pk_bf16_f32 v176, v128, v129\n"
  "v_cvt_pk_bf16_f32 v177, v130, v131\n"
  "v_accvgpr_read_b32 v136, a204\n"
  "v_accvgpr_read_b32 v137, a205\n"
  "v_accvgpr_read_b32 v138, a206\n"
  "v_accvgpr_read_b32 v139, a207\n"
  "v_accvgpr_read_b32 v140, a208\n"
  "v_accvgpr_read_b32 v141, a209\n"
  "v_accvgpr_read_b32 v142, a210\n"
  "v_accvgpr_read_b32 v143, a211\n"
  "v_accvgpr_read_b32 v144, a212\n"
  "v_accvgpr_read_b32 v145, a213\n"
  "v_accvgpr_read_b32 v146, a214\n"
  "v_accvgpr_read_b32 v147, a215\n"
  "v_pk_fma_f32 v[136:137], v[144:145], s[76:77], v[136:137]\n"
  "v_pk_fma_f32 v[140:141], v[144:145], s[76:77], v[140:141]\n"
  "v_pk_fma_f32 v[138:139], v[146:147], s[76:77], v[138:139]\n"
  "v_pk_fma_f32 v[142:143], v[146:147], s[76:77], v[142:143]\n"
  "v_cvt_pk_bf16_f32 v174, v136, v137\n"
  "v_cvt_pk_bf16_f32 v175, v138, v139\n"
  "v_cvt_pk_bf16_f32 v178, v140, v141\n"
  "v_cvt_pk_bf16_f32 v179, v142, v143\n"
  "v_accvgpr_read_b32 v148, a216\n"
  "v_accvgpr_read_b32 v149, a217\n"
  "v_accvgpr_read_b32 v150, a218\n"
  "s_mov_b64 vcc, s[66:67]\n"
  "v_cndmask_b32_dpp v234, v176, v172, vcc quad_perm:[1,0,3,2] row_mask:0xf bank_mask:0xf\n"
  "v_cndmask_b32_dpp v235, v177, v173, vcc quad_perm:[1,0,3,2] row_mask:0xf bank_mask:0xf\n"
  "v_cndmask_b32_dpp v236, v178, v174, vcc quad_perm:[1,0,3,2] row_mask:0xf bank_mask:0xf\n"
  "v_cndmask_b32_dpp v237, v179, v175, vcc quad_perm:[1,0,3,2] row_mask:0xf bank_mask:0xf\n"
  "v_accvgpr_read_b32 v151, a219\n"
  "s_add_i32 s64, s84, 0\n"
  "buffer_store_dwordx4 v[234:237], v250, s[72:75], s64 offen sc1\n"
  "s_not_b64 vcc, vcc\n"
  "v_cndmask_b32_dpp v238, v172, v176, vcc quad_perm:[1,0,3,2] row_mask:0xf bank_mask:0xf\n"
  "v_cndmask_b32_dpp v239, v173, v177, vcc quad_perm:[1,0,3,2] row_mask:0xf bank_mask:0xf\n"
  "v_cndmask_b32_dpp v240, v174, v178, vcc quad_perm:[1,0,3,2] row_mask:0xf bank_mask:0xf\n"
  "v_cndmask_b32_dpp v241, v175, v179, vcc quad_perm:[1,0,3,2] row_mask:0xf bank_mask:0xf\n"
  "s_add_i32 s65, s84, 65536\n"
  "buffer_store_dwordx4 v[238:241], v250, s[72:75], s65 offen sc1\n"
  "v_accvgpr_read_b32 v152, a220\n"
  "v_accvgpr_read_b32 v153, a221\n"
  "v_accvgpr_read_b32 v154, a222\n"
  "v_accvgpr_read_b32 v155, a223\n"
  "v_accvgpr_read_b32 v156, a224\n"
  "v_accvgpr_read_b32 v157, a225\n"
  "v_accvgpr_read_b32 v158, a226\n"
  "v_accvgpr_read_b32 v159, a227\n"
  "v_pk_fma_f32 v[148:149], v[156:157], s[76:77], v[148:149]\n"
  "v_pk_fma_f32 v[152:153], v[156:157], s[76:77], v[152:153]\n"
  "v_pk_fma_f32 v[150:151], v[158:159], s[76:77], v[150:151]\n"
  "v_pk_fma_f32 v[154:155], v[158:159], s[76:77], v[154:155]\n"
  "v_cvt_pk_bf16_f32 v172, v148, v149\n"
  "v_cvt_pk_bf16_f32 v173, v150, v151\n"
  "v_cvt_pk_bf16_f32 v176, v152, v153\n"
  "v_cvt_pk_bf16_f32 v177, v154, v155\n"
  "v_accvgpr_read_b32 v160, a228\n"
  "v_accvgpr_read_b32 v161, a229\n"
  "v_accvgpr_read_b32 v162, a230\n"
  "v_accvgpr_read_b32 v163, a231\n"
  "v_accvgpr_read_b32 v164, a232\n"
  "v_accvgpr_read_b32 v165, a233\n"
  "v_accvgpr_read_b32 v166, a234\n"
  "v_accvgpr_read_b32 v167, a235\n"
  "v_accvgpr_read_b32 v168, a236\n"
  "v_accvgpr_read_b32 v169, a237\n"
  "v_accvgpr_read_b32 v170, a238\n"
  "v_accvgpr_read_b32 v171, a239\n"
  "v_pk_fma_f32 v[160:161], v[168:169], s[76:77], v[160:161]\n"
  "v_pk_fma_f32 v[164:165], v[168:169], s[76:77], v[164:165]\n"
  "v_pk_fma_f32 v[162:163], v[170:171], s[76:77], v[162:163]\n"
  "v_pk_fma_f32 v[166:167], v[170:171], s[76:77], v[166:167]\n"
  "v_cvt_pk_bf16_f32 v174, v160, v161\n"
  "v_cvt_pk_bf16_f32 v175, v162, v163\n"
  "v_cvt_pk_bf16_f32 v178, v164, v165\n"
  "v_cvt_pk_bf16_f32 v179, v166, v167\n"
  "s_mov_b64 vcc, s[66:67]\n"
  "v_cndmask_b32_dpp v234, v176, v172, vcc quad_perm:[1,0,3,2] row_mask:0xf bank_mask:0xf\n"
  "v_cndmask_b32_dpp v235, v177, v173, vcc quad_perm:[1,0,3,2] row_mask:0xf bank_mask:0xf\n"
  "v_cndmask_b32_dpp v236, v178, v174, vcc quad_perm:[1,0,3,2] row_mask:0xf bank_mask:0xf\n"
  "v_cndmask_b32_dpp v237, v179, v175, vcc quad_perm:[1,0,3,2] row_mask:0xf bank_mask:0xf\n"
  "s_add_i32 s64, s84, 4096\n"
  "buffer_store_dwordx4 v[234:237], v250, s[72:75], s64 offen sc1\n"
  "s_not_b64 vcc, vcc\n"
  "v_cndmask_b32_dpp v238, v172, v176, vcc quad_perm:[1,0,3,2] row_mask:0xf bank_mask:0xf\n"
  "v_cndmask_b32_dpp v239, v173, v177, vcc quad_perm:[1,0,3,2] row_mask:0xf bank_mask:0xf\n"
  "v_cndmask_b32_dpp v240, v174, v178, vcc quad_perm:[1,0,3,2] row_mask:0xf bank_mask:0xf\n"
  "v_cndmask_b32_dpp v241, v175, v179, vcc quad_perm:[1,0,3,2] row_mask:0xf bank_mask:0xf\n"
  "s_add_i32 s65, s84, 69632\n"
  "buffer_store_dwordx4 v[238:241], v250, s[72:75], s65 offen sc1\n"
  "s_waitcnt vmcnt(20)\n"
  "s_branch .Lend_%=\n"
  ".Lpace_s0_None_%=:\n"
  "s_memrealtime s[70:71]\n"
  "s_waitcnt lgkmcnt(0)\n"
  "s_sub_i32 s64, s70, s61\n"
  "s_cmp_lt_i32 s64, 0\n"
  "s_cbranch_scc0 .Lpace2_s0_None_%=\n"
  "s_sleep 1\n"
  "s_branch .Lpace_s0_None_%=\n"
  ".Lpace2_s0_None_%=:\n"
  "s_add_i32 s61, s70, s62\n"
  "s_add_i32 s61, s61, 8\n"
  "s_sub_i32 s62, s62, 14\n"
  "s_branch .Lpaced_s0_None_%=\n"
  ".Lpace_s1_None_%=:\n"
  "s_memrealtime s[70:71]\n"
  "s_waitcnt lgkmcnt(0)\n"
  "s_sub_i32 s64, s70, s61\n"
  "s_cmp_lt_i32 s64, 0\n"
  "s_cbranch_scc0 .Lpace2_s1_None_%=\n"
  "s_sleep 1\n"
  "s_branch .Lpace_s1_None_%=\n"
  ".Lpace2_s1_None_%=:\n"
  "s_add_i32 s61, s70, s62\n"
  "s_add_i32 s61, s61, 0\n"
  "s_sub_i32 s62, s62, 14\n"
  "s_branch .Lpaced_s1_None_%=\n"
  ".Lspin_pe_None_%=:\n"
  "s_mov_b32 s64, 0\n"
  ".Lspin2_pe_None_%=:\n"
  "s_sleep 3\n"
  "buffer_load_dword v247, v246, s[40:43], s54 offen sc1\n"
  "s_add_u32 s64, s64, 1\n"
  "s_waitcnt vmcnt(0)\n"
  "s_cmp_gt_u32 s64, 0x200000\n"
  "s_cbranch_scc1 .Lspun_pe_None_%=\n"
  "v_cmp_eq_u32 vcc, 0, v247\n"
  "s_cbranch_vccnz .Lspin2_pe_None_%=\n"
  "s_branch .Lspun_pe_None_%=\n"
  ".Lpace_pe_None_%=:\n"
  "s_memrealtime s[70:71]\n"
  "s_waitcnt lgkmcnt(0)\n"
  "s_sub_i32 s64, s70, s61\n"
  "s_cmp_lt_i32 s64, 0\n"
  "s_cbranch_scc0 .Lpace2_pe_None_%=\n"
  "s_sleep 1\n"
  "s_branch .Lpace_pe_None_%=\n"
  ".Lpace2_pe_None_%=:\n"
  "s_add_i32 s61, s70, s62\n"
  "s_add_i32 s61, s61, 8\n"
  "s_sub_i32 s62, s62, 14\n"
  "s_branch .Lpaced_pe_None_%=\n"
  ".Lpace_po_None_%=:\n"
  "s_memrealtime s[70:71]\n"
  "s_waitcnt lgkmcnt(0)\n"
  "s_sub_i32 s64, s70, s61\n"
  "s_cmp_lt_i32 s64, 0\n"
  "s_cbranch_scc0 .Lpace2_po_None_%=\n"
  "s_sleep 1\n"
  "s_branch .Lpace_po_None_%=\n"
  ".Lpace2_po_None_%=:\n"
  "s_add_i32 s61, s70, s62\n"
  "s_add_i32 s61, s61, 0\n"
  "s_sub_i32 s62, s62, 14\n"
  "s_branch .Lpaced_po_None_%=\n"
  ".Lspin_s22_None_%=:\n"
  "s_mov_b32 s64, 0\n"
  ".Lspin2_s22_None_%=:\n"
  "s_sleep 3\n"
  "buffer_load_dword v247, v246, s[40:43], s54 offen sc1\n"
  "s_add_u32 s64, s64, 1\n"
  "s_waitcnt vmcnt(0)\n"
  "s_cmp_gt_u32 s64, 0x200000\n"
  "s_cbranch_scc1 .Lspun_s22_None_%=\n"
  "v_cmp_eq_u32 vcc, 0, v247\n"
  "s_cbranch_vccnz .Lspin2_s22_None_%=\n"
  "s_branch .Lspun_s22_None_%=\n"
  ".Lspin_ne_None_%=:\n"
  "s_mov_b32 s64, 0\n"
  ".Lspin2_ne_None_%=:\n"
  "s_sleep 3\n"
  "buffer_load_dword v247, v246, s[40:43], s54 offen sc1\n"
  "s_add_u32 s64, s64, 1\n"
  "s_waitcnt vmcnt(0)\n"
  "s_cmp_gt_u32 s64, 0x200000\n"
  "s_cbranch_scc1 .Lspun_ne_None_%=\n"
  "v_cmp_eq_u32 vcc, 0, v247\n"
  "s_cbranch_vccnz .Lspin2_ne_None_%=\n"
  "s_branch .Lspun_ne_None_%=\n"
  ".Lspin_s28_None_%=:\n"
  "s_mov_b32 s64, 0\n"
  ".Lspin2_s28_None_%=:\n"
  "s_sleep 3\n"
  "buffer_load_dword v247, v246, s[40:43], s54 offen sc1\n"
  "s_add_u32 s64, s64, 1\n"
  "s_waitcnt vmcnt(0)\n"
  "s_cmp_gt_u32 s64, 0x200000\n"
  "s_cbranch_scc1 .Lspun_s28_None_%=\n"
  "v_cmp_eq_u32 vcc, 0, v247\n"
  "s_cbranch_vccnz .Lspin2_s28_None_%=\n"
  "s_branch .Lspun_s28_None_%=\n"
  ".Lend_%=:\n"
    : "={v[180:211]}"(o3), "={v[212:227]}"(o4), "={v[176:179]}"(o5), "={v[80:95]}"(o0), "={v[156:163]}"(o1)
    : "{v[180:211]}"(h0), "{v[212:227]}"(h1), "{v[176:179]}"(hd.vb), "{v[80:95]}"(st0), "{v[156:163]}"(st1), "{s[40:43]}"(cx.rs)
    : "v0", "v1", "v2", "v3", "v4", "v5", "v6", "v7", "v8", "v9", "v10", "v11", "v12", "v13", "v14", "v15", "v16", "v17", "v18", "v19", "v20", "v21", "v22", "v23", "v24", "v25", "v26", "v27", "v28", "v29", "v30", "v31", "v32", "v33", "v34", "v35", "v36", "v37", "v38", "v39", "v40", "v41", "v42", "v43", "v44", "v45", "v46", "v47", "v48", "v49", "v50", "v51", "v52", "v53", "v54", "v55", "v56", "v57", "v58", "v59", "v60", "v61", "v62", "v63", "v64", "v65", "v66", "v67", "v68", "v69", "v70", "v71", "v72", "v73", "v74", "v75", "v76", "v77", "v78", "v79", "v96", "v97", "v98", "v99", "v100", "v101", "v102", "v103", "v104", "v105", "v106", "v107", "v108", "v109", "v110", "v111", "v112", "v113", "v114", "v115", "v116", "v117", "v118", "v119", "v120", "v121", "v122", "v123", "v124", "v125", "v126", "v127", "v128", "v129", "v130", "v131", "v132", "v133", "v134", "v135", "v136", "v137", "v138", "v139", "v140", "v141", "v142", "v143", "v144", "v145", "v146", "v147", "v148", "v149", "v150", "v151", "v152", "v153", "v154", "v155", "v164", "v165", "v166", "v167", "v168", "v169", "v170", "v171", "v172", "v173", "v174", "v175", "v228", "v229", "v230", "v231", "v232", "v233", "v234", "v235", "v236", "v237", "v238", "v239", "v240", "v241", "v242", "v243", "v244", "v245", "v246", "v247", "v248", "v249", "v250", "v251", "v252", "v253", "v254", "v255", "a0", "a1", "a2", "a3", "a4", "a5", "a6", "a7", "a8", "a9", "a10", "a11", "a12", "a13", "a14", "a15", "a16", "a17", "a18", "a19", "a20", "a21", "a22", "a23", "a24", "a25", "a26", "a27", "a28", "a29", "a30", "a31", "a32", "a33", "a34", "a35", "a36", "a37", "a38", "a39", "a40", "a41", "a42", "a43", "a44", "a45", "a46", "a47", "a48", "a49", "a50", "a51", "a52", "a53", "a54", "a55", "a56", "a57", "a58", "a59", "a60", "a61", "a62", "a63", "a64", "a65", "a66", "a67", "a68", "a69", "a70", "a71", "a72", "a73", "a74", "a75", "a76", "a77", "a78", "a79", "a80", "a81", "a82", "a83", "a84", "a85", "a86", "a87", "a88", "a89", "a90", "a91", "a92", "a93", "a94", "a95", "a96", "a97", "a98", "a99", "a100", "a101", "a102", "a103", "a104", "a105", "a106", "a107", "a108", "a109", "a110", "a111", "a112", "a113", "a114", "a115", "a116", "a117", "a118", "a119", "a120", "a121", "a122", "a123", "a124", "a125", "a126", "a127", "a128", "a129", "a130", "a131", "a132", "a133", "a134", "a135", "a136", "a137", "a138", "a139", "a140", "a141", "a142", "a143", "a144", "a145", "a146", "a147", "a148", "a149", "a150", "a151", "a152", "a153", "a154", "a155", "a156", "a157", "a158", "a159", "a160", "a161", "a162", "a163", "a164", "a165", "a166", "a167", "a168", "a169", "a170", "a171", "a172", "a173", "a174", "a175", "a176", "a177", "a178", "a179", "a180", "a181", "a182", "a183", "a184", "a185", "a186", "a187", "a188", "a189", "a190", "a191", "a192", "a193", "a194", "a195", "a196", "a197", "a198", "a199", "a200", "a201", "a202", "a203", "a204", "a205", "a206", "a207", "a208", "a209", "a210", "a211", "a212", "a213", "a214", "a215", "a216", "a217", "a218", "a219", "a220", "a221", "a222", "a223", "a224", "a225", "a226", "a227", "a228", "a229", "a230", "a231", "a232", "a233", "a234", "a235", "a236", "a237", "a238", "a239", "a240", "a241", "a242", "a243", "a244", "a245", "a246", "a247", "a248", "a249", "a250", "a251", "a252", "a253", "a254", "a255", "s44", "s45", "s46", "s47", "s48", "s49", "s50", "s51", "s52", "s53", "s54", "s55", "s56", "s57", "s58", "s59", "s60", "s61", "s62", "s63", "s64", "s65", "s66", "s67", "s68", "s69", "s70", "s71", "s72", "s73", "s74", "s75", "s76", "s77", "s78", "s79", "s80", "s81", "s82", "s83", "s84", "s85", "s86", "s87", "s88", "s89", "vcc", "scc", "m0", "memory");
}

__device__ __forceinline__ void imfma0(f32x4& d, const bf16x8& a, const bf16x8& b, const f32x4& c) {
  asm volatile("v_mfma_f32_16x16x32_bf16 %0, %1, %2, %3" : "=&v"(d) : "v"(a), "a"(b), "v"(c));
}
__device__ __forceinline__ void imfma1(f32x4& d, const bf16x8& a, const bf16x8& b) {
  asm volatile("v_mfma_f32_16x16x32_bf16 %0, %1, %2, %0" : "+v"(d) : "v"(a), "a"(b));
}
#define DPPADD(d, s, CTRL, BANKS) asm volatile("v_add_f32_dpp %0, %1, %1 " CTRL " row_mask:0xf bank_mask:" BANKS : "+v"(d) : "v"(s))
template <int CTRL>
__device__ __forceinline__ float dpp_add(float v) {
  asm("" : "+v"(v));
  return v + __builtin_bit_cast(float, __builtin_amdgcn_update_dpp(0, __builtin_bit_cast(int, v), CTRL, 0xf, 0xf, true));
}

extern "C" __global__ __launch_bounds__(256, 1) void conv_fft(char* ws, const unsigned short* __restrict__ x,
                                                   const unsigned short* __restrict__ w,
                                                   const unsigned short* __restrict__ gamma,
                                                   const unsigned short* __restrict__ beta,
                                                   unsigned short* __restrict__ out) {
  __shared__ __attribute__((aligned(16))) char smem[160 * 1024];
  const unsigned lds0 = (unsigned)(uintptr_t)(lds_void*)smem;
  const int wv = __builtin_amdgcn_readfirstlane(threadIdx.x >> 6);
  Ctx cx;

  {
    unsigned len;
    asm volatile("s_getpc_b64 %0\n.Lcf_a:\n\ts_mov_b32 %1, .Lcf_b-.Lcf_a" : "=s"(cx.pc), "=s"(len));
    cx.wv = wv;
    cx.pfo = len;
    unsigned xcc;
    asm volatile("s_getreg_b32 %0, hwreg(HW_REG_XCC_ID, 0, 3)" : "=s"(xcc));
    unsigned id = blockIdx.x;
    asm volatile("" : "+s"(id));
    cx.id = id;
    cx.vb = (int)((id & 248u) | (id & 256u ? (id >> 10) & 7u : xcc));
    const char* pb = ws + WS_PUB;
    u32x4 &sx = cx.sx, &sw = cx.sw;
    cx.xcc = xcc;
    if ((id | wv) == 0) {
      const unsigned l = threadIdx.x;
      const u32x4 z = {0u, 0u, 0u, 0u};
      {
        const unsigned so = l * (31u * 1024u + 16u) + (4u << 20);
        asm volatile("global_load_dwordx4 %0, %2, %3 nt\n\tglobal_load_dwordx4 %1, %4, %5 nt"
                     : "=&v"(sx), "=&v"(sw) : "v"(so + (l & 3u) * (8u << 20)), "s"(x), "v"(so + (l & 1u) * (6u << 20)), "s"(w) : "memory");
      }
      {
#pragma unroll
        for (int k = 0; k < 2; ++k)
          asm volatile("global_store_dwordx4 %0, %1, %2 sc1" :: "v"(32768 + (l & 7) * 16 + (l & 8) * 512 + (l & 16) * 1024 + (l & 32) * 32 + k * 8192), "v"(z), "s"(pb) : "memory");
        asm volatile("global_store_dwordx4 %0, %1, %2 sc1" :: "v"(l * 16 + 1024), "v"(z), "s"(pb) : "memory");
      }
    }
    if (id) code_touch(cx);
    SB0;
  }
  cx.smem = smem;
  cx.ws = ws;
  cx.gamma = gamma;
  cx.beta = beta;
  run12(cx, x, w);
  ws = cx.ws;
  const int bid = cx.vb, idx = bid >> 3;
  const int slot = (bid & 7) * 4 + (idx >> 3), b = (idx >> 2) & 1, nt = idx & 3;
  const int lane = (int)__builtin_amdgcn_mbcnt_hi(~0u, __builtin_amdgcn_mbcnt_lo(~0u, 0u));
  const int c64i = nt * 2 + ((bid >> 2) & 1), rg = (bid & 1) * 8 + (slot & 1) * 4 + (bid & 2) + (idx >> 4);
  const int bs = 10 * rg, hw = wv & 1, par = wv >> 1;
  const int g = lane >> 4, qq = (lane >> 2) & 3, pp = lane & 3, fr = lane & 15;
  const int lend = min(HOP * (bs + 10), L_);
  const u32x4 rsf = make_rsrc(ws + WS_GRP + nt * 256, 16 * 1024);
  unsigned fw = (unsigned)((lane + (lane & 4)) * 1024 + (b * 32 + slot) * 4);
  unsigned fp = (unsigned)(((bid & 7) + (bid & 4)) * 1024 + (b * 32 + ((lane & 31) ^ ((lane & 31) == slot))) * 4);
  f32x4 y[30], ye[2], cin[2], npv[2];
  {
    const int ee = fr & 1 ? cx.es : -2000;
#pragma unroll
    for (int s = 0; s < 2; ++s) {
      cin[s] = *reinterpret_cast<const f32x4*>(smem + CBUF + (par + (fr & 14)) * 256 + hw * 128 + g * 32 + s * 16);
#pragma unroll
      for (int r = 0; r < 4; ++r) cin[s][r] = __builtin_amdgcn_ldexpf(cin[s][r], ee);
    }
  }
  char* my = smem + wv * 20480;
  unsigned src = (unsigned)(uintptr_t)(lds_void*)my + (unsigned)((4 * g + qq) * 64 + ((g & 1) << 5) + pp * 8), src1 = src ^ 32u;
  unsigned vo = (unsigned)((lane >> 2) * 64 + (((lane & 3) ^ ((lane >> 3) & 2)) << 4)), vp = (unsigned)(g * 8);
  const u32x4 rsy = make_rsrc(ws + WS_Y, ROWS * 65536);
  unsigned so[6];
  {
    const unsigned t0 = (unsigned)(b * NBLK * 65536 + (c64i * 2 + hw) * 4096);
#pragma unroll
    for (int i = 0; i < 6; ++i) {
      const unsigned t = __builtin_amdgcn_readfirstlane(i < 5 ? t0 + (unsigned)(bs + par + 2 * i) * 65536 : t0);
      so[i] = t ^ ((t >> 8) & 8192u);
      asm volatile("" : "+s"(so[i]));
    }
  }
  asm volatile("" : "+v"(cin[0]), "+v"(cin[1]), "+v"(src), "+v"(src1), "+v"(vo), "+v"(vp), "+v"(fw), "+v"(fp));
  SB0;
  wait_vm<0>();
  __builtin_amdgcn_s_barrier();
  unsigned fv = 1u;
  if (wv == 1 && lane < 8) asm volatile("buffer_store_dword %0, %1, %2, 0 offen sc1" :: "v"(fv), "v"(fw), "s"(rsf) : "memory");
  if (wv == 0) asm volatile("buffer_load_dword %0, %1, %2, 0 offen sc1" : "=v"(fv) : "v"(fp), "s"(rsf) : "memory");
  bf16x8 gb[16];
  {
    const unsigned l16 = (unsigned)lane * 16u;
    const char* gt = (const char*)&TAB.G[0][0][0][0][0];
#pragma unroll
    for (int i = 0; i < 16; ++i)
      asm volatile("global_load_dwordx4 %0, %1, %2 offset:%3" : "=a"(gb[i]) : "v"(l16), "s"(gt + (i >> 2) * 4096), "i"((i & 3) * 1024) : "memory");
  }
  SB0;
  float gam = bf_lo(*reinterpret_cast<const unsigned short*>(smem + GAM + wv * 32 + fr * 2));
  float bet = bf_lo(*reinterpret_cast<const unsigned short*>(smem + GAM + 128 + wv * 32 + fr * 2));
  float eps = fmaxf(__builtin_amdgcn_ldexpf(1e-5f, 2 * cx.es), 1e-30f);
  const int l0 = HOP * (bs + par) + fr;
  const int le = fr < 10 ? HOP * (bs + par + 2 * (fr >> 1)) + 48 + (fr & 1) : 1 << 20;
  unsigned oo = (unsigned)((l0 * D_ + (c64i * 2 + hw) * 32 + 8 * g) * 2), oe = (unsigned)((le * D_ + (c64i * 2 + hw) * 32 + 8 * g) * 2);
  float v4[4];
#pragma unroll
  for (int tt = 0; tt < 4; ++tt) v4[tt] = (tt < 3 ? l0 + 8 * HOP + 16 * tt : le) < lend ? 1.f : 0.f;
  unsigned long long* stats = reinterpret_cast<unsigned long long*>(ws + WS_STATS);
  const u32x4 rss = make_rsrc(stats + (b * 32 + (c64i & 1) * 4) * 256 + nt * 64, 65536);
  unsigned sp = (unsigned)((g * 256 + 16 * wv + fr) * 8);
  const unsigned red = lds0 + 8 * 12288, aff = red + 4096;
  unsigned rw_ = red + (unsigned)((par * 64 + hw * 32 + 8 * g + (fr & 7)) * 8 + (fr >> 3) * 4), aw_ = aff + (unsigned)(((g & 1) * 64 + 16 * wv + fr) * 4);
  unsigned ar_ = aff + (unsigned)((hw * 32 + 8 * g) * 4);
  asm volatile("" : "+v"(gam), "+v"(bet), "+v"(eps), "+v"(oo), "+v"(oe), "+v"(sp), "+v"(rw_), "+v"(aw_), "+v"(ar_), "+v"(v4[0]), "+v"(v4[1]), "+v"(v4[2]), "+v"(v4[3]));
  SB0;
  if (wv == 0) {
    wait_vm<16>();
    asm volatile("" : "+v"(fv));
    for (unsigned spins = 0; !__all(fv != 0u) && spins < (1u << 21); ++spins)
      asm volatile("s_sleep 1\n\tbuffer_load_dword %0, %1, %2, 0 offen sc1\n\ts_waitcnt vmcnt(0)" : "=v"(fv) : "v"(fp), "s"(rsf) : "memory");
  }
  __builtin_amdgcn_s_barrier();
  u32x2 pv[2];
  asm volatile("buffer_load_dwordx2 %0, %2, %3, %4 offen sc1\n\tbuffer_load_dwordx2 %1, %2, %3, %4 offen offset:32 sc1"
               : "=&v"(pv[0]), "=&v"(pv[1]) : "v"(vp), "s"(rsy), "s"(so[5]) : "memory");
#pragma unroll
  for (int i = 0; i < 5; ++i)
    asm volatile("s_mov_b32 m0, %0\n\ts_nop 0\n\tbuffer_load_dwordx4 %1, %2, %3 offen sc1 lds\n\tbuffer_load_dwordx4 %1, %2, %3 offen offset:1024 sc1 lds\n\t"
                 "buffer_load_dwordx4 %1, %2, %3 offen offset:2048 sc1 lds\n\tbuffer_load_dwordx4 %1, %2, %3 offen offset:3072 sc1 lds"
                 :: "s"(lds0 + wv * 20480 + i * 4096), "v"(vo), "s"(rsy), "s"(so[i]) : "memory");
  float s1[8], s2[8];
  {
    bf16x8 a[2][4];
    auto rd = [&](int i, int q) __attribute__((always_inline)) {
      const lds_s16x4* p = (const lds_s16x4*)(uintptr_t)((q & 2 ? src1 : src) + i * 4096 + (q & 1) * 2048);
      a[i & 1][q] = cat(__builtin_amdgcn_ds_read_tr16_b64_v4i16((lds_s16x4*)p), __builtin_amdgcn_ds_read_tr16_b64_v4i16((lds_s16x4*)p + 128));
    };
    f32x4 t3[2];
    auto sums = [&](int cp, int q) __attribute__((always_inline)) {
      const int i = cp >> 3, s = (cp >> 2) & 1, tt = cp & 3, e = 4 * s + q;
      if (tt == 3) {
        if (i) ye[s][q] = (fr >> 1) == i ? t3[s][q] : ye[s][q];
        asm volatile("" : "+v"(ye[s]));
        return;
      }
      float d = y[6 * i + 3 * s + tt][q];
      if (i == 4) d *= v4[tt];
      if (i == 0 && tt == 0) { s1[e] = d; s2[e] = d * d; }
      else { s1[e] += d; s2[e] = __builtin_fmaf(d, d, s2[e]); }
      asm volatile("" : "+v"(s1[e]), "+v"(s2[e]));
    };
    wait_vm<16>();
    SB0;
#pragma unroll
    for (int q = 0; q < 4; ++q) { rd(0, q); SB0; }
    asm volatile("" : "+v"(pv[0]), "+v"(pv[1]));
#pragma unroll
    for (int s = 0; s < 2; ++s) {
      npv[s] = f32x4{bf_lo(pv[s][0]), bf_hi(pv[s][0]), bf_lo(pv[s][1]), bf_hi(pv[s][1])} * (-1.0f / 64.0f);
      cin[s] += npv[s];
    }
    asm volatile("" : "+v"(npv[0]), "+v"(npv[1]), "+v"(cin[0]), "+v"(cin[1]));
    asm volatile("s_nop 1");
    SB0;
    sfor<0, 42>([&](auto cc) __attribute__((always_inline)) {
      constexpr int c = cc.v, i = c >> 3, is = (c >> 2) & 1, tt = c & 3;
      if constexpr ((c & 7) == 4 && i < 4) wait_vm<12 - 4 * i>();
      sfor<0, 4>([&](auto qq_) __attribute__((always_inline)) {
        constexpr int q = qq_.v;
        if constexpr (c < 40) {
          f32x4& d = tt < 3 ? y[6 * i + 3 * is + (tt < 3 ? tt : 0)] : i ? t3[is] : ye[is];
          if constexpr (q == 0) imfma0(d, a[i & 1][2 * is], gb[4 * tt + 1], tt == 3 ? cin[is] : npv[is]);
          else imfma1(d, a[i & 1][2 * is + (q >> 1)], gb[4 * tt + (q ^ 1)]);
        }
        SB0;
        if constexpr (((c & 7) == 4 || (c & 7) == 5) && i < 4) { if (q & 1) rd(i + 1, (c & 1) * 2 + (q >> 1)); }
        if constexpr (c >= 2) sums(c - 2, q);
        SB0;
      });
    });
    asm volatile("" :: "v"(npv[0]), "v"(npv[1]), "v"(cin[0]), "v"(cin[1]));
#pragma unroll
    for (int e = 0; e < 8; ++e) {
      const float d = v4[3] != 0.f ? ye[e >> 2][e & 3] : 0.f;
      s1[e] += d;
      s2[e] = __builtin_fmaf(d, d, s2[e]);
    }
  }
  {
    float v[8];
#pragma unroll
    for (int j = 0; j < 8; ++j) { v[j] = s1[j]; }
#pragma unroll
    for (int j = 0; j < 8; ++j) DPPADD(v[j], s1[j], "row_ror:8", "0x3");
#pragma unroll
    for (int j = 0; j < 8; ++j) DPPADD(v[j], s2[j], "row_ror:8", "0xc");
    asm volatile("s_nop 0");
#pragma unroll
    for (int j = 0; j < 4; ++j) DPPADD(v[j], v[j], "row_half_mirror", "0x5");
#pragma unroll
    for (int j = 0; j < 4; ++j) DPPADD(v[j], v[j + 4], "row_half_mirror", "0xa");
    asm volatile("s_nop 0");
#pragma unroll
    for (int j = 0; j < 4; ++j) v[j] = dpp_add<0x4E>(v[j]);
    SB0;
#pragma unroll
    for (int j = 0; j < 4; ++j) v[j] = dpp_add<0xB1>(v[j]);
    const float w0 = fr & 1 ? v[1] : v[0], w1 = fr & 1 ? v[3] : v[2];
    *(__attribute__((address_space(3))) float*)(uintptr_t)rw_ = fr & 2 ? w1 : w0;
  }
  asm volatile("s_waitcnt lgkmcnt(0)" ::: "memory");
  __builtin_amdgcn_s_barrier();
  if (wv == 0) {
    typedef __attribute__((address_space(3))) f32x2 lds_f32x2;
    const f32x2 sm = *(lds_f32x2*)(uintptr_t)(red + lane * 8) + *(lds_f32x2*)(uintptr_t)(red + 512 + lane * 8);
    u32x2 pk = {__float_as_uint(sm[0]), __float_as_uint(sm[1])};
    if (pk[0] == ~0u) pk[0] = ~1u;
    asm volatile("buffer_store_dwordx2 %0, %1, %2, %3 offen sc1" :: "v"(pk), "v"(lane * 8), "s"(rss), "s"((rg + (rg & 12)) * 2048) : "memory");
  }
  {
    u32x2 pk[4];
    for (unsigned spins = 0;; ++spins) {
      asm volatile("buffer_load_dwordx2 %0, %4, %5, 0 offen sc1\n\tbuffer_load_dwordx2 %1, %4, %5, %6 offen sc1\n\tbuffer_load_dwordx2 %2, %4, %5, %7 offen sc1\n\t"
                   "buffer_load_dwordx2 %3, %4, %5, %8 offen sc1\n\ts_waitcnt vmcnt(0)"
                   : "=&v"(pk[0]), "=&v"(pk[1]), "=&v"(pk[2]), "=&v"(pk[3]) : "v"(sp), "s"(rss), "s"(16384), "s"(32768), "s"(49152) : "memory");
      if (__all(max(max(pk[0][0], pk[1][0]), max(pk[2][0], pk[3][0])) != ~0u) || spins > (1u << 20)) break;
      __builtin_amdgcn_s_sleep(1);
    }
    float a = 0.f, q = 0.f;
#pragma unroll
    for (int k = 0; k < 4; ++k) { a += __uint_as_float(pk[k][0]); q += __uint_as_float(pk[k][1]); }
    auto r0 = __builtin_amdgcn_permlane16_swap(__float_as_uint(a), __float_as_uint(q), false, false);
    const unsigned c0 = __float_as_uint(__uint_as_float(r0[0]) + __uint_as_float(r0[1]));
    auto r1 = __builtin_amdgcn_permlane32_swap(c0, c0, false, false);
    const unsigned c1 = __float_as_uint(__uint_as_float(r1[0]) + __uint_as_float(r1[1]));
    auto r2 = __builtin_amdgcn_permlane16_swap(c1, c1, false, false);
    const float mean = __uint_as_float(r2[0]) * (1.0f / (float)L_);
    const float var = fmaxf(__builtin_fmaf(-mean, mean, __uint_as_float(r2[1]) * (1.0f / (float)L_)), 0.f);
    const float gg = gam * __builtin_amdgcn_rsqf(var + eps);
    *(__attribute__((address_space(3))) float*)(uintptr_t)aw_ = g & 1 ? __builtin_fmaf(-mean, gg, bet) : gg;
    asm volatile("s_waitcnt lgkmcnt(0)" ::: "memory");
    __builtin_amdgcn_s_barrier();
  }
  {
    const u32x4 ro = make_rsrc(out + (size_t)b * L_ * D_, lend * (D_ * 2));
    f32x4 ga[2], be[2];
#pragma unroll
    for (int s = 0; s < 2; ++s) {
      typedef __attribute__((address_space(3))) f32x4 lds_f32x4;
      ga[s] = *(lds_f32x4*)(uintptr_t)(ar_ + 16 * s);
      be[s] = *(lds_f32x4*)(uintptr_t)(ar_ + 256 + 16 * s);
    }
    const float k1 = -2.0f * 0.7978845608028654f * 1.4426950408889634f, k2 = k1 * 0.044715f;
    f32x4 kk1 = {k1, k1, k1, k1}, kk2 = {k2, k2, k2, k2};
    asm volatile("" : "+v"(kk1), "+v"(kk2));
#pragma unroll
    for (int n = 0; n < 16; ++n) {
      const int i = n / 3, tt = n % 3;
      f32x4 o[2];
#pragma unroll
      for (int s = 0; s < 2; ++s) {
        const f32x4 z = (n < 15 ? y[(6 * i + 3 * s + tt) % 30] : ye[s]) * ga[s] + be[s];
        const f32x4 a = z * (z * z * kk2 + kk1);
        f32x4 e;
#pragma unroll
        for (int r = 0; r < 4; ++r) e[r] = __builtin_amdgcn_exp2f(a[r]);
        e += 1.0f;
#pragma unroll
        for (int r = 0; r < 4; ++r) e[r] = __builtin_amdgcn_rcpf(e[r]);
        o[s] = z * e;
      }
      st16(pack8(o[0], o[1]), ro, n < 15 ? oo : oe, n < 15 ? (2 * HOP * i + 16 * tt) * (D_ * 2) : 0);
      SB0;
    }
  }

  asm volatile(".Lcf_b:\n\ts_nop 0" ::: "memory");
}

extern "C" void kernel_launch(void* const* d_in, const int* in_sizes, int n_in,
                              void* d_out, int out_size, void* d_ws, size_t ws_size,
                              hipStream_t stream) {
  const unsigned short* x = (const unsigned short*)d_in[0];
  const unsigned short* w = (const unsigned short*)d_in[1];
  const unsigned short* gamma = (const unsigned short*)d_in[2];
  const unsigned short* beta = (const unsigned short*)d_in[3];
  char* ws = (char*)(((uintptr_t)d_ws + (2u << 20) - 1) & ~(uintptr_t)((2u << 20) - 1));
  conv_fft<<<256, 256, 0, stream>>>(ws, x, w, gamma, beta, (unsigned short*)d_out);
}
```

```cpp
#include <hip/hip_runtime.h>
#include <stdint.h>

typedef __attribute__((ext_vector_type(8))) short bf16x8;
typedef __attribute__((ext_vector_type(4))) short s16x4;
typedef __attribute__((ext_vector_type(4))) float f32x4;
typedef __attribute__((ext_vector_type(2))) float f32x2;
typedef __attribute__((ext_vector_type(2))) __bf16 bf16x2v;
typedef __attribute__((ext_vector_type(4))) unsigned int u32x4;
typedef __attribute__((ext_vector_type(2))) unsigned int u32x2;
constexpr int NH = 6, NST = 3;
typedef __attribute__((ext_vector_type(2 * NH))) unsigned int u32xh;
typedef __attribute__((ext_vector_type(8))) _Float16 f16x8;
typedef __attribute__((ext_vector_type(2))) _Float16 f16x2;
typedef __attribute__((ext_vector_type(2))) unsigned short u16x2;
typedef __attribute__((address_space(1))) unsigned long long gu64;
typedef __attribute__((address_space(1))) unsigned gu32;
typedef __attribute__((address_space(3))) void lds_void;
typedef __attribute__((address_space(3))) s16x4 lds_s16x4;

constexpr int T_ = 16000, D_ = 512, L_ = 7985;
constexpr int HOP = 50, NBLK = 160, ROWS = 2 * NBLK;

constexpr unsigned WS_S = 0;
constexpr unsigned SBLK = 4u << 20;
constexpr unsigned sbase(unsigned sl) { return sl * 131072 + (sl >> 4) * 8192; }
constexpr unsigned FLG = 32u * SBLK;
constexpr unsigned WS_GRP = WS_S + FLG + 16 * 8192;
constexpr unsigned WS_D = WS_S + FLG + 20 * 8192;
constexpr unsigned WS_L = WS_D + ROWS * 1024;
constexpr unsigned WS_Y = 40u << 20;
constexpr unsigned WS_STATS = 129u << 20;
constexpr unsigned WS_FA = WS_STATS + (1u << 17);
constexpr unsigned WS_PUB = WS_STATS + (1u << 19);
static_assert(WS_L + D_ * D_ * 2 <= WS_STATS && FLG % (16 * 8192) == 0, "workspace layout");

#define RLX_AGENT __ATOMIC_RELAXED, __HIP_MEMORY_SCOPE_AGENT

constexpr double COS64[17] = {1.0, 0.99518472667219688624, 0.98078528040323044913, 0.95694033573220886494,
    0.92387953251128675613, 0.88192126434835502971, 0.83146961230254523708, 0.77301045336273696081,
    0.70710678118654752440, 0.63439328416364549822, 0.55557023301960222474, 0.47139673682599764856,
    0.38268343236508977173, 0.29028467725446236764, 0.19509032201612826785, 0.09801714032956060199, 0.0};
constexpr double c64(int k) { k &= 63; if (k > 32) k = 64 - k; return k <= 16 ? COS64[k] : -COS64[32 - k]; }
constexpr double s64(int k) { return c64(k + 48); }
constexpr unsigned short bf_rne(double v) {
  const unsigned u = __builtin_bit_cast(unsigned, (float)v);
  return (unsigned short)((u + 0x7fffu + ((u >> 16) & 1u)) >> 16);
}
constexpr double bf_val(unsigned short h) { return (double)__builtin_bit_cast(float, (unsigned)h << 16); }
constexpr unsigned short hilo(double v, int lo) { return lo ? bf_rne(v - bf_val(bf_rne(v))) : bf_rne(v); }
constexpr unsigned short nz(unsigned short h) { return h & 0x7fff ? h : (unsigned short)(97 << 7); }
constexpr double R3 = 0.86602540378443864676;
constexpr double dft(int c, int t, double sgn) {
  return (c >> 1) == 0 ? ((c & t) & 1 ? -1.0 : 1.0)
                       : ((c & 1) ? -0.5 * c64((c >> 1) * t) - R3 * sgn * s64((c >> 1) * t) : c64((c >> 1) * t));
}
constexpr double idft(int c, int t) {
  return (c >> 1) == 0 ? ((c & t) & 1 ? -1.0 : 1.0) / 64.0
                       : ((c & 1) ? -s64((c >> 1) * t) / R3 - c64((c >> 1) * t) * (2.0 / 3.0) : c64((c >> 1) * t) * (4.0 / 3.0)) / 32.0;
}
constexpr double UP = 1.0 + 0.72134752 / 256.0;
constexpr int kidx(int g, int j) { return j < 4 ? 4 * g + j : 12 + 4 * g + j; }
struct Tables {
  unsigned short F[4][2][2][64][8];
  unsigned short V[2][4][64][8];
  unsigned short G[4][2][2][64][8];
};
constexpr Tables make_tables() {
  Tables tb{};
  for (int l = 0; l < 64; ++l)
    for (int j = 0; j < 8; ++j) {
      const int g = l >> 4, n = l & 15;
      for (int ct = 0; ct < 4; ++ct) {
        for (int ks = 0; ks < 2; ++ks)
          for (int h = 0; h < 2; ++h) tb.F[ct][ks][h][l][j] = nz(hilo(UP * dft(16 * ct + n, 32 * ks + kidx(g, j), -1.0), h));
        for (int p = 0; p < 2; ++p) {
          const int tap = 4 * g + (j & 3);
          tb.V[p][ct][l][j] = (p == 1 && tap == 15) ? (unsigned short)0 : nz(hilo(UP * dft(16 * ct + n, tap, 1.0), j >> 2));
        }
      }
      for (int tt = 0; tt < 4; ++tt)
        for (int ks = 0; ks < 2; ++ks)
          for (int h = 0; h < 2; ++h) {
            const int c = 32 * ks + kidx(g, j);
            tb.G[tt][ks][h][l][j] = hilo(idft(c, 16 * tt + (tt < 3 ? n : n & 1)), h);
          }
    }
  return tb;
}
__device__ const __attribute__((aligned(16))) Tables TAB = make_tables();

__device__ __forceinline__ unsigned pk_bf16(float lo, float hi) {
  return __builtin_bit_cast(unsigned, __builtin_convertvector(f32x2{lo, hi}, bf16x2v));
}
__device__ __forceinline__ float bf_lo(unsigned v) { return __uint_as_float(v << 16); }
__device__ __forceinline__ float bf_hi(unsigned v) { return __uint_as_float(v & 0xffff0000u); }
__device__ __forceinline__ u32x4 pack8(f32x4 a, f32x4 b) {
  return u32x4{pk_bf16(a[0], a[1]), pk_bf16(a[2], a[3]), pk_bf16(b[0], b[1]), pk_bf16(b[2], b[3])};
}
__device__ __forceinline__ unsigned pk_f16(float lo, float hi) {
  return (__builtin_bit_cast(unsigned, __builtin_amdgcn_cvt_pkrtz(lo, hi)) & 0xfff8fff8u);
}
__device__ __forceinline__ u32x4 pack8h(f32x4 a, f32x4 b) {
  return u32x4{pk_f16(a[0], a[1]), pk_f16(a[2], a[3]), pk_f16(b[0], b[1]), pk_f16(b[2], b[3])};
}
__device__ __forceinline__ unsigned add_h(const bf16x8& a, const bf16x8& b, int d) {
  const unsigned x = __builtin_bit_cast(u32x4, a)[d], y = __builtin_bit_cast(u32x4, b)[d];
  unsigned r = __builtin_bit_cast(unsigned, __builtin_bit_cast(f16x2, x) + __builtin_bit_cast(f16x2, y));
  return r;
}
__device__ __forceinline__ unsigned amax(unsigned m, unsigned v) {
  return __builtin_bit_cast(unsigned, __builtin_elementwise_max(__builtin_bit_cast(u16x2, m), __builtin_bit_cast(u16x2, v & 0x7fff7fffu)));
}
struct Rng { f16x2 hi = {0, 0}, lo = {0, 0}; };
__device__ __forceinline__ void widen(Rng& r, unsigned a, unsigned b) {
  const f16x2 x = __builtin_bit_cast(f16x2, a), y = __builtin_bit_cast(f16x2, b);
  r.hi = __builtin_elementwise_maximum(__builtin_elementwise_maximum(r.hi, x), y);
  r.lo = __builtin_elementwise_minimum(__builtin_elementwise_minimum(r.lo, x), y);
}
__device__ __forceinline__ unsigned amax(const Rng& r) { return amax(amax(0, __builtin_bit_cast(unsigned, r.hi)), __builtin_bit_cast(unsigned, r.lo)); }
__device__ __forceinline__ unsigned expo(unsigned m) { return max(m & 0xffffu, m >> 16) >> 7; }
__device__ __forceinline__ unsigned wave_max(unsigned v) {
  unsigned m = 0;
#pragma unroll 1
  for (int l = 0; l < 64; ++l) m = max(m, (unsigned)__builtin_amdgcn_readlane((int)v, l));
  return m;
}
__device__ __forceinline__ void scale8(bf16x8& v, int e) {
  u32x4 u = __builtin_bit_cast(u32x4, v);
#pragma unroll
  for (int d = 0; d < 4; ++d)
    u[d] = __builtin_amdgcn_perm(__float_as_uint(__builtin_amdgcn_ldexpf(bf_hi(u[d]), e)), __float_as_uint(__builtin_amdgcn_ldexpf(bf_lo(u[d]), e)), 0x07060302u);
  v = __builtin_bit_cast(bf16x8, u);
}
__device__ __forceinline__ void restart(unsigned id) {
  asm volatile("s_waitcnt vmcnt(0)\n\ts_barrier\n\ts_branch conv_fft" :: "{s[0:1]}"(__builtin_amdgcn_kernarg_segment_ptr()), "{s2}"(id), "{v0}"(threadIdx.x) : "memory");
}
__device__ __forceinline__ s16x4 tr_read(const char* p) {
  return __builtin_amdgcn_ds_read_tr16_b64_v4i16((lds_s16x4*)p);
}
__device__ __forceinline__ bf16x8 cat(s16x4 a, s16x4 b) {
  const u32x2 x = __builtin_bit_cast(u32x2, a), y = __builtin_bit_cast(u32x2, b);
  return __builtin_bit_cast(bf16x8, u32x4{x[0], x[1], y[0], y[1]});
}
#define MFMA(a, b, c) __builtin_amdgcn_mfma_f32_16x16x32_bf16(a, b, c, 0, 0, 0)
#define MFMH(a, b, c) __builtin_amdgcn_mfma_f32_16x16x32_f16(__builtin_bit_cast(f16x8, a), __builtin_bit_cast(f16x8, b), c, 0, 0, 0)
#define SB0 __builtin_amdgcn_sched_barrier(0)
#define ZERO4 (f32x4{0.f, 0.f, 0.f, 0.f})
#define DMA16(POL, rs, lds, voff, soff) \
  asm volatile("s_mov_b32 m0, %0\n\ts_nop 0\n\tbuffer_load_dwordx4 %1, %2, %3 offen " POL " lds" \
               :: "s"(lds), "v"(voff), "s"(rs), "s"(soff) : "memory")
__device__ __forceinline__ void st16(const u32x4& v, const u32x4& rs, unsigned voff, unsigned soff) {
  asm volatile("buffer_store_dwordx4 %0, %1, %2, %3 offen sc1\n\ts_nop 1" :: "v"(v), "v"(voff), "s"(rs), "s"(soff) : "memory");
}
__device__ __forceinline__ void ld16(bf16x8& v, const void* p) {
  asm volatile("global_load_dwordx4 %0, %1, off" : "=v"(v) : "v"(p) : "memory");
}
template <int N> __device__ __forceinline__ void wait_vm() { asm volatile("s_waitcnt vmcnt(%0)" :: "i"(N) : "memory"); }
__device__ __forceinline__ u32x4 make_rsrc(const void* p, unsigned bytes) {
  const unsigned long long a = (unsigned long long)p;
  return u32x4{(unsigned)a, (unsigned)(a >> 32) & 0xffffu, bytes, 0x00020000u};
}
__device__ __forceinline__ void spin_flags(const char* p) {
  for (unsigned spins = 0; !__all(__hip_atomic_load((const gu32*)p, RLX_AGENT) != 0u) && spins < (1u << 21); ++spins)
    __builtin_amdgcn_s_sleep(3);
}

typedef __attribute__((ext_vector_type(8))) unsigned int u32x8;
typedef __attribute__((ext_vector_type(16))) unsigned int u32x16;
typedef __attribute__((ext_vector_type(32))) unsigned int u32x32;
constexpr int GW = 10;
constexpr int NQ1 = GW / 2, NQW = 16 - NQ1;
constexpr int STG = 36 * 1024;
constexpr int RAWB = 67 * 1024, XT1 = RAWB + NQ1 * 4096, SINK = XT1 + RAWB;
constexpr int RAWL = 3 * STG;
constexpr int RAW0 = RAWL + 12 * 4096;
constexpr int GAM = RAWL + 36864, CBUF = RAWL + 40960;

struct Ctx {

  char* smem;
  char* ws;
  const unsigned short *gamma, *beta;
  u32x4 rs;
  unsigned lds0;
  unsigned voffw, voffx;
  unsigned poff, xo;
  unsigned id;
  unsigned tag[2];
  int es;
  int wv, vb;
  unsigned long long pc;
  unsigned pfo;
  u32x4 sx, sw;
  unsigned xcc;
};
__device__ __forceinline__ void code_touch(const Ctx& cx) {
  if ((cx.wv ^ (cx.vb >> 3)) & 3) return;
  const unsigned l = __builtin_amdgcn_mbcnt_hi(~0u, __builtin_amdgcn_mbcnt_lo(~0u, cx.wv * 64u));
#pragma unroll
  for (int k = 0; k < 2; ++k)
    asm volatile("s_mov_b32 m0, %0\n\ts_nop 0\n\tglobal_load_lds_dword %1, %2" :: "s"(SINK + cx.wv * 512 + k * 256), "v"(min((l + k * 256) * 128u, cx.pfo)), "s"(cx.pc) : "memory");
}
__device__ __forceinline__ void stage_issue(const Ctx& cx, int kc) {
  const unsigned slot = (unsigned)((kc + 1) % 3) * STG;
#pragma unroll
  for (int i = 0; i < 9; ++i) {
    const int j = i < 4 ? i : i - 4;
    const bool ahead = i < 4 || j == 0 || j == 3;
    const unsigned lo = i < 4 ? j * 4096 : j < 3 ? 16384 + j * 4096 : j == 3 ? 16384 + 10240 : 16384 + 14336;
    const unsigned go = i < 4 ? j * 4096 : (j >> 1) * 16384 + (j & 1) * 4096;
    if (kc >= 0 || ahead)
      DMA16("sc1", cx.rs, cx.lds0 + slot + lo + (i == 6 ? (cx.wv & 2) * 4096 : 0), i < 4 ? cx.voffw : cx.voffx, (unsigned)(kc + ahead) * SBLK + go);
  }
}
__device__ __forceinline__ unsigned x_place(unsigned kb, unsigned blk) { return kb + (blk + (blk & 192u)) * 128u; }
template <int V> struct IC { static constexpr int v = V; constexpr operator int() const { return V; } };
template <int A, int B, class F> __device__ __forceinline__ void sfor(F f) {
  if constexpr (A < B) { f(IC<A>{}); sfor<A + 1, B>(f); }
}
__device__ __forceinline__ unsigned dpp_swap1(unsigned v) {
  return (unsigned)__builtin_amdgcn_update_dpp(0, (int)v, 0xB1, 0xf, 0xf, false);
}

__device__ __forceinline__ int run_bs(int vb, int t) { return 5 * ((vb >> 4) + 16 * ((vb ^ t) & 1)); }
__device__ __forceinline__ int run_q(int vb, int t) { return (((vb >> 1) & 1) + ((vb >> 2) & 2) + 2 * t) & 3; }
__device__ __forceinline__ unsigned x_addr(int vb, int t, int wv) {
  const int lane = threadIdx.x & 63, q = run_q(vb, t);
  const unsigned c = (unsigned)((lane & 15) ^ (((4 * wv + (lane >> 4)) & 7) << 1));
  return (unsigned)(2 * (HOP * run_bs(vb, t) + 4 * wv + (lane >> 4)) + ((t ^ q ^ (q >> 1)) & 1)) * 1024u + c * 16 + q * 256;
}
__device__ __forceinline__ u32x4 x_rsrc(const unsigned short* x, int vb) { return make_rsrc(x + (size_t)((vb >> 2) & 1) * T_ * D_, T_ * D_ * 2); }
__device__ __forceinline__ void issue_x(int t, int wv, unsigned a, const u32x4& rx, unsigned lds0, int j0 = 0, int j1 = 17) {
#pragma unroll
  for (int jj = 0; jj < 17; ++jj)
    if (jj >= j0 && jj < j1 && (jj < 16 || wv < 3)) DMA16("nt", rx, lds0 + t * XT1 + wv * 1024 + jj * 4096, a, jj * 32768u);
}
#define CKP(i) CK(i)
#define CKQ(p)
#define CKE(i)
#define CKI(i)
__device__ __forceinline__ void mfma0(f32x4& d, const bf16x8& a, const bf16x8& b) {
  asm volatile("v_mfma_f32_16x16x32_bf16 %0, %1, %2, 0" : "=&v"(d) : "v"(a), "v"(b));
}
__device__ __forceinline__ void mfma1(f32x4& d, const bf16x8& a, const bf16x8& b) {
  asm volatile("v_mfma_f32_16x16x32_bf16 %0, %1, %2, %0" : "+v"(d) : "v"(a), "v"(b));
}
__device__ __forceinline__ bf16x8 dup(s16x4 a) {
  u32x2 lo = __builtin_bit_cast(u32x2, a), hi;
  asm volatile("v_mov_b64 %0, %1" : "=v"(hi) : "v"(lo));
  return __builtin_bit_cast(bf16x8, u32x4{lo[0], lo[1], hi[0], hi[1]});
}
__device__ __forceinline__ void scale8p(bf16x8& v, int e) {
  typedef __attribute__((ext_vector_type(8))) unsigned short u16x8;
  const unsigned short h = (unsigned short)(e << 7);
  v = __builtin_bit_cast(bf16x8, (u16x8)(__builtin_bit_cast(u16x8, v) + h));
}
__device__ __forceinline__ void pin(u32x4& v) { asm volatile("" : "+v"(v)); }
struct XR { s16x4 t[8]; };
#define P0_ 16
#define SLP_ 3
#define GPO_ 113u
#define GPE_ 137u
#define CT_ 0
#define PRK_ 16
#define TE_ 6
#define LDN_ 4
#define NA_ 14
#define SX0_ 9
#define SCN_ 8
#define PG_ 42
#define PS_ 42
#define NFA_ 2
constexpr int P0 = P0_, P1 = 46;
constexpr int PG = PG_, NFA = NFA_;
constexpr int TE = TE_;
constexpr int NLD = 17 - TE + 1 + NQW + 1;
constexpr int ld_pos(int l) { return 2 * l / LDN_; }
constexpr int ld_slot(int l) { int o = 0; for (int k = 0; k < l; ++k) o += ld_pos(k) == ld_pos(l); return 1 + 2 * o; }
constexpr int NIT = 46, NS = 40, NA = NA_, NB = 26;
static_assert(NH == 6 && NST == 3, "x blocks stored in the loop");
constexpr int ord(int p) { return p == 0 ? 0 : p < 6 ? p + 2 : p < 8 ? p - 5 : p; }
constexpr int st_w(int j) { return j < 2 ? j : j < 7 ? -1 : j < 15 ? j - 5 : -1; }
constexpr int st_x(int j) { return j < 7 ? j - 2 : j - 10; }
constexpr int st_pos(int j) { return st_w(j) >= 0 ? (j < 9 ? 0 : j < 13 ? 6 : 7) : j < 7 ? j - 1 : j - 7; }
#define NBP_ 4
constexpr int NBP = NBP_;
constexpr int st_first(int h) {
  int J = h <= P0 ? 0 : h >= P1 ? NS : h <= P0 + NBP ? (h - P0) * (NA + NFA) / NBP : NA + NFA + (h - P0 - NBP) * (NS - NA - NFA) / (P1 - P0 - NBP);
  for (int k = 0; k < J; ++k)
    if (2 * st_pos(k >> 1) + (k & 1) + 2 > h - 1) return k;
  return J;
}
constexpr int sc_pos(int k) { return k < 17 ? k * SX0_ / 17 : SX0_ + (k - 17) * (P0 - SX0_) / 17; }
constexpr int sc_raw(int k) { return k < NQ1 ? k : P0 + (k - NQ1) * (NBP - 1) / NQW; }
constexpr int sc_cnt(int h) { int c = 0; for (int k = 0; k < 34; ++k) c += sc_pos(k) == h; return c; }
constexpr bool sc_ok() { for (int h = 0; h < NIT; ++h) if (sc_cnt(h) > 3) return false; return true; }
constexpr int sc_wait(int h) {
  int j = -1;
  for (int k = 17; k < 34; ++k) if (sc_pos(k) == h) j = k - 17;
  if (j < 0) return -1;
  int c = j < TE ? TE - 1 - j : 0;
  for (int l = 0; l < NLD; ++l)
    if (ld_pos(l) < h && l > j - TE) c += l != 16 - TE;
  return c;
}
constexpr int XBAR = st_first(25) > NA + NFA - 1 ? -1 : 24;
static_assert(ld_pos(NLD - 1) < P0 && P0 < 24 && LDN_ <= 8, "loads before the first store");
static_assert(sc_ok() && st_first(P0 + NBP) == NA + NFA && st_first(P0 + NBP - 1) <= NA + NFA - 1 && SX0_ < P0, "range check before the first hand-off");
struct Hand {
  u32x4 held[2 * NH], st[2 * NST];
  bf16x8 vb;
};
constexpr int A_RAW = 144, A_WL = 184, A_VEC = 188, A_SCA = 214, A_XA = 240;
template <int I> __device__ __forceinline__ void stash(unsigned v) { asm volatile("v_accvgpr_write_b32 a%c0, %1" :: "i"(I), "v"(v)); }
template <int I> __device__ __forceinline__ unsigned unstash() {
  unsigned v;
  asm volatile("v_accvgpr_read_b32 %0, a%c1" : "=v"(v) : "i"(I));
  return v;
}
__device__ __forceinline__ void stash4(int a, const u32x4& v) {
  asm volatile("v_accvgpr_write_b32 a%c0, %4\n\tv_accvgpr_write_b32 a%c1, %5\n\tv_accvgpr_write_b32 a%c2, %6\n\tv_accvgpr_write_b32 a%c3, %7"
               :: "i"(a), "i"(a + 1), "i"(a + 2), "i"(a + 3), "v"(v[0]), "v"(v[1]), "v"(v[2]), "v"(v[3]));
}
constexpr int hw_reg(int mg, int q, int half) { return ((mg * 5 + q) * 2 + half) * 4; }
template <int A> __device__ __forceinline__ void load_acc(unsigned vo, const u32x4& rs, unsigned so) {
  asm volatile("buffer_load_dwordx4 a[%c3:%c4], %0, %1, %2 offen nt" :: "v"(vo), "s"(rs), "s"(so), "i"(A), "i"(A + 3) : "memory");
}
__device__ __forceinline__ void ld8(u32x2& v, const char* p, unsigned o) {
  asm volatile("global_load_dwordx2 %0, %1, %2 sc1" : "=v"(v) : "v"(o), "s"(p) : "memory");
}
__device__ __forceinline__ void loop_setup(const Ctx& cx, unsigned pwr, unsigned prd, unsigned hst) {
  const int bid = cx.vb, idx = bid >> 3, wv = cx.wv, lane = threadIdx.x & 63;
  const int slot = (bid & 7) * 4 + (idx >> 3), b = (idx >> 2) & 1, nt = idx & 3, xb = (bid >> 2) & 1;
  const int mg = wv >> 1, nh = wv & 1, g = lane >> 4, qq = (lane >> 2) & 3, pp = lane & 3, rt = 4 * g + qq;
  const int rg = (bid & 1) * 8 + (slot & 1) * 4 + (bid & 2) + (idx >> 4);
  sfor<0, 4>([&](auto c) __attribute__((always_inline)) {
    stash<A_VEC + c.v>((unsigned)(rt * 512 + (((nh * 4 + c.v) ^ (rt & 7)) << 5) + pp * 8));
    stash<A_VEC + 5 + c.v>((RAW0 + prd) ^ (unsigned)(c.v << 5));
  });
  { const int r = lane & 15; stash<A_VEC + 4>((unsigned)(16384 + mg * 5 * 2048 + (r >> 3) * 1024 + (r & 7) * 64 + ((g ^ ((r >> 3) << 1)) << 4))); }
  stash<A_VEC + 9>(cx.voffw);
  stash<A_VEC + 10>(cx.voffx);
  {
    const int kr8 = lane >> 3;
    stash<A_VEC + 11>((unsigned)(WS_L - WS_S + (8 * wv + kr8) * 1024 + (nt * 2 + xb) * 128 + (((lane & 7) ^ ((kr8 & 2) | (wv & 1) << 2)) << 4)));
  }
  stash<A_VEC + 12>((unsigned)(WS_D - WS_S + (b * NBLK + 10 * rg + min(lane & 15, 9)) * 1024 + g * 16));
  stash<A_VEC + 13>((unsigned)(RAWL + (8 * g + qq) * 128 + ((wv ^ ((qq >> 1) | (g & 1) << 1)) << 5) + pp * 8));
  stash<A_VEC + 14>(cx.poff);
  stash<A_VEC + 15>(cx.xo);
  stash<A_VEC + 16>(hst);
  stash<A_VEC + 17>(wv == 0 && lane < 8 ? (unsigned)(lane * 1024 + (bid >> 2) * 4) : 0x80000000u);
  stash<A_VEC + 18>((unsigned)((bid & 7) * 1024 + wv * 256 + lane * 4));
  {
    const char* gp = (const char*)(lane < 32 ? cx.gamma : cx.beta) + (nt * 2 + xb) * 128 + (lane & 31) * 4;
    stash<A_VEC + 20>((unsigned)(uintptr_t)gp);
    stash<A_VEC + 21>((unsigned)((uintptr_t)gp >> 32));
  }
  stash<A_VEC + 22>((unsigned)((b * NBLK + mg * 80 + (lane & 14)) * 65536 + nt * 16384 + g * 16 + (2 * slot + (lane & 1)) * 64));
  {
    unsigned len;
    asm volatile("s_mov_b32 %0, .Lcf_b-.Lcf_t-64" : "=s"(len));
    stash<A_VEC + 23>(min((unsigned)(wv * 64 + lane) * 128u, len));
  }
  stash<A_VEC + 24>(RAWL + pwr);
  stash<A_VEC + 25>((unsigned)(CBUF + (lane & 15) * 256 + wv * 64 + g * 16));
  const unsigned long long gt = (unsigned long long)&TAB.G[0][0][0][0][0];
  unsigned sc[15] = {cx.lds0, (unsigned)(wv & 2) * 4096u, FLG + 2 * 8192 + (bid & 3) * 256, wv == nt ? 0x3ffcu : 0x444u,
                     NQ1 * 2 * SBLK + ((bid + NQ1) & 3) * 16384, wv == 3 ? (bid & 1 ? GPO_ : GPE_) - 14u : 0u, slot ? 0xbf800000u : 0u,
                     0, 0, 0, 0, 0, ((wv ^ idx) & 3) == 0 ? RAWL + 32768 + wv * 256u : 0u, (unsigned)gt, (unsigned)(gt >> 32)};
#pragma unroll
  for (int m = 0; m < 5; ++m) {
    const int xt = mg ? (m + 1) % 5 : m;
    sc[7 + m] = (unsigned)(xt * 16 * 65536 + ((b * 10 + mg * 5 + 2 * nh + xt) & 2) * 4096);
  }
  unsigned sv = 0;
#pragma unroll
  for (int i = 0; i < 15; ++i)
    asm volatile("v_writelane_b32 %0, %1, %2" : "+v"(sv) : "s"(__builtin_amdgcn_readfirstlane((int)sc[i])), "i"(i));
  stash<A_SCA>(sv);
}
__device__ __forceinline__ void phase1(Ctx& cx, const unsigned short* __restrict__ x, const unsigned short* __restrict__ w, Hand& hd) {
  u32x4 rq0;
  static_assert(NQ1 == 5, "phase-1 wait counts assume five quanta");
  char* smem = cx.smem;
  const unsigned lds0 = (unsigned)(uintptr_t)(lds_void*)smem;
  const int idx = cx.vb >> 3, wv = cx.wv, lane = threadIdx.x & 63;
  const u32x4 rw = make_rsrc(w, 31 * D_ * D_ * 2);
  const int tap = threadIdx.x >> 4, pc = threadIdx.x & 15;
  const unsigned roff = (unsigned)(tap * (2 * D_ * D_ * 2) + pc * 16);
  const int ng = wv;
  const int g = lane >> 4, qq = (lane >> 2) & 3, pp = lane & 3;
  u32x4 r1[NQ1];
  bf16x8 fb[2][2];
  unsigned long long tag;
  const char* rec = cx.ws + WS_PUB + ((cx.id & 7) * 8 + wv) * 16;
  u32x4 pr;
  unsigned pwr, prd, pvo, pww, xb0[4], pq[4], xa[2];
  const int kr = (cx.vb >> 7) * 32 + (cx.vb & 4) * 4 + (idx & 15);
  auto raw_so = [&](int q) {
    return (unsigned)(kr + 64 * (2 * (q & 3) + ((q >> 2) & 1)) + 512 * (((q >> 3) ^ q ^ (q >> 1)) & 1)) * 1024u + (unsigned)((cx.vb + q) & 3) * 256u;
  };
  auto raw_load = [&](u32x4& r, int q) {
    asm volatile("buffer_load_dwordx4 %0, %1, %2, %3 offen nt" : "=v"(r) : "v"(roff), "s"(rw), "s"(raw_so(q)) : "memory");
  };
  const unsigned xa1 = x_addr(cx.vb, 1, wv);
  {
    const int vb = cx.vb;
    auto setup = [&]() __attribute__((always_inline)) {
    {
      const int bid = vb, xb = (bid >> 2) & 1, krow = kr;
      const int slot = (bid & 7) * 4 + (idx >> 3), b = (idx >> 2) & 1, nt = idx & 3;
      const int rt = 4 * g + qq;
      pvo = (unsigned)((bid & 7) * 1024 + wv * 256 + lane * 4);
      pwr = (unsigned)((pc >> 3) * 2048 + tap * 128 + ((((pc >> 1) ^ (tap >> 1)) & 3) << 5) + (pc & 1) * 16);
      prd = (unsigned)(rt * 128 + (((rt >> 1) & 3) << 5) + pp * 8);
      cx.poff = (unsigned)(sbase(8 * ng + ((lane >> 1) & 7)) + (krow >> 5) * SBLK + (krow & 31) * 512 + (lane & 1) * 64 + g * 16);
      cx.xo = (unsigned)(sbase(8 * ng + ((lane >> 1) & 7)) + 65536 + xb * 8192 + (lane & 1) * 64 + g * 16);
      cx.lds0 = lds0 + wv * 1024;
#pragma unroll
      for (int k = 0; k < 4; ++k) {
        xb0[k] = (unsigned)((4 * g + qq) * 256 + (((4 * g + qq + 2 * k) & 7) << 5) + pp * 8);
        pq[k] = (prd + RAWB) ^ (unsigned)(k << 5);
      }
      pww = pwr + RAWB;
      asm volatile("" : "+v"(xb0[0]), "+v"(xb0[1]), "+v"(xb0[2]), "+v"(xb0[3]), "+v"(pq[0]), "+v"(pq[1]), "+v"(pq[2]), "+v"(pq[3]), "+v"(pww));
      {
        const int row = 2 * wv + (lane >> 5), p = ((lane & 31) >> 1) ^ (row & 7);
        const int chunk = ((p >> 2) & 1) * 8 + ((p >> 1) & 1) * 4 + ((p >> 3) & 1) * 2 + (p & 1);
        cx.voffw = (unsigned)(sbase(slot) + (nt * 32 + row) * 512 + chunk * 32 + (lane & 1) * 16);
        cx.voffx = (unsigned)(sbase(slot) + 65536 + b * 8192 + (8 * wv + ((lane >> 2) & 7)) * 128 + (lane >> 5) * 64 + (((lane & 3) ^ ((wv & 1) << 1)) << 4));
      }
    }
    asm volatile("" : "+v"(pvo), "+v"(cx.poff), "+v"(cx.xo), "+v"(cx.voffw), "+v"(cx.voffx));
    {
      const unsigned so1 = (unsigned)(16 + 2 * run_q(vb, 1)) * SBLK;
      const int e = lane & 31, n = (lane < 32 ? 20 - NH : 10) + (e >> 1);
      unsigned hst = e < (lane < 32 ? 2 * NH : 8) ? x_place(so1 + (n >= 15 ? 8 * SBLK : 0) + (e & 1) * SBLK, (unsigned)(run_bs(vb, 1) + n % 5)) : 0xf0000000u;
      if ((lane >> 1) == 20 && run_q(vb, 0)) hst = x_place((unsigned)(8 + 2 * run_q(vb, 0) + (e & 1)) * SBLK, (unsigned)(run_bs(vb, 0) + 4));
      loop_setup(cx, pwr, prd, hst);
    }
      };
    if (cx.id | wv) issue_x(0, wv, x_addr(vb, 0, wv), x_rsrc(x, vb), lds0);
    if (cx.id == 0 && wv) {
#pragma unroll
      for (int k = 0; k < 6; ++k)
        if (k < 5 || wv < 3) DMA16("nt", x_rsrc(x, vb), lds0 + (3 * k + wv - 1) * 4096, x_addr(vb, 0, 0), (3 * k + wv - 1) * 32768u);
    }
    SB0;
#pragma unroll
    for (int k = 0; k < NQ1; ++k) raw_load(r1[k], k);
#pragma unroll
    for (int i = 0; i < 4; ++i) ld16(fb[i >> 1][i & 1], TAB.F[ng][i >> 1][i & 1][lane]);
    ld16(hd.vb, TAB.V[0][ng][lane]);
    if (cx.id | wv) asm volatile("global_load_dwordx4 %0, %1, %2 sc1" : "=v"(pr) : "v"(0u), "s"(rec) : "memory");
    SB0;
    setup();
    SB0;
    if ((cx.id | wv) == 0) {
      wait_vm<3 + NQ1 + 5>();
      u32x4 &sx = cx.sx, &sw = cx.sw;
      asm volatile("" : "+v"(sx), "+v"(sw));
      unsigned m = expo(amax(amax(amax(amax(0, sx[0]), sx[1]), sx[2]), sx[3])) | expo(amax(amax(amax(amax(0, sw[0]), sw[1]), sw[2]), sw[3])) << 16;
      m = amax(m, (unsigned)__builtin_amdgcn_update_dpp(0, (int)m, 0x128, 0xf, 0xf, false));
      m = amax(m, (unsigned)__builtin_amdgcn_update_dpp(0, (int)m, 0x124, 0xf, 0xf, false));
      m = amax(m, (unsigned)__builtin_amdgcn_update_dpp(0, (int)m, 0x122, 0xf, 0xf, false));
      m = amax(m, (unsigned)__builtin_amdgcn_update_dpp(0, (int)m, 0x121, 0xf, 0xf, false));
      unsigned ex = 28, ew = 28;
#pragma unroll
      for (int l = 0; l < 64; l += 16) {
        const unsigned v = (unsigned)__builtin_amdgcn_readlane((int)m, l);
        ex = max(ex + 2, v & 255u) - 2;
        ew = max(ew + 3, v >> 16) - 3;
      }
      const unsigned long long tg = __builtin_amdgcn_s_memrealtime();
      const unsigned lo = (unsigned)tg << 4 | cx.xcc << 1, hi = ((unsigned)(tg >> 28) & 0xffffu) | min(ex, 220u) << 16 | min(ew, 220u) << 24;
      asm volatile("global_store_dwordx4 %0, %1, %2 sc1\n\ts_nop 1" :: "v"(threadIdx.x * 16), "v"(u32x4{lo, hi, ~lo, ~hi}), "s"(cx.ws + WS_PUB) : "memory");
      pr = u32x4{lo, hi, ~lo, ~hi};
      wait_vm<NQ1 + 5 + 1>();
      unsigned g[2] = {~0u, ~0u};
#pragma unroll 1
      for (int k = -1; k < PRK_ && ((k & 1) || min(g[0], g[1]) > 440u); ++k) {
        unsigned long long t0, t1;
        unsigned d;
        asm volatile("s_memtime %0\n\ts_waitcnt lgkmcnt(0)\n\ts_load_dword %2, %3, 0x0\n\ts_waitcnt lgkmcnt(0)\n\ts_memtime %1\n\ts_waitcnt lgkmcnt(0)"
                     : "=&s"(t0), "=&s"(t1), "=&s"(d) : "s"(rec + 32768 + (k < 0 ? 128 : (k & 1) * 8192 + (k & 2) * 2048 + (k & 4) * 4096 + (k & 8) * 128)) : "memory");
        if (k >= 0) { if (k & 1) g[1] = min(g[1], (unsigned)(t1 - t0)); else g[0] = min(g[0], (unsigned)(t1 - t0)); }
      }
      asm volatile("global_store_dwordx4 %0, %1, %2 sc1\n\ts_nop 1" :: "v"(lane * 16 + 2048), "v"(u32x4{lo | (((g[0] < g[1] ? 0u : 1u) ^ (cx.vb >> 2)) & 1u), hi, lo | (((g[0] < g[1] ? 0u : 1u) ^ (cx.vb >> 2)) & 1u), hi}), "s"(rec) : "memory");
    }
    issue_x(1, wv, xa1, x_rsrc(x, vb), lds0, 0, TE);
  }
  Rng gx, gw;
  wait_vm<6 + TE>();
#pragma unroll
  for (int q = 0; q < NQ1; ++q) {
    asm volatile("" : "+v"(r1[q]));
    *reinterpret_cast<u32x4*>(smem + (pww + q * 4096)) = r1[q];
  }
  SB0;
  unsigned near;
  {
    wait_vm<TE>();
    unsigned d0, d1;
    for (unsigned spins = 0;; ++spins) {
      asm volatile("" : "+v"(pr));
      d0 = __builtin_amdgcn_readfirstlane(pr[0]);
      d1 = __builtin_amdgcn_readfirstlane(pr[1]);
      const unsigned d2 = __builtin_amdgcn_readfirstlane(pr[2]), d3 = __builtin_amdgcn_readfirstlane(pr[3]);
      if (__builtin_expect((d0 == ~d2 && d1 == ~d3) || spins > (1u << 21), 1)) break;
      __builtin_amdgcn_s_sleep(2);
      asm volatile("global_load_dwordx4 %0, %1, %2 sc1\n\ts_waitcnt vmcnt(0)" : "=v"(pr) : "v"(0u), "s"(rec) : "memory");
    }
    tag = d0 | (unsigned long long)d1 << 32;
    const unsigned vn = (cx.id + (d0 >> 1)) & 7u;
    if (__builtin_expect(vn != (cx.vb & 7u), 0)) restart(cx.id | 256u | vn << 10);
  }
  const unsigned ee = (cx.id & 512u ? cx.id : (unsigned)(tag >> 32)) >> 16, ex = ee & 255u, ew = ee >> 8;
  cx.es = 256 - (int)(ex + ew);
  char* ws;
  const int bid = cx.vb;
  bf16x8& vb = hd.vb;
  u32x4 f0, f1;
  const u32x4* fa;
  u32x2 ov;
  unsigned pollv;
  const int rb[2] = {run_bs(bid, 0), run_bs(bid, 1)};
  asm volatile("" : "+v"(fb[0][0]), "+v"(fb[0][1]), "+v"(fb[1][0]), "+v"(fb[1][1]), "+v"(vb));
  scale8p(vb, 128 - (int)ew);
#pragma unroll
  for (int i = 0; i < 4; ++i) scale8p(fb[i >> 1][i & 1], 128 - (int)ex);
  asm volatile("" : "+v"(fb[0][0]), "+v"(fb[0][1]), "+v"(fb[1][0]), "+v"(fb[1][1]), "+v"(vb));
  asm volatile("s_waitcnt lgkmcnt(0)" ::: "memory");
  __builtin_amdgcn_s_barrier();
  SB0;
  u32x4 hx[2][20], hwl;
  f32x4 pend[2];
  auto rd = [&](int h, int i) __attribute__((always_inline)) {
    const int it = ord(h >> 1), mg = h & 1;
    if (it < 3) return tr_read(smem + (pq[i & 3] + mg * 2048 + (2 * it + (i >> 2)) * 4096));
    const int n = it - 3;
    const int c = 4 * ((n / 5) & 1) + 2 * mg + (i & 1);
    return tr_read(smem + ((c ? xa[i & 1] : xb0[n % 5 & 3]) + n % 5 * (HOP * 256) + (i >> 1) * (16 * 256)));
  };
  auto xaddr = [&](int h) __attribute__((always_inline)) {
    const int n = ord(h >> 1) - 3, mg = h & 1;
    if (h >= NIT || n < 0) return;
    if (h == 26) {
#pragma unroll
      for (int k = 0; k < 4; ++k) xb0[k] += XT1;
      asm volatile("" : "+v"(xb0[0]), "+v"(xb0[1]), "+v"(xb0[2]), "+v"(xb0[3]));
    }
#pragma unroll
    for (int i = 0; i < 2; ++i) {
      const int c = 4 * ((n / 5) & 1) + 2 * mg + i;
      if (c) { xa[i] = xb0[n % 5 & 3] ^ (unsigned)(c << 5); asm volatile("" : "+v"(xa[i])); }
    }
  };
  auto dst = [&](int h) -> u32x4& { const int it = ord(h >> 1); return it < 3 || it == 12 || it == 16 ? hwl : hx[h & 1][it - 3]; };
  auto item = [&](int h, XR& xr, auto side) __attribute__((always_inline)) {
    const XR c = xr;
    const int it = ord(h >> 1), mg = h & 1;
    const int nm = it == 2 ? 4 : 8, nr = h + 1 < NIT && ord((h + 1) >> 1) == 2 ? 4 : 8;
    const int pn = h > 0 && ord((h - 1) >> 1) == 2 ? 4 : 8;
    f32x4 r[8];
    bf16x8 dq[3];
    if (it < 3) { dq[0] = dup(c.t[0]); dq[1] = dup(c.t[1]); asm volatile("s_nop 0"); }
#pragma unroll
    for (int i = 0; i < 8; ++i) {
      if (i >= nm) break;
      const int s2 = i & 1, ks = (i >> 1) & 1;
      if (it < 3) mfma0(r[i], dq[i % 3], vb);
      else if (i < 2) mfma0(r[6 + i], cat(c.t[4 * ks + s2], c.t[4 * ks + 2 + s2]), fb[ks][1]);
      else mfma1(r[6 + s2], cat(c.t[4 * ks + s2], c.t[4 * ks + 2 + s2]), fb[ks][i < 4]);
      SB0;
      if (it < 3 && i + 2 < nm) dq[(i + 2) % 3] = dup(c.t[i + 2]);
      if (h + 1 < NIT && i < 4) {
        const bool nq = ord((h + 1) >> 1) < 3;
#pragma unroll
        for (int u = 0; u < 2; ++u) {
          const int k = 2 * i + u, j = nq ? k : (k & 4) + ((k >> 1) & 1) + (k & 1) * 2;
          if (j < nr) xr.t[j] = rd(h + 1, j);
        }
      }
      if (i == (nm == 4 ? 3 : 4)) xaddr(h + 2);
      if (h > 0 && (nm == 4 ? i >= 2 : i >= 4)) {
        u32x4& d = dst(h - 1);
#pragma unroll
        for (int e = 0; e < 4; ++e)
          if ((nm == 4 ? 2 + (e >> 1) : 4 + e) == i) d[e] = pk_f16(pend[e >> 1][(e & 1) * 2], pend[e >> 1][(e & 1) * 2 + 1]);
        if (i == nm - 1) {
          const int pi = ord((h - 1) >> 1);
          if (pi < 3) stash4(hw_reg((h - 1) & 1, pi == 2 ? 4 : 2 * pi + 1, 1), d);
          else if (pi == 12 || pi == 16) stash4(A_XA + (pi == 12 ? 8 : 0) + ((h - 1) & 1) * 4, d);
          else asm volatile("" : "+v"(d));
        }
      }
      if (it < 3 && i >= 3 && (i & 1)) {
        stash4(hw_reg(mg, 2 * it + ((i - 3) >> 2), ((i - 3) >> 1) & 1), pack8h(r[i - 3], r[i - 2]));
      }
      side(i * (8 / nm));
      if (nm == 4) side(2 * i + 1);
      SB0;
    }
    (void)pn;
    pend[0] = r[it < 3 ? nm - 2 : 6];
    pend[1] = r[it < 3 ? nm - 1 : 7];
  };
  const unsigned so0 = (unsigned)(2 * run_q(bid, 0)) * SBLK, so1 = (unsigned)(16 + 2 * run_q(bid, 1)) * SBLK;
  auto store = [&](int J) __attribute__((always_inline)) {
    const int j = J >> 1, mg = J & 1;
    if (J == 0) {
      wait_vm<0>();
      for (unsigned spins = 0;; ++spins) {
        asm volatile("" : "+v"(ov));
        near = __builtin_amdgcn_readfirstlane(ov[0]) ^ (unsigned)tag;
        if (__builtin_expect((near < 2u && __builtin_amdgcn_readfirstlane(ov[1]) == (unsigned)(tag >> 32)) || spins > (1u << 21), 1)) break;
        __builtin_amdgcn_s_sleep(2);
        asm volatile("global_load_dwordx2 %0, %1, %2 sc1\n\ts_waitcnt vmcnt(0)" : "=v"(ov) : "v"(2048u), "s"(rec) : "memory");
      }
      ws = cx.ws += (near & 1u) * 8192;
      cx.rs = make_rsrc(ws + WS_S, WS_L - WS_S + D_ * D_ * 2);
    }
    if (J == 1) {
      if (wv == 1) {
        const unsigned o = (lane < 32 ? WS_STATS : WS_S + FLG + 16384 - 512) + bid * 512 + lane * 16;
        const unsigned v = lane < 32 ? ~0u : 0u;
        asm volatile("global_store_dwordx4 %0, %1, %2 sc1\n\ts_nop 1" :: "v"(o), "v"(u32x4{v, v, v, v}), "s"(ws) : "memory");
        if (lane < 8) __hip_atomic_store((gu32*)(ws + WS_S + FLG + lane * 1024 + ((bid + 2) & 3) * 256 + (bid >> 2) * 4), 0u, RLX_AGENT);
      }
    }
    if (J == NB + NFA) {
      if (((bid >> 1) ^ (bid >> 3) ^ 1) & 1 && threadIdx.x < 160) {
        const int t = threadIdx.x >= 80, blk = (threadIdx.x >> 4) - 5 * t;
        const unsigned o = (unsigned)(t * XT1 + blk * (HOP * 256) + (((lane & 15) ^ ((blk & 3) << 2)) << 4));
        const u32x4 lo = *reinterpret_cast<const u32x4*>(smem + o), hi = *reinterpret_cast<const u32x4*>(smem + o + 64 * 256);
        u32x4 d;
#pragma unroll
        for (int e = 0; e < 4; ++e) d[e] = pk_bf16(bf_lo(hi[e]) - bf_lo(lo[e]), bf_hi(hi[e]) - bf_hi(lo[e]));
        st16(d, cx.rs, (unsigned)((((bid >> 2) & 1) * NBLK + (t ? rb[1] : rb[0]) + blk) * 1024 + run_q(bid, t) * 256 + (lane & 15) * 16), WS_D - WS_S);
      }
      asm volatile("buffer_store_dwordx4 a[%c3:%c4], %0, %1, %2 offen sc1" :: "v"(wv >= 2 ? (threadIdx.x & 127) * 16 : 0x80000000u), "s"(cx.rs), "s"(WS_L - WS_S + bid * 2048), "i"(A_WL), "i"(A_WL + 3) : "memory");
    }
    const int wq = st_w(j), xn = st_x(j);
    if (wq >= 0)
      asm volatile("buffer_store_dwordx4 a[%c3:%c4], %0, %1, %2 offen sc1" :: "v"(cx.poff), "s"(cx.rs),
                   "s"((unsigned)(wq >> 1) * (2 * SBLK) + (unsigned)((bid + (wq >> 1)) & 3) * 16384u + (wq & 1) * 128u + mg * 256u),
                   "i"(hw_reg(mg, wq >> 1, wq & 1)), "i"(hw_reg(mg, wq >> 1, wq & 1) + 3) : "memory");
    else if (xn == 9)
      asm volatile("buffer_store_dwordx4 a[%c3:%c4], %0, %1, %2 offen sc1" :: "v"(cx.xo), "s"(cx.rs),
                   "s"(run_q(bid, 0) ? 0xf0000000u : x_place(so0 + 8 * SBLK + mg * SBLK, (unsigned)(rb[0] + 4))), "i"(A_XA + 8 + mg * 4), "i"(A_XA + 11 + mg * 4) : "memory");
    else st16(hx[mg][xn], cx.rs, cx.xo, x_place((xn < 10 ? so0 : so1) + ((xn / 5) & 1) * (8 * SBLK) + mg * SBLK, (unsigned)(rb[xn / 10] + xn % 5)));
    if (J == NA + NFA - 1) {
      const unsigned mx = amax(gx), mw = amax(gw);
      if (__builtin_expect(__any(expo(mx) > ex + 7 || expo(mw) > ew + 9) && !(cx.id & 512u), 0)) {
        const u32x2 m = {wave_max(expo(mx)), wave_max(expo(mw))};
        asm volatile("global_atomic_umax %0, %1, %3 sc1\n\tglobal_atomic_umax %0, %2, %3 offset:4 sc1\n\ts_waitcnt vmcnt(0)"
                     :: "v"(lane * 16 + 1024), "v"(m[0]), "v"(m[1]), "s"(rec - ((cx.id & 7) * 8 + wv) * 16) : "memory");
      }
    }
    if (J == NA + NFA - 1 || J == NB + NFA - 1) {
      if (J < NB) {
      } else {
      }
      wait_vm<NFA>();
      if (J < NB) {
      } else {
      }
      __builtin_amdgcn_s_barrier();
      if (J < NB) {
      } else {
      }
      if (wv == 0 && lane < 8) {
        if (J < NB) __hip_atomic_store((gu64*)(ws + WS_FA + (cx.id & 512u) * 32 + lane * 2048) + bid, tag, RLX_AGENT);
        else __hip_atomic_store((gu32*)(ws + WS_S + FLG + lane * 1024 + ((bid + 2) & 3) * 256 + (bid >> 2) * 4), 1u, RLX_AGENT);
      }
    }
    SB0;
  };
  u32x4 sv[3];
  auto check = [&](auto hc, int s) __attribute__((always_inline)) {
    constexpr int h = hc.v;
    if constexpr (sc_wait(h) >= 0) { if (s == 0) wait_vm<sc_wait(h) < 0 ? 0 : sc_wait(h)>(); }
#pragma unroll
    for (int k = 0; k < 34; ++k) {
      if (sc_pos(k) != h) continue;
      int o = 0;
#pragma unroll
      for (int k2 = 0; k2 < k; ++k2) o += sc_pos(k2) == h;
      const int t = k / 17, j = k % 17;
      if (s == o) sv[o] = *reinterpret_cast<const u32x4*>(smem + (t * XT1 + (j < 16 || wv < 3 ? j : 15) * 4096 + threadIdx.x * 16));
      if (s == 5 + o) {
        widen(gx, sv[o][0], sv[o][1]);
        widen(gx, sv[o][2], sv[o][3]);
      }
    }
    if (s == 4)
      sfor<0, 16>([&](auto k) __attribute__((always_inline)) {
        if (sc_raw(k.v) != h) return;
        u32x4 r;
        if constexpr (k.v <= NQ1) {
          u32x4& q = k.v < NQ1 ? r1[k.v < NQ1 ? k.v : 0] : rq0;
          pin(q);
          r = q;
        } else {
          constexpr int a = A_RAW + 4 * (k.v - NQ1 - 1);
          r = u32x4{unstash<a>(), unstash<a + 1>(), unstash<a + 2>(), unstash<a + 3>()};
        }
        widen(gw, r[0], r[1]);
        widen(gw, r[2], r[3]);
      });
    if (s == 1 && h <= P0 + NBP) asm volatile("" : "+v"(gx.hi), "+v"(gx.lo), "+v"(gw.hi), "+v"(gw.lo));
  };
  auto loads = [&](auto p, int s) __attribute__((always_inline)) {
    sfor<0, NLD>([&](auto l) __attribute__((always_inline)) {
      if (ld_pos(l.v) != p.v || ld_slot(l.v) != s) return;
      constexpr int n = l.v - (17 - TE);
      if constexpr (n < 0) {
        issue_x(1, wv, xa1, x_rsrc(x, bid), lds0, TE + l.v, TE + l.v + 1);
      } else if constexpr (n == 0) {
        load_acc<A_WL>(wv >= 2 ? (threadIdx.x & 127) * 16 : 0x80000000u, rw, 30 * D_ * D_ * 2 + bid * 2048);
      } else if constexpr (n == 1) {
        raw_load(rq0, NQ1);
      } else if constexpr (n <= NQW) {
        load_acc<A_RAW + 4 * (n - 2)>(roff, rw, raw_so(NQ1 + n - 1));
      } else {
        ld8(ov, rec, 2048u);
      }
    });
  };
  XR xr;
#pragma unroll
  for (int j = 0; j < 8; ++j) xr.t[j] = rd(0, j);
  static_assert(ord(0) < 3 && ord(1) >= 3 && ord(0) != 2, "positions 0, 1: weights");
  SB0;
  static_assert(st_first(NIT) == NS && PG >= 28 && st_first(PG - 4) > NB + NFA && NA + NFA <= NB && NFA >= 1 && NH <= 7, "order of the hand-off steps");
  sfor<0, NIT>([&](auto p) __attribute__((always_inline)) {
    constexpr int j0 = st_first(p.v), j1 = st_first(p.v + 1);
    if (SLP_ && p >= 24 && p < PG && !(p & 1)) { if (!(bid & 1) && cx.id) __builtin_amdgcn_s_sleep(SLP_); }
    if (p == XBAR) __builtin_amdgcn_s_barrier();
    if (p == PG - 4) {
      code_touch(cx);
      fa = reinterpret_cast<const u32x4*>(ws + WS_FA + (cx.id & 512u) * 32 + (bid & 7) * 2048) + lane;
      if (wv == 0)
        asm volatile("global_load_dwordx4 %0, %2, off sc1\n\tglobal_load_dwordx4 %1, %2, off offset:1024 sc1" : "=&v"(f0), "=&v"(f1) : "v"(fa) : "memory");
    }
    if (p == PG) {
      if (wv == 0) {
        wait_vm<st_first(PG) - st_first(PG - 4)>();
        for (unsigned spins = 0; spins < (1u << 21); ++spins) {
          asm volatile("" : "+v"(f0), "+v"(f1));
          const unsigned t0 = (unsigned)tag, t1 = (unsigned)(tag >> 32);
          if (__all(f0[0] == t0 && f0[1] == t1 && f0[2] == t0 && f0[3] == t1 && f1[0] == t0 && f1[1] == t1 && f1[2] == t0 && f1[3] == t1)) break;
          __builtin_amdgcn_s_sleep(2);
          asm volatile("global_load_dwordx4 %0, %2, off sc1\n\tglobal_load_dwordx4 %1, %2, off offset:1024 sc1\n\ts_waitcnt vmcnt(0)"
                       : "=&v"(f0), "=&v"(f1) : "v"(fa) : "memory");
        }
      }
      __builtin_amdgcn_s_barrier();
      stage_issue(cx, -1);
      asm volatile("global_load_dwordx2 %0, %1, %2 sc1" : "=v"(ov) : "v"(1024u), "s"(rec) : "memory");
      stage_issue(cx, 0);
      SB0;
    }
    if (p == NIT - 2)
      asm volatile("buffer_load_dword %0, %1, %2, %3 offen sc1" : "=v"(pollv) : "v"(pvo), "s"(cx.rs), "s"(FLG) : "memory");
    item(p, xr, [&](int i) __attribute__((always_inline)) {
      if ((i * (j1 - j0) + 7) / 8 < ((i + 1) * (j1 - j0) + 7) / 8) store(j0 + (i * (j1 - j0) + 7) / 8);
      check(p, i);
      loads(p, i);
    });
  });
  hx[1][19] = pack8h(pend[0], pend[1]);
#pragma unroll
  for (int e = 0; e < 2 * NH; ++e) hd.held[e] = hx[e & 1][20 - NH + (e >> 1)];
#pragma unroll
  for (int e = 0; e < 2 * NST; ++e) hd.st[e] = hx[e & 1][10 + (e >> 1)];
  *reinterpret_cast<u32x4*>(smem + RAW0 + pwr) = rq0;
  wait_vm<NS - st_first(NIT - 2)>();
  {
    asm volatile("" : "+v"(ov));
    const unsigned ox = __builtin_amdgcn_readfirstlane(ov[0]), ow = __builtin_amdgcn_readfirstlane(ov[1]);
    if (__builtin_expect((ox | ow) && !(cx.id & 512u), 0))
      restart((cx.id & 255u) | 768u | (bid & 7u) << 10 | min(max(ex + 7, ox) - 7, 220u) << 16 | min(max(ew + 9, ow) - 9, 220u) << 24);
    if ((cx.id & 255u) == 0 && wv == 0)
      asm volatile("global_store_dwordx4 %0, %1, %2 sc1\n\ts_nop 1" :: "v"(lane * 16), "v"(u32x4{0u, 0u, 0u, 0u}), "s"(rec) : "memory");
  }
  asm volatile("" : "+v"(pollv));
  if (!__all(pollv != 0u)) spin_flags(ws + WS_S + FLG + pvo);
  asm volatile("s_waitcnt lgkmcnt(0)" ::: "memory");
  __builtin_amdgcn_s_barrier();
  SB0;
}

__device__ __forceinline__ void run12(Ctx& cx, const unsigned short* __restrict__ x, const unsigned short* __restrict__ w) {
  Hand hd;
  phase1(cx, x, w, hd);
  u32x16 h1, st0;
  u32x8 st1;
  u32x32 h0;
#pragma unroll
  for (int i = 0; i < 32; ++i) {
    h0[i] = hd.held[i >> 2][i & 3];
    if (i < 16) { h1[i] = hd.held[8 + (i >> 2)][i & 3]; st0[i] = hd.st[i >> 2][i & 3]; }
    if (i < 8) st1[i] = hd.st[4 + (i >> 2)][i & 3];
  }
  u32x16 o0, o4;
  u32x8 o1;
  u32x4 o5;
  u32x32 o3;
  asm volatile(
  "v_accvgpr_read_b32 v228, a188\n"
  "v_accvgpr_read_b32 v229, a189\n"
  "v_accvgpr_read_b32 v230, a190\n"
  "v_accvgpr_read_b32 v231, a191\n"
  "v_accvgpr_read_b32 v232, a192\n"
  "ds_read_b64_tr_b16 v[0:1], v228 offset:0\n"
  "ds_read_b64_tr_b16 v[2:3], v228 offset:8192\n"
  "ds_read_b64_tr_b16 v[16:17], v228 offset:256\n"
  "ds_read_b64_tr_b16 v[18:19], v228 offset:8448\n"
  "ds_read_b64_tr_b16 v[4:5], v229 offset:0\n"
  "ds_read_b64_tr_b16 v[6:7], v229 offset:8192\n"
  "ds_read_b64_tr_b16 v[20:21], v229 offset:256\n"
  "ds_read_b64_tr_b16 v[22:23], v229 offset:8448\n"
  "ds_read_b64_tr_b16 v[8:9], v230 offset:0\n"
  "ds_read_b64_tr_b16 v[10:11], v230 offset:8192\n"
  "ds_read_b64_tr_b16 v[24:25], v230 offset:256\n"
  "ds_read_b64_tr_b16 v[26:27], v230 offset:8448\n"
  "ds_read_b64_tr_b16 v[12:13], v231 offset:0\n"
  "ds_read_b64_tr_b16 v[14:15], v231 offset:8192\n"
  "ds_read_b64_tr_b16 v[28:29], v231 offset:256\n"
  "ds_read_b64_tr_b16 v[30:31], v231 offset:8448\n"
  "ds_read_b128 v[96:99], v232 offset:0\n"
  "ds_read_b128 v[100:103], v232 offset:512\n"
  "ds_read_b128 v[104:107], v232 offset:2048\n"
  "ds_read_b128 v[108:111], v232 offset:2560\n"
  "v_accvgpr_read_b32 v255, a214\n"
  "v_accvgpr_read_b32 v237, a197\n"
  "v_accvgpr_read_b32 v238, a198\n"
  "v_readlane_b32 s44, v255, 0\n"
  "v_readlane_b32 s45, v255, 1\n"
  "v_readlane_b32 s62, v255, 5\n"
  "v_accvgpr_read_b32 v233, a193\n"
  "v_accvgpr_read_b32 v234, a194\n"
  "v_accvgpr_read_b32 v235, a195\n"
  "v_accvgpr_read_b32 v236, a196\n"
  "v_accvgpr_read_b32 v239, a199\n"
  "v_accvgpr_read_b32 v240, a200\n"
  "v_accvgpr_read_b32 v241, a201\n"
  "v_accvgpr_read_b32 v242, a202\n"
  "v_accvgpr_read_b32 v243, a203\n"
  "v_accvgpr_read_b32 v244, a204\n"
  "v_accvgpr_read_b32 v245, a205\n"
  "v_accvgpr_read_b32 v246, a206\n"
  "v_accvgpr_read_b32 v248, a208\n"
  "v_accvgpr_read_b32 v249, a209\n"
  "v_accvgpr_read_b32 v250, a210\n"
  "v_accvgpr_read_b32 v251, a211\n"
  "v_accvgpr_read_b32 v252, a212\n"
  "v_accvgpr_read_b32 v253, a213\n"
  "s_add_i32 s67, s44, s45\n"
  "s_add_i32 m0, s44, 73728\n"
  "s_mov_b32 s64, 8388608\n"
  "buffer_load_dwordx4 v237, s[40:43], s64 offen sc1 lds\n"
  "s_add_i32 m0, s44, 77824\n"
  "s_mov_b32 s64, 8392704\n"
  "buffer_load_dwordx4 v237, s[40:43], s64 offen sc1 lds\n"
  "s_add_i32 m0, s44, 81920\n"
  "s_mov_b32 s64, 8396800\n"
  "buffer_load_dwordx4 v237, s[40:43], s64 offen sc1 lds\n"
  "s_add_i32 m0, s44, 86016\n"
  "s_mov_b32 s64, 8400896\n"
  "buffer_load_dwordx4 v237, s[40:43], s64 offen sc1 lds\n"
  "s_add_i32 m0, s44, 90112\n"
  "s_mov_b32 s64, 8388608\n"
  "buffer_load_dwordx4 v238, s[40:43], s64 offen sc1 lds\n"
  "s_add_i32 m0, s44, 94208\n"
  "s_mov_b32 s64, 4198400\n"
  "buffer_load_dwordx4 v238, s[40:43], s64 offen sc1 lds\n"
  "s_add_i32 m0, s67, 98304\n"
  "s_mov_b32 s64, 4210688\n"
  "buffer_load_dwordx4 v238, s[40:43], s64 offen sc1 lds\n"
  "s_add_i32 m0, s44, 100352\n"
  "s_mov_b32 s64, 8409088\n"
  "buffer_load_dwordx4 v238, s[40:43], s64 offen sc1 lds\n"
  "s_add_i32 m0, s44, 104448\n"
  "s_mov_b32 s64, 4227072\n"
  "buffer_load_dwordx4 v238, s[40:43], s64 offen sc1 lds\n"
  "s_memrealtime s[70:71]\n"
  "v_readlane_b32 s51, v255, 2\n"
  "v_readlane_b32 s52, v255, 3\n"
  "v_readlane_b32 s55, v255, 4\n"
  "v_readlane_b32 s56, v255, 4\n"
  "v_readlane_b32 s76, v255, 6\n"
  "v_readlane_b32 s77, v255, 6\n"
  "v_readlane_b32 s80, v255, 7\n"
  "v_readlane_b32 s81, v255, 8\n"
  "v_readlane_b32 s82, v255, 9\n"
  "v_readlane_b32 s83, v255, 10\n"
  "v_readlane_b32 s84, v255, 11\n"
  "v_readlane_b32 s85, v255, 12\n"
  "v_readlane_b32 s88, v255, 13\n"
  "v_readlane_b32 s89, v255, 14\n"
  "v_mov_b32 v172, 0\n"
  "v_mov_b32 v173, 0\n"
  "v_mov_b32 v174, 0\n"
  "v_mov_b32 v175, 0\n"
  "v_mov_b32 v247, 1\n"
  "s_mov_b32 s46, 36864\n"
  "s_mov_b32 s47, s44\n"
  "s_mov_b32 s48, s67\n"
  "s_mov_b32 s49, 36864\n"
  "s_mov_b32 s50, 8388608\n"
  "s_mov_b32 s53, 134217728\n"
  "s_mov_b32 s54, 134217728\n"
  "s_mov_b32 s57, -131072\n"
  "s_mov_b32 s58, 0xf0000000\n"
  "s_mov_b32 s59, 0xf0000000\n"
  "s_mov_b32 s60, -1\n"
  "s_mov_b32 s68, -73728\n"
  "s_mov_b32 s69, 36864\n"
  "s_add_u32 s72, s40, 41943040\n"
  "s_addc_u32 s73, s41, 0\n"
  "s_mov_b32 s74, 20971520\n"
  "s_mov_b32 s75, 0x20000\n"
  "s_waitcnt lgkmcnt(0)\n"
  "s_add_i32 s61, s70, s62\n"
  "s_add_i32 s61, s61, 14\n"
  "v_pk_add_f16 v32, v0, v16\n"
  "v_pk_add_f16 v33, v1, v17\n"
  "v_pk_add_f16 v34, v2, v18\n"
  "v_pk_add_f16 v35, v3, v19\n"
  "v_pk_add_f16 v36, v4, v20\n"
  "v_pk_add_f16 v37, v5, v21\n"
  "v_pk_add_f16 v38, v6, v22\n"
  "v_pk_add_f16 v39, v7, v23\n"
  "v_pk_add_f16 v40, v8, v24\n"
  "v_pk_add_f16 v41, v9, v25\n"
  "v_pk_add_f16 v42, v10, v26\n"
  "v_pk_add_f16 v43, v11, v27\n"
  "v_pk_add_f16 v44, v12, v28\n"
  "v_pk_add_f16 v45, v13, v29\n"
  "v_pk_add_f16 v46, v14, v30\n"
  "v_pk_add_f16 v47, v15, v31\n"
  "v_add_u32 v228, 36864, v228\n"
  "v_add_u32 v229, 36864, v229\n"
  "v_add_u32 v230, 36864, v230\n"
  "v_add_u32 v231, 36864, v231\n"
  "v_add_u32 v232, 36864, v232\n"
  "s_barrier\n"
  "v_mfma_f32_16x16x32_f16 a[0:3], v[0:3], v[96:99], 0\n"
  "v_readlane_b32 s65, v244, 32\n"
  "v_pk_add_f16 v120, v96, v100\n"
  "v_mfma_f32_16x16x32_f16 a[12:15], v[4:7], v[96:99], 0\n"
  "v_pk_add_f16 v121, v97, v101\n"
  "v_pk_add_f16 v122, v98, v102\n"
  "v_mfma_f32_16x16x32_f16 a[24:27], v[8:11], v[96:99], 0\n"
  "s_add_i32 m0, s47, 0\n"
  "ds_read_b64_tr_b16 v[48:49], v228 offset:0\n"
  "v_pk_add_f16 v123, v99, v103\n"
  "v_mfma_f32_16x16x32_f16 a[36:39], v[12:15], v[96:99], 0\n"
  "s_add_i32 s64, s50, 4194304\n"
  "buffer_load_dwordx4 v237, s[40:43], s64 offen sc1 lds\n"
  "v_readlane_b32 s66, v244, 33\n"
  "v_mfma_f32_16x16x32_f16 a[4:7], v[16:19], v[100:103], 0\n"
  "s_cmp_eq_u32 s46, 73728\n"
  "s_cselect_b32 s49, s68, s69\n"
  "ds_read_b64_tr_b16 v[124:125], v233 offset:0\n"
  "v_mfma_f32_16x16x32_f16 a[16:19], v[20:23], v[100:103], 0\n"
  "ds_read_b64_tr_b16 v[128:129], v234 offset:0\n"
  "ds_read_b64_tr_b16 v[50:51], v228 offset:8192\n"
  "v_mfma_f32_16x16x32_f16 a[28:31], v[24:27], v[100:103], 0\n"
  "buffer_store_dwordx4 v[80:83], v243, s[40:43], s65 offen sc1\n"
  "ds_read_b128 v[96:99], v232 offset:4096\n"
  "v_mfma_f32_16x16x32_f16 a[40:43], v[28:31], v[100:103], 0\n"
  "ds_read_b64_tr_b16 v[132:133], v235 offset:0\n"
  "v_readlane_b32 s65, v244, 34\n"
  "v_mfma_f32_16x16x32_f16 a[8:11], v[32:35], v[120:123], 0\n"
  "s_add_i32 m0, s47, 4096\n"
  "ds_read_b64_tr_b16 v[64:65], v228 offset:256\n"
  "ds_read_b128 v[100:103], v232 offset:4608\n"
  "v_mfma_f32_16x16x32_f16 a[20:23], v[36:39], v[120:123], 0\n"
  "s_add_i32 s64, s50, 4198400\n"
  "buffer_load_dwordx4 v237, s[40:43], s64 offen sc1 lds\n"
  "ds_read_b64_tr_b16 v[136:137], v236 offset:0\n"
  "v_mfma_f32_16x16x32_f16 a[32:35], v[40:43], v[120:123], 0\n"
  "ds_read_b64_tr_b16 v[66:67], v228 offset:8448\n"
  "v_add_u32 v228, s49, v228\n"
  "v_mfma_f32_16x16x32_f16 a[44:47], v[44:47], v[120:123], 0\n"
  "ds_read_b64_tr_b16 v[52:53], v229 offset:0\n"
  "ds_read_b64_tr_b16 v[54:55], v229 offset:8192\n"
  "v_mfma_f32_16x16x32_f16 a[48:51], v[0:3], v[104:107], 0\n"
  "buffer_store_dwordx4 v[84:87], v243, s[40:43], s66 offen sc1\n"
  "v_pk_add_f16 v120, v104, v108\n"
  "v_mfma_f32_16x16x32_f16 a[60:63], v[4:7], v[104:107], 0\n"
  "v_pk_add_f16 v121, v105, v109\n"
  "v_pk_add_f16 v122, v106, v110\n"
  "v_mfma_f32_16x16x32_f16 a[72:75], v[8:11], v[104:107], 0\n"
  "s_add_i32 m0, s47, 8192\n"
  "ds_read_b64_tr_b16 v[68:69], v229 offset:256\n"
  "v_pk_add_f16 v123, v107, v111\n"
  "v_mfma_f32_16x16x32_f16 a[84:87], v[12:15], v[104:107], 0\n"
  "s_add_i32 s64, s50, 4202496\n"
  "buffer_load_dwordx4 v237, s[40:43], s64 offen sc1 lds\n"
  "v_readlane_b32 s66, v244, 35\n"
  "v_mfma_f32_16x16x32_f16 a[52:55], v[16:19], v[108:111], 0\n"
  "s_waitcnt lgkmcnt(4)\n"
  "ds_read_b64_tr_b16 v[70:71], v229 offset:8448\n"
  "v_mov_b64 v[126:127], v[124:125]\n"
  "v_mfma_f32_16x16x32_f16 a[64:67], v[20:23], v[108:111], 0\n"
  "s_waitcnt lgkmcnt(4)\n"
  "ds_read_b128 v[104:107], v232 offset:6144\n"
  "v_mov_b64 v[130:131], v[128:129]\n"
  "v_mfma_f32_16x16x32_f16 a[76:79], v[24:27], v[108:111], 0\n"
  "buffer_store_dwordx4 v[88:91], v243, s[40:43], s65 offen sc1\n"
  "v_mov_b64 v[134:135], v[132:133]\n"
  "v_mfma_f32_16x16x32_f16 a[88:91], v[28:31], v[108:111], 0\n"
  "ds_read_b64_tr_b16 v[56:57], v230 offset:0\n"
  "v_readlane_b32 s65, v244, 36\n"
  "v_mfma_f32_16x16x32_f16 a[56:59], v[32:35], v[120:123], 0\n"
  "s_add_i32 m0, s47, 12288\n"
  "ds_read_b128 v[108:111], v232 offset:6656\n"
  "v_mov_b64 v[138:139], v[136:137]\n"
  "v_mfma_f32_16x16x32_f16 a[68:71], v[36:39], v[120:123], 0\n"
  "s_add_i32 s64, s50, 4206592\n"
  "buffer_load_dwordx4 v237, s[40:43], s64 offen sc1 lds\n"
  "v_mfma_f32_16x16x32_f16 a[80:83], v[40:43], v[120:123], 0\n"
  "ds_read_b64_tr_b16 v[58:59], v230 offset:8192\n"
  "v_pk_add_f16 v80, v48, v64\n"
  "v_mfma_f32_16x16x32_f16 a[92:95], v[44:47], v[120:123], 0\n"
  "s_waitcnt lgkmcnt(4)\n"
  "ds_read_b64_tr_b16 v[72:73], v230 offset:256\n"
  "v_pk_add_f16 v81, v49, v65\n"
  "v_mfma_f32_16x16x32_f16 a[96:99], v[0:3], v[96:99], 0\n"
  "buffer_store_dwordx4 v[92:95], v243, s[40:43], s66 offen sc1\n"
  "v_pk_add_f16 v120, v96, v100\n"
  "v_mfma_f32_16x16x32_bf16 v[140:143], v[124:127], v[176:179], 0\n"
  "ds_read_b64_tr_b16 v[74:75], v230 offset:8448\n"
  "v_pk_add_f16 v121, v97, v101\n"
  "v_mfma_f32_16x16x32_f16 a[108:111], v[4:7], v[96:99], 0\n"
  "s_add_i32 m0, s47, 16384\n"
  "v_pk_add_f16 v122, v98, v102\n"
  "v_pk_add_f16 v123, v99, v103\n"
  "v_mfma_f32_16x16x32_f16 a[120:123], v[8:11], v[96:99], 0\n"
  "s_add_i32 s64, s50, 4194304\n"
  "buffer_load_dwordx4 v238, s[40:43], s64 offen sc1 lds\n"
  "v_readlane_b32 s66, v244, 37\n"
  "v_mfma_f32_16x16x32_f16 a[132:135], v[12:15], v[96:99], 0\n"
  "ds_read_b64_tr_b16 v[60:61], v231 offset:0\n"
  "v_cvt_pkrtz_f16_f32 v124, v140, v141\n"
  "v_mfma_f32_16x16x32_bf16 v[144:147], v[128:131], v[176:179], 0\n"
  "ds_read_b64_tr_b16 v[62:63], v231 offset:8192\n"
  "v_cvt_pkrtz_f16_f32 v125, v142, v143\n"
  "v_mfma_f32_16x16x32_f16 a[100:103], v[16:19], v[100:103], 0\n"
  "buffer_store_dwordx4 v[156:159], v243, s[40:43], s65 offen sc1\n"
  "v_and_b32 v124, 0xfff8fff8, v124\n"
  "v_mfma_f32_16x16x32_f16 a[112:115], v[20:23], v[100:103], 0\n"
  "ds_read_b128 v[96:99], v232 offset:0\n"
  "v_and_b32 v125, 0xfff8fff8, v125\n"
  "v_mfma_f32_16x16x32_f16 a[124:127], v[24:27], v[100:103], 0\n"
  "s_add_i32 m0, s47, 20480\n"
  "ds_read_b128 v[112:115], v232 offset:8192\n"
  "v_cvt_pkrtz_f16_f32 v126, v144, v145\n"
  "v_mfma_f32_16x16x32_bf16 v[148:151], v[132:135], v[176:179], 0\n"
  "s_add_i32 s64, s50, 4096\n"
  "buffer_load_dwordx4 v238, s[40:43], s64 offen sc1 lds\n"
  "v_cvt_pkrtz_f16_f32 v127, v146, v147\n"
  "v_mfma_f32_16x16x32_f16 a[136:139], v[28:31], v[100:103], 0\n"
  "s_waitcnt lgkmcnt(4)\n"
  "ds_write_b128 v252, a[144:147] offset:4096\n"
  "v_and_b32 v126, 0xfff8fff8, v126\n"
  "v_mfma_f32_16x16x32_f16 a[104:107], v[32:35], v[120:123], 0\n"
  "ds_read_b64_tr_b16 v[76:77], v231 offset:256\n"
  "v_and_b32 v127, 0xfff8fff8, v127\n"
  "v_mfma_f32_16x16x32_f16 a[116:119], v[36:39], v[120:123], 0\n"
  "buffer_store_dwordx4 v[160:163], v243, s[40:43], s66 offen sc1\n"
  "v_cvt_pkrtz_f16_f32 v128, v148, v149\n"
  "v_mfma_f32_16x16x32_bf16 v[152:155], v[136:139], v[176:179], 0\n"
  "ds_write_b128 v252, a[156:159] offset:16384\n"
  "v_cvt_pkrtz_f16_f32 v129, v150, v151\n"
  "v_mfma_f32_16x16x32_f16 a[128:131], v[40:43], v[120:123], 0\n"
  "s_add_i32 m0, s48, 24576\n"
  "ds_read_b128 v[100:103], v232 offset:512\n"
  "v_and_b32 v128, 0xfff8fff8, v128\n"
  "v_mfma_f32_16x16x32_f16 a[140:143], v[44:47], v[120:123], 0\n"
  "s_add_i32 s64, s50, 16384\n"
  "buffer_load_dwordx4 v238, s[40:43], s64 offen sc1 lds\n"
  "v_and_b32 v129, 0xfff8fff8, v129\n"
  "v_mfma_f32_16x16x32_f16 a[144:147], v[0:3], v[104:107], 0\n"
  "ds_read_b128 v[116:119], v232 offset:8704\n"
  "v_pk_add_f16 v120, v104, v108\n"
  "v_mfma_f32_16x16x32_f16 a[156:159], v[4:7], v[104:107], 0\n"
  "ds_write_b128 v252, a[168:171] offset:28672\n"
  "v_pk_add_f16 v121, v105, v109\n"
  "v_mfma_f32_16x16x32_f16 a[168:171], v[8:11], v[104:107], 0\n"
  "buffer_store_dwordx4 v[124:127], v242, s[40:43], s56 offen offset:0 sc1\n"
  "ds_read_b64_tr_b16 v[78:79], v231 offset:8448\n"
  "ds_write_b128 v252, a[180:183] offset:40960\n"
  "v_mfma_f32_16x16x32_f16 a[180:183], v[12:15], v[104:107], 0\n"
  "ds_write_b128 v252, a[148:151] offset:8192\n"
  "v_pk_add_f16 v122, v106, v110\n"
  "v_mfma_f32_16x16x32_f16 a[148:151], v[16:19], v[108:111], 0\n"
  "s_add_i32 m0, s47, 26624\n"
  "ds_write_b128 v252, a[160:163] offset:20480\n"
  "v_pk_add_f16 v123, v107, v111\n"
  "v_mfma_f32_16x16x32_f16 a[160:163], v[20:23], v[108:111], 0\n"
  "s_add_i32 s64, s50, 4214784\n"
  "buffer_load_dwordx4 v238, s[40:43], s64 offen sc1 lds\n"
  "ds_write_b128 v252, a[172:175] offset:32768\n"
  "v_mfma_f32_16x16x32_f16 a[172:175], v[24:27], v[108:111], 0\n"
  "ds_write_b128 v252, a[152:155] offset:12288\n"
  "v_cvt_pkrtz_f16_f32 v130, v152, v153\n"
  "v_mfma_f32_16x16x32_f16 a[184:187], v[28:31], v[108:111], 0\n"
  "s_add_i32 m0, s47, 30720\n"
  "ds_write_b128 v252, a[164:167] offset:24576\n"
  "v_cvt_pkrtz_f16_f32 v131, v154, v155\n"
  "v_mfma_f32_16x16x32_f16 a[152:155], v[32:35], v[120:123], 0\n"
  "s_add_i32 s64, s50, 32768\n"
  "buffer_load_dwordx4 v238, s[40:43], s64 offen sc1 lds\n"
  "ds_write_b128 v252, a[176:179] offset:36864\n"
  "v_mfma_f32_16x16x32_f16 a[164:167], v[36:39], v[120:123], 0\n"
  "s_waitcnt lgkmcnt(9)\n"
  "v_and_b32 v130, 0xfff8fff8, v130\n"
  "v_and_b32 v131, 0xfff8fff8, v131\n"
  "v_mfma_f32_16x16x32_f16 a[176:179], v[40:43], v[120:123], 0\n"
  "s_add_i32 s47, s44, s46\n"
  "s_waitcnt lgkmcnt(7)\n"
  "ds_read_b128 v[104:107], v232 offset:2048\n"
  "v_mfma_f32_16x16x32_f16 a[188:191], v[44:47], v[120:123], 0\n"
  "s_add_i32 s48, s47, s45\n"
  "buffer_store_dwordx4 v[128:131], v242, s[40:43], s56 offen offset:128 sc1\n"
  "v_pk_add_f16 v82, v50, v66\n"
  "v_mfma_f32_16x16x32_f16 a[192:195], v[0:3], v[112:115], 0\n"
  "s_add_i32 s46, s46, s49\n"
  "ds_read_b128 v[108:111], v232 offset:2560\n"
  "v_pk_add_f16 v120, v112, v116\n"
  "v_mfma_f32_16x16x32_f16 a[204:207], v[4:7], v[112:115], 0\n"
  "s_add_i32 s50, s50, 4194304\n"
  "v_pk_add_f16 v121, v113, v117\n"
  "v_pk_add_f16 v122, v114, v118\n"
  "v_mfma_f32_16x16x32_f16 a[216:219], v[8:11], v[112:115], 0\n"
  "v_pk_add_f16 v123, v115, v119\n"
  "v_pk_add_f16 v83, v51, v67\n"
  "v_mfma_f32_16x16x32_f16 a[228:231], v[12:15], v[112:115], 0\n"
  "v_pk_add_f16 v84, v52, v68\n"
  "v_pk_add_f16 v85, v53, v69\n"
  "v_mfma_f32_16x16x32_f16 a[196:199], v[16:19], v[116:119], 0\n"
  "v_pk_add_f16 v86, v54, v70\n"
  "v_pk_add_f16 v87, v55, v71\n"
  "v_mfma_f32_16x16x32_f16 a[208:211], v[20:23], v[116:119], 0\n"
  "v_pk_add_f16 v88, v56, v72\n"
  "v_pk_add_f16 v89, v57, v73\n"
  "v_mfma_f32_16x16x32_f16 a[220:223], v[24:27], v[116:119], 0\n"
  "v_pk_add_f16 v90, v58, v74\n"
  "v_pk_add_f16 v91, v59, v75\n"
  "v_mfma_f32_16x16x32_f16 a[232:235], v[28:31], v[116:119], 0\n"
  "v_pk_add_f16 v92, v60, v76\n"
  "v_pk_add_f16 v93, v61, v77\n"
  "v_mfma_f32_16x16x32_f16 a[200:203], v[32:35], v[120:123], 0\n"
  "v_pk_add_f16 v94, v62, v78\n"
  "v_pk_add_f16 v95, v63, v79\n"
  "s_waitcnt vmcnt(17) lgkmcnt(0)\n"
  "v_mfma_f32_16x16x32_f16 a[212:215], v[36:39], v[120:123], 0\n"
  "s_cmp_gt_u32 s62, 88\n"
  "s_cbranch_scc1 .Lpace_s0_None_%=\n"
  ".Lpaced_s0_None_%=:\n"
  "v_mfma_f32_16x16x32_f16 a[224:227], v[40:43], v[120:123], 0\n"
  "v_add_u32 v229, s49, v229\n"
  "v_add_u32 v230, s49, v230\n"
  "s_barrier\n"
  "v_mfma_f32_16x16x32_f16 a[236:239], v[44:47], v[120:123], 0\n"
  "v_add_u32 v231, s49, v231\n"
  "v_add_u32 v232, s49, v232\n"
  "v_mfma_f32_16x16x32_f16 a[0:3], v[48:51], v[96:99], a[0:3]\n"
  "v_pk_add_f16 v120, v96, v100\n"
  "v_pk_add_f16 v121, v97, v101\n"
  "v_mfma_f32_16x16x32_f16 a[12:15], v[52:55], v[96:99], a[12:15]\n"
  "v_pk_add_f16 v122, v98, v102\n"
  "v_pk_add_f16 v123, v99, v103\n"
  "v_mfma_f32_16x16x32_f16 a[24:27], v[56:59], v[96:99], a[24:27]\n"
  "s_and_b32 s65, s51, 0xffffe300\n"
  "ds_read_b64_tr_b16 v[0:1], v228 offset:0\n"
  "v_readlane_b32 s66, v244, 38\n"
  "v_mfma_f32_16x16x32_f16 a[36:39], v[60:63], v[96:99], a[36:39]\n"
  "buffer_load_dwordx4 v[164:167], v240, s[40:43], s59 offen sc1\n"
  "v_readlane_b32 s67, v244, 39\n"
  "v_mfma_f32_16x16x32_f16 a[4:7], v[64:67], v[100:103], a[4:7]\n"
  "s_cmp_eq_u32 s46, 73728\n"
  "s_cselect_b32 s49, s68, s69\n"
  "ds_read_b64_tr_b16 v[124:125], v233 offset:2048\n"
  "v_mfma_f32_16x16x32_f16 a[16:19], v[68:71], v[100:103], a[16:19]\n"
  "s_add_i32 m0, s44, 155648\n"
  "ds_read_b64_tr_b16 v[128:129], v234 offset:2048\n"
  "v_add_u32 v233, -45056, v233\n"
  "v_mfma_f32_16x16x32_f16 a[28:31], v[72:75], v[100:103], a[28:31]\n"
  "buffer_load_dwordx4 v239, s[40:43], s58 offen sc1 lds\n"
  "v_add_u32 v234, -45056, v234\n"
  "v_mfma_f32_16x16x32_f16 a[40:43], v[76:79], v[100:103], a[40:43]\n"
  "s_add_i32 s57, s57, 32768\n"
  "ds_read_b64_tr_b16 v[2:3], v228 offset:8192\n"
  "ds_read_b128 v[96:99], v232 offset:4096\n"
  "v_mfma_f32_16x16x32_f16 a[8:11], v[80:83], v[120:123], a[8:11]\n"
  "s_add_i32 m0, s47, 0\n"
  "s_lshr_b32 s59, s57, 9\n"
  "ds_read_b64_tr_b16 v[132:133], v235 offset:2048\n"
  "v_mfma_f32_16x16x32_f16 a[20:23], v[84:87], v[120:123], a[20:23]\n"
  "s_add_i32 s64, s50, 4194304\n"
  "buffer_load_dwordx4 v237, s[40:43], s64 offen sc1 lds\n"
  "v_add_u32 v235, -45056, v235\n"
  "v_mfma_f32_16x16x32_f16 a[32:35], v[88:91], v[120:123], a[32:35]\n"
  "s_add_i32 s53, s53, 8192\n"
  "s_lshr_b32 s52, s52, 1\n"
  "ds_read_b64_tr_b16 v[16:17], v228 offset:256\n"
  "v_mfma_f32_16x16x32_f16 a[44:47], v[92:95], v[120:123], a[44:47]\n"
  "s_bitcmp1_b32 s52, 0\n"
  "s_cselect_b32 s54, s53, 134217728\n"
  "ds_read_b128 v[100:103], v232 offset:4608\n"
  "v_mfma_f32_16x16x32_f16 a[48:51], v[48:51], v[104:107], a[48:51]\n"
  "s_add_i32 s51, s51, 8448\n"
  "buffer_store_dword v232, v245, s[40:43], s65 offen sc1\n"
  "v_pk_add_f16 v120, v104, v108\n"
  "v_mfma_f32_16x16x32_f16 a[60:63], v[52:55], v[104:107], a[60:63]\n"
  "ds_read_b64_tr_b16 v[136:137], v236 offset:2048\n"
  "v_pk_add_f16 v121, v105, v109\n"
  "v_mfma_f32_16x16x32_f16 a[72:75], v[56:59], v[104:107], a[72:75]\n"
  "ds_read_b64_tr_b16 v[18:19], v228 offset:8448\n"
  "v_pk_add_f16 v122, v106, v110\n"
  "v_mfma_f32_16x16x32_f16 a[84:87], v[60:63], v[104:107], a[84:87]\n"
  "buffer_store_dwordx4 a[240:243], v243, s[40:43], s66 offen sc1\n"
  "v_pk_add_f16 v123, v107, v111\n"
  "v_mfma_f32_16x16x32_f16 a[52:55], v[64:67], v[108:111], a[52:55]\n"
  "ds_read_b64_tr_b16 v[4:5], v229 offset:0\n"
  "v_readlane_b32 s66, v244, 40\n"
  "v_mfma_f32_16x16x32_f16 a[64:67], v[68:71], v[108:111], a[64:67]\n"
  "s_add_i32 m0, s47, 4096\n"
  "ds_read_b64_tr_b16 v[6:7], v229 offset:8192\n"
  "v_add_u32 v228, s49, v228\n"
  "v_mfma_f32_16x16x32_f16 a[76:79], v[72:75], v[108:111], a[76:79]\n"
  "s_add_i32 s64, s50, 4198400\n"
  "buffer_load_dwordx4 v237, s[40:43], s64 offen sc1 lds\n"
  "v_add_u32 v236, -45056, v236\n"
  "v_mfma_f32_16x16x32_f16 a[88:91], v[76:79], v[108:111], a[88:91]\n"
  "ds_read_b64_tr_b16 v[20:21], v229 offset:256\n"
  "ds_read_b64_tr_b16 v[22:23], v229 offset:8448\n"
  "v_mfma_f32_16x16x32_f16 a[56:59], v[80:83], v[120:123], a[56:59]\n"
  "s_waitcnt lgkmcnt(5)\n"
  "ds_read_b128 v[104:107], v232 offset:6144\n"
  "v_mov_b64 v[126:127], v[124:125]\n"
  "v_mfma_f32_16x16x32_f16 a[68:71], v[84:87], v[120:123], a[68:71]\n"
  "buffer_store_dwordx4 a[244:247], v243, s[40:43], s67 offen sc1\n"
  "v_mov_b64 v[130:131], v[128:129]\n"
  "v_mfma_f32_16x16x32_f16 a[80:83], v[88:91], v[120:123], a[80:83]\n"
  "s_waitcnt lgkmcnt(5)\n"
  "ds_read_b64_tr_b16 v[8:9], v230 offset:0\n"
  "v_readlane_b32 s67, v244, 41\n"
  "v_mfma_f32_16x16x32_f16 a[92:95], v[92:95], v[120:123], a[92:95]\n"
  "s_add_i32 m0, s47, 8192\n"
  "ds_read_b128 v[108:111], v232 offset:6656\n"
  "v_mov_b64 v[134:135], v[132:133]\n"
  "v_mfma_f32_16x16x32_f16 a[96:99], v[48:51], v[96:99], a[96:99]\n"
  "s_add_i32 s64, s50, 4202496\n"
  "buffer_load_dwordx4 v237, s[40:43], s64 offen sc1 lds\n"
  "v_pk_add_f16 v120, v96, v100\n"
  "v_mfma_f32_16x16x32_bf16 v[140:143], v[124:127], v[176:179], 0\n"
  "ds_read_b64_tr_b16 v[10:11], v230 offset:8192\n"
  "v_pk_add_f16 v121, v97, v101\n"
  "v_mfma_f32_16x16x32_f16 a[108:111], v[52:55], v[96:99], a[108:111]\n"
  "s_waitcnt lgkmcnt(4)\n"
  "ds_read_b64_tr_b16 v[24:25], v230 offset:256\n"
  "v_pk_add_f16 v122, v98, v102\n"
  "v_mfma_f32_16x16x32_f16 a[120:123], v[56:59], v[96:99], a[120:123]\n"
  "buffer_store_dwordx4 a[248:251], v243, s[40:43], s66 offen sc1\n"
  "v_pk_add_f16 v123, v99, v103\n"
  "v_mfma_f32_16x16x32_f16 a[132:135], v[60:63], v[96:99], a[132:135]\n"
  "ds_read_b64_tr_b16 v[26:27], v230 offset:8448\n"
  "v_mov_b64 v[138:139], v[136:137]\n"
  "v_mfma_f32_16x16x32_bf16 v[144:147], v[128:131], v[176:179], 0\n"
  "s_add_i32 m0, s47, 12288\n"
  "ds_read_b64_tr_b16 v[12:13], v231 offset:0\n"
  "v_cvt_pkrtz_f16_f32 v124, v140, v141\n"
  "v_mfma_f32_16x16x32_f16 a[100:103], v[64:67], v[100:103], a[100:103]\n"
  "s_add_i32 s64, s50, 4206592\n"
  "buffer_load_dwordx4 v237, s[40:43], s64 offen sc1 lds\n"
  "v_cvt_pkrtz_f16_f32 v125, v142, v143\n"
  "v_mfma_f32_16x16x32_f16 a[112:115], v[68:71], v[100:103], a[112:115]\n"
  "ds_read_b64_tr_b16 v[14:15], v231 offset:8192\n"
  "v_and_b32 v124, 0xfff8fff8, v124\n"
  "v_mfma_f32_16x16x32_f16 a[124:127], v[72:75], v[100:103], a[124:127]\n"
  "ds_read_b128 v[96:99], v232 offset:0\n"
  "v_cvt_pkrtz_f16_f32 v126, v144, v145\n"
  "v_mfma_f32_16x16x32_bf16 v[148:151], v[132:135], v[176:179], 0\n"
  "buffer_store_dwordx4 a[252:255], v243, s[40:43], s67 offen sc1\n"
  "v_cvt_pkrtz_f16_f32 v127, v146, v147\n"
  "v_mfma_f32_16x16x32_f16 a[136:139], v[76:79], v[100:103], a[136:139]\n"
  "ds_read_b128 v[112:115], v232 offset:8192\n"
  "v_and_b32 v125, 0xfff8fff8, v125\n"
  "v_mfma_f32_16x16x32_f16 a[104:107], v[80:83], v[120:123], a[104:107]\n"
  "s_add_i32 m0, s47, 16384\n"
  "ds_read_b64_tr_b16 v[28:29], v231 offset:256\n"
  "v_and_b32 v126, 0xfff8fff8, v126\n"
  "v_mfma_f32_16x16x32_f16 a[116:119], v[84:87], v[120:123], a[116:119]\n"
  "s_waitcnt lgkmcnt(8)\n"
  "s_add_i32 s64, s50, 4194304\n"
  "buffer_load_dwordx4 v238, s[40:43], s64 offen sc1 lds\n"
  "v_mfma_f32_16x16x32_bf16 v[152:155], v[136:139], v[176:179], 0\n"
  "s_waitcnt lgkmcnt(5)\n"
  "ds_read_b128 v[100:103], v232 offset:512\n"
  "v_and_b32 v127, 0xfff8fff8, v127\n"
  "v_mfma_f32_16x16x32_f16 a[128:131], v[88:91], v[120:123], a[128:131]\n"
  "s_add_i32 m0, s47, 20480\n"
  "ds_read_b128 v[116:119], v232 offset:8704\n"
  "v_cvt_pkrtz_f16_f32 v128, v148, v149\n"
  "v_mfma_f32_16x16x32_f16 a[140:143], v[92:95], v[120:123], a[140:143]\n"
  "s_add_i32 s64, s50, 4096\n"
  "buffer_load_dwordx4 v238, s[40:43], s64 offen sc1 lds\n"
  "v_cvt_pkrtz_f16_f32 v129, v150, v151\n"
  "v_mfma_f32_16x16x32_f16 a[144:147], v[48:51], v[104:107], a[144:147]\n"
  "ds_read_b64_tr_b16 v[30:31], v231 offset:8448\n"
  "v_pk_add_f16 v120, v104, v108\n"
  "v_mfma_f32_16x16x32_f16 a[156:159], v[52:55], v[104:107], a[156:159]\n"
  "s_add_i32 m0, s48, 24576\n"
  "v_pk_add_f16 v121, v105, v109\n"
  "v_pk_add_f16 v122, v106, v110\n"
  "v_mfma_f32_16x16x32_f16 a[168:171], v[56:59], v[104:107], a[168:171]\n"
  "s_add_i32 s64, s50, 16384\n"
  "buffer_load_dwordx4 v238, s[40:43], s64 offen sc1 lds\n"
  "v_pk_add_f16 v123, v107, v111\n"
  "v_mfma_f32_16x16x32_f16 a[180:183], v[60:63], v[104:107], a[180:183]\n"
  "ds_read_b128 v[104:107], v232 offset:2048\n"
  "v_cvt_pkrtz_f16_f32 v130, v152, v153\n"
  "v_mfma_f32_16x16x32_f16 a[148:151], v[64:67], v[108:111], a[148:151]\n"
  "v_cvt_pkrtz_f16_f32 v131, v154, v155\n"
  "v_and_b32 v128, 0xfff8fff8, v128\n"
  "v_mfma_f32_16x16x32_f16 a[160:163], v[68:71], v[108:111], a[160:163]\n"
  "buffer_store_dwordx4 v[124:127], v242, s[40:43], s56 offen offset:256 sc1\n"
  "v_and_b32 v129, 0xfff8fff8, v129\n"
  "v_mfma_f32_16x16x32_f16 a[172:175], v[72:75], v[108:111], a[172:175]\n"
  "v_and_b32 v130, 0xfff8fff8, v130\n"
  "v_and_b32 v131, 0xfff8fff8, v131\n"
  "v_mfma_f32_16x16x32_f16 a[184:187], v[76:79], v[108:111], a[184:187]\n"
  "s_add_i32 m0, s47, 26624\n"
  "s_waitcnt lgkmcnt(1)\n"
  "ds_read_b128 v[108:111], v232 offset:2560\n"
  "v_mfma_f32_16x16x32_f16 a[152:155], v[80:83], v[120:123], a[152:155]\n"
  "s_add_i32 s64, s50, 4214784\n"
  "buffer_load_dwordx4 v238, s[40:43], s64 offen sc1 lds\n"
  "v_pk_add_f16 v32, v0, v16\n"
  "v_mfma_f32_16x16x32_f16 a[164:167], v[84:87], v[120:123], a[164:167]\n"
  "v_pk_add_f16 v33, v1, v17\n"
  "v_pk_add_f16 v34, v2, v18\n"
  "v_mfma_f32_16x16x32_f16 a[176:179], v[88:91], v[120:123], a[176:179]\n"
  "s_add_i32 m0, s47, 30720\n"
  "v_pk_add_f16 v35, v3, v19\n"
  "v_pk_add_f16 v36, v4, v20\n"
  "v_mfma_f32_16x16x32_f16 a[188:191], v[92:95], v[120:123], a[188:191]\n"
  "s_add_i32 s64, s50, 32768\n"
  "buffer_load_dwordx4 v238, s[40:43], s64 offen sc1 lds\n"
  "v_pk_add_f16 v37, v5, v21\n"
  "v_mfma_f32_16x16x32_f16 a[192:195], v[48:51], v[112:115], a[192:195]\n"
  "s_add_i32 s47, s44, s46\n"
  "v_pk_add_f16 v120, v112, v116\n"
  "v_pk_add_f16 v121, v113, v117\n"
  "v_mfma_f32_16x16x32_f16 a[204:207], v[52:55], v[112:115], a[204:207]\n"
  "s_add_i32 s48, s47, s45\n"
  "v_pk_add_f16 v122, v114, v118\n"
  "v_pk_add_f16 v123, v115, v119\n"
  "v_mfma_f32_16x16x32_f16 a[216:219], v[56:59], v[112:115], a[216:219]\n"
  "s_add_i32 s46, s46, s49\n"
  "buffer_store_dwordx4 v[128:131], v242, s[40:43], s56 offen offset:384 sc1\n"
  "v_pk_add_f16 v38, v6, v22\n"
  "v_mfma_f32_16x16x32_f16 a[228:231], v[60:63], v[112:115], a[228:231]\n"
  "s_cmp_lt_u32 s57, 0x80000\n"
  "s_cselect_b32 s58, s57, 0xf0000000\n"
  "s_cselect_b32 s59, s59, 0xf0000000\n"
  "v_mfma_f32_16x16x32_f16 a[196:199], v[64:67], v[116:119], a[196:199]\n"
  "s_add_i32 s55, s55, 8404992\n"
  "v_pk_add_f16 v39, v7, v23\n"
  "v_pk_add_f16 v40, v8, v24\n"
  "v_mfma_f32_16x16x32_f16 a[208:211], v[68:71], v[116:119], a[208:211]\n"
  "s_add_i32 s50, s50, 4194304\n"
  "v_pk_add_f16 v41, v9, v25\n"
  "v_pk_add_f16 v42, v10, v26\n"
  "v_mfma_f32_16x16x32_f16 a[220:223], v[72:75], v[116:119], a[220:223]\n"
  "s_and_b32 s56, s55, 0xff80c000\n"
  "v_pk_add_f16 v43, v11, v27\n"
  "v_pk_add_f16 v44, v12, v28\n"
  "v_mfma_f32_16x16x32_f16 a[232:235], v[76:79], v[116:119], a[232:235]\n"
  "v_pk_add_f16 v45, v13, v29\n"
  "v_pk_add_f16 v46, v14, v30\n"
  "v_mfma_f32_16x16x32_f16 a[200:203], v[80:83], v[120:123], a[200:203]\n"
  "v_pk_add_f16 v47, v15, v31\n"
  "v_add_u32 v229, s49, v229\n"
  "s_waitcnt vmcnt(16) lgkmcnt(0)\n"
  "v_mfma_f32_16x16x32_f16 a[212:215], v[84:87], v[120:123], a[212:215]\n"
  "s_cmp_gt_u32 s62, 88\n"
  "s_cbranch_scc1 .Lpace_s1_None_%=\n"
  ".Lpaced_s1_None_%=:\n"
  "v_mfma_f32_16x16x32_f16 a[224:227], v[88:91], v[120:123], a[224:227]\n"
  "v_add_u32 v230, s49, v230\n"
  "v_add_u32 v231, s49, v231\n"
  "s_barrier\n"
  "v_mfma_f32_16x16x32_f16 a[236:239], v[92:95], v[120:123], a[236:239]\n"
  "v_add_u32 v232, s49, v232\n"
  "s_mov_b32 s63, 10\n"
  ".p2align 6\n"
  ".LloopP_%=:\n"
  "v_mfma_f32_16x16x32_f16 a[0:3], v[0:3], v[96:99], a[0:3]\n"
  "buffer_load_dword v247, v246, s[40:43], s54 offen sc1\n"
  "v_pk_add_f16 v120, v96, v100\n"
  "v_mfma_f32_16x16x32_f16 a[12:15], v[4:7], v[96:99], a[12:15]\n"
  "v_pk_add_f16 v121, v97, v101\n"
  "v_pk_add_f16 v122, v98, v102\n"
  "v_mfma_f32_16x16x32_f16 a[24:27], v[8:11], v[96:99], a[24:27]\n"
  "s_min_u32 s67, s60, 11\n"
  "s_lshl_b32 s67, s67, 2\n"
  "v_pk_add_f16 v123, v99, v103\n"
  "v_mfma_f32_16x16x32_f16 a[36:39], v[12:15], v[96:99], a[36:39]\n"
  "ds_read_b64_tr_b16 v[48:49], v228 offset:0\n"
  "v_readlane_b32 s66, v244, s60\n"
  "v_mfma_f32_16x16x32_f16 a[4:7], v[16:19], v[100:103], a[4:7]\n"
  "s_cmp_eq_u32 s46, 73728\n"
  "s_cselect_b32 s49, s68, s69\n"
  "buffer_load_dwordx4 v[168:171], v240, s[40:43], s59 offen sc1\n"
  "v_mfma_f32_16x16x32_f16 a[16:19], v[20:23], v[100:103], a[16:19]\n"
  "ds_read_b64_tr_b16 v[124:125], v233 offset:0\n"
  "ds_read_b64_tr_b16 v[128:129], v234 offset:0\n"
  "v_mfma_f32_16x16x32_f16 a[28:31], v[24:27], v[100:103], a[28:31]\n"
  "ds_read_b64_tr_b16 v[50:51], v228 offset:8192\n"
  "ds_read_b128 v[96:99], v232 offset:4096\n"
  "v_mfma_f32_16x16x32_f16 a[40:43], v[28:31], v[100:103], a[40:43]\n"
  "s_add_i32 m0, s44, 110592\n"
  "ds_read_b64_tr_b16 v[160:161], v241 offset:45056\n"
  "ds_read_b64_tr_b16 v[162:163], v241 offset:45568\n"
  "v_mfma_f32_16x16x32_f16 a[8:11], v[32:35], v[120:123], a[8:11]\n"
  "buffer_load_dwordx4 v239, s[40:43], s58 offen sc1 lds\n"
  "ds_read_b64_tr_b16 v[132:133], v235 offset:0\n"
  "v_mfma_f32_16x16x32_f16 a[20:23], v[36:39], v[120:123], a[20:23]\n"
  "s_add_i32 s57, s57, 32768\n"
  "ds_read_b64_tr_b16 v[64:65], v228 offset:256\n"
  "ds_read_b128 v[100:103], v232 offset:4608\n"
  "v_mfma_f32_16x16x32_f16 a[32:35], v[40:43], v[120:123], a[32:35]\n"
  "s_lshr_b32 s59, s57, 9\n"
  "s_add_i32 s60, s60, 1\n"
  "ds_read_b64_tr_b16 v[136:137], v236 offset:0\n"
  "v_mfma_f32_16x16x32_f16 a[44:47], v[44:47], v[120:123], a[44:47]\n"
  "s_add_i32 m0, s47, 0\n"
  "ds_read_b64_tr_b16 v[66:67], v228 offset:8448\n"
  "v_add_u32 v228, s49, v228\n"
  "v_mfma_f32_16x16x32_f16 a[48:51], v[0:3], v[104:107], a[48:51]\n"
  "s_add_i32 s64, s50, 4194304\n"
  "buffer_load_dwordx4 v237, s[40:43], s64 offen sc1 lds\n"
  "v_pk_add_f16 v120, v104, v108\n"
  "v_mfma_f32_16x16x32_f16 a[60:63], v[4:7], v[104:107], a[60:63]\n"
  "s_set_gpr_idx_on s67, gpr_idx(SRC0)\n"
  "v_mov_b64 v[156:157], v[180:181]\n"
  "v_mov_b64 v[158:159], v[182:183]\n"
  "s_set_gpr_idx_off\n"
  "v_mfma_f32_16x16x32_f16 a[72:75], v[8:11], v[104:107], a[72:75]\n"
  "s_waitcnt lgkmcnt(5)\n"
  "ds_read_b64_tr_b16 v[52:53], v229 offset:0\n"
  "v_pk_add_f16 v121, v105, v109\n"
  "v_mfma_f32_16x16x32_f16 a[84:87], v[12:15], v[104:107], a[84:87]\n"
  "v_pk_add_f16 v122, v106, v110\n"
  "v_pk_add_f16 v123, v107, v111\n"
  "v_mfma_f32_16x16x32_f16 a[52:55], v[16:19], v[108:111], a[52:55]\n"
  "buffer_store_dwordx4 v[156:159], v243, s[40:43], s66 offen sc1\n"
  "ds_read_b64_tr_b16 v[54:55], v229 offset:8192\n"
  "v_mfma_f32_16x16x32_f16 a[64:67], v[20:23], v[108:111], a[64:67]\n"
  "s_waitcnt lgkmcnt(3)\n"
  "ds_read_b64_tr_b16 v[68:69], v229 offset:256\n"
  "v_mov_b64 v[126:127], v[124:125]\n"
  "v_mfma_f32_16x16x32_f16 a[76:79], v[24:27], v[108:111], a[76:79]\n"
  "s_waitcnt lgkmcnt(3)\n"
  "ds_read_b64_tr_b16 v[70:71], v229 offset:8448\n"
  "v_mov_b64 v[130:131], v[128:129]\n"
  "v_mfma_f32_16x16x32_bf16 v[172:175], v[160:163], v[164:167], v[172:175]\n"
  "s_add_i32 m0, s47, 4096\n"
  "ds_read_b128 v[104:107], v232 offset:6144\n"
  "v_mov_b64 v[134:135], v[132:133]\n"
  "v_mfma_f32_16x16x32_f16 a[88:91], v[28:31], v[108:111], a[88:91]\n"
  "s_add_i32 s64, s50, 4198400\n"
  "buffer_load_dwordx4 v237, s[40:43], s64 offen sc1 lds\n"
  "v_mov_b64 v[138:139], v[136:137]\n"
  "v_mfma_f32_16x16x32_f16 a[56:59], v[32:35], v[120:123], a[56:59]\n"
  "ds_read_b64_tr_b16 v[56:57], v230 offset:0\n"
  "v_pk_add_f16 v80, v48, v64\n"
  "v_mfma_f32_16x16x32_f16 a[68:71], v[36:39], v[120:123], a[68:71]\n"
  "ds_read_b128 v[108:111], v232 offset:6656\n"
  "v_pk_add_f16 v81, v49, v65\n"
  "v_mfma_f32_16x16x32_f16 a[80:83], v[40:43], v[120:123], a[80:83]\n"
  "s_add_i32 m0, s47, 8192\n"
  "ds_read_b64_tr_b16 v[58:59], v230 offset:8192\n"
  "v_pk_add_f16 v82, v50, v66\n"
  "v_mfma_f32_16x16x32_f16 a[92:95], v[44:47], v[120:123], a[92:95]\n"
  "s_add_i32 s64, s50, 4202496\n"
  "buffer_load_dwordx4 v237, s[40:43], s64 offen sc1 lds\n"
  "v_pk_add_f16 v83, v51, v67\n"
  "v_mfma_f32_16x16x32_f16 a[96:99], v[0:3], v[96:99], a[96:99]\n"
  "s_waitcnt lgkmcnt(4)\n"
  "v_pk_add_f16 v120, v96, v100\n"
  "v_pk_add_f16 v121, v97, v101\n"
  "v_mfma_f32_16x16x32_bf16 v[140:143], v[124:127], v[176:179], 0\n"
  "ds_read_b64_tr_b16 v[72:73], v230 offset:256\n"
  "v_pk_add_f16 v84, v52, v68\n"
  "v_mfma_f32_16x16x32_f16 a[108:111], v[4:7], v[96:99], a[108:111]\n"
  "s_add_i32 m0, s47, 12288\n"
  "v_pk_add_f16 v122, v98, v102\n"
  "v_pk_add_f16 v123, v99, v103\n"
  "v_mfma_f32_16x16x32_f16 a[120:123], v[8:11], v[96:99], a[120:123]\n"
  "s_add_i32 s64, s50, 4206592\n"
  "buffer_load_dwordx4 v237, s[40:43], s64 offen sc1 lds\n"
  "v_pk_add_f16 v85, v53, v69\n"
  "v_mfma_f32_16x16x32_f16 a[132:135], v[12:15], v[96:99], a[132:135]\n"
  "ds_read_b64_tr_b16 v[74:75], v230 offset:8448\n"
  "v_cvt_pkrtz_f16_f32 v124, v140, v141\n"
  "v_mfma_f32_16x16x32_bf16 v[144:147], v[128:131], v[176:179], 0\n"
  "ds_read_b64_tr_b16 v[60:61], v231 offset:0\n"
  "v_cvt_pkrtz_f16_f32 v125, v142, v143\n"
  "v_mfma_f32_16x16x32_f16 a[100:103], v[16:19], v[100:103], a[100:103]\n"
  "s_add_i32 m0, s47, 16384\n"
  "ds_read_b64_tr_b16 v[62:63], v231 offset:8192\n"
  "v_and_b32 v124, 0xfff8fff8, v124\n"
  "v_mfma_f32_16x16x32_f16 a[112:115], v[20:23], v[100:103], a[112:115]\n"
  "s_add_i32 s64, s50, 4194304\n"
  "buffer_load_dwordx4 v238, s[40:43], s64 offen sc1 lds\n"
  "v_and_b32 v125, 0xfff8fff8, v125\n"
  "v_mfma_f32_16x16x32_f16 a[124:127], v[24:27], v[100:103], a[124:127]\n"
  "ds_read_b128 v[96:99], v232 offset:0\n"
  "v_cvt_pkrtz_f16_f32 v126, v144, v145\n"
  "v_mfma_f32_16x16x32_bf16 v[148:151], v[132:135], v[176:179], 0\n"
  "ds_read_b128 v[112:115], v232 offset:8192\n"
  "v_cvt_pkrtz_f16_f32 v127, v146, v147\n"
  "v_mfma_f32_16x16x32_f16 a[136:139], v[28:31], v[100:103], a[136:139]\n"
  "s_add_i32 m0, s47, 20480\n"
  "ds_read_b64_tr_b16 v[76:77], v231 offset:256\n"
  "v_and_b32 v126, 0xfff8fff8, v126\n"
  "v_mfma_f32_16x16x32_f16 a[104:107], v[32:35], v[120:123], a[104:107]\n"
  "s_add_i32 s64, s50, 4096\n"
  "buffer_load_dwordx4 v238, s[40:43], s64 offen sc1 lds\n"
  "v_and_b32 v127, 0xfff8fff8, v127\n"
  "v_mfma_f32_16x16x32_f16 a[116:119], v[36:39], v[120:123], a[116:119]\n"
  "s_waitcnt lgkmcnt(8)\n"
  "ds_read_b128 v[100:103], v232 offset:512\n"
  "v_cvt_pkrtz_f16_f32 v128, v148, v149\n"
  "v_mfma_f32_16x16x32_bf16 v[152:155], v[136:139], v[176:179], 0\n"
  "s_waitcnt lgkmcnt(6)\n"
  "ds_read_b128 v[116:119], v232 offset:8704\n"
  "v_cvt_pkrtz_f16_f32 v129, v150, v151\n"
  "v_mfma_f32_16x16x32_f16 a[128:131], v[40:43], v[120:123], a[128:131]\n"
  "s_add_i32 m0, s48, 24576\n"
  "ds_read_b64_tr_b16 v[78:79], v231 offset:8448\n"
  "v_and_b32 v128, 0xfff8fff8, v128\n"
  "v_mfma_f32_16x16x32_f16 a[140:143], v[44:47], v[120:123], a[140:143]\n"
  "s_add_i32 s64, s50, 16384\n"
  "buffer_load_dwordx4 v238, s[40:43], s64 offen sc1 lds\n"
  "v_and_b32 v129, 0xfff8fff8, v129\n"
  "v_mfma_f32_16x16x32_f16 a[144:147], v[0:3], v[104:107], a[144:147]\n"
  "v_pk_add_f16 v120, v104, v108\n"
  "v_pk_add_f16 v121, v105, v109\n"
  "v_mfma_f32_16x16x32_f16 a[156:159], v[4:7], v[104:107], a[156:159]\n"
  "v_pk_add_f16 v122, v106, v110\n"
  "v_pk_add_f16 v123, v107, v111\n"
  "v_mfma_f32_16x16x32_f16 a[168:171], v[8:11], v[104:107], a[168:171]\n"
  "v_cvt_pkrtz_f16_f32 v130, v152, v153\n"
  "v_cvt_pkrtz_f16_f32 v131, v154, v155\n"
  "v_mfma_f32_16x16x32_f16 a[180:183], v[12:15], v[104:107], a[180:183]\n"
  "buffer_store_dwordx4 v[124:127], v242, s[40:43], s56 offen offset:0 sc1\n"
  "v_and_b32 v130, 0xfff8fff8, v130\n"
  "v_mfma_f32_16x16x32_f16 a[148:151], v[16:19], v[108:111], a[148:151]\n"
  "ds_read_b128 v[104:107], v232 offset:2048\n"
  "v_and_b32 v131, 0xfff8fff8, v131\n"
  "v_mfma_f32_16x16x32_f16 a[160:163], v[20:23], v[108:111], a[160:163]\n"
  "s_cmp_lt_u32 s57, 0x80000\n"
  "s_cselect_b32 s58, s57, 0xf0000000\n"
  "s_cselect_b32 s59, s59, 0xf0000000\n"
  "v_mfma_f32_16x16x32_f16 a[172:175], v[24:27], v[108:111], a[172:175]\n"
  "s_add_i32 m0, s47, 26624\n"
  "s_waitcnt lgkmcnt(1)\n"
  "v_pk_add_f16 v86, v54, v70\n"
  "v_mfma_f32_16x16x32_f16 a[184:187], v[28:31], v[108:111], a[184:187]\n"
  "s_add_i32 s64, s50, 4214784\n"
  "buffer_load_dwordx4 v238, s[40:43], s64 offen sc1 lds\n"
  "v_pk_add_f16 v87, v55, v71\n"
  "v_mfma_f32_16x16x32_f16 a[152:155], v[32:35], v[120:123], a[152:155]\n"
  "ds_read_b128 v[108:111], v232 offset:2560\n"
  "v_pk_add_f16 v88, v56, v72\n"
  "v_mfma_f32_16x16x32_f16 a[164:167], v[36:39], v[120:123], a[164:167]\n"
  "v_pk_add_f16 v89, v57, v73\n"
  "v_pk_add_f16 v90, v58, v74\n"
  "v_mfma_f32_16x16x32_f16 a[176:179], v[40:43], v[120:123], a[176:179]\n"
  "s_add_i32 m0, s47, 30720\n"
  "v_pk_add_f16 v91, v59, v75\n"
  "v_pk_add_f16 v92, v60, v76\n"
  "v_mfma_f32_16x16x32_f16 a[188:191], v[44:47], v[120:123], a[188:191]\n"
  "s_add_i32 s64, s50, 32768\n"
  "buffer_load_dwordx4 v238, s[40:43], s64 offen sc1 lds\n"
  "v_pk_add_f16 v93, v61, v77\n"
  "v_mfma_f32_16x16x32_f16 a[192:195], v[0:3], v[112:115], a[192:195]\n"
  "s_add_i32 s47, s44, s46\n"
  "v_pk_add_f16 v120, v112, v116\n"
  "v_pk_add_f16 v121, v113, v117\n"
  "v_mfma_f32_16x16x32_f16 a[204:207], v[4:7], v[112:115], a[204:207]\n"
  "s_add_i32 s48, s47, s45\n"
  "v_pk_add_f16 v122, v114, v118\n"
  "v_pk_add_f16 v123, v115, v119\n"
  "v_mfma_f32_16x16x32_f16 a[216:219], v[8:11], v[112:115], a[216:219]\n"
  "s_add_i32 s46, s46, s49\n"
  "v_pk_add_f16 v94, v62, v78\n"
  "v_pk_add_f16 v95, v63, v79\n"
  "v_mfma_f32_16x16x32_f16 a[228:231], v[12:15], v[112:115], a[228:231]\n"
  "s_add_i32 s50, s50, 4194304\n"
  "buffer_store_dwordx4 v[128:131], v242, s[40:43], s56 offen offset:128 sc1\n"
  "v_add_u32 v229, s49, v229\n"
  "v_mfma_f32_16x16x32_f16 a[196:199], v[16:19], v[116:119], a[196:199]\n"
  "v_add_u32 v230, s49, v230\n"
  "v_add_u32 v231, s49, v231\n"
  "v_mfma_f32_16x16x32_f16 a[208:211], v[20:23], v[116:119], a[208:211]\n"
  "v_add_u32 v232, s49, v232\n"
  "v_mfma_f32_16x16x32_f16 a[220:223], v[24:27], v[116:119], a[220:223]\n"
  "v_mfma_f32_16x16x32_f16 a[232:235], v[28:31], v[116:119], a[232:235]\n"
  "v_mfma_f32_16x16x32_f16 a[200:203], v[32:35], v[120:123], a[200:203]\n"
  "s_waitcnt vmcnt(12) lgkmcnt(0)\n"
  "v_mfma_f32_16x16x32_f16 a[212:215], v[36:39], v[120:123], a[212:215]\n"
  "v_cmp_eq_u32 vcc, 0, v247\n"
  "s_cbranch_vccnz .Lspin_pe_None_%=\n"
  ".Lspun_pe_None_%=:\n"
  "s_cmp_gt_u32 s62, 88\n"
  "s_cbranch_scc1 .Lpace_pe_None_%=\n"
  ".Lpaced_pe_None_%=:\n"
  "v_mfma_f32_16x16x32_f16 a[224:227], v[40:43], v[120:123], a[224:227]\n"
  "s_barrier\n"
  "v_mfma_f32_16x16x32_f16 a[236:239], v[44:47], v[120:123], a[236:239]\n"
  "v_mfma_f32_16x16x32_f16 a[0:3], v[48:51], v[96:99], a[0:3]\n"
  "buffer_load_dwordx4 v[164:167], v240, s[40:43], s59 offen sc1\n"
  "v_pk_add_f16 v120, v96, v100\n"
  "v_mfma_f32_16x16x32_f16 a[12:15], v[52:55], v[96:99], a[12:15]\n"
  "v_pk_add_f16 v121, v97, v101\n"
  "v_pk_add_f16 v122, v98, v102\n"
  "v_mfma_f32_16x16x32_f16 a[24:27], v[56:59], v[96:99], a[24:27]\n"
  "s_min_u32 s67, s60, 11\n"
  "s_lshl_b32 s67, s67, 2\n"
  "v_pk_add_f16 v123, v99, v103\n"
  "v_mfma_f32_16x16x32_f16 a[36:39], v[60:63], v[96:99], a[36:39]\n"
  "s_add_i32 m0, s44, 155648\n"
  "s_and_b32 s65, s51, 0xffffe300\n"
  "ds_read_b64_tr_b16 v[0:1], v228 offset:0\n"
  "v_mfma_f32_16x16x32_f16 a[4:7], v[64:67], v[100:103], a[4:7]\n"
  "buffer_load_dwordx4 v239, s[40:43], s58 offen sc1 lds\n"
  "v_readlane_b32 s66, v244, s60\n"
  "v_mfma_f32_16x16x32_f16 a[16:19], v[68:71], v[100:103], a[16:19]\n"
  "s_cmp_eq_u32 s46, 73728\n"
  "s_cselect_b32 s49, s68, s69\n"
  "ds_read_b64_tr_b16 v[124:125], v233 offset:2048\n"
  "v_mfma_f32_16x16x32_f16 a[28:31], v[72:75], v[100:103], a[28:31]\n"
  "s_add_i32 s57, s57, 32768\n"
  "ds_read_b64_tr_b16 v[128:129], v234 offset:2048\n"
  "v_add_u32 v233, 4096, v233\n"
  "v_mfma_f32_16x16x32_f16 a[40:43], v[76:79], v[100:103], a[40:43]\n"
  "s_add_i32 m0, s47, 0\n"
  "s_lshr_b32 s59, s57, 9\n"
  "ds_read_b64_tr_b16 v[2:3], v228 offset:8192\n"
  "v_mfma_f32_16x16x32_f16 a[8:11], v[80:83], v[120:123], a[8:11]\n"
  "s_add_i32 s64, s50, 4194304\n"
  "buffer_load_dwordx4 v237, s[40:43], s64 offen sc1 lds\n"
  "v_add_u32 v234, 4096, v234\n"
  "v_mfma_f32_16x16x32_f16 a[20:23], v[84:87], v[120:123], a[20:23]\n"
  "ds_read_b128 v[96:99], v232 offset:4096\n"
  "ds_read_b64_tr_b16 v[160:161], v241 offset:0\n"
  "v_mfma_f32_16x16x32_f16 a[32:35], v[88:91], v[120:123], a[32:35]\n"
  "s_add_i32 s53, s53, 8192\n"
  "s_lshr_b32 s52, s52, 1\n"
  "ds_read_b64_tr_b16 v[162:163], v241 offset:512\n"
  "v_mfma_f32_16x16x32_f16 a[44:47], v[92:95], v[120:123], a[44:47]\n"
  "s_add_i32 s60, s60, 1\n"
  "ds_read_b64_tr_b16 v[132:133], v235 offset:2048\n"
  "v_add_u32 v235, 4096, v235\n"
  "v_mfma_f32_16x16x32_f16 a[48:51], v[48:51], v[104:107], a[48:51]\n"
  "s_add_i32 s51, s51, 8448\n"
  "buffer_store_dword v232, v245, s[40:43], s65 offen sc1\n"
  "ds_read_b64_tr_b16 v[16:17], v228 offset:256\n"
  "v_mfma_f32_16x16x32_f16 a[60:63], v[52:55], v[104:107], a[60:63]\n"
  "ds_read_b128 v[100:103], v232 offset:4608\n"
  "v_pk_add_f16 v120, v104, v108\n"
  "v_mfma_f32_16x16x32_f16 a[72:75], v[56:59], v[104:107], a[72:75]\n"
  "ds_read_b64_tr_b16 v[136:137], v236 offset:2048\n"
  "ds_read_b64_tr_b16 v[18:19], v228 offset:8448\n"
  "s_set_gpr_idx_on s67, gpr_idx(SRC0)\n"
  "v_mov_b64 v[156:157], v[180:181]\n"
  "v_mov_b64 v[158:159], v[182:183]\n"
  "s_set_gpr_idx_off\n"
  "v_mfma_f32_16x16x32_f16 a[84:87], v[60:63], v[104:107], a[84:87]\n"
  "ds_read_b64_tr_b16 v[4:5], v229 offset:0\n"
  "v_pk_add_f16 v121, v105, v109\n"
  "v_mfma_f32_16x16x32_f16 a[52:55], v[64:67], v[108:111], a[52:55]\n"
  "buffer_store_dwordx4 v[156:159], v243, s[40:43], s66 offen sc1\n"
  "v_pk_add_f16 v122, v106, v110\n"
  "v_mfma_f32_16x16x32_f16 a[64:67], v[68:71], v[108:111], a[64:67]\n"
  "s_waitcnt lgkmcnt(6)\n"
  "ds_read_b64_tr_b16 v[6:7], v229 offset:8192\n"
  "v_pk_add_f16 v123, v107, v111\n"
  "v_mfma_f32_16x16x32_f16 a[76:79], v[72:75], v[108:111], a[76:79]\n"
  "s_bitcmp1_b32 s52, 0\n"
  "s_cselect_b32 s54, s53, 134217728\n"
  "ds_read_b64_tr_b16 v[20:21], v229 offset:256\n"
  "v_mfma_f32_16x16x32_bf16 v[172:175], v[160:163], v[168:171], v[172:175]\n"
  "s_add_i32 m0, s47, 4096\n"
  "ds_read_b64_tr_b16 v[22:23], v229 offset:8448\n"
  "v_add_u32 v228, s49, v228\n"
  "v_mfma_f32_16x16x32_f16 a[88:91], v[76:79], v[108:111], a[88:91]\n"
  "s_add_i32 s64, s50, 4198400\n"
  "buffer_load_dwordx4 v237, s[40:43], s64 offen sc1 lds\n"
  "v_add_u32 v229, s49, v229\n"
  "v_mfma_f32_16x16x32_f16 a[56:59], v[80:83], v[120:123], a[56:59]\n"
  "s_waitcnt lgkmcnt(5)\n"
  "ds_read_b128 v[104:107], v232 offset:6144\n"
  "v_mov_b64 v[126:127], v[124:125]\n"
  "v_mfma_f32_16x16x32_f16 a[68:71], v[84:87], v[120:123], a[68:71]\n"
  "ds_read_b64_tr_b16 v[8:9], v230 offset:0\n"
  "v_mov_b64 v[130:131], v[128:129]\n"
  "v_mfma_f32_16x16x32_f16 a[80:83], v[88:91], v[120:123], a[80:83]\n"
  "s_add_i32 m0, s47, 8192\n"
  "ds_read_b128 v[108:111], v232 offset:6656\n"
  "v_mov_b64 v[134:135], v[132:133]\n"
  "v_mfma_f32_16x16x32_f16 a[92:95], v[92:95], v[120:123], a[92:95]\n"
  "s_add_i32 s64, s50, 4202496\n"
  "buffer_load_dwordx4 v237, s[40:43], s64 offen sc1 lds\n"
  "v_mov_b64 v[138:139], v[136:137]\n"
  "v_mfma_f32_16x16x32_f16 a[96:99], v[48:51], v[96:99], a[96:99]\n"
  "s_waitcnt lgkmcnt(7)\n"
  "ds_read_b64_tr_b16 v[10:11], v230 offset:8192\n"
  "v_pk_add_f16 v120, v96, v100\n"
  "v_mfma_f32_16x16x32_bf16 v[140:143], v[124:127], v[176:179], 0\n"
  "s_waitcnt lgkmcnt(4)\n"
  "ds_read_b64_tr_b16 v[24:25], v230 offset:256\n"
  "v_pk_add_f16 v121, v97, v101\n"
  "v_mfma_f32_16x16x32_f16 a[108:111], v[52:55], v[96:99], a[108:111]\n"
  "s_add_i32 m0, s47, 12288\n"
  "v_pk_add_f16 v122, v98, v102\n"
  "v_pk_add_f16 v123, v99, v103\n"
  "v_mfma_f32_16x16x32_f16 a[120:123], v[56:59], v[96:99], a[120:123]\n"
  "s_add_i32 s64, s50, 4206592\n"
  "buffer_load_dwordx4 v237, s[40:43], s64 offen sc1 lds\n"
  "v_pk_add_f16 v32, v0, v16\n"
  "v_mfma_f32_16x16x32_f16 a[132:135], v[60:63], v[96:99], a[132:135]\n"
  "ds_read_b64_tr_b16 v[26:27], v230 offset:8448\n"
  "v_cvt_pkrtz_f16_f32 v124, v140, v141\n"
  "v_mfma_f32_16x16x32_bf16 v[144:147], v[128:131], v[176:179], 0\n"
  "ds_read_b64_tr_b16 v[12:13], v231 offset:0\n"
  "v_cvt_pkrtz_f16_f32 v125, v142, v143\n"
  "v_mfma_f32_16x16x32_f16 a[100:103], v[64:67], v[100:103], a[100:103]\n"
  "s_add_i32 m0, s47, 16384\n"
  "ds_read_b64_tr_b16 v[14:15], v231 offset:8192\n"
  "v_and_b32 v124, 0xfff8fff8, v124\n"
  "v_mfma_f32_16x16x32_f16 a[112:115], v[68:71], v[100:103], a[112:115]\n"
  "s_add_i32 s64, s50, 4194304\n"
  "buffer_load_dwordx4 v238, s[40:43], s64 offen sc1 lds\n"
  "v_and_b32 v125, 0xfff8fff8, v125\n"
  "v_mfma_f32_16x16x32_f16 a[124:127], v[72:75], v[100:103], a[124:127]\n"
  "ds_read_b128 v[96:99], v232 offset:0\n"
  "v_cvt_pkrtz_f16_f32 v126, v144, v145\n"
  "v_mfma_f32_16x16x32_bf16 v[148:151], v[132:135], v[176:179], 0\n"
  "ds_read_b128 v[112:115], v232 offset:8192\n"
  "v_cvt_pkrtz_f16_f32 v127, v146, v147\n"
  "v_mfma_f32_16x16x32_f16 a[136:139], v[76:79], v[100:103], a[136:139]\n"
  "s_add_i32 m0, s47, 20480\n"
  "ds_read_b64_tr_b16 v[28:29], v231 offset:256\n"
  "v_and_b32 v126, 0xfff8fff8, v126\n"
  "v_mfma_f32_16x16x32_f16 a[104:107], v[80:83], v[120:123], a[104:107]\n"
  "s_add_i32 s64, s50, 4096\n"
  "buffer_load_dwordx4 v238, s[40:43], s64 offen sc1 lds\n"
  "v_and_b32 v127, 0xfff8fff8, v127\n"
  "v_mfma_f32_16x16x32_f16 a[116:119], v[84:87], v[120:123], a[116:119]\n"
  "s_waitcnt lgkmcnt(8)\n"
  "ds_read_b128 v[100:103], v232 offset:512\n"
  "v_cvt_pkrtz_f16_f32 v128, v148, v149\n"
  "v_mfma_f32_16x16x32_bf16 v[152:155], v[136:139], v[176:179], 0\n"
  "s_waitcnt lgkmcnt(6)\n"
  "ds_read_b128 v[116:119], v232 offset:8704\n"
  "v_cvt_pkrtz_f16_f32 v129, v150, v151\n"
  "v_mfma_f32_16x16x32_f16 a[128:131], v[88:91], v[120:123], a[128:131]\n"
  "s_add_i32 m0, s48, 24576\n"
  "ds_read_b64_tr_b16 v[30:31], v231 offset:8448\n"
  "v_and_b32 v128, 0xfff8fff8, v128\n"
  "v_mfma_f32_16x16x32_f16 a[140:143], v[92:95], v[120:123], a[140:143]\n"
  "s_add_i32 s64, s50, 16384\n"
  "buffer_load_dwordx4 v238, s[40:43], s64 offen sc1 lds\n"
  "v_and_b32 v129, 0xfff8fff8, v129\n"
  "v_mfma_f32_16x16x32_f16 a[144:147], v[48:51], v[104:107], a[144:147]\n"
  "v_pk_add_f16 v120, v104, v108\n"
  "v_pk_add_f16 v121, v105, v109\n"
  "v_mfma_f32_16x16x32_f16 a[156:159], v[52:55], v[104:107], a[156:159]\n"
  "v_pk_add_f16 v122, v106, v110\n"
  "v_pk_add_f16 v123, v107, v111\n"
  "v_mfma_f32_16x16x32_f16 a[168:171], v[56:59], v[104:107], a[168:171]\n"
  "v_cvt_pkrtz_f16_f32 v130, v152, v153\n"
  "v_cvt_pkrtz_f16_f32 v131, v154, v155\n"
  "v_mfma_f32_16x16x32_f16 a[180:183], v[60:63], v[104:107], a[180:183]\n"
  "buffer_store_dwordx4 v[124:127], v242, s[40:43], s56 offen offset:256 sc1\n"
  "v_and_b32 v130, 0xfff8fff8, v130\n"
  "v_mfma_f32_16x16x32_f16 a[148:151], v[64:67], v[108:111], a[148:151]\n"
  "ds_read_b128 v[104:107], v232 offset:2048\n"
  "v_and_b32 v131, 0xfff8fff8, v131\n"
  "v_mfma_f32_16x16x32_f16 a[160:163], v[68:71], v[108:111], a[160:163]\n"
  "s_cmp_lt_u32 s57, 0x80000\n"
  "s_cselect_b32 s58, s57, 0xf0000000\n"
  "s_cselect_b32 s59, s59, 0xf0000000\n"
  "v_mfma_f32_16x16x32_f16 a[172:175], v[72:75], v[108:111], a[172:175]\n"
  "s_add_i32 m0, s47, 26624\n"
  "s_waitcnt lgkmcnt(1)\n"
  "v_pk_add_f16 v33, v1, v17\n"
  "v_mfma_f32_16x16x32_f16 a[184:187], v[76:79], v[108:111], a[184:187]\n"
  "s_add_i32 s64, s50, 4214784\n"
  "buffer_load_dwordx4 v238, s[40:43], s64 offen sc1 lds\n"
  "v_pk_add_f16 v34, v2, v18\n"
  "v_mfma_f32_16x16x32_f16 a[152:155], v[80:83], v[120:123], a[152:155]\n"
  "ds_read_b128 v[108:111], v232 offset:2560\n"
  "v_pk_add_f16 v35, v3, v19\n"
  "v_mfma_f32_16x16x32_f16 a[164:167], v[84:87], v[120:123], a[164:167]\n"
  "v_pk_add_f16 v36, v4, v20\n"
  "v_pk_add_f16 v37, v5, v21\n"
  "v_mfma_f32_16x16x32_f16 a[176:179], v[88:91], v[120:123], a[176:179]\n"
  "s_add_i32 m0, s47, 30720\n"
  "v_pk_add_f16 v38, v6, v22\n"
  "v_pk_add_f16 v39, v7, v23\n"
  "v_mfma_f32_16x16x32_f16 a[188:191], v[92:95], v[120:123], a[188:191]\n"
  "s_add_i32 s64, s50, 32768\n"
  "buffer_load_dwordx4 v238, s[40:43], s64 offen sc1 lds\n"
  "v_pk_add_f16 v40, v8, v24\n"
  "v_mfma_f32_16x16x32_f16 a[192:195], v[48:51], v[112:115], a[192:195]\n"
  "s_add_i32 s47, s44, s46\n"
  "v_pk_add_f16 v120, v112, v116\n"
  "v_pk_add_f16 v121, v113, v117\n"
  "v_mfma_f32_16x16x32_f16 a[204:207], v[52:55], v[112:115], a[204:207]\n"
  "s_add_i32 s48, s47, s45\n"
  "v_pk_add_f16 v122, v114, v118\n"
  "v_pk_add_f16 v123, v115, v119\n"
  "v_mfma_f32_16x16x32_f16 a[216:219], v[56:59], v[112:115], a[216:219]\n"
  "s_add_i32 s46, s46, s49\n"
  "v_pk_add_f16 v41, v9, v25\n"
  "v_pk_add_f16 v42, v10, v26\n"
  "v_mfma_f32_16x16x32_f16 a[228:231], v[60:63], v[112:115], a[228:231]\n"
  "s_add_i32 s50, s50, 4194304\n"
  "buffer_store_dwordx4 v[128:131], v242, s[40:43], s56 offen offset:384 sc1\n"
  "v_pk_add_f16 v43, v11, v27\n"
  "v_mfma_f32_16x16x32_f16 a[196:199], v[64:67], v[116:119], a[196:199]\n"
  "s_add_i32 s55, s55, 8404992\n"
  "v_pk_add_f16 v44, v12, v28\n"
  "v_pk_add_f16 v45, v13, v29\n"
  "v_mfma_f32_16x16x32_f16 a[208:211], v[68:71], v[116:119], a[208:211]\n"
  "s_and_b32 s56, s55, 0xff80c000\n"
  "v_pk_add_f16 v46, v14, v30\n"
  "v_pk_add_f16 v47, v15, v31\n"
  "v_mfma_f32_16x16x32_f16 a[220:223], v[72:75], v[116:119], a[220:223]\n"
  "v_add_u32 v230, s49, v230\n"
  "v_add_u32 v231, s49, v231\n"
  "v_mfma_f32_16x16x32_f16 a[232:235], v[76:79], v[116:119], a[232:235]\n"
  "v_add_u32 v232, s49, v232\n"
  "v_add_u32 v236, 4096, v236\n"
  "v_mfma_f32_16x16x32_f16 a[200:203], v[80:83], v[120:123], a[200:203]\n"
  "s_waitcnt vmcnt(13) lgkmcnt(0)\n"
  "v_mfma_f32_16x16x32_f16 a[212:215], v[84:87], v[120:123], a[212:215]\n"
  "s_cmp_gt_u32 s62, 88\n"
  "s_cbranch_scc1 .Lpace_po_None_%=\n"
  ".Lpaced_po_None_%=:\n"
  "v_mfma_f32_16x16x32_f16 a[224:227], v[88:91], v[120:123], a[224:227]\n"
  "s_barrier\n"
  "v_mfma_f32_16x16x32_f16 a[236:239], v[92:95], v[120:123], a[236:239]\n"
  "s_sub_i32 s63, s63, 1\n"
  "s_cmp_lg_u32 s63, 0\n"
  "s_cbranch_scc1 .LloopP_%=\n"
  ".Lcf_t:\n"
  "s_cmp_eq_u32 s85, 0\n"
  "s_cbranch_scc1 .Lnt_%=\n"
  "s_getpc_b64 s[86:87]\n"
  "s_mov_b32 m0, s85\n"
  "s_nop 0\n"
  "global_load_lds_dword v251, s[86:87]\n"
  ".Lnt_%=:\n"
  "v_mfma_f32_16x16x32_f16 a[0:3], v[0:3], v[96:99], a[0:3]\n"
  "buffer_load_dword v247, v246, s[40:43], s54 offen sc1\n"
  "v_pk_add_f16 v120, v96, v100\n"
  "v_mfma_f32_16x16x32_f16 a[12:15], v[4:7], v[96:99], a[12:15]\n"
  "v_pk_add_f16 v121, v97, v101\n"
  "v_pk_add_f16 v122, v98, v102\n"
  "v_mfma_f32_16x16x32_f16 a[24:27], v[8:11], v[96:99], a[24:27]\n"
  "ds_read_b64_tr_b16 v[48:49], v228 offset:0\n"
  "v_pk_add_f16 v123, v99, v103\n"
  "v_mfma_f32_16x16x32_f16 a[36:39], v[12:15], v[96:99], a[36:39]\n"
  "s_add_i32 m0, s47, 0\n"
  "ds_read_b64_tr_b16 v[50:51], v228 offset:8192\n"
  "v_accvgpr_read_b32 v124, a0\n"
  "v_mfma_f32_16x16x32_f16 a[4:7], v[16:19], v[100:103], a[4:7]\n"
  "s_add_i32 s64, s50, 4194304\n"
  "buffer_load_dwordx4 v237, s[40:43], s64 offen sc1 lds\n"
  "v_accvgpr_read_b32 v125, a1\n"
  "v_mfma_f32_16x16x32_f16 a[16:19], v[20:23], v[100:103], a[16:19]\n"
  "s_cmp_eq_u32 s46, 73728\n"
  "s_cselect_b32 s49, s68, s69\n"
  "ds_read_b128 v[96:99], v232 offset:4096\n"
  "v_mfma_f32_16x16x32_f16 a[28:31], v[24:27], v[100:103], a[28:31]\n"
  "ds_read_b64_tr_b16 v[64:65], v228 offset:256\n"
  "v_accvgpr_read_b32 v126, a2\n"
  "v_mfma_f32_16x16x32_f16 a[40:43], v[28:31], v[100:103], a[40:43]\n"
  "s_add_i32 m0, s47, 4096\n"
  "ds_read_b128 v[100:103], v232 offset:4608\n"
  "v_accvgpr_read_b32 v127, a3\n"
  "v_mfma_f32_16x16x32_f16 a[8:11], v[32:35], v[120:123], a[8:11]\n"
  "s_add_i32 s64, s50, 4198400\n"
  "buffer_load_dwordx4 v237, s[40:43], s64 offen sc1 lds\n"
  "v_accvgpr_read_b32 v128, a4\n"
  "v_mfma_f32_16x16x32_f16 a[20:23], v[36:39], v[120:123], a[20:23]\n"
  "ds_read_b64_tr_b16 v[66:67], v228 offset:8448\n"
  "v_accvgpr_read_b32 v129, a5\n"
  "v_mfma_f32_16x16x32_f16 a[32:35], v[40:43], v[120:123], a[32:35]\n"
  "ds_read_b64_tr_b16 v[52:53], v229 offset:0\n"
  "v_accvgpr_read_b32 v130, a6\n"
  "v_mfma_f32_16x16x32_f16 a[44:47], v[44:47], v[120:123], a[44:47]\n"
  "s_add_i32 m0, s47, 8192\n"
  "ds_read_b64_tr_b16 v[54:55], v229 offset:8192\n"
  "v_accvgpr_read_b32 v131, a7\n"
  "v_mfma_f32_16x16x32_f16 a[48:51], v[0:3], v[104:107], a[48:51]\n"
  "s_add_i32 s64, s50, 4202496\n"
  "buffer_load_dwordx4 v237, s[40:43], s64 offen sc1 lds\n"
  "v_pk_add_f16 v120, v104, v108\n"
  "v_mfma_f32_16x16x32_f16 a[60:63], v[4:7], v[104:107], a[60:63]\n"
  "v_pk_add_f16 v121, v105, v109\n"
  "v_pk_add_f16 v122, v106, v110\n"
  "v_mfma_f32_16x16x32_f16 a[72:75], v[8:11], v[104:107], a[72:75]\n"
  "ds_read_b64_tr_b16 v[68:69], v229 offset:256\n"
  "v_pk_add_f16 v123, v107, v111\n"
  "v_mfma_f32_16x16x32_f16 a[84:87], v[12:15], v[104:107], a[84:87]\n"
  "s_add_i32 m0, s47, 12288\n"
  "ds_read_b64_tr_b16 v[70:71], v229 offset:8448\n"
  "v_accvgpr_read_b32 v132, a8\n"
  "v_mfma_f32_16x16x32_f16 a[52:55], v[16:19], v[108:111], a[52:55]\n"
  "s_add_i32 s64, s50, 4206592\n"
  "buffer_load_dwordx4 v237, s[40:43], s64 offen sc1 lds\n"
  "v_accvgpr_read_b32 v133, a9\n"
  "v_mfma_f32_16x16x32_f16 a[64:67], v[20:23], v[108:111], a[64:67]\n"
  "s_waitcnt lgkmcnt(4)\n"
  "ds_read_b128 v[104:107], v232 offset:6144\n"
  "v_accvgpr_read_b32 v134, a10\n"
  "v_mfma_f32_16x16x32_f16 a[76:79], v[24:27], v[108:111], a[76:79]\n"
  "ds_read_b64_tr_b16 v[56:57], v230 offset:0\n"
  "v_accvgpr_read_b32 v135, a11\n"
  "v_mfma_f32_16x16x32_f16 a[88:91], v[28:31], v[108:111], a[88:91]\n"
  "s_add_i32 m0, s47, 16384\n"
  "ds_read_b128 v[108:111], v232 offset:6656\n"
  "v_accvgpr_read_b32 v136, a12\n"
  "v_mfma_f32_16x16x32_f16 a[56:59], v[32:35], v[120:123], a[56:59]\n"
  "s_add_i32 s64, s50, 4194304\n"
  "buffer_load_dwordx4 v238, s[40:43], s64 offen sc1 lds\n"
  "v_accvgpr_read_b32 v137, a13\n"
  "v_mfma_f32_16x16x32_f16 a[68:71], v[36:39], v[120:123], a[68:71]\n"
  "ds_read_b64_tr_b16 v[58:59], v230 offset:8192\n"
  "v_accvgpr_read_b32 v138, a14\n"
  "v_mfma_f32_16x16x32_f16 a[80:83], v[40:43], v[120:123], a[80:83]\n"
  "s_waitcnt lgkmcnt(4)\n"
  "ds_read_b64_tr_b16 v[72:73], v230 offset:256\n"
  "v_accvgpr_read_b32 v139, a15\n"
  "v_mfma_f32_16x16x32_f16 a[92:95], v[44:47], v[120:123], a[92:95]\n"
  "s_add_i32 m0, s47, 20480\n"
  "ds_read_b64_tr_b16 v[74:75], v230 offset:8448\n"
  "v_accvgpr_read_b32 v140, a16\n"
  "v_mfma_f32_16x16x32_f16 a[96:99], v[0:3], v[96:99], a[96:99]\n"
  "s_add_i32 s64, s50, 4096\n"
  "buffer_load_dwordx4 v238, s[40:43], s64 offen sc1 lds\n"
  "v_pk_add_f16 v120, v96, v100\n"
  "v_mfma_f32_16x16x32_f16 a[108:111], v[4:7], v[96:99], a[108:111]\n"
  "v_pk_add_f16 v121, v97, v101\n"
  "v_pk_add_f16 v122, v98, v102\n"
  "v_mfma_f32_16x16x32_f16 a[120:123], v[8:11], v[96:99], a[120:123]\n"
  "ds_read_b64_tr_b16 v[60:61], v231 offset:0\n"
  "v_pk_add_f16 v123, v99, v103\n"
  "v_mfma_f32_16x16x32_f16 a[132:135], v[12:15], v[96:99], a[132:135]\n"
  "s_add_i32 m0, s48, 24576\n"
  "ds_read_b64_tr_b16 v[62:63], v231 offset:8192\n"
  "v_accvgpr_read_b32 v141, a17\n"
  "v_mfma_f32_16x16x32_f16 a[100:103], v[16:19], v[100:103], a[100:103]\n"
  "s_add_i32 s64, s50, 16384\n"
  "buffer_load_dwordx4 v238, s[40:43], s64 offen sc1 lds\n"
  "v_accvgpr_read_b32 v142, a18\n"
  "v_mfma_f32_16x16x32_f16 a[112:115], v[20:23], v[100:103], a[112:115]\n"
  "ds_read_b128 v[96:99], v232 offset:0\n"
  "v_accvgpr_read_b32 v143, a19\n"
  "v_mfma_f32_16x16x32_f16 a[124:127], v[24:27], v[100:103], a[124:127]\n"
  "s_waitcnt lgkmcnt(3)\n"
  "ds_read_b128 v[112:115], v232 offset:8192\n"
  "v_accvgpr_read_b32 v144, a20\n"
  "v_mfma_f32_16x16x32_f16 a[136:139], v[28:31], v[100:103], a[136:139]\n"
  "s_add_i32 m0, s47, 26624\n"
  "ds_read_b64_tr_b16 v[76:77], v231 offset:256\n"
  "v_accvgpr_read_b32 v145, a21\n"
  "v_mfma_f32_16x16x32_f16 a[104:107], v[32:35], v[120:123], a[104:107]\n"
  "s_add_i32 s64, s50, 4214784\n"
  "buffer_load_dwordx4 v238, s[40:43], s64 offen sc1 lds\n"
  "v_accvgpr_read_b32 v146, a22\n"
  "v_mfma_f32_16x16x32_f16 a[116:119], v[36:39], v[120:123], a[116:119]\n"
  "ds_read_b128 v[100:103], v232 offset:512\n"
  "v_accvgpr_read_b32 v147, a23\n"
  "v_mfma_f32_16x16x32_f16 a[128:131], v[40:43], v[120:123], a[128:131]\n"
  "ds_read_b128 v[116:119], v232 offset:8704\n"
  "v_accvgpr_read_b32 v148, a24\n"
  "v_mfma_f32_16x16x32_f16 a[140:143], v[44:47], v[120:123], a[140:143]\n"
  "s_add_i32 m0, s47, 30720\n"
  "ds_read_b64_tr_b16 v[78:79], v231 offset:8448\n"
  "v_accvgpr_read_b32 v149, a25\n"
  "v_mfma_f32_16x16x32_f16 a[144:147], v[0:3], v[104:107], a[144:147]\n"
  "s_add_i32 s64, s50, 32768\n"
  "buffer_load_dwordx4 v238, s[40:43], s64 offen sc1 lds\n"
  "v_pk_add_f16 v120, v104, v108\n"
  "v_mfma_f32_16x16x32_f16 a[156:159], v[4:7], v[104:107], a[156:159]\n"
  "v_pk_add_f16 v121, v105, v109\n"
  "v_pk_add_f16 v122, v106, v110\n"
  "v_mfma_f32_16x16x32_f16 a[168:171], v[8:11], v[104:107], a[168:171]\n"
  "v_pk_add_f16 v123, v107, v111\n"
  "v_accvgpr_read_b32 v150, a26\n"
  "v_mfma_f32_16x16x32_f16 a[180:183], v[12:15], v[104:107], a[180:183]\n"
  "ds_read_b128 v[104:107], v232 offset:2048\n"
  "v_accvgpr_read_b32 v151, a27\n"
  "v_mfma_f32_16x16x32_f16 a[148:151], v[16:19], v[108:111], a[148:151]\n"
  "s_add_i32 s47, s44, s46\n"
  "v_accvgpr_read_b32 v152, a28\n"
  "v_accvgpr_read_b32 v153, a29\n"
  "v_mfma_f32_16x16x32_f16 a[160:163], v[20:23], v[108:111], a[160:163]\n"
  "s_add_i32 s48, s47, s45\n"
  "v_accvgpr_read_b32 v154, a30\n"
  "v_accvgpr_read_b32 v155, a31\n"
  "v_mfma_f32_16x16x32_f16 a[172:175], v[24:27], v[108:111], a[172:175]\n"
  "s_waitcnt lgkmcnt(1)\n"
  "v_accvgpr_read_b32 v156, a32\n"
  "v_accvgpr_read_b32 v157, a33\n"
  "v_mfma_f32_16x16x32_f16 a[184:187], v[28:31], v[108:111], a[184:187]\n"
  "s_add_i32 s46, s46, s49\n"
  "ds_read_b128 v[108:111], v232 offset:2560\n"
  "v_accvgpr_read_b32 v158, a34\n"
  "v_mfma_f32_16x16x32_f16 a[152:155], v[32:35], v[120:123], a[152:155]\n"
  "s_add_i32 s50, s50, 4194304\n"
  "v_accvgpr_read_b32 v159, a35\n"
  "v_accvgpr_read_b32 v160, a36\n"
  "v_mfma_f32_16x16x32_f16 a[164:167], v[36:39], v[120:123], a[164:167]\n"
  "v_accvgpr_read_b32 v161, a37\n"
  "v_accvgpr_read_b32 v162, a38\n"
  "v_mfma_f32_16x16x32_f16 a[176:179], v[40:43], v[120:123], a[176:179]\n"
  "v_accvgpr_read_b32 v163, a39\n"
  "v_accvgpr_read_b32 v164, a40\n"
  "v_mfma_f32_16x16x32_f16 a[188:191], v[44:47], v[120:123], a[188:191]\n"
  "v_accvgpr_read_b32 v165, a41\n"
  "v_accvgpr_read_b32 v166, a42\n"
  "v_mfma_f32_16x16x32_f16 a[192:195], v[0:3], v[112:115], a[192:195]\n"
  "v_pk_add_f16 v120, v112, v116\n"
  "v_pk_add_f16 v121, v113, v117\n"
  "v_mfma_f32_16x16x32_f16 a[204:207], v[4:7], v[112:115], a[204:207]\n"
  "v_pk_add_f16 v122, v114, v118\n"
  "v_pk_add_f16 v123, v115, v119\n"
  "v_mfma_f32_16x16x32_f16 a[216:219], v[8:11], v[112:115], a[216:219]\n"
  "v_accvgpr_read_b32 v167, a43\n"
  "v_accvgpr_read_b32 v168, a44\n"
  "v_mfma_f32_16x16x32_f16 a[228:231], v[12:15], v[112:115], a[228:231]\n"
  "v_accvgpr_read_b32 v169, a45\n"
  "v_accvgpr_read_b32 v170, a46\n"
  "v_mfma_f32_16x16x32_f16 a[196:199], v[16:19], v[116:119], a[196:199]\n"
  "v_accvgpr_read_b32 v171, a47\n"
  "v_pk_add_f16 v80, v48, v64\n"
  "v_mfma_f32_16x16x32_f16 a[208:211], v[20:23], v[116:119], a[208:211]\n"
  "v_pk_add_f16 v81, v49, v65\n"
  "v_pk_add_f16 v82, v50, v66\n"
  "v_mfma_f32_16x16x32_f16 a[220:223], v[24:27], v[116:119], a[220:223]\n"
  "v_pk_add_f16 v83, v51, v67\n"
  "v_pk_add_f16 v84, v52, v68\n"
  "v_mfma_f32_16x16x32_f16 a[232:235], v[28:31], v[116:119], a[232:235]\n"
  "v_pk_add_f16 v85, v53, v69\n"
  "v_pk_add_f16 v86, v54, v70\n"
  "v_mfma_f32_16x16x32_f16 a[200:203], v[32:35], v[120:123], a[200:203]\n"
  "v_pk_add_f16 v87, v55, v71\n"
  "v_pk_add_f16 v88, v56, v72\n"
  "s_waitcnt vmcnt(9) lgkmcnt(0)\n"
  "v_mfma_f32_16x16x32_f16 a[212:215], v[36:39], v[120:123], a[212:215]\n"
  "v_cmp_eq_u32 vcc, 0, v247\n"
  "s_cbranch_vccnz .Lspin_s22_None_%=\n"
  ".Lspun_s22_None_%=:\n"
  "v_mfma_f32_16x16x32_f16 a[224:227], v[40:43], v[120:123], a[224:227]\n"
  "v_pk_add_f16 v89, v57, v73\n"
  "v_pk_add_f16 v90, v58, v74\n"
  "s_barrier\n"
  "v_mfma_f32_16x16x32_f16 a[236:239], v[44:47], v[120:123], a[236:239]\n"
  "v_pk_add_f16 v91, v59, v75\n"
  "v_pk_add_f16 v92, v60, v76\n"
  "v_pk_add_f16 v93, v61, v77\n"
  "v_pk_add_f16 v94, v62, v78\n"
  "v_pk_add_f16 v95, v63, v79\n"
  "v_add_u32 v228, s49, v228\n"
  "v_add_u32 v229, s49, v229\n"
  "v_add_u32 v230, s49, v230\n"
  "v_add_u32 v231, s49, v231\n"
  "v_add_u32 v232, s49, v232\n"
  "v_mfma_f32_16x16x32_f16 v[124:127], v[48:51], v[96:99], v[124:127]\n"
  "v_pk_add_f16 v120, v96, v100\n"
  "v_pk_add_f16 v121, v97, v101\n"
  "v_mfma_f32_16x16x32_f16 v[136:139], v[52:55], v[96:99], v[136:139]\n"
  "v_pk_add_f16 v122, v98, v102\n"
  "v_pk_add_f16 v123, v99, v103\n"
  "v_mfma_f32_16x16x32_f16 v[148:151], v[56:59], v[96:99], v[148:151]\n"
  "s_and_b32 s65, s51, 0xffffe300\n"
  "ds_read_b64_tr_b16 v[0:1], v228 offset:0\n"
  "ds_read_b64_tr_b16 v[2:3], v228 offset:8192\n"
  "v_mfma_f32_16x16x32_f16 v[160:163], v[60:63], v[96:99], v[160:163]\n"
  "s_add_i32 m0, s47, 0\n"
  "ds_read_b128 v[96:99], v232 offset:4096\n"
  "ds_read_b64_tr_b16 v[16:17], v228 offset:256\n"
  "v_mfma_f32_16x16x32_f16 v[128:131], v[64:67], v[100:103], v[128:131]\n"
  "s_add_i32 s64, s50, 4194304\n"
  "buffer_load_dwordx4 v237, s[40:43], s64 offen sc1 lds\n"
  "ds_read_b64_tr_b16 v[18:19], v228 offset:8448\n"
  "v_mfma_f32_16x16x32_f16 v[140:143], v[68:71], v[100:103], v[140:143]\n"
  "s_cmp_eq_u32 s46, 73728\n"
  "s_cselect_b32 s49, s68, s69\n"
  "ds_read_b64_tr_b16 v[4:5], v229 offset:0\n"
  "v_mfma_f32_16x16x32_f16 v[152:155], v[72:75], v[100:103], v[152:155]\n"
  "ds_read_b64_tr_b16 v[6:7], v229 offset:8192\n"
  "v_add_u32 v228, s49, v228\n"
  "v_mfma_f32_16x16x32_f16 v[164:167], v[76:79], v[100:103], v[164:167]\n"
  "ds_read_b128 v[100:103], v232 offset:4608\n"
  "ds_read_b64_tr_b16 v[20:21], v229 offset:256\n"
  "v_mfma_f32_16x16x32_f16 v[132:135], v[80:83], v[120:123], v[132:135]\n"
  "buffer_store_dword v232, v245, s[40:43], s65 offen sc1\n"
  "ds_read_b64_tr_b16 v[22:23], v229 offset:8448\n"
  "v_mfma_f32_16x16x32_f16 v[144:147], v[84:87], v[120:123], v[144:147]\n"
  "ds_read_b64_tr_b16 v[8:9], v230 offset:0\n"
  "v_add_u32 v229, s49, v229\n"
  "v_mfma_f32_16x16x32_f16 v[156:159], v[88:91], v[120:123], v[156:159]\n"
  "s_add_i32 s53, s53, 8192\n"
  "s_lshr_b32 s52, s52, 1\n"
  "ds_read_b64_tr_b16 v[10:11], v230 offset:8192\n"
  "v_mfma_f32_16x16x32_f16 v[168:171], v[92:95], v[120:123], v[168:171]\n"
  "s_add_i32 m0, s47, 4096\n"
  "s_waitcnt lgkmcnt(7)\n"
  "ds_read_b64_tr_b16 v[24:25], v230 offset:256\n"
  "v_mfma_f32_16x16x32_f16 a[48:51], v[48:51], v[104:107], a[48:51]\n"
  "s_add_i32 s64, s50, 4198400\n"
  "buffer_load_dwordx4 v237, s[40:43], s64 offen sc1 lds\n"
  "v_pk_add_f16 v120, v104, v108\n"
  "v_mfma_f32_16x16x32_f16 a[60:63], v[52:55], v[104:107], a[60:63]\n"
  "s_add_i32 s51, s51, 8448\n"
  "v_pk_add_f16 v121, v105, v109\n"
  "v_pk_add_f16 v122, v106, v110\n"
  "v_mfma_f32_16x16x32_f16 a[72:75], v[56:59], v[104:107], a[72:75]\n"
  "ds_read_b64_tr_b16 v[26:27], v230 offset:8448\n"
  "v_pk_add_f16 v123, v107, v111\n"
  "v_mfma_f32_16x16x32_f16 a[84:87], v[60:63], v[104:107], a[84:87]\n"
  "s_add_i32 m0, s47, 8192\n"
  "ds_read_b128 v[104:107], v232 offset:6144\n"
  "v_accvgpr_read_b32 v180, a48\n"
  "v_mfma_f32_16x16x32_f16 a[52:55], v[64:67], v[108:111], a[52:55]\n"
  "s_add_i32 s64, s50, 4202496\n"
  "buffer_load_dwordx4 v237, s[40:43], s64 offen sc1 lds\n"
  "v_accvgpr_read_b32 v181, a49\n"
  "v_mfma_f32_16x16x32_f16 a[64:67], v[68:71], v[108:111], a[64:67]\n"
  "s_bitcmp1_b32 s52, 0\n"
  "s_cselect_b32 s54, s53, 134217728\n"
  "ds_read_b64_tr_b16 v[12:13], v231 offset:0\n"
  "v_mfma_f32_16x16x32_f16 a[76:79], v[72:75], v[108:111], a[76:79]\n"
  "s_waitcnt lgkmcnt(6)\n"
  "ds_read_b64_tr_b16 v[14:15], v231 offset:8192\n"
  "v_accvgpr_read_b32 v182, a50\n"
  "v_mfma_f32_16x16x32_f16 a[88:91], v[76:79], v[108:111], a[88:91]\n"
  "s_add_i32 m0, s47, 12288\n"
  "ds_read_b128 v[108:111], v232 offset:6656\n"
  "v_accvgpr_read_b32 v183, a51\n"
  "v_mfma_f32_16x16x32_f16 a[56:59], v[80:83], v[120:123], a[56:59]\n"
  "s_add_i32 s64, s50, 4206592\n"
  "buffer_load_dwordx4 v237, s[40:43], s64 offen sc1 lds\n"
  "v_accvgpr_read_b32 v184, a52\n"
  "v_mfma_f32_16x16x32_f16 a[68:71], v[84:87], v[120:123], a[68:71]\n"
  "ds_read_b128 v[112:115], v232 offset:8192\n"
  "v_accvgpr_read_b32 v185, a53\n"
  "v_mfma_f32_16x16x32_f16 a[80:83], v[88:91], v[120:123], a[80:83]\n"
  "s_waitcnt lgkmcnt(5)\n"
  "ds_read_b64_tr_b16 v[28:29], v231 offset:256\n"
  "v_accvgpr_read_b32 v186, a54\n"
  "v_mfma_f32_16x16x32_f16 a[92:95], v[92:95], v[120:123], a[92:95]\n"
  "s_add_i32 m0, s47, 16384\n"
  "ds_read_b128 v[116:119], v232 offset:8704\n"
  "v_accvgpr_read_b32 v187, a55\n"
  "v_mfma_f32_16x16x32_f16 a[96:99], v[48:51], v[96:99], a[96:99]\n"
  "s_add_i32 s64, s50, 4194304\n"
  "buffer_load_dwordx4 v238, s[40:43], s64 offen sc1 lds\n"
  "v_pk_add_f16 v120, v96, v100\n"
  "v_mfma_f32_16x16x32_f16 a[108:111], v[52:55], v[96:99], a[108:111]\n"
  "v_pk_add_f16 v121, v97, v101\n"
  "v_pk_add_f16 v122, v98, v102\n"
  "v_mfma_f32_16x16x32_f16 a[120:123], v[56:59], v[96:99], a[120:123]\n"
  "ds_read_b64_tr_b16 v[30:31], v231 offset:8448\n"
  "v_pk_add_f16 v123, v99, v103\n"
  "v_mfma_f32_16x16x32_f16 a[132:135], v[60:63], v[96:99], a[132:135]\n"
  "s_add_i32 m0, s47, 20480\n"
  "ds_read_b128 v[96:99], v232 offset:0\n"
  "v_accvgpr_read_b32 v188, a56\n"
  "v_mfma_f32_16x16x32_f16 a[100:103], v[64:67], v[100:103], a[100:103]\n"
  "s_add_i32 s64, s50, 4096\n"
  "buffer_load_dwordx4 v238, s[40:43], s64 offen sc1 lds\n"
  "v_accvgpr_read_b32 v189, a57\n"
  "v_mfma_f32_16x16x32_f16 a[112:115], v[68:71], v[100:103], a[112:115]\n"
  "v_accvgpr_read_b32 v190, a58\n"
  "v_accvgpr_read_b32 v191, a59\n"
  "v_mfma_f32_16x16x32_f16 a[124:127], v[72:75], v[100:103], a[124:127]\n"
  "v_accvgpr_read_b32 v192, a60\n"
  "v_accvgpr_read_b32 v193, a61\n"
  "v_mfma_f32_16x16x32_f16 a[136:139], v[76:79], v[100:103], a[136:139]\n"
  "s_add_i32 m0, s48, 24576\n"
  "ds_read_b128 v[100:103], v232 offset:512\n"
  "v_accvgpr_read_b32 v194, a62\n"
  "v_mfma_f32_16x16x32_f16 a[104:107], v[80:83], v[120:123], a[104:107]\n"
  "s_add_i32 s64, s50, 16384\n"
  "buffer_load_dwordx4 v238, s[40:43], s64 offen sc1 lds\n"
  "v_accvgpr_read_b32 v195, a63\n"
  "v_mfma_f32_16x16x32_f16 a[116:119], v[84:87], v[120:123], a[116:119]\n"
  "s_waitcnt lgkmcnt(6)\n"
  "v_accvgpr_read_b32 v196, a64\n"
  "v_accvgpr_read_b32 v197, a65\n"
  "v_mfma_f32_16x16x32_f16 a[128:131], v[88:91], v[120:123], a[128:131]\n"
  "s_waitcnt lgkmcnt(2)\n"
  "v_accvgpr_read_b32 v198, a66\n"
  "v_accvgpr_read_b32 v199, a67\n"
  "v_mfma_f32_16x16x32_f16 a[140:143], v[92:95], v[120:123], a[140:143]\n"
  "s_add_i32 m0, s47, 26624\n"
  "v_accvgpr_read_b32 v200, a68\n"
  "v_accvgpr_read_b32 v201, a69\n"
  "v_mfma_f32_16x16x32_f16 a[144:147], v[48:51], v[104:107], a[144:147]\n"
  "s_add_i32 s64, s50, 4214784\n"
  "buffer_load_dwordx4 v238, s[40:43], s64 offen sc1 lds\n"
  "v_pk_add_f16 v120, v104, v108\n"
  "v_mfma_f32_16x16x32_f16 a[156:159], v[52:55], v[104:107], a[156:159]\n"
  "v_pk_add_f16 v121, v105, v109\n"
  "v_pk_add_f16 v122, v106, v110\n"
  "v_mfma_f32_16x16x32_f16 a[168:171], v[56:59], v[104:107], a[168:171]\n"
  "v_pk_add_f16 v123, v107, v111\n"
  "v_accvgpr_read_b32 v202, a70\n"
  "v_mfma_f32_16x16x32_f16 a[180:183], v[60:63], v[104:107], a[180:183]\n"
  "s_add_i32 m0, s47, 30720\n"
  "ds_read_b128 v[104:107], v232 offset:2048\n"
  "v_accvgpr_read_b32 v203, a71\n"
  "v_mfma_f32_16x16x32_f16 a[148:151], v[64:67], v[108:111], a[148:151]\n"
  "s_add_i32 s64, s50, 32768\n"
  "buffer_load_dwordx4 v238, s[40:43], s64 offen sc1 lds\n"
  "v_accvgpr_read_b32 v204, a72\n"
  "v_mfma_f32_16x16x32_f16 a[160:163], v[68:71], v[108:111], a[160:163]\n"
  "s_add_i32 s47, s44, s46\n"
  "v_accvgpr_read_b32 v205, a73\n"
  "v_accvgpr_read_b32 v206, a74\n"
  "v_mfma_f32_16x16x32_f16 a[172:175], v[72:75], v[108:111], a[172:175]\n"
  "s_add_i32 s48, s47, s45\n"
  "v_accvgpr_read_b32 v207, a75\n"
  "v_accvgpr_read_b32 v208, a76\n"
  "v_mfma_f32_16x16x32_f16 a[184:187], v[76:79], v[108:111], a[184:187]\n"
  "s_add_i32 s46, s46, s49\n"
  "ds_read_b128 v[108:111], v232 offset:2560\n"
  "v_accvgpr_read_b32 v209, a77\n"
  "v_mfma_f32_16x16x32_f16 a[152:155], v[80:83], v[120:123], a[152:155]\n"
  "s_add_i32 s50, s50, 4194304\n"
  "v_accvgpr_read_b32 v210, a78\n"
  "v_accvgpr_read_b32 v211, a79\n"
  "v_mfma_f32_16x16x32_f16 a[164:167], v[84:87], v[120:123], a[164:167]\n"
  "v_accvgpr_read_b32 v212, a80\n"
  "v_accvgpr_read_b32 v213, a81\n"
  "v_mfma_f32_16x16x32_f16 a[176:179], v[88:91], v[120:123], a[176:179]\n"
  "v_accvgpr_read_b32 v214, a82\n"
  "v_accvgpr_read_b32 v215, a83\n"
  "v_mfma_f32_16x16x32_f16 a[188:191], v[92:95], v[120:123], a[188:191]\n"
  "v_accvgpr_read_b32 v216, a84\n"
  "v_accvgpr_read_b32 v217, a85\n"
  "v_mfma_f32_16x16x32_f16 a[192:195], v[48:51], v[112:115], a[192:195]\n"
  "v_pk_add_f16 v120, v112, v116\n"
  "v_pk_add_f16 v121, v113, v117\n"
  "v_mfma_f32_16x16x32_f16 a[204:207], v[52:55], v[112:115], a[204:207]\n"
  "v_pk_add_f16 v122, v114, v118\n"
  "v_pk_add_f16 v123, v115, v119\n"
  "v_mfma_f32_16x16x32_f16 a[216:219], v[56:59], v[112:115], a[216:219]\n"
  "v_accvgpr_read_b32 v218, a86\n"
  "v_accvgpr_read_b32 v219, a87\n"
  "v_mfma_f32_16x16x32_f16 a[228:231], v[60:63], v[112:115], a[228:231]\n"
  "v_accvgpr_read_b32 v220, a88\n"
  "v_accvgpr_read_b32 v221, a89\n"
  "v_mfma_f32_16x16x32_f16 a[196:199], v[64:67], v[116:119], a[196:199]\n"
  "v_accvgpr_read_b32 v222, a90\n"
  "v_accvgpr_read_b32 v223, a91\n"
  "v_mfma_f32_16x16x32_f16 a[208:211], v[68:71], v[116:119], a[208:211]\n"
  "v_accvgpr_read_b32 v224, a92\n"
  "v_accvgpr_read_b32 v225, a93\n"
  "v_mfma_f32_16x16x32_f16 a[220:223], v[72:75], v[116:119], a[220:223]\n"
  "v_accvgpr_read_b32 v226, a94\n"
  "v_accvgpr_read_b32 v227, a95\n"
  "v_mfma_f32_16x16x32_f16 a[232:235], v[76:79], v[116:119], a[232:235]\n"
  "v_pk_add_f16 v32, v0, v16\n"
  "v_pk_add_f16 v33, v1, v17\n"
  "v_mfma_f32_16x16x32_f16 a[200:203], v[80:83], v[120:123], a[200:203]\n"
  "v_pk_add_f16 v34, v2, v18\n"
  "v_pk_add_f16 v35, v3, v19\n"
  "s_waitcnt vmcnt(10) lgkmcnt(0)\n"
  "v_mfma_f32_16x16x32_f16 a[212:215], v[84:87], v[120:123], a[212:215]\n"
  "v_pk_add_f16 v36, v4, v20\n"
  "v_pk_add_f16 v37, v5, v21\n"
  "v_mfma_f32_16x16x32_f16 a[224:227], v[88:91], v[120:123], a[224:227]\n"
  "v_pk_add_f16 v38, v6, v22\n"
  "v_pk_add_f16 v39, v7, v23\n"
  "s_barrier\n"
  "v_mfma_f32_16x16x32_f16 a[236:239], v[92:95], v[120:123], a[236:239]\n"
  "v_pk_add_f16 v40, v8, v24\n"
  "v_pk_add_f16 v41, v9, v25\n"
  "v_pk_add_f16 v42, v10, v26\n"
  "v_pk_add_f16 v43, v11, v27\n"
  "v_pk_add_f16 v44, v12, v28\n"
  "v_pk_add_f16 v45, v13, v29\n"
  "v_pk_add_f16 v46, v14, v30\n"
  "v_pk_add_f16 v47, v15, v31\n"
  "v_add_u32 v230, s49, v230\n"
  "v_add_u32 v231, s49, v231\n"
  "v_add_u32 v232, s49, v232\n"
  "s_mov_b32 s63, 2\n"
  ".p2align 6\n"
  ".LloopN_%=:\n"
  "v_mfma_f32_16x16x32_f16 v[124:127], v[0:3], v[96:99], v[124:127]\n"
  "buffer_load_dword v247, v246, s[40:43], s54 offen sc1\n"
  "v_pk_add_f16 v120, v96, v100\n"
  "v_mfma_f32_16x16x32_f16 v[136:139], v[4:7], v[96:99], v[136:139]\n"
  "v_pk_add_f16 v121, v97, v101\n"
  "v_pk_add_f16 v122, v98, v102\n"
  "v_mfma_f32_16x16x32_f16 v[148:151], v[8:11], v[96:99], v[148:151]\n"
  "ds_read_b64_tr_b16 v[48:49], v228 offset:0\n"
  "v_pk_add_f16 v123, v99, v103\n"
  "v_mfma_f32_16x16x32_f16 v[160:163], v[12:15], v[96:99], v[160:163]\n"
  "s_add_i32 m0, s47, 0\n"
  "ds_read_b64_tr_b16 v[50:51], v228 offset:8192\n"
  "ds_read_b128 v[96:99], v232 offset:4096\n"
  "v_mfma_f32_16x16x32_f16 v[128:131], v[16:19], v[100:103], v[128:131]\n"
  "s_add_i32 s64, s50, 4194304\n"
  "buffer_load_dwordx4 v237, s[40:43], s64 offen sc1 lds\n"
  "ds_read_b64_tr_b16 v[64:65], v228 offset:256\n"
  "v_mfma_f32_16x16x32_f16 v[140:143], v[20:23], v[100:103], v[140:143]\n"
  "s_cmp_eq_u32 s46, 73728\n"
  "s_cselect_b32 s49, s68, s69\n"
  "ds_read_b64_tr_b16 v[66:67], v228 offset:8448\n"
  "v_mfma_f32_16x16x32_f16 v[152:155], v[24:27], v[100:103], v[152:155]\n"
  "ds_read_b64_tr_b16 v[52:53], v229 offset:0\n"
  "v_add_u32 v228, s49, v228\n"
  "v_mfma_f32_16x16x32_f16 v[164:167], v[28:31], v[100:103], v[164:167]\n"
  "s_add_i32 m0, s47, 4096\n"
  "ds_read_b128 v[100:103], v232 offset:4608\n"
  "ds_read_b64_tr_b16 v[54:55], v229 offset:8192\n"
  "v_mfma_f32_16x16x32_f16 v[132:135], v[32:35], v[120:123], v[132:135]\n"
  "s_add_i32 s64, s50, 4198400\n"
  "buffer_load_dwordx4 v237, s[40:43], s64 offen sc1 lds\n"
  "ds_read_b64_tr_b16 v[68:69], v229 offset:256\n"
  "v_mfma_f32_16x16x32_f16 v[144:147], v[36:39], v[120:123], v[144:147]\n"
  "ds_read_b64_tr_b16 v[70:71], v229 offset:8448\n"
  "v_add_u32 v229, s49, v229\n"
  "v_mfma_f32_16x16x32_f16 v[156:159], v[40:43], v[120:123], v[156:159]\n"
  "ds_read_b64_tr_b16 v[56:57], v230 offset:0\n"
  "ds_read_b64_tr_b16 v[58:59], v230 offset:8192\n"
  "v_mfma_f32_16x16x32_f16 v[168:171], v[44:47], v[120:123], v[168:171]\n"
  "s_add_i32 m0, s47, 8192\n"
  "ds_read_b64_tr_b16 v[72:73], v230 offset:256\n"
  "ds_read_b64_tr_b16 v[74:75], v230 offset:8448\n"
  "v_mfma_f32_16x16x32_f16 v[180:183], v[0:3], v[104:107], v[180:183]\n"
  "s_add_i32 s64, s50, 4202496\n"
  "buffer_load_dwordx4 v237, s[40:43], s64 offen sc1 lds\n"
  "v_pk_add_f16 v120, v104, v108\n"
  "v_mfma_f32_16x16x32_f16 v[192:195], v[4:7], v[104:107], v[192:195]\n"
  "s_waitcnt lgkmcnt(9)\n"
  "v_pk_add_f16 v121, v105, v109\n"
  "v_pk_add_f16 v122, v106, v110\n"
  "v_mfma_f32_16x16x32_f16 v[204:207], v[8:11], v[104:107], v[204:207]\n"
  "ds_read_b64_tr_b16 v[60:61], v231 offset:0\n"
  "v_pk_add_f16 v123, v107, v111\n"
  "v_mfma_f32_16x16x32_f16 v[216:219], v[12:15], v[104:107], v[216:219]\n"
  "s_add_i32 m0, s47, 12288\n"
  "ds_read_b128 v[104:107], v232 offset:6144\n"
  "v_pk_add_f16 v80, v48, v64\n"
  "v_mfma_f32_16x16x32_f16 v[184:187], v[16:19], v[108:111], v[184:187]\n"
  "s_waitcnt lgkmcnt(6)\n"
  "s_add_i32 s64, s50, 4206592\n"
  "buffer_load_dwordx4 v237, s[40:43], s64 offen sc1 lds\n"
  "v_mfma_f32_16x16x32_f16 v[196:199], v[20:23], v[108:111], v[196:199]\n"
  "ds_read_b64_tr_b16 v[62:63], v231 offset:8192\n"
  "v_pk_add_f16 v81, v49, v65\n"
  "v_mfma_f32_16x16x32_f16 v[208:211], v[24:27], v[108:111], v[208:211]\n"
  "s_waitcnt lgkmcnt(3)\n"
  "ds_read_b128 v[112:115], v232 offset:8192\n"
  "v_pk_add_f16 v82, v50, v66\n"
  "v_mfma_f32_16x16x32_f16 v[220:223], v[28:31], v[108:111], v[220:223]\n"
  "s_add_i32 m0, s47, 16384\n"
  "ds_read_b128 v[108:111], v232 offset:6656\n"
  "v_pk_add_f16 v83, v51, v67\n"
  "v_mfma_f32_16x16x32_f16 v[188:191], v[32:35], v[120:123], v[188:191]\n"
  "s_add_i32 s64, s50, 4194304\n"
  "buffer_load_dwordx4 v238, s[40:43], s64 offen sc1 lds\n"
  "v_pk_add_f16 v84, v52, v68\n"
  "v_mfma_f32_16x16x32_f16 v[200:203], v[36:39], v[120:123], v[200:203]\n"
  "ds_read_b64_tr_b16 v[76:77], v231 offset:256\n"
  "v_pk_add_f16 v85, v53, v69\n"
  "v_mfma_f32_16x16x32_f16 v[212:215], v[40:43], v[120:123], v[212:215]\n"
  "ds_read_b128 v[116:119], v232 offset:8704\n"
  "v_pk_add_f16 v86, v54, v70\n"
  "v_mfma_f32_16x16x32_f16 v[224:227], v[44:47], v[120:123], v[224:227]\n"
  "s_add_i32 m0, s47, 20480\n"
  "ds_read_b64_tr_b16 v[78:79], v231 offset:8448\n"
  "v_pk_add_f16 v87, v55, v71\n"
  "v_mfma_f32_16x16x32_f16 a[96:99], v[0:3], v[96:99], a[96:99]\n"
  "s_add_i32 s64, s50, 4096\n"
  "buffer_load_dwordx4 v238, s[40:43], s64 offen sc1 lds\n"
  "v_pk_add_f16 v120, v96, v100\n"
  "v_mfma_f32_16x16x32_f16 a[108:111], v[4:7], v[96:99], a[108:111]\n"
  "v_pk_add_f16 v121, v97, v101\n"
  "v_pk_add_f16 v122, v98, v102\n"
  "v_mfma_f32_16x16x32_f16 a[120:123], v[8:11], v[96:99], a[120:123]\n"
  "v_pk_add_f16 v123, v99, v103\n"
  "v_pk_add_f16 v88, v56, v72\n"
  "v_mfma_f32_16x16x32_f16 a[132:135], v[12:15], v[96:99], a[132:135]\n"
  "s_add_i32 m0, s48, 24576\n"
  "ds_read_b128 v[96:99], v232 offset:0\n"
  "v_pk_add_f16 v89, v57, v73\n"
  "v_mfma_f32_16x16x32_f16 a[100:103], v[16:19], v[100:103], a[100:103]\n"
  "s_add_i32 s64, s50, 16384\n"
  "buffer_load_dwordx4 v238, s[40:43], s64 offen sc1 lds\n"
  "v_pk_add_f16 v90, v58, v74\n"
  "v_mfma_f32_16x16x32_f16 a[112:115], v[20:23], v[100:103], a[112:115]\n"
  "v_pk_add_f16 v91, v59, v75\n"
  "v_add_u32 v230, s49, v230\n"
  "v_mfma_f32_16x16x32_f16 a[124:127], v[24:27], v[100:103], a[124:127]\n"
  "s_waitcnt lgkmcnt(1)\n"
  "v_pk_add_f16 v92, v60, v76\n"
  "v_pk_add_f16 v93, v61, v77\n"
  "v_mfma_f32_16x16x32_f16 a[136:139], v[28:31], v[100:103], a[136:139]\n"
  "s_add_i32 m0, s47, 26624\n"
  "ds_read_b128 v[100:103], v232 offset:512\n"
  "v_pk_add_f16 v94, v62, v78\n"
  "v_mfma_f32_16x16x32_f16 a[104:107], v[32:35], v[120:123], a[104:107]\n"
  "s_add_i32 s64, s50, 4214784\n"
  "buffer_load_dwordx4 v238, s[40:43], s64 offen sc1 lds\n"
  "v_pk_add_f16 v95, v63, v79\n"
  "v_mfma_f32_16x16x32_f16 a[116:119], v[36:39], v[120:123], a[116:119]\n"
  "v_add_u32 v231, s49, v231\n"
  "v_mfma_f32_16x16x32_f16 a[128:131], v[40:43], v[120:123], a[128:131]\n"
  "v_mfma_f32_16x16x32_f16 a[140:143], v[44:47], v[120:123], a[140:143]\n"
  "s_add_i32 m0, s47, 30720\n"
  "v_mfma_f32_16x16x32_f16 a[144:147], v[0:3], v[104:107], a[144:147]\n"
  "s_add_i32 s64, s50, 32768\n"
  "buffer_load_dwordx4 v238, s[40:43], s64 offen sc1 lds\n"
  "v_pk_add_f16 v120, v104, v108\n"
  "v_mfma_f32_16x16x32_f16 a[156:159], v[4:7], v[104:107], a[156:159]\n"
  "v_pk_add_f16 v121, v105, v109\n"
  "v_pk_add_f16 v122, v106, v110\n"
  "v_mfma_f32_16x16x32_f16 a[168:171], v[8:11], v[104:107], a[168:171]\n"
  "v_pk_add_f16 v123, v107, v111\n"
  "v_mfma_f32_16x16x32_f16 a[180:183], v[12:15], v[104:107], a[180:183]\n"
  "ds_read_b128 v[104:107], v232 offset:2048\n"
  "v_mfma_f32_16x16x32_f16 a[148:151], v[16:19], v[108:111], a[148:151]\n"
  "s_add_i32 s47, s44, s46\n"
  "s_add_i32 s50, s50, 4194304\n"
  "v_mfma_f32_16x16x32_f16 a[160:163], v[20:23], v[108:111], a[160:163]\n"
  "s_add_i32 s48, s47, s45\n"
  "s_add_i32 s46, s46, s49\n"
  "v_mfma_f32_16x16x32_f16 a[172:175], v[24:27], v[108:111], a[172:175]\n"
  "v_mfma_f32_16x16x32_f16 a[184:187], v[28:31], v[108:111], a[184:187]\n"
  "ds_read_b128 v[108:111], v232 offset:2560\n"
  "v_add_u32 v232, s49, v232\n"
  "v_mfma_f32_16x16x32_f16 a[152:155], v[32:35], v[120:123], a[152:155]\n"
  "v_mfma_f32_16x16x32_f16 a[164:167], v[36:39], v[120:123], a[164:167]\n"
  "v_mfma_f32_16x16x32_f16 a[176:179], v[40:43], v[120:123], a[176:179]\n"
  "v_mfma_f32_16x16x32_f16 a[188:191], v[44:47], v[120:123], a[188:191]\n"
  "v_mfma_f32_16x16x32_f16 a[192:195], v[0:3], v[112:115], a[192:195]\n"
  "v_pk_add_f16 v120, v112, v116\n"
  "v_pk_add_f16 v121, v113, v117\n"
  "v_mfma_f32_16x16x32_f16 a[204:207], v[4:7], v[112:115], a[204:207]\n"
  "v_pk_add_f16 v122, v114, v118\n"
  "v_pk_add_f16 v123, v115, v119\n"
  "v_mfma_f32_16x16x32_f16 a[216:219], v[8:11], v[112:115], a[216:219]\n"
  "v_mfma_f32_16x16x32_f16 a[228:231], v[12:15], v[112:115], a[228:231]\n"
  "v_mfma_f32_16x16x32_f16 a[196:199], v[16:19], v[116:119], a[196:199]\n"
  "v_mfma_f32_16x16x32_f16 a[208:211], v[20:23], v[116:119], a[208:211]\n"
  "v_mfma_f32_16x16x32_f16 a[220:223], v[24:27], v[116:119], a[220:223]\n"
  "v_mfma_f32_16x16x32_f16 a[232:235], v[28:31], v[116:119], a[232:235]\n"
  "v_mfma_f32_16x16x32_f16 a[200:203], v[32:35], v[120:123], a[200:203]\n"
  "s_waitcnt vmcnt(9) lgkmcnt(0)\n"
  "v_mfma_f32_16x16x32_f16 a[212:215], v[36:39], v[120:123], a[212:215]\n"
  "v_cmp_eq_u32 vcc, 0, v247\n"
  "s_cbranch_vccnz .Lspin_ne_None_%=\n"
  ".Lspun_ne_None_%=:\n"
  "v_mfma_f32_16x16x32_f16 a[224:227], v[40:43], v[120:123], a[224:227]\n"
  "s_barrier\n"
  "v_mfma_f32_16x16x32_f16 a[236:239], v[44:47], v[120:123], a[236:239]\n"
  "v_mfma_f32_16x16x32_f16 v[124:127], v[48:51], v[96:99], v[124:127]\n"
  "v_pk_add_f16 v120, v96, v100\n"
  "v_pk_add_f16 v121, v97, v101\n"
  "v_mfma_f32_16x16x32_f16 v[136:139], v[52:55], v[96:99], v[136:139]\n"
  "v_pk_add_f16 v122, v98, v102\n"
  "v_pk_add_f16 v123, v99, v103\n"
  "v_mfma_f32_16x16x32_f16 v[148:151], v[56:59], v[96:99], v[148:151]\n"
  "s_and_b32 s65, s51, 0xffffe300\n"
  "ds_read_b64_tr_b16 v[0:1], v228 offset:0\n"
  "ds_read_b64_tr_b16 v[2:3], v228 offset:8192\n"
  "v_mfma_f32_16x16x32_f16 v[160:163], v[60:63], v[96:99], v[160:163]\n"
  "s_add_i32 m0, s47, 0\n"
  "ds_read_b128 v[96:99], v232 offset:4096\n"
  "ds_read_b64_tr_b16 v[16:17], v228 offset:256\n"
  "v_mfma_f32_16x16x32_f16 v[128:131], v[64:67], v[100:103], v[128:131]\n"
  "s_add_i32 s64, s50, 4194304\n"
  "buffer_load_dwordx4 v237, s[40:43], s64 offen sc1 lds\n"
  "ds_read_b64_tr_b16 v[18:19], v228 offset:8448\n"
  "v_mfma_f32_16x16x32_f16 v[140:143], v[68:71], v[100:103], v[140:143]\n"
  "s_cmp_eq_u32 s46, 73728\n"
  "s_cselect_b32 s49, s68, s69\n"
  "ds_read_b64_tr_b16 v[4:5], v229 offset:0\n"
  "v_mfma_f32_16x16x32_f16 v[152:155], v[72:75], v[100:103], v[152:155]\n"
  "ds_read_b64_tr_b16 v[6:7], v229 offset:8192\n"
  "v_add_u32 v228, s49, v228\n"
  "v_mfma_f32_16x16x32_f16 v[164:167], v[76:79], v[100:103], v[164:167]\n"
  "ds_read_b128 v[100:103], v232 offset:4608\n"
  "ds_read_b64_tr_b16 v[20:21], v229 offset:256\n"
  "v_mfma_f32_16x16x32_f16 v[132:135], v[80:83], v[120:123], v[132:135]\n"
  "buffer_store_dword v232, v245, s[40:43], s65 offen sc1\n"
  "ds_read_b64_tr_b16 v[22:23], v229 offset:8448\n"
  "v_mfma_f32_16x16x32_f16 v[144:147], v[84:87], v[120:123], v[144:147]\n"
  "ds_read_b64_tr_b16 v[8:9], v230 offset:0\n"
  "v_add_u32 v229, s49, v229\n"
  "v_mfma_f32_16x16x32_f16 v[156:159], v[88:91], v[120:123], v[156:159]\n"
  "s_add_i32 s53, s53, 8192\n"
  "s_lshr_b32 s52, s52, 1\n"
  "ds_read_b64_tr_b16 v[10:11], v230 offset:8192\n"
  "v_mfma_f32_16x16x32_f16 v[168:171], v[92:95], v[120:123], v[168:171]\n"
  "s_add_i32 m0, s47, 4096\n"
  "s_waitcnt lgkmcnt(7)\n"
  "ds_read_b64_tr_b16 v[24:25], v230 offset:256\n"
  "v_mfma_f32_16x16x32_f16 v[180:183], v[48:51], v[104:107], v[180:183]\n"
  "s_add_i32 s64, s50, 4198400\n"
  "buffer_load_dwordx4 v237, s[40:43], s64 offen sc1 lds\n"
  "v_pk_add_f16 v120, v104, v108\n"
  "v_mfma_f32_16x16x32_f16 v[192:195], v[52:55], v[104:107], v[192:195]\n"
  "s_add_i32 s51, s51, 8448\n"
  "v_pk_add_f16 v121, v105, v109\n"
  "v_pk_add_f16 v122, v106, v110\n"
  "v_mfma_f32_16x16x32_f16 v[204:207], v[56:59], v[104:107], v[204:207]\n"
  "ds_read_b64_tr_b16 v[26:27], v230 offset:8448\n"
  "v_pk_add_f16 v123, v107, v111\n"
  "v_mfma_f32_16x16x32_f16 v[216:219], v[60:63], v[104:107], v[216:219]\n"
  "s_add_i32 m0, s47, 8192\n"
  "s_waitcnt lgkmcnt(4)\n"
  "ds_read_b128 v[104:107], v232 offset:6144\n"
  "v_mfma_f32_16x16x32_f16 v[184:187], v[64:67], v[108:111], v[184:187]\n"
  "s_add_i32 s64, s50, 4202496\n"
  "buffer_load_dwordx4 v237, s[40:43], s64 offen sc1 lds\n"
  "v_pk_add_f16 v32, v0, v16\n"
  "v_mfma_f32_16x16x32_f16 v[196:199], v[68:71], v[108:111], v[196:199]\n"
  "s_bitcmp1_b32 s52, 0\n"
  "s_cselect_b32 s54, s53, 134217728\n"
  "ds_read_b64_tr_b16 v[12:13], v231 offset:0\n"
  "v_mfma_f32_16x16x32_f16 v[208:211], v[72:75], v[108:111], v[208:211]\n"
  "ds_read_b64_tr_b16 v[14:15], v231 offset:8192\n"
  "v_pk_add_f16 v33, v1, v17\n"
  "v_mfma_f32_16x16x32_f16 v[220:223], v[76:79], v[108:111], v[220:223]\n"
  "s_add_i32 m0, s47, 12288\n"
  "ds_read_b128 v[108:111], v232 offset:6656\n"
  "v_pk_add_f16 v34, v2, v18\n"
  "v_mfma_f32_16x16x32_f16 v[188:191], v[80:83], v[120:123], v[188:191]\n"
  "s_add_i32 s64, s50, 4206592\n"
  "buffer_load_dwordx4 v237, s[40:43], s64 offen sc1 lds\n"
  "v_pk_add_f16 v35, v3, v19\n"
  "v_mfma_f32_16x16x32_f16 v[200:203], v[84:87], v[120:123], v[200:203]\n"
  "s_waitcnt lgkmcnt(4)\n"
  "ds_read_b128 v[112:115], v232 offset:8192\n"
  "v_mfma_f32_16x16x32_f16 v[212:215], v[88:91], v[120:123], v[212:215]\n"
  "ds_read_b64_tr_b16 v[28:29], v231 offset:256\n"
  "v_pk_add_f16 v36, v4, v20\n"
  "v_mfma_f32_16x16x32_f16 v[224:227], v[92:95], v[120:123], v[224:227]\n"
  "s_add_i32 m0, s47, 16384\n"
  "ds_read_b128 v[116:119], v232 offset:8704\n"
  "v_pk_add_f16 v37, v5, v21\n"
  "v_mfma_f32_16x16x32_f16 a[96:99], v[48:51], v[96:99], a[96:99]\n"
  "s_add_i32 s64, s50, 4194304\n"
  "buffer_load_dwordx4 v238, s[40:43], s64 offen sc1 lds\n"
  "v_pk_add_f16 v120, v96, v100\n"
  "v_mfma_f32_16x16x32_f16 a[108:111], v[52:55], v[96:99], a[108:111]\n"
  "v_pk_add_f16 v121, v97, v101\n"
  "v_pk_add_f16 v122, v98, v102\n"
  "v_mfma_f32_16x16x32_f16 a[120:123], v[56:59], v[96:99], a[120:123]\n"
  "ds_read_b64_tr_b16 v[30:31], v231 offset:8448\n"
  "v_pk_add_f16 v123, v99, v103\n"
  "v_mfma_f32_16x16x32_f16 a[132:135], v[60:63], v[96:99], a[132:135]\n"
  "s_add_i32 m0, s47, 20480\n"
  "ds_read_b128 v[96:99], v232 offset:0\n"
  "v_pk_add_f16 v38, v6, v22\n"
  "v_mfma_f32_16x16x32_f16 a[100:103], v[64:67], v[100:103], a[100:103]\n"
  "s_add_i32 s64, s50, 4096\n"
  "buffer_load_dwordx4 v238, s[40:43], s64 offen sc1 lds\n"
  "v_pk_add_f16 v39, v7, v23\n"
  "v_mfma_f32_16x16x32_f16 a[112:115], v[68:71], v[100:103], a[112:115]\n"
  "v_pk_add_f16 v40, v8, v24\n"
  "v_pk_add_f16 v41, v9, v25\n"
  "v_mfma_f32_16x16x32_f16 a[124:127], v[72:75], v[100:103], a[124:127]\n"
  "v_pk_add_f16 v42, v10, v26\n"
  "v_pk_add_f16 v43, v11, v27\n"
  "v_mfma_f32_16x16x32_f16 a[136:139], v[76:79], v[100:103], a[136:139]\n"
  "s_add_i32 m0, s48, 24576\n"
  "ds_read_b128 v[100:103], v232 offset:512\n"
  "v_add_u32 v230, s49, v230\n"
  "v_mfma_f32_16x16x32_f16 a[104:107], v[80:83], v[120:123], a[104:107]\n"
  "s_add_i32 s64, s50, 16384\n"
  "buffer_load_dwordx4 v238, s[40:43], s64 offen sc1 lds\n"
  "v_add_u32 v231, s49, v231\n"
  "v_mfma_f32_16x16x32_f16 a[116:119], v[84:87], v[120:123], a[116:119]\n"
  "s_waitcnt lgkmcnt(6)\n"
  "s_waitcnt lgkmcnt(2)\n"
  "v_pk_add_f16 v44, v12, v28\n"
  "v_mfma_f32_16x16x32_f16 a[128:131], v[88:91], v[120:123], a[128:131]\n"
  "v_pk_add_f16 v45, v13, v29\n"
  "v_pk_add_f16 v46, v14, v30\n"
  "v_mfma_f32_16x16x32_f16 a[140:143], v[92:95], v[120:123], a[140:143]\n"
  "s_add_i32 m0, s47, 26624\n"
  "v_pk_add_f16 v47, v15, v31\n"
  "v_mfma_f32_16x16x32_f16 a[144:147], v[48:51], v[104:107], a[144:147]\n"
  "s_add_i32 s64, s50, 4214784\n"
  "buffer_load_dwordx4 v238, s[40:43], s64 offen sc1 lds\n"
  "v_pk_add_f16 v120, v104, v108\n"
  "v_mfma_f32_16x16x32_f16 a[156:159], v[52:55], v[104:107], a[156:159]\n"
  "v_pk_add_f16 v121, v105, v109\n"
  "v_pk_add_f16 v122, v106, v110\n"
  "v_mfma_f32_16x16x32_f16 a[168:171], v[56:59], v[104:107], a[168:171]\n"
  "v_pk_add_f16 v123, v107, v111\n"
  "v_mfma_f32_16x16x32_f16 a[180:183], v[60:63], v[104:107], a[180:183]\n"
  "s_add_i32 m0, s47, 30720\n"
  "ds_read_b128 v[104:107], v232 offset:2048\n"
  "v_mfma_f32_16x16x32_f16 a[148:151], v[64:67], v[108:111], a[148:151]\n"
  "s_add_i32 s64, s50, 32768\n"
  "buffer_load_dwordx4 v238, s[40:43], s64 offen sc1 lds\n"
  "v_mfma_f32_16x16x32_f16 a[160:163], v[68:71], v[108:111], a[160:163]\n"
  "s_add_i32 s47, s44, s46\n"
  "s_add_i32 s50, s50, 4194304\n"
  "v_mfma_f32_16x16x32_f16 a[172:175], v[72:75], v[108:111], a[172:175]\n"
  "s_add_i32 s48, s47, s45\n"
  "s_add_i32 s46, s46, s49\n"
  "v_mfma_f32_16x16x32_f16 a[184:187], v[76:79], v[108:111], a[184:187]\n"
  "ds_read_b128 v[108:111], v232 offset:2560\n"
  "v_add_u32 v232, s49, v232\n"
  "v_mfma_f32_16x16x32_f16 a[152:155], v[80:83], v[120:123], a[152:155]\n"
  "v_mfma_f32_16x16x32_f16 a[164:167], v[84:87], v[120:123], a[164:167]\n"
  "v_mfma_f32_16x16x32_f16 a[176:179], v[88:91], v[120:123], a[176:179]\n"
  "v_mfma_f32_16x16x32_f16 a[188:191], v[92:95], v[120:123], a[188:191]\n"
  "v_mfma_f32_16x16x32_f16 a[192:195], v[48:51], v[112:115], a[192:195]\n"
  "v_pk_add_f16 v120, v112, v116\n"
  "v_pk_add_f16 v121, v113, v117\n"
  "v_mfma_f32_16x16x32_f16 a[204:207], v[52:55], v[112:115], a[204:207]\n"
  "v_pk_add_f16 v122, v114, v118\n"
  "v_pk_add_f16 v123, v115, v119\n"
  "v_mfma_f32_16x16x32_f16 a[216:219], v[56:59], v[112:115], a[216:219]\n"
  "v_mfma_f32_16x16x32_f16 a[228:231], v[60:63], v[112:115], a[228:231]\n"
  "v_mfma_f32_16x16x32_f16 a[196:199], v[64:67], v[116:119], a[196:199]\n"
  "v_mfma_f32_16x16x32_f16 a[208:211], v[68:71], v[116:119], a[208:211]\n"
  "v_mfma_f32_16x16x32_f16 a[220:223], v[72:75], v[116:119], a[220:223]\n"
  "v_mfma_f32_16x16x32_f16 a[232:235], v[76:79], v[116:119], a[232:235]\n"
  "v_mfma_f32_16x16x32_f16 a[200:203], v[80:83], v[120:123], a[200:203]\n"
  "s_waitcnt vmcnt(10) lgkmcnt(0)\n"
  "v_mfma_f32_16x16x32_f16 a[212:215], v[84:87], v[120:123], a[212:215]\n"
  "v_mfma_f32_16x16x32_f16 a[224:227], v[88:91], v[120:123], a[224:227]\n"
  "s_barrier\n"
  "v_mfma_f32_16x16x32_f16 a[236:239], v[92:95], v[120:123], a[236:239]\n"
  "s_sub_i32 s63, s63, 1\n"
  "s_cmp_lg_u32 s63, 0\n"
  "s_cbranch_scc1 .LloopN_%=\n"
  "v_mfma_f32_16x16x32_f16 v[124:127], v[0:3], v[96:99], v[124:127]\n"
  "s_add_i32 s67, s44, s45\n"
  "buffer_load_dword v247, v246, s[40:43], s54 offen sc1\n"
  "v_pk_add_f16 v120, v96, v100\n"
  "v_mfma_f32_16x16x32_f16 v[136:139], v[4:7], v[96:99], v[136:139]\n"
  "v_pk_add_f16 v121, v97, v101\n"
  "v_pk_add_f16 v122, v98, v102\n"
  "v_mfma_f32_16x16x32_f16 v[148:151], v[8:11], v[96:99], v[148:151]\n"
  "ds_read_b64_tr_b16 v[48:49], v228 offset:0\n"
  "v_pk_add_f16 v123, v99, v103\n"
  "v_mfma_f32_16x16x32_f16 v[160:163], v[12:15], v[96:99], v[160:163]\n"
  "s_add_i32 m0, s47, 0\n"
  "ds_read_b64_tr_b16 v[50:51], v228 offset:8192\n"
  "ds_read_b128 v[96:99], v232 offset:4096\n"
  "v_mfma_f32_16x16x32_f16 v[128:131], v[16:19], v[100:103], v[128:131]\n"
  "s_add_i32 s64, s50, 4194304\n"
  "buffer_load_dwordx4 v237, s[40:43], s64 offen sc1 lds\n"
  "ds_read_b64_tr_b16 v[64:65], v228 offset:256\n"
  "v_mfma_f32_16x16x32_f16 v[140:143], v[20:23], v[100:103], v[140:143]\n"
  "s_cmp_eq_u32 s46, 73728\n"
  "s_cselect_b32 s49, s68, s69\n"
  "ds_read_b64_tr_b16 v[66:67], v228 offset:8448\n"
  "v_mfma_f32_16x16x32_f16 v[152:155], v[24:27], v[100:103], v[152:155]\n"
  "ds_read_b64_tr_b16 v[52:53], v229 offset:0\n"
  "v_add_u32 v228, s49, v228\n"
  "v_mfma_f32_16x16x32_f16 v[164:167], v[28:31], v[100:103], v[164:167]\n"
  "s_add_i32 m0, s47, 4096\n"
  "ds_read_b128 v[100:103], v232 offset:4608\n"
  "ds_read_b64_tr_b16 v[54:55], v229 offset:8192\n"
  "v_mfma_f32_16x16x32_f16 v[132:135], v[32:35], v[120:123], v[132:135]\n"
  "s_add_i32 s64, s50, 4198400\n"
  "buffer_load_dwordx4 v237, s[40:43], s64 offen sc1 lds\n"
  "ds_read_b64_tr_b16 v[68:69], v229 offset:256\n"
  "v_mfma_f32_16x16x32_f16 v[144:147], v[36:39], v[120:123], v[144:147]\n"
  "ds_read_b64_tr_b16 v[70:71], v229 offset:8448\n"
  "v_add_u32 v229, s49, v229\n"
  "v_mfma_f32_16x16x32_f16 v[156:159], v[40:43], v[120:123], v[156:159]\n"
  "ds_read_b64_tr_b16 v[56:57], v230 offset:0\n"
  "ds_read_b64_tr_b16 v[58:59], v230 offset:8192\n"
  "v_mfma_f32_16x16x32_f16 v[168:171], v[44:47], v[120:123], v[168:171]\n"
  "s_add_i32 m0, s47, 8192\n"
  "ds_read_b64_tr_b16 v[72:73], v230 offset:256\n"
  "ds_read_b64_tr_b16 v[74:75], v230 offset:8448\n"
  "v_mfma_f32_16x16x32_f16 v[180:183], v[0:3], v[104:107], v[180:183]\n"
  "s_add_i32 s64, s50, 4202496\n"
  "buffer_load_dwordx4 v237, s[40:43], s64 offen sc1 lds\n"
  "v_pk_add_f16 v120, v104, v108\n"
  "v_mfma_f32_16x16x32_f16 v[192:195], v[4:7], v[104:107], v[192:195]\n"
  "s_waitcnt lgkmcnt(9)\n"
  "v_pk_add_f16 v121, v105, v109\n"
  "v_pk_add_f16 v122, v106, v110\n"
  "v_mfma_f32_16x16x32_f16 v[204:207], v[8:11], v[104:107], v[204:207]\n"
  "ds_read_b64_tr_b16 v[60:61], v231 offset:0\n"
  "v_pk_add_f16 v123, v107, v111\n"
  "v_mfma_f32_16x16x32_f16 v[216:219], v[12:15], v[104:107], v[216:219]\n"
  "s_add_i32 m0, s47, 12288\n"
  "ds_read_b128 v[104:107], v232 offset:6144\n"
  "v_pk_add_f16 v80, v48, v64\n"
  "v_mfma_f32_16x16x32_f16 v[184:187], v[16:19], v[108:111], v[184:187]\n"
  "s_waitcnt lgkmcnt(6)\n"
  "s_add_i32 s64, s50, 4206592\n"
  "buffer_load_dwordx4 v237, s[40:43], s64 offen sc1 lds\n"
  "v_mfma_f32_16x16x32_f16 v[196:199], v[20:23], v[108:111], v[196:199]\n"
  "ds_read_b64_tr_b16 v[62:63], v231 offset:8192\n"
  "v_pk_add_f16 v81, v49, v65\n"
  "v_mfma_f32_16x16x32_f16 v[208:211], v[24:27], v[108:111], v[208:211]\n"
  "s_waitcnt lgkmcnt(3)\n"
  "ds_read_b128 v[112:115], v232 offset:8192\n"
  "v_pk_add_f16 v82, v50, v66\n"
  "v_mfma_f32_16x16x32_f16 v[220:223], v[28:31], v[108:111], v[220:223]\n"
  "s_add_i32 m0, s47, 16384\n"
  "ds_read_b128 v[108:111], v232 offset:6656\n"
  "v_pk_add_f16 v83, v51, v67\n"
  "v_mfma_f32_16x16x32_f16 v[188:191], v[32:35], v[120:123], v[188:191]\n"
  "s_add_i32 s64, s50, 4194304\n"
  "buffer_load_dwordx4 v238, s[40:43], s64 offen sc1 lds\n"
  "v_pk_add_f16 v84, v52, v68\n"
  "v_mfma_f32_16x16x32_f16 v[200:203], v[36:39], v[120:123], v[200:203]\n"
  "ds_read_b64_tr_b16 v[76:77], v231 offset:256\n"
  "v_pk_add_f16 v85, v53, v69\n"
  "v_mfma_f32_16x16x32_f16 v[212:215], v[40:43], v[120:123], v[212:215]\n"
  "ds_read_b128 v[116:119], v232 offset:8704\n"
  "v_pk_add_f16 v86, v54, v70\n"
  "v_mfma_f32_16x16x32_f16 v[224:227], v[44:47], v[120:123], v[224:227]\n"
  "s_add_i32 m0, s47, 20480\n"
  "ds_read_b64_tr_b16 v[78:79], v231 offset:8448\n"
  "v_pk_add_f16 v87, v55, v71\n"
  "v_mfma_f32_16x16x32_f16 a[96:99], v[0:3], v[96:99], a[96:99]\n"
  "s_add_i32 s64, s50, 4096\n"
  "buffer_load_dwordx4 v238, s[40:43], s64 offen sc1 lds\n"
  "v_pk_add_f16 v120, v96, v100\n"
  "v_mfma_f32_16x16x32_f16 a[108:111], v[4:7], v[96:99], a[108:111]\n"
  "v_pk_add_f16 v121, v97, v101\n"
  "v_pk_add_f16 v122, v98, v102\n"
  "v_mfma_f32_16x16x32_f16 a[120:123], v[8:11], v[96:99], a[120:123]\n"
  "ds_write_b128 v253, v[172:175]\n"
  "v_pk_add_f16 v123, v99, v103\n"
  "v_mfma_f32_16x16x32_f16 a[132:135], v[12:15], v[96:99], a[132:135]\n"
  "s_add_i32 m0, s48, 24576\n"
  "ds_read_b128 v[96:99], v232 offset:0\n"
  "v_pk_add_f16 v88, v56, v72\n"
  "v_mfma_f32_16x16x32_f16 a[100:103], v[16:19], v[100:103], a[100:103]\n"
  "s_add_i32 s64, s50, 16384\n"
  "buffer_load_dwordx4 v238, s[40:43], s64 offen sc1 lds\n"
  "v_pk_add_f16 v89, v57, v73\n"
  "v_mfma_f32_16x16x32_f16 a[112:115], v[20:23], v[100:103], a[112:115]\n"
  "v_pk_add_f16 v90, v58, v74\n"
  "v_pk_add_f16 v91, v59, v75\n"
  "v_mfma_f32_16x16x32_f16 a[124:127], v[24:27], v[100:103], a[124:127]\n"
  "s_waitcnt lgkmcnt(2)\n"
  "v_pk_add_f16 v92, v60, v76\n"
  "v_pk_add_f16 v93, v61, v77\n"
  "v_mfma_f32_16x16x32_f16 a[136:139], v[28:31], v[100:103], a[136:139]\n"
  "s_add_i32 m0, s47, 26624\n"
  "ds_read_b128 v[100:103], v232 offset:512\n"
  "v_pk_add_f16 v94, v62, v78\n"
  "v_mfma_f32_16x16x32_f16 a[104:107], v[32:35], v[120:123], a[104:107]\n"
  "s_add_i32 s64, s50, 4214784\n"
  "buffer_load_dwordx4 v238, s[40:43], s64 offen sc1 lds\n"
  "v_pk_add_f16 v95, v63, v79\n"
  "v_mfma_f32_16x16x32_f16 a[116:119], v[36:39], v[120:123], a[116:119]\n"
  "v_add_u32 v230, s49, v230\n"
  "v_add_u32 v231, s49, v231\n"
  "v_mfma_f32_16x16x32_f16 a[128:131], v[40:43], v[120:123], a[128:131]\n"
  "v_mfma_f32_16x16x32_f16 a[140:143], v[44:47], v[120:123], a[140:143]\n"
  "s_add_i32 m0, s47, 30720\n"
  "v_mfma_f32_16x16x32_f16 a[144:147], v[0:3], v[104:107], a[144:147]\n"
  "s_add_i32 s64, s50, 32768\n"
  "buffer_load_dwordx4 v238, s[40:43], s64 offen sc1 lds\n"
  "v_pk_add_f16 v120, v104, v108\n"
  "v_mfma_f32_16x16x32_f16 a[156:159], v[4:7], v[104:107], a[156:159]\n"
  "v_pk_add_f16 v121, v105, v109\n"
  "v_pk_add_f16 v122, v106, v110\n"
  "v_mfma_f32_16x16x32_f16 a[168:171], v[8:11], v[104:107], a[168:171]\n"
  "v_pk_add_f16 v123, v107, v111\n"
  "v_mfma_f32_16x16x32_f16 a[180:183], v[12:15], v[104:107], a[180:183]\n"
  "s_add_i32 m0, s44, 114688\n"
  "ds_read_b128 v[104:107], v232 offset:2048\n"
  "v_mfma_f32_16x16x32_f16 a[148:151], v[16:19], v[108:111], a[148:151]\n"
  "s_add_i32 s47, s44, s46\n"
  "s_add_i32 s64, s50, 4198400\n"
  "buffer_load_dwordx4 v238, s[40:43], s64 offen sc1 lds\n"
  "v_mfma_f32_16x16x32_f16 a[160:163], v[20:23], v[108:111], a[160:163]\n"
  "s_add_i32 s48, s47, s45\n"
  "s_add_i32 s46, s46, s49\n"
  "v_mfma_f32_16x16x32_f16 a[172:175], v[24:27], v[108:111], a[172:175]\n"
  "v_mfma_f32_16x16x32_f16 a[184:187], v[28:31], v[108:111], a[184:187]\n"
  "s_add_i32 m0, s67, 118784\n"
  "ds_read_b128 v[108:111], v232 offset:2560\n"
  "v_add_u32 v232, s49, v232\n"
  "v_mfma_f32_16x16x32_f16 a[152:155], v[32:35], v[120:123], a[152:155]\n"
  "s_add_i32 s64, s50, 4210688\n"
  "buffer_load_dwordx4 v238, s[40:43], s64 offen sc1 lds\n"
  "v_mfma_f32_16x16x32_f16 a[164:167], v[36:39], v[120:123], a[164:167]\n"
  "v_mfma_f32_16x16x32_f16 a[176:179], v[40:43], v[120:123], a[176:179]\n"
  "v_mfma_f32_16x16x32_f16 a[188:191], v[44:47], v[120:123], a[188:191]\n"
  "s_add_i32 m0, s44, 124928\n"
  "v_mfma_f32_16x16x32_f16 a[192:195], v[0:3], v[112:115], a[192:195]\n"
  "s_add_i32 s64, s50, 4227072\n"
  "buffer_load_dwordx4 v238, s[40:43], s64 offen sc1 lds\n"
  "v_pk_add_f16 v120, v112, v116\n"
  "v_mfma_f32_16x16x32_f16 a[204:207], v[4:7], v[112:115], a[204:207]\n"
  "s_add_i32 s50, s50, 4194304\n"
  "v_pk_add_f16 v121, v113, v117\n"
  "v_pk_add_f16 v122, v114, v118\n"
  "v_mfma_f32_16x16x32_f16 a[216:219], v[8:11], v[112:115], a[216:219]\n"
  "v_pk_add_f16 v123, v115, v119\n"
  "v_mfma_f32_16x16x32_f16 a[228:231], v[12:15], v[112:115], a[228:231]\n"
  "v_mfma_f32_16x16x32_f16 a[196:199], v[16:19], v[116:119], a[196:199]\n"
  "v_mfma_f32_16x16x32_f16 a[208:211], v[20:23], v[116:119], a[208:211]\n"
  "v_mfma_f32_16x16x32_f16 a[220:223], v[24:27], v[116:119], a[220:223]\n"
  "v_mfma_f32_16x16x32_f16 a[232:235], v[28:31], v[116:119], a[232:235]\n"
  "v_mfma_f32_16x16x32_f16 a[200:203], v[32:35], v[120:123], a[200:203]\n"
  "s_waitcnt vmcnt(12) lgkmcnt(0)\n"
  "v_mfma_f32_16x16x32_f16 a[212:215], v[36:39], v[120:123], a[212:215]\n"
  "v_cmp_eq_u32 vcc, 0, v247\n"
  "s_cbranch_vccnz .Lspin_s28_None_%=\n"
  ".Lspun_s28_None_%=:\n"
  "v_mfma_f32_16x16x32_f16 a[224:227], v[40:43], v[120:123], a[224:227]\n"
  "s_barrier\n"
  "v_mfma_f32_16x16x32_f16 a[236:239], v[44:47], v[120:123], a[236:239]\n"
  "v_mfma_f32_16x16x32_f16 v[124:127], v[48:51], v[96:99], v[124:127]\n"
  "v_mbcnt_lo_u32_b32 v252, -1, 0\n"
  "v_mbcnt_hi_u32_b32 v252, -1, v252\n"
  "v_mfma_f32_16x16x32_f16 v[136:139], v[52:55], v[96:99], v[136:139]\n"
  "v_pk_add_f16 v120, v96, v100\n"
  "v_pk_add_f16 v121, v97, v101\n"
  "v_mfma_f32_16x16x32_f16 v[148:151], v[56:59], v[96:99], v[148:151]\n"
  "v_lshlrev_b32 v252, 7, v252\n"
  "v_pk_add_f16 v122, v98, v102\n"
  "v_mfma_f32_16x16x32_f16 v[160:163], v[60:63], v[96:99], v[160:163]\n"
  "s_mov_b32 m0, 147456\n"
  "v_pk_add_f16 v123, v99, v103\n"
  "v_add_u32 v252, s45, v252\n"
  "v_mfma_f32_16x16x32_f16 v[128:131], v[64:67], v[100:103], v[128:131]\n"
  "s_cmp_eq_u32 s46, 73728\n"
  "s_cselect_b32 s49, s68, s69\n"
  "global_load_lds_dword v[248:249], off\n"
  "v_mfma_f32_16x16x32_f16 v[140:143], v[68:71], v[100:103], v[140:143]\n"
  "ds_read_b64_tr_b16 v[0:1], v228 offset:0\n"
  "ds_read_b64_tr_b16 v[2:3], v228 offset:8192\n"
  "v_mfma_f32_16x16x32_f16 v[152:155], v[72:75], v[100:103], v[152:155]\n"
  "ds_read_b128 v[96:99], v232 offset:4096\n"
  "ds_read_b64_tr_b16 v[16:17], v228 offset:256\n"
  "v_mfma_f32_16x16x32_f16 v[164:167], v[76:79], v[100:103], v[164:167]\n"
  "ds_read_b128 v[100:103], v232 offset:4608\n"
  "ds_read_b64_tr_b16 v[18:19], v228 offset:8448\n"
  "v_mfma_f32_16x16x32_f16 v[132:135], v[80:83], v[120:123], v[132:135]\n"
  "ds_read_b64_tr_b16 v[4:5], v229 offset:0\n"
  "v_add_u32 v228, s49, v228\n"
  "v_mfma_f32_16x16x32_f16 v[144:147], v[84:87], v[120:123], v[144:147]\n"
  "ds_read_b64_tr_b16 v[6:7], v229 offset:8192\n"
  "ds_read_b64_tr_b16 v[20:21], v229 offset:256\n"
  "v_mfma_f32_16x16x32_f16 v[156:159], v[88:91], v[120:123], v[156:159]\n"
  "ds_read_b64_tr_b16 v[22:23], v229 offset:8448\n"
  "v_add_u32 v229, s49, v229\n"
  "v_mfma_f32_16x16x32_f16 v[168:171], v[92:95], v[120:123], v[168:171]\n"
  "s_mov_b32 m0, 144384\n"
  "ds_read_b64_tr_b16 v[8:9], v230 offset:0\n"
  "ds_read_b64_tr_b16 v[10:11], v230 offset:8192\n"
  "v_mfma_f32_16x16x32_f16 v[180:183], v[48:51], v[104:107], v[180:183]\n"
  "global_load_lds_dword v252, s[88:89]\n"
  "v_pk_add_f16 v120, v104, v108\n"
  "v_mfma_f32_16x16x32_f16 v[192:195], v[52:55], v[104:107], v[192:195]\n"
  "v_pk_add_f16 v121, v105, v109\n"
  "v_pk_add_f16 v122, v106, v110\n"
  "v_mfma_f32_16x16x32_f16 v[204:207], v[56:59], v[104:107], v[204:207]\n"
  "s_waitcnt lgkmcnt(6)\n"
  "ds_read_b64_tr_b16 v[24:25], v230 offset:256\n"
  "v_pk_add_f16 v123, v107, v111\n"
  "v_mfma_f32_16x16x32_f16 v[216:219], v[60:63], v[104:107], v[216:219]\n"
  "ds_read_b128 v[104:107], v232 offset:6144\n"
  "v_pk_add_f16 v32, v0, v16\n"
  "v_mfma_f32_16x16x32_f16 v[184:187], v[64:67], v[108:111], v[184:187]\n"
  "ds_read_b64_tr_b16 v[26:27], v230 offset:8448\n"
  "v_pk_add_f16 v33, v1, v17\n"
  "v_mfma_f32_16x16x32_f16 v[196:199], v[68:71], v[108:111], v[196:199]\n"
  "s_waitcnt lgkmcnt(5)\n"
  "ds_read_b64_tr_b16 v[12:13], v231 offset:0\n"
  "v_pk_add_f16 v34, v2, v18\n"
  "v_mfma_f32_16x16x32_f16 v[208:211], v[72:75], v[108:111], v[208:211]\n"
  "ds_read_b64_tr_b16 v[14:15], v231 offset:8192\n"
  "v_pk_add_f16 v35, v3, v19\n"
  "v_mfma_f32_16x16x32_f16 v[220:223], v[76:79], v[108:111], v[220:223]\n"
  "ds_read_b128 v[108:111], v232 offset:6656\n"
  "v_pk_add_f16 v36, v4, v20\n"
  "v_mfma_f32_16x16x32_f16 v[188:191], v[80:83], v[120:123], v[188:191]\n"
  "ds_read_b128 v[112:115], v232 offset:8192\n"
  "v_pk_add_f16 v37, v5, v21\n"
  "v_mfma_f32_16x16x32_f16 v[200:203], v[84:87], v[120:123], v[200:203]\n"
  "ds_read_b64_tr_b16 v[28:29], v231 offset:256\n"
  "v_pk_add_f16 v38, v6, v22\n"
  "v_mfma_f32_16x16x32_f16 v[212:215], v[88:91], v[120:123], v[212:215]\n"
  "ds_read_b128 v[116:119], v232 offset:8704\n"
  "v_pk_add_f16 v39, v7, v23\n"
  "v_mfma_f32_16x16x32_f16 v[224:227], v[92:95], v[120:123], v[224:227]\n"
  "s_waitcnt lgkmcnt(6)\n"
  "ds_read_b64_tr_b16 v[30:31], v231 offset:8448\n"
  "v_pk_add_f16 v40, v8, v24\n"
  "v_mfma_f32_16x16x32_f16 a[96:99], v[48:51], v[96:99], a[96:99]\n"
  "v_pk_add_f16 v120, v96, v100\n"
  "v_pk_add_f16 v121, v97, v101\n"
  "v_mfma_f32_16x16x32_f16 a[108:111], v[52:55], v[96:99], a[108:111]\n"
  "v_pk_add_f16 v122, v98, v102\n"
  "v_pk_add_f16 v123, v99, v103\n"
  "v_mfma_f32_16x16x32_f16 a[120:123], v[56:59], v[96:99], a[120:123]\n"
  "v_pk_add_f16 v41, v9, v25\n"
  "v_pk_add_f16 v42, v10, v26\n"
  "v_mfma_f32_16x16x32_f16 a[132:135], v[60:63], v[96:99], a[132:135]\n"
  "ds_read_b128 v[96:99], v232 offset:0\n"
  "v_pk_add_f16 v43, v11, v27\n"
  "v_mfma_f32_16x16x32_f16 a[100:103], v[64:67], v[100:103], a[100:103]\n"
  "v_add_u32 v230, s49, v230\n"
  "v_add_u32 v231, s49, v231\n"
  "v_mfma_f32_16x16x32_f16 a[112:115], v[68:71], v[100:103], a[112:115]\n"
  "v_mfma_f32_16x16x32_f16 a[124:127], v[72:75], v[100:103], a[124:127]\n"
  "s_waitcnt lgkmcnt(1)\n"
  "v_pk_add_f16 v44, v12, v28\n"
  "v_pk_add_f16 v45, v13, v29\n"
  "v_mfma_f32_16x16x32_f16 a[136:139], v[76:79], v[100:103], a[136:139]\n"
  "ds_read_b128 v[100:103], v232 offset:512\n"
  "v_pk_add_f16 v46, v14, v30\n"
  "v_mfma_f32_16x16x32_f16 a[104:107], v[80:83], v[120:123], a[104:107]\n"
  "v_pk_add_f16 v47, v15, v31\n"
  "v_mfma_f32_16x16x32_f16 a[116:119], v[84:87], v[120:123], a[116:119]\n"
  "v_mfma_f32_16x16x32_f16 a[128:131], v[88:91], v[120:123], a[128:131]\n"
  "v_mfma_f32_16x16x32_f16 a[140:143], v[92:95], v[120:123], a[140:143]\n"
  "v_mfma_f32_16x16x32_f16 a[144:147], v[48:51], v[104:107], a[144:147]\n"
  "v_pk_add_f16 v120, v104, v108\n"
  "v_pk_add_f16 v121, v105, v109\n"
  "v_mfma_f32_16x16x32_f16 a[156:159], v[52:55], v[104:107], a[156:159]\n"
  "v_pk_add_f16 v122, v106, v110\n"
  "v_pk_add_f16 v123, v107, v111\n"
  "v_mfma_f32_16x16x32_f16 a[168:171], v[56:59], v[104:107], a[168:171]\n"
  "v_mfma_f32_16x16x32_f16 a[180:183], v[60:63], v[104:107], a[180:183]\n"
  "ds_read_b128 v[104:107], v232 offset:2048\n"
  "v_mfma_f32_16x16x32_f16 a[148:151], v[64:67], v[108:111], a[148:151]\n"
  "s_add_i32 s47, s44, s46\n"
  "s_add_i32 s50, s50, 4194304\n"
  "v_mfma_f32_16x16x32_f16 a[160:163], v[68:71], v[108:111], a[160:163]\n"
  "s_add_i32 s48, s47, s45\n"
  "s_add_i32 s46, s46, s49\n"
  "v_mfma_f32_16x16x32_f16 a[172:175], v[72:75], v[108:111], a[172:175]\n"
  "v_mfma_f32_16x16x32_f16 a[184:187], v[76:79], v[108:111], a[184:187]\n"
  "ds_read_b128 v[108:111], v232 offset:2560\n"
  "v_add_u32 v232, s49, v232\n"
  "v_mfma_f32_16x16x32_f16 a[152:155], v[80:83], v[120:123], a[152:155]\n"
  "v_mfma_f32_16x16x32_f16 a[164:167], v[84:87], v[120:123], a[164:167]\n"
  "v_mfma_f32_16x16x32_f16 a[176:179], v[88:91], v[120:123], a[176:179]\n"
  "v_mfma_f32_16x16x32_f16 a[188:191], v[92:95], v[120:123], a[188:191]\n"
  "v_mfma_f32_16x16x32_f16 a[192:195], v[48:51], v[112:115], a[192:195]\n"
  "v_pk_add_f16 v120, v112, v116\n"
  "v_pk_add_f16 v121, v113, v117\n"
  "v_mfma_f32_16x16x32_f16 a[204:207], v[52:55], v[112:115], a[204:207]\n"
  "v_pk_add_f16 v122, v114, v118\n"
  "v_pk_add_f16 v123, v115, v119\n"
  "v_mfma_f32_16x16x32_f16 a[216:219], v[56:59], v[112:115], a[216:219]\n"
  "v_mfma_f32_16x16x32_f16 a[228:231], v[60:63], v[112:115], a[228:231]\n"
  "v_mfma_f32_16x16x32_f16 a[196:199], v[64:67], v[116:119], a[196:199]\n"
  "v_mfma_f32_16x16x32_f16 a[208:211], v[68:71], v[116:119], a[208:211]\n"
  "v_mfma_f32_16x16x32_f16 a[220:223], v[72:75], v[116:119], a[220:223]\n"
  "v_mfma_f32_16x16x32_f16 a[232:235], v[76:79], v[116:119], a[232:235]\n"
  "v_mfma_f32_16x16x32_f16 a[200:203], v[80:83], v[120:123], a[200:203]\n"
  "s_waitcnt vmcnt(2) lgkmcnt(0)\n"
  "v_mfma_f32_16x16x32_f16 a[212:215], v[84:87], v[120:123], a[212:215]\n"
  "v_mfma_f32_16x16x32_f16 a[224:227], v[88:91], v[120:123], a[224:227]\n"
  "s_barrier\n"
  "v_mfma_f32_16x16x32_f16 a[236:239], v[92:95], v[120:123], a[236:239]\n"
  "v_mfma_f32_16x16x32_f16 v[124:127], v[0:3], v[96:99], v[124:127]\n"
  "v_pk_add_f16 v120, v96, v100\n"
  "v_pk_add_f16 v121, v97, v101\n"
  "v_mfma_f32_16x16x32_f16 v[136:139], v[4:7], v[96:99], v[136:139]\n"
  "v_pk_add_f16 v122, v98, v102\n"
  "v_pk_add_f16 v123, v99, v103\n"
  "v_mfma_f32_16x16x32_f16 v[148:151], v[8:11], v[96:99], v[148:151]\n"
  "ds_read_b128 v[112:115], v232 offset:0\n"
  "v_add_u32 v242, 57344, v232\n"
  "v_mfma_f32_16x16x32_f16 v[160:163], v[12:15], v[96:99], v[160:163]\n"
  "s_mov_b32 s66, 0x55555555\n"
  "s_mov_b32 s67, 0x55555555\n"
  "ds_read_b128 v[116:119], v232 offset:512\n"
  "v_mfma_f32_16x16x32_f16 v[128:131], v[16:19], v[100:103], v[128:131]\n"
  "ds_read_b64_tr_b16 v[48:49], v228 offset:0\n"
  "ds_read_b64_tr_b16 v[50:51], v228 offset:8192\n"
  "v_mfma_f32_16x16x32_f16 v[140:143], v[20:23], v[100:103], v[140:143]\n"
  "ds_read_b64_tr_b16 v[64:65], v228 offset:256\n"
  "ds_read_b64_tr_b16 v[66:67], v228 offset:8448\n"
  "v_mfma_f32_16x16x32_f16 v[152:155], v[24:27], v[100:103], v[152:155]\n"
  "ds_read_b64_tr_b16 v[52:53], v229 offset:0\n"
  "ds_read_b64_tr_b16 v[54:55], v229 offset:8192\n"
  "v_mfma_f32_16x16x32_f16 v[164:167], v[28:31], v[100:103], v[164:167]\n"
  "ds_read_b64_tr_b16 v[68:69], v229 offset:256\n"
  "ds_read_b64_tr_b16 v[70:71], v229 offset:8448\n"
  "v_mfma_f32_16x16x32_f16 v[132:135], v[32:35], v[120:123], v[132:135]\n"
  "ds_read_b64_tr_b16 v[56:57], v230 offset:0\n"
  "ds_read_b64_tr_b16 v[58:59], v230 offset:8192\n"
  "v_mfma_f32_16x16x32_f16 v[144:147], v[36:39], v[120:123], v[144:147]\n"
  "ds_read_b64_tr_b16 v[72:73], v230 offset:256\n"
  "ds_read_b64_tr_b16 v[74:75], v230 offset:8448\n"
  "v_mfma_f32_16x16x32_f16 v[156:159], v[40:43], v[120:123], v[156:159]\n"
  "ds_read_b64_tr_b16 v[60:61], v231 offset:0\n"
  "ds_read_b64_tr_b16 v[62:63], v231 offset:8192\n"
  "v_mfma_f32_16x16x32_f16 v[168:171], v[44:47], v[120:123], v[168:171]\n"
  "ds_read_b64_tr_b16 v[76:77], v231 offset:256\n"
  "ds_read_b64_tr_b16 v[78:79], v231 offset:8448\n"
  "v_mfma_f32_16x16x32_f16 v[180:183], v[0:3], v[104:107], v[180:183]\n"
  "s_waitcnt lgkmcnt(12)\n"
  "v_pk_add_f16 v120, v104, v108\n"
  "v_pk_add_f16 v121, v105, v109\n"
  "v_mfma_f32_16x16x32_f16 v[192:195], v[4:7], v[104:107], v[192:195]\n"
  "v_pk_add_f16 v122, v106, v110\n"
  "v_pk_add_f16 v123, v107, v111\n"
  "v_mfma_f32_16x16x32_f16 v[204:207], v[8:11], v[104:107], v[204:207]\n"
  "s_waitcnt lgkmcnt(8)\n"
  "v_pk_add_f16 v80, v48, v64\n"
  "v_pk_add_f16 v81, v49, v65\n"
  "v_mfma_f32_16x16x32_f16 v[216:219], v[12:15], v[104:107], v[216:219]\n"
  "v_pk_add_f16 v82, v50, v66\n"
  "v_pk_add_f16 v83, v51, v67\n"
  "v_mfma_f32_16x16x32_f16 v[184:187], v[16:19], v[108:111], v[184:187]\n"
  "s_waitcnt lgkmcnt(4)\n"
  "ds_read_b128 v[96:99], v232 offset:2048\n"
  "v_pk_add_f16 v84, v52, v68\n"
  "v_mfma_f32_16x16x32_f16 v[196:199], v[20:23], v[108:111], v[196:199]\n"
  "ds_read_b128 v[100:103], v232 offset:2560\n"
  "v_pk_add_f16 v85, v53, v69\n"
  "v_mfma_f32_16x16x32_f16 v[208:211], v[24:27], v[108:111], v[208:211]\n"
  "s_waitcnt lgkmcnt(2)\n"
  "v_pk_add_f16 v86, v54, v70\n"
  "v_pk_add_f16 v87, v55, v71\n"
  "v_mfma_f32_16x16x32_f16 v[220:223], v[28:31], v[108:111], v[220:223]\n"
  "v_pk_add_f16 v88, v56, v72\n"
  "v_pk_add_f16 v89, v57, v73\n"
  "v_mfma_f32_16x16x32_f16 v[188:191], v[32:35], v[120:123], v[188:191]\n"
  "v_pk_add_f16 v90, v58, v74\n"
  "v_pk_add_f16 v91, v59, v75\n"
  "v_mfma_f32_16x16x32_f16 v[200:203], v[36:39], v[120:123], v[200:203]\n"
  "v_pk_add_f16 v92, v60, v76\n"
  "v_pk_add_f16 v93, v61, v77\n"
  "v_mfma_f32_16x16x32_f16 v[212:215], v[40:43], v[120:123], v[212:215]\n"
  "v_pk_add_f16 v94, v62, v78\n"
  "v_pk_add_f16 v95, v63, v79\n"
  "v_mfma_f32_16x16x32_f16 v[224:227], v[44:47], v[120:123], v[224:227]\n"
  "v_mfma_f32_16x16x32_f16 v[124:127], v[48:51], v[112:115], v[124:127]\n"
  "v_pk_add_f16 v120, v112, v116\n"
  "v_pk_add_f16 v121, v113, v117\n"
  "v_mfma_f32_16x16x32_f16 v[136:139], v[52:55], v[112:115], v[136:139]\n"
  "v_pk_add_f16 v122, v114, v118\n"
  "v_pk_add_f16 v123, v115, v119\n"
  "v_mfma_f32_16x16x32_f16 v[128:131], v[64:67], v[116:119], v[128:131]\n"
  "ds_read_b128 v[104:107], v232 offset:4096\n"
  "ds_read_b128 v[108:111], v232 offset:4608\n"
  "v_mfma_f32_16x16x32_f16 v[140:143], v[68:71], v[116:119], v[140:143]\n"
  "v_mfma_f32_16x16x32_f16 v[132:135], v[80:83], v[120:123], v[132:135]\n"
  "v_mfma_f32_16x16x32_f16 v[144:147], v[84:87], v[120:123], v[144:147]\n"
  "v_mfma_f32_16x16x32_f16 v[148:151], v[56:59], v[112:115], v[148:151]\n"
  "v_mfma_f32_16x16x32_f16 v[160:163], v[60:63], v[112:115], v[160:163]\n"
  "v_fmac_f32 v124, s76, v132\n"
  "v_fmac_f32 v128, s76, v132\n"
  "v_mfma_f32_16x16x32_f16 v[152:155], v[72:75], v[116:119], v[152:155]\n"
  "s_waitcnt lgkmcnt(2)\n"
  "v_fmac_f32 v125, s76, v133\n"
  "v_fmac_f32 v129, s76, v133\n"
  "v_mfma_f32_16x16x32_f16 v[164:167], v[76:79], v[116:119], v[164:167]\n"
  "v_fmac_f32 v126, s76, v134\n"
  "v_fmac_f32 v130, s76, v134\n"
  "v_mfma_f32_16x16x32_f16 v[156:159], v[88:91], v[120:123], v[156:159]\n"
  "v_fmac_f32 v127, s76, v135\n"
  "v_fmac_f32 v131, s76, v135\n"
  "v_mfma_f32_16x16x32_f16 v[168:171], v[92:95], v[120:123], v[168:171]\n"
  "v_cvt_pk_bf16_f32 v172, v124, v125\n"
  "v_cvt_pk_bf16_f32 v173, v126, v127\n"
  "v_mfma_f32_16x16x32_f16 v[180:183], v[48:51], v[96:99], v[180:183]\n"
  "v_pk_add_f16 v120, v96, v100\n"
  "v_pk_add_f16 v121, v97, v101\n"
  "v_mfma_f32_16x16x32_f16 v[192:195], v[52:55], v[96:99], v[192:195]\n"
  "v_pk_add_f16 v122, v98, v102\n"
  "v_pk_add_f16 v123, v99, v103\n"
  "v_mfma_f32_16x16x32_f16 v[184:187], v[64:67], v[100:103], v[184:187]\n"
  "ds_read_b128 v[112:115], v242 offset:4096\n"
  "v_cvt_pk_bf16_f32 v176, v128, v129\n"
  "v_cvt_pk_bf16_f32 v177, v130, v131\n"
  "v_fmac_f32 v136, s76, v144\n"
  "v_fmac_f32 v140, s76, v144\n"
  "v_mfma_f32_16x16x32_f16 v[196:199], v[68:71], v[100:103], v[196:199]\n"
  "ds_read_b128 v[116:119], v242 offset:4608\n"
  "v_fmac_f32 v137, s76, v145\n"
  "v_fmac_f32 v141, s76, v145\n"
  "v_fmac_f32 v138, s76, v146\n"
  "v_fmac_f32 v142, s76, v146\n"
  "v_mfma_f32_16x16x32_f16 v[188:191], v[80:83], v[120:123], v[188:191]\n"
  "v_fmac_f32 v139, s76, v147\n"
  "v_fmac_f32 v143, s76, v147\n"
  "v_cvt_pk_bf16_f32 v174, v136, v137\n"
  "v_cvt_pk_bf16_f32 v175, v138, v139\n"
  "v_cvt_pk_bf16_f32 v178, v140, v141\n"
  "v_mfma_f32_16x16x32_f16 v[200:203], v[84:87], v[120:123], v[200:203]\n"
  "v_cvt_pk_bf16_f32 v179, v142, v143\n"
  "v_fmac_f32 v148, s76, v156\n"
  "v_fmac_f32 v152, s76, v156\n"
  "v_fmac_f32 v149, s76, v157\n"
  "v_fmac_f32 v153, s76, v157\n"
  "v_mfma_f32_16x16x32_f16 v[204:207], v[56:59], v[96:99], v[204:207]\n"
  "s_mov_b64 vcc, s[66:67]\n"
  "v_cndmask_b32_dpp v234, v176, v172, vcc quad_perm:[1,0,3,2] row_mask:0xf bank_mask:0xf\n"
  "v_cndmask_b32_dpp v235, v177, v173, vcc quad_perm:[1,0,3,2] row_mask:0xf bank_mask:0xf\n"
  "v_cndmask_b32_dpp v236, v178, v174, vcc quad_perm:[1,0,3,2] row_mask:0xf bank_mask:0xf\n"
  "v_cndmask_b32_dpp v237, v179, v175, vcc quad_perm:[1,0,3,2] row_mask:0xf bank_mask:0xf\n"
  "v_fmac_f32 v150, s76, v158\n"
  "v_mfma_f32_16x16x32_f16 v[216:219], v[60:63], v[96:99], v[216:219]\n"
  "s_add_i32 s64, s80, 0\n"
  "buffer_store_dwordx4 v[234:237], v250, s[72:75], s64 offen sc1\n"
  "s_not_b64 vcc, vcc\n"
  "v_cndmask_b32_dpp v238, v172, v176, vcc quad_perm:[1,0,3,2] row_mask:0xf bank_mask:0xf\n"
  "v_cndmask_b32_dpp v239, v173, v177, vcc quad_perm:[1,0,3,2] row_mask:0xf bank_mask:0xf\n"
  "v_cndmask_b32_dpp v240, v174, v178, vcc quad_perm:[1,0,3,2] row_mask:0xf bank_mask:0xf\n"
  "v_cndmask_b32_dpp v241, v175, v179, vcc quad_perm:[1,0,3,2] row_mask:0xf bank_mask:0xf\n"
  "v_mfma_f32_16x16x32_f16 v[208:211], v[72:75], v[100:103], v[208:211]\n"
  "s_waitcnt lgkmcnt(2)\n"
  "s_add_i32 s65, s80, 65536\n"
  "buffer_store_dwordx4 v[238:241], v250, s[72:75], s65 offen sc1\n"
  "v_fmac_f32 v154, s76, v158\n"
  "v_fmac_f32 v151, s76, v159\n"
  "v_fmac_f32 v155, s76, v159\n"
  "v_cvt_pk_bf16_f32 v172, v148, v149\n"
  "v_mfma_f32_16x16x32_f16 v[220:223], v[76:79], v[100:103], v[220:223]\n"
  "v_cvt_pk_bf16_f32 v173, v150, v151\n"
  "v_cvt_pk_bf16_f32 v176, v152, v153\n"
  "v_cvt_pk_bf16_f32 v177, v154, v155\n"
  "v_fmac_f32 v160, s76, v168\n"
  "v_fmac_f32 v164, s76, v168\n"
  "v_mfma_f32_16x16x32_f16 v[212:215], v[88:91], v[120:123], v[212:215]\n"
  "v_fmac_f32 v161, s76, v169\n"
  "v_fmac_f32 v165, s76, v169\n"
  "v_fmac_f32 v162, s76, v170\n"
  "v_fmac_f32 v166, s76, v170\n"
  "v_fmac_f32 v163, s76, v171\n"
  "v_mfma_f32_16x16x32_f16 v[224:227], v[92:95], v[120:123], v[224:227]\n"
  "v_fmac_f32 v167, s76, v171\n"
  "v_cvt_pk_bf16_f32 v174, v160, v161\n"
  "v_cvt_pk_bf16_f32 v175, v162, v163\n"
  "v_cvt_pk_bf16_f32 v178, v164, v165\n"
  "v_cvt_pk_bf16_f32 v179, v166, v167\n"
  "v_mfma_f32_16x16x32_f16 a[96:99], v[0:3], v[104:107], a[96:99]\n"
  "ds_read_b128 v[96:99], v232 offset:6144\n"
  "v_pk_add_f16 v120, v104, v108\n"
  "v_pk_add_f16 v121, v105, v109\n"
  "v_pk_add_f16 v122, v106, v110\n"
  "v_pk_add_f16 v123, v107, v111\n"
  "v_mfma_f32_16x16x32_f16 a[108:111], v[4:7], v[104:107], a[108:111]\n"
  "ds_read_b128 v[100:103], v232 offset:6656\n"
  "s_mov_b64 vcc, s[66:67]\n"
  "v_cndmask_b32_dpp v234, v176, v172, vcc quad_perm:[1,0,3,2] row_mask:0xf bank_mask:0xf\n"
  "v_cndmask_b32_dpp v235, v177, v173, vcc quad_perm:[1,0,3,2] row_mask:0xf bank_mask:0xf\n"
  "v_cndmask_b32_dpp v236, v178, v174, vcc quad_perm:[1,0,3,2] row_mask:0xf bank_mask:0xf\n"
  "v_cndmask_b32_dpp v237, v179, v175, vcc quad_perm:[1,0,3,2] row_mask:0xf bank_mask:0xf\n"
  "v_mfma_f32_16x16x32_f16 a[120:123], v[8:11], v[104:107], a[120:123]\n"
  "s_add_i32 s64, s80, 4096\n"
  "buffer_store_dwordx4 v[234:237], v250, s[72:75], s64 offen sc1\n"
  "s_not_b64 vcc, vcc\n"
  "v_cndmask_b32_dpp v238, v172, v176, vcc quad_perm:[1,0,3,2] row_mask:0xf bank_mask:0xf\n"
  "v_cndmask_b32_dpp v239, v173, v177, vcc quad_perm:[1,0,3,2] row_mask:0xf bank_mask:0xf\n"
  "v_cndmask_b32_dpp v240, v174, v178, vcc quad_perm:[1,0,3,2] row_mask:0xf bank_mask:0xf\n"
  "v_cndmask_b32_dpp v241, v175, v179, vcc quad_perm:[1,0,3,2] row_mask:0xf bank_mask:0xf\n"
  "v_mfma_f32_16x16x32_f16 a[132:135], v[12:15], v[104:107], a[132:135]\n"
  "s_add_i32 s65, s80, 69632\n"
  "buffer_store_dwordx4 v[238:241], v250, s[72:75], s65 offen sc1\n"
  "v_fmac_f32 v180, s76, v188\n"
  "v_fmac_f32 v184, s76, v188\n"
  "v_fmac_f32 v181, s76, v189\n"
  "v_fmac_f32 v185, s76, v189\n"
  "v_mfma_f32_16x16x32_f16 a[100:103], v[16:19], v[108:111], a[100:103]\n"
  "v_fmac_f32 v182, s76, v190\n"
  "v_fmac_f32 v186, s76, v190\n"
  "v_fmac_f32 v183, s76, v191\n"
  "v_fmac_f32 v187, s76, v191\n"
  "v_cvt_pk_bf16_f32 v172, v180, v181\n"
  "v_mfma_f32_16x16x32_f16 a[112:115], v[20:23], v[108:111], a[112:115]\n"
  "v_cvt_pk_bf16_f32 v173, v182, v183\n"
  "v_cvt_pk_bf16_f32 v176, v184, v185\n"
  "v_cvt_pk_bf16_f32 v177, v186, v187\n"
  "v_fmac_f32 v192, s76, v200\n"
  "v_fmac_f32 v196, s76, v200\n"
  "v_mfma_f32_16x16x32_f16 a[124:127], v[24:27], v[108:111], a[124:127]\n"
  "v_fmac_f32 v193, s76, v201\n"
  "v_fmac_f32 v197, s76, v201\n"
  "v_fmac_f32 v194, s76, v202\n"
  "v_fmac_f32 v198, s76, v202\n"
  "v_fmac_f32 v195, s76, v203\n"
  "v_mfma_f32_16x16x32_f16 a[136:139], v[28:31], v[108:111], a[136:139]\n"
  "v_fmac_f32 v199, s76, v203\n"
  "v_cvt_pk_bf16_f32 v174, v192, v193\n"
  "v_cvt_pk_bf16_f32 v175, v194, v195\n"
  "v_cvt_pk_bf16_f32 v178, v196, v197\n"
  "v_cvt_pk_bf16_f32 v179, v198, v199\n"
  "v_mfma_f32_16x16x32_f16 a[104:107], v[32:35], v[120:123], a[104:107]\n"
  "s_waitcnt lgkmcnt(2)\n"
  "s_mov_b64 vcc, s[66:67]\n"
  "v_cndmask_b32_dpp v234, v176, v172, vcc quad_perm:[1,0,3,2] row_mask:0xf bank_mask:0xf\n"
  "v_cndmask_b32_dpp v235, v177, v173, vcc quad_perm:[1,0,3,2] row_mask:0xf bank_mask:0xf\n"
  "v_cndmask_b32_dpp v236, v178, v174, vcc quad_perm:[1,0,3,2] row_mask:0xf bank_mask:0xf\n"
  "v_cndmask_b32_dpp v237, v179, v175, vcc quad_perm:[1,0,3,2] row_mask:0xf bank_mask:0xf\n"
  "v_fmac_f32 v204, s76, v212\n"
  "v_mfma_f32_16x16x32_f16 a[116:119], v[36:39], v[120:123], a[116:119]\n"
  "s_add_i32 s64, s81, 0\n"
  "buffer_store_dwordx4 v[234:237], v250, s[72:75], s64 offen sc1\n"
  "s_not_b64 vcc, vcc\n"
  "v_cndmask_b32_dpp v238, v172, v176, vcc quad_perm:[1,0,3,2] row_mask:0xf bank_mask:0xf\n"
  "v_cndmask_b32_dpp v239, v173, v177, vcc quad_perm:[1,0,3,2] row_mask:0xf bank_mask:0xf\n"
  "v_cndmask_b32_dpp v240, v174, v178, vcc quad_perm:[1,0,3,2] row_mask:0xf bank_mask:0xf\n"
  "v_cndmask_b32_dpp v241, v175, v179, vcc quad_perm:[1,0,3,2] row_mask:0xf bank_mask:0xf\n"
  "v_mfma_f32_16x16x32_f16 a[128:131], v[40:43], v[120:123], a[128:131]\n"
  "s_add_i32 s65, s81, 65536\n"
  "buffer_store_dwordx4 v[238:241], v250, s[72:75], s65 offen sc1\n"
  "v_fmac_f32 v208, s76, v212\n"
  "v_fmac_f32 v205, s76, v213\n"
  "v_fmac_f32 v209, s76, v213\n"
  "v_fmac_f32 v206, s76, v214\n"
  "v_mfma_f32_16x16x32_f16 a[140:143], v[44:47], v[120:123], a[140:143]\n"
  "v_fmac_f32 v210, s76, v214\n"
  "v_fmac_f32 v207, s76, v215\n"
  "v_fmac_f32 v211, s76, v215\n"
  "v_cvt_pk_bf16_f32 v172, v204, v205\n"
  "v_cvt_pk_bf16_f32 v173, v206, v207\n"
  "v_mfma_f32_16x16x32_f16 a[96:99], v[48:51], v[112:115], a[96:99]\n"
  "ds_read_b128 v[104:107], v242 offset:6144\n"
  "v_pk_add_f16 v120, v112, v116\n"
  "v_pk_add_f16 v121, v113, v117\n"
  "v_pk_add_f16 v122, v114, v118\n"
  "v_pk_add_f16 v123, v115, v119\n"
  "v_mfma_f32_16x16x32_f16 a[108:111], v[52:55], v[112:115], a[108:111]\n"
  "ds_read_b128 v[108:111], v242 offset:6656\n"
  "v_cvt_pk_bf16_f32 v176, v208, v209\n"
  "v_cvt_pk_bf16_f32 v177, v210, v211\n"
  "v_fmac_f32 v216, s76, v224\n"
  "v_fmac_f32 v220, s76, v224\n"
  "v_mfma_f32_16x16x32_f16 a[100:103], v[64:67], v[116:119], a[100:103]\n"
  "v_fmac_f32 v217, s76, v225\n"
  "v_fmac_f32 v221, s76, v225\n"
  "v_fmac_f32 v218, s76, v226\n"
  "v_fmac_f32 v222, s76, v226\n"
  "v_fmac_f32 v219, s76, v227\n"
  "v_mfma_f32_16x16x32_f16 a[112:115], v[68:71], v[116:119], a[112:115]\n"
  "v_fmac_f32 v223, s76, v227\n"
  "v_cvt_pk_bf16_f32 v174, v216, v217\n"
  "v_cvt_pk_bf16_f32 v175, v218, v219\n"
  "v_cvt_pk_bf16_f32 v178, v220, v221\n"
  "v_cvt_pk_bf16_f32 v179, v222, v223\n"
  "v_mfma_f32_16x16x32_f16 a[104:107], v[80:83], v[120:123], a[104:107]\n"
  "s_mov_b64 vcc, s[66:67]\n"
  "v_cndmask_b32_dpp v234, v176, v172, vcc quad_perm:[1,0,3,2] row_mask:0xf bank_mask:0xf\n"
  "v_cndmask_b32_dpp v235, v177, v173, vcc quad_perm:[1,0,3,2] row_mask:0xf bank_mask:0xf\n"
  "v_cndmask_b32_dpp v236, v178, v174, vcc quad_perm:[1,0,3,2] row_mask:0xf bank_mask:0xf\n"
  "v_cndmask_b32_dpp v237, v179, v175, vcc quad_perm:[1,0,3,2] row_mask:0xf bank_mask:0xf\n"
  "v_accvgpr_read_b32 v124, a96\n"
  "v_mfma_f32_16x16x32_f16 a[116:119], v[84:87], v[120:123], a[116:119]\n"
  "s_add_i32 s64, s81, 4096\n"
  "buffer_store_dwordx4 v[234:237], v250, s[72:75], s64 offen sc1\n"
  "s_not_b64 vcc, vcc\n"
  "v_cndmask_b32_dpp v238, v172, v176, vcc quad_perm:[1,0,3,2] row_mask:0xf bank_mask:0xf\n"
  "v_cndmask_b32_dpp v239, v173, v177, vcc quad_perm:[1,0,3,2] row_mask:0xf bank_mask:0xf\n"
  "v_cndmask_b32_dpp v240, v174, v178, vcc quad_perm:[1,0,3,2] row_mask:0xf bank_mask:0xf\n"
  "v_cndmask_b32_dpp v241, v175, v179, vcc quad_perm:[1,0,3,2] row_mask:0xf bank_mask:0xf\n"
  "v_mfma_f32_16x16x32_f16 a[120:123], v[56:59], v[112:115], a[120:123]\n"
  "s_add_i32 s65, s81, 69632\n"
  "buffer_store_dwordx4 v[238:241], v250, s[72:75], s65 offen sc1\n"
  "v_accvgpr_read_b32 v125, a97\n"
  "v_accvgpr_read_b32 v126, a98\n"
  "v_accvgpr_read_b32 v127, a99\n"
  "v_accvgpr_read_b32 v128, a100\n"
  "v_mfma_f32_16x16x32_f16 a[132:135], v[60:63], v[112:115], a[132:135]\n"
  "v_accvgpr_read_b32 v129, a101\n"
  "v_accvgpr_read_b32 v130, a102\n"
  "v_accvgpr_read_b32 v131, a103\n"
  "v_accvgpr_read_b32 v132, a104\n"
  "v_accvgpr_read_b32 v133, a105\n"
  "v_mfma_f32_16x16x32_f16 a[124:127], v[72:75], v[116:119], a[124:127]\n"
  "s_waitcnt lgkmcnt(2)\n"
  "v_accvgpr_read_b32 v134, a106\n"
  "v_accvgpr_read_b32 v135, a107\n"
  "v_fmac_f32 v124, s76, v132\n"
  "v_fmac_f32 v128, s76, v132\n"
  "v_fmac_f32 v125, s76, v133\n"
  "v_mfma_f32_16x16x32_f16 a[136:139], v[76:79], v[116:119], a[136:139]\n"
  "v_fmac_f32 v129, s76, v133\n"
  "v_fmac_f32 v126, s76, v134\n"
  "v_fmac_f32 v130, s76, v134\n"
  "v_fmac_f32 v127, s76, v135\n"
  "v_fmac_f32 v131, s76, v135\n"
  "v_mfma_f32_16x16x32_f16 a[128:131], v[88:91], v[120:123], a[128:131]\n"
  "v_cvt_pk_bf16_f32 v172, v124, v125\n"
  "v_cvt_pk_bf16_f32 v173, v126, v127\n"
  "v_cvt_pk_bf16_f32 v176, v128, v129\n"
  "v_cvt_pk_bf16_f32 v177, v130, v131\n"
  "v_accvgpr_read_b32 v136, a108\n"
  "v_mfma_f32_16x16x32_f16 a[140:143], v[92:95], v[120:123], a[140:143]\n"
  "v_accvgpr_read_b32 v137, a109\n"
  "v_accvgpr_read_b32 v138, a110\n"
  "v_accvgpr_read_b32 v139, a111\n"
  "v_accvgpr_read_b32 v140, a112\n"
  "v_accvgpr_read_b32 v141, a113\n"
  "v_mfma_f32_16x16x32_f16 a[144:147], v[0:3], v[96:99], a[144:147]\n"
  "ds_read_b128 v[112:115], v232 offset:8192\n"
  "v_pk_add_f16 v120, v96, v100\n"
  "v_pk_add_f16 v121, v97, v101\n"
  "v_pk_add_f16 v122, v98, v102\n"
  "v_pk_add_f16 v123, v99, v103\n"
  "v_mfma_f32_16x16x32_f16 a[156:159], v[4:7], v[96:99], a[156:159]\n"
  "ds_read_b128 v[116:119], v232 offset:8704\n"
  "v_accvgpr_read_b32 v142, a114\n"
  "v_accvgpr_read_b32 v143, a115\n"
  "v_accvgpr_read_b32 v144, a116\n"
  "v_accvgpr_read_b32 v145, a117\n"
  "v_mfma_f32_16x16x32_f16 a[168:171], v[8:11], v[96:99], a[168:171]\n"
  "v_accvgpr_read_b32 v146, a118\n"
  "v_accvgpr_read_b32 v147, a119\n"
  "v_fmac_f32 v136, s76, v144\n"
  "v_fmac_f32 v140, s76, v144\n"
  "v_fmac_f32 v137, s76, v145\n"
  "v_mfma_f32_16x16x32_f16 a[180:183], v[12:15], v[96:99], a[180:183]\n"
  "v_fmac_f32 v141, s76, v145\n"
  "v_fmac_f32 v138, s76, v146\n"
  "v_fmac_f32 v142, s76, v146\n"
  "v_fmac_f32 v139, s76, v147\n"
  "v_fmac_f32 v143, s76, v147\n"
  "v_mfma_f32_16x16x32_f16 a[148:151], v[16:19], v[100:103], a[148:151]\n"
  "v_cvt_pk_bf16_f32 v174, v136, v137\n"
  "v_cvt_pk_bf16_f32 v175, v138, v139\n"
  "v_cvt_pk_bf16_f32 v178, v140, v141\n"
  "v_cvt_pk_bf16_f32 v179, v142, v143\n"
  "v_accvgpr_read_b32 v148, a120\n"
  "v_mfma_f32_16x16x32_f16 a[160:163], v[20:23], v[100:103], a[160:163]\n"
  "s_mov_b64 vcc, s[66:67]\n"
  "v_cndmask_b32_dpp v234, v176, v172, vcc quad_perm:[1,0,3,2] row_mask:0xf bank_mask:0xf\n"
  "v_cndmask_b32_dpp v235, v177, v173, vcc quad_perm:[1,0,3,2] row_mask:0xf bank_mask:0xf\n"
  "v_cndmask_b32_dpp v236, v178, v174, vcc quad_perm:[1,0,3,2] row_mask:0xf bank_mask:0xf\n"
  "v_cndmask_b32_dpp v237, v179, v175, vcc quad_perm:[1,0,3,2] row_mask:0xf bank_mask:0xf\n"
  "v_accvgpr_read_b32 v149, a121\n"
  "v_mfma_f32_16x16x32_f16 a[172:175], v[24:27], v[100:103], a[172:175]\n"
  "s_add_i32 s64, s82, 0\n"
  "buffer_store_dwordx4 v[234:237], v250, s[72:75], s64 offen sc1\n"
  "s_not_b64 vcc, vcc\n"
  "v_cndmask_b32_dpp v238, v172, v176, vcc quad_perm:[1,0,3,2] row_mask:0xf bank_mask:0xf\n"
  "v_cndmask_b32_dpp v239, v173, v177, vcc quad_perm:[1,0,3,2] row_mask:0xf bank_mask:0xf\n"
  "v_cndmask_b32_dpp v240, v174, v178, vcc quad_perm:[1,0,3,2] row_mask:0xf bank_mask:0xf\n"
  "v_cndmask_b32_dpp v241, v175, v179, vcc quad_perm:[1,0,3,2] row_mask:0xf bank_mask:0xf\n"
  "v_mfma_f32_16x16x32_f16 a[184:187], v[28:31], v[100:103], a[184:187]\n"
  "s_add_i32 s65, s82, 65536\n"
  "buffer_store_dwordx4 v[238:241], v250, s[72:75], s65 offen sc1\n"
  "v_accvgpr_read_b32 v150, a122\n"
  "v_accvgpr_read_b32 v151, a123\n"
  "v_accvgpr_read_b32 v152, a124\n"
  "v_accvgpr_read_b32 v153, a125\n"
  "v_mfma_f32_16x16x32_f16 a[152:155], v[32:35], v[120:123], a[152:155]\n"
  "s_waitcnt lgkmcnt(2)\n"
  "v_accvgpr_read_b32 v154, a126\n"
  "v_accvgpr_read_b32 v155, a127\n"
  "v_accvgpr_read_b32 v156, a128\n"
  "v_accvgpr_read_b32 v157, a129\n"
  "v_accvgpr_read_b32 v158, a130\n"
  "v_mfma_f32_16x16x32_f16 a[164:167], v[36:39], v[120:123], a[164:167]\n"
  "v_accvgpr_read_b32 v159, a131\n"
  "v_fmac_f32 v148, s76, v156\n"
  "v_fmac_f32 v152, s76, v156\n"
  "v_fmac_f32 v149, s76, v157\n"
  "v_fmac_f32 v153, s76, v157\n"
  "v_mfma_f32_16x16x32_f16 a[176:179], v[40:43], v[120:123], a[176:179]\n"
  "v_fmac_f32 v150, s76, v158\n"
  "v_fmac_f32 v154, s76, v158\n"
  "v_fmac_f32 v151, s76, v159\n"
  "v_fmac_f32 v155, s76, v159\n"
  "v_cvt_pk_bf16_f32 v172, v148, v149\n"
  "v_mfma_f32_16x16x32_f16 a[188:191], v[44:47], v[120:123], a[188:191]\n"
  "v_cvt_pk_bf16_f32 v173, v150, v151\n"
  "v_cvt_pk_bf16_f32 v176, v152, v153\n"
  "v_cvt_pk_bf16_f32 v177, v154, v155\n"
  "v_accvgpr_read_b32 v160, a132\n"
  "v_accvgpr_read_b32 v161, a133\n"
  "v_mfma_f32_16x16x32_f16 a[144:147], v[48:51], v[104:107], a[144:147]\n"
  "ds_read_b128 v[96:99], v242 offset:8192\n"
  "v_pk_add_f16 v120, v104, v108\n"
  "v_pk_add_f16 v121, v105, v109\n"
  "v_pk_add_f16 v122, v106, v110\n"
  "v_pk_add_f16 v123, v107, v111\n"
  "v_mfma_f32_16x16x32_f16 a[156:159], v[52:55], v[104:107], a[156:159]\n"
  "ds_read_b128 v[100:103], v242 offset:8704\n"
  "v_accvgpr_read_b32 v162, a134\n"
  "v_accvgpr_read_b32 v163, a135\n"
  "v_accvgpr_read_b32 v164, a136\n"
  "v_accvgpr_read_b32 v165, a137\n"
  "v_mfma_f32_16x16x32_f16 a[148:151], v[64:67], v[108:111], a[148:151]\n"
  "v_accvgpr_read_b32 v166, a138\n"
  "v_accvgpr_read_b32 v167, a139\n"
  "v_accvgpr_read_b32 v168, a140\n"
  "v_accvgpr_read_b32 v169, a141\n"
  "v_accvgpr_read_b32 v170, a142\n"
  "v_mfma_f32_16x16x32_f16 a[160:163], v[68:71], v[108:111], a[160:163]\n"
  "v_accvgpr_read_b32 v171, a143\n"
  "v_fmac_f32 v160, s76, v168\n"
  "v_fmac_f32 v164, s76, v168\n"
  "v_fmac_f32 v161, s76, v169\n"
  "v_fmac_f32 v165, s76, v169\n"
  "v_mfma_f32_16x16x32_f16 a[152:155], v[80:83], v[120:123], a[152:155]\n"
  "v_fmac_f32 v162, s76, v170\n"
  "v_fmac_f32 v166, s76, v170\n"
  "v_fmac_f32 v163, s76, v171\n"
  "v_fmac_f32 v167, s76, v171\n"
  "v_cvt_pk_bf16_f32 v174, v160, v161\n"
  "v_mfma_f32_16x16x32_f16 a[164:167], v[84:87], v[120:123], a[164:167]\n"
  "v_cvt_pk_bf16_f32 v175, v162, v163\n"
  "v_cvt_pk_bf16_f32 v178, v164, v165\n"
  "v_cvt_pk_bf16_f32 v179, v166, v167\n"
  "v_accvgpr_read_b32 v180, a144\n"
  "v_accvgpr_read_b32 v181, a145\n"
  "v_mfma_f32_16x16x32_f16 a[168:171], v[56:59], v[104:107], a[168:171]\n"
  "s_mov_b64 vcc, s[66:67]\n"
  "v_cndmask_b32_dpp v234, v176, v172, vcc quad_perm:[1,0,3,2] row_mask:0xf bank_mask:0xf\n"
  "v_cndmask_b32_dpp v235, v177, v173, vcc quad_perm:[1,0,3,2] row_mask:0xf bank_mask:0xf\n"
  "v_cndmask_b32_dpp v236, v178, v174, vcc quad_perm:[1,0,3,2] row_mask:0xf bank_mask:0xf\n"
  "v_cndmask_b32_dpp v237, v179, v175, vcc quad_perm:[1,0,3,2] row_mask:0xf bank_mask:0xf\n"
  "v_accvgpr_read_b32 v182, a146\n"
  "v_mfma_f32_16x16x32_f16 a[180:183], v[60:63], v[104:107], a[180:183]\n"
  "s_add_i32 s64, s82, 4096\n"
  "buffer_store_dwordx4 v[234:237], v250, s[72:75], s64 offen sc1\n"
  "s_not_b64 vcc, vcc\n"
  "v_cndmask_b32_dpp v238, v172, v176, vcc quad_perm:[1,0,3,2] row_mask:0xf bank_mask:0xf\n"
  "v_cndmask_b32_dpp v239, v173, v177, vcc quad_perm:[1,0,3,2] row_mask:0xf bank_mask:0xf\n"
  "v_cndmask_b32_dpp v240, v174, v178, vcc quad_perm:[1,0,3,2] row_mask:0xf bank_mask:0xf\n"
  "v_cndmask_b32_dpp v241, v175, v179, vcc quad_perm:[1,0,3,2] row_mask:0xf bank_mask:0xf\n"
  "v_mfma_f32_16x16x32_f16 a[172:175], v[72:75], v[108:111], a[172:175]\n"
  "s_waitcnt lgkmcnt(2)\n"
  "s_add_i32 s65, s82, 69632\n"
  "buffer_store_dwordx4 v[238:241], v250, s[72:75], s65 offen sc1\n"
  "v_accvgpr_read_b32 v183, a147\n"
  "v_accvgpr_read_b32 v184, a148\n"
  "v_accvgpr_read_b32 v185, a149\n"
  "v_accvgpr_read_b32 v186, a150\n"
  "v_mfma_f32_16x16x32_f16 a[184:187], v[76:79], v[108:111], a[184:187]\n"
  "v_accvgpr_read_b32 v187, a151\n"
  "v_accvgpr_read_b32 v188, a152\n"
  "v_accvgpr_read_b32 v189, a153\n"
  "v_accvgpr_read_b32 v190, a154\n"
  "v_accvgpr_read_b32 v191, a155\n"
  "v_mfma_f32_16x16x32_f16 a[176:179], v[88:91], v[120:123], a[176:179]\n"
  "v_fmac_f32 v180, s76, v188\n"
  "v_fmac_f32 v184, s76, v188\n"
  "v_fmac_f32 v181, s76, v189\n"
  "v_fmac_f32 v185, s76, v189\n"
  "v_fmac_f32 v182, s76, v190\n"
  "v_mfma_f32_16x16x32_f16 a[188:191], v[92:95], v[120:123], a[188:191]\n"
  "v_fmac_f32 v186, s76, v190\n"
  "v_fmac_f32 v183, s76, v191\n"
  "v_fmac_f32 v187, s76, v191\n"
  "v_cvt_pk_bf16_f32 v172, v180, v181\n"
  "v_cvt_pk_bf16_f32 v173, v182, v183\n"
  "v_mfma_f32_16x16x32_f16 a[192:195], v[0:3], v[112:115], a[192:195]\n"
  "v_pk_add_f16 v120, v112, v116\n"
  "v_pk_add_f16 v121, v113, v117\n"
  "v_pk_add_f16 v122, v114, v118\n"
  "v_pk_add_f16 v123, v115, v119\n"
  "v_cvt_pk_bf16_f32 v176, v184, v185\n"
  "v_mfma_f32_16x16x32_f16 a[204:207], v[4:7], v[112:115], a[204:207]\n"
  "v_cvt_pk_bf16_f32 v177, v186, v187\n"
  "v_accvgpr_read_b32 v192, a156\n"
  "v_accvgpr_read_b32 v193, a157\n"
  "v_accvgpr_read_b32 v194, a158\n"
  "v_accvgpr_read_b32 v195, a159\n"
  "v_mfma_f32_16x16x32_f16 a[216:219], v[8:11], v[112:115], a[216:219]\n"
  "v_accvgpr_read_b32 v196, a160\n"
  "v_accvgpr_read_b32 v197, a161\n"
  "v_accvgpr_read_b32 v198, a162\n"
  "v_accvgpr_read_b32 v199, a163\n"
  "v_accvgpr_read_b32 v200, a164\n"
  "v_mfma_f32_16x16x32_f16 a[228:231], v[12:15], v[112:115], a[228:231]\n"
  "v_accvgpr_read_b32 v201, a165\n"
  "v_accvgpr_read_b32 v202, a166\n"
  "v_accvgpr_read_b32 v203, a167\n"
  "v_fmac_f32 v192, s76, v200\n"
  "v_fmac_f32 v196, s76, v200\n"
  "v_mfma_f32_16x16x32_f16 a[196:199], v[16:19], v[116:119], a[196:199]\n"
  "v_fmac_f32 v193, s76, v201\n"
  "v_fmac_f32 v197, s76, v201\n"
  "v_fmac_f32 v194, s76, v202\n"
  "v_fmac_f32 v198, s76, v202\n"
  "v_fmac_f32 v195, s76, v203\n"
  "v_mfma_f32_16x16x32_f16 a[208:211], v[20:23], v[116:119], a[208:211]\n"
  "v_fmac_f32 v199, s76, v203\n"
  "v_cvt_pk_bf16_f32 v174, v192, v193\n"
  "v_cvt_pk_bf16_f32 v175, v194, v195\n"
  "v_cvt_pk_bf16_f32 v178, v196, v197\n"
  "v_cvt_pk_bf16_f32 v179, v198, v199\n"
  "v_mfma_f32_16x16x32_f16 a[220:223], v[24:27], v[116:119], a[220:223]\n"
  "s_mov_b64 vcc, s[66:67]\n"
  "v_cndmask_b32_dpp v234, v176, v172, vcc quad_perm:[1,0,3,2] row_mask:0xf bank_mask:0xf\n"
  "v_cndmask_b32_dpp v235, v177, v173, vcc quad_perm:[1,0,3,2] row_mask:0xf bank_mask:0xf\n"
  "v_cndmask_b32_dpp v236, v178, v174, vcc quad_perm:[1,0,3,2] row_mask:0xf bank_mask:0xf\n"
  "v_cndmask_b32_dpp v237, v179, v175, vcc quad_perm:[1,0,3,2] row_mask:0xf bank_mask:0xf\n"
  "v_accvgpr_read_b32 v204, a168\n"
  "v_mfma_f32_16x16x32_f16 a[232:235], v[28:31], v[116:119], a[232:235]\n"
  "s_add_i32 s64, s83, 0\n"
  "buffer_store_dwordx4 v[234:237], v250, s[72:75], s64 offen sc1\n"
  "s_not_b64 vcc, vcc\n"
  "v_cndmask_b32_dpp v238, v172, v176, vcc quad_perm:[1,0,3,2] row_mask:0xf bank_mask:0xf\n"
  "v_cndmask_b32_dpp v239, v173, v177, vcc quad_perm:[1,0,3,2] row_mask:0xf bank_mask:0xf\n"
  "v_cndmask_b32_dpp v240, v174, v178, vcc quad_perm:[1,0,3,2] row_mask:0xf bank_mask:0xf\n"
  "v_cndmask_b32_dpp v241, v175, v179, vcc quad_perm:[1,0,3,2] row_mask:0xf bank_mask:0xf\n"
  "v_mfma_f32_16x16x32_f16 a[200:203], v[32:35], v[120:123], a[200:203]\n"
  "s_waitcnt lgkmcnt(0)\n"
  "s_add_i32 s65, s83, 65536\n"
  "buffer_store_dwordx4 v[238:241], v250, s[72:75], s65 offen sc1\n"
  "v_accvgpr_read_b32 v205, a169\n"
  "v_accvgpr_read_b32 v206, a170\n"
  "v_accvgpr_read_b32 v207, a171\n"
  "v_accvgpr_read_b32 v208, a172\n"
  "v_mfma_f32_16x16x32_f16 a[212:215], v[36:39], v[120:123], a[212:215]\n"
  "v_accvgpr_read_b32 v209, a173\n"
  "v_accvgpr_read_b32 v210, a174\n"
  "v_accvgpr_read_b32 v211, a175\n"
  "v_accvgpr_read_b32 v212, a176\n"
  "v_accvgpr_read_b32 v213, a177\n"
  "v_mfma_f32_16x16x32_f16 a[224:227], v[40:43], v[120:123], a[224:227]\n"
  "v_accvgpr_read_b32 v214, a178\n"
  "v_accvgpr_read_b32 v215, a179\n"
  "v_fmac_f32 v204, s76, v212\n"
  "v_fmac_f32 v208, s76, v212\n"
  "v_fmac_f32 v205, s76, v213\n"
  "v_mfma_f32_16x16x32_f16 a[236:239], v[44:47], v[120:123], a[236:239]\n"
  "v_fmac_f32 v209, s76, v213\n"
  "v_fmac_f32 v206, s76, v214\n"
  "v_fmac_f32 v210, s76, v214\n"
  "v_fmac_f32 v207, s76, v215\n"
  "v_fmac_f32 v211, s76, v215\n"
  "v_mfma_f32_16x16x32_f16 a[192:195], v[48:51], v[96:99], a[192:195]\n"
  "v_pk_add_f16 v120, v96, v100\n"
  "v_pk_add_f16 v121, v97, v101\n"
  "v_pk_add_f16 v122, v98, v102\n"
  "v_pk_add_f16 v123, v99, v103\n"
  "v_cvt_pk_bf16_f32 v172, v204, v205\n"
  "v_mfma_f32_16x16x32_f16 a[204:207], v[52:55], v[96:99], a[204:207]\n"
  "v_cvt_pk_bf16_f32 v173, v206, v207\n"
  "v_cvt_pk_bf16_f32 v176, v208, v209\n"
  "v_cvt_pk_bf16_f32 v177, v210, v211\n"
  "v_accvgpr_read_b32 v216, a180\n"
  "v_accvgpr_read_b32 v217, a181\n"
  "v_mfma_f32_16x16x32_f16 a[196:199], v[64:67], v[100:103], a[196:199]\n"
  "v_accvgpr_read_b32 v218, a182\n"
  "v_accvgpr_read_b32 v219, a183\n"
  "v_accvgpr_read_b32 v220, a184\n"
  "v_accvgpr_read_b32 v221, a185\n"
  "v_accvgpr_read_b32 v222, a186\n"
  "v_mfma_f32_16x16x32_f16 a[208:211], v[68:71], v[100:103], a[208:211]\n"
  "v_accvgpr_read_b32 v223, a187\n"
  "v_accvgpr_read_b32 v224, a188\n"
  "v_accvgpr_read_b32 v225, a189\n"
  "v_accvgpr_read_b32 v226, a190\n"
  "v_accvgpr_read_b32 v227, a191\n"
  "v_mfma_f32_16x16x32_f16 a[200:203], v[80:83], v[120:123], a[200:203]\n"
  "v_fmac_f32 v216, s76, v224\n"
  "v_fmac_f32 v220, s76, v224\n"
  "v_fmac_f32 v217, s76, v225\n"
  "v_fmac_f32 v221, s76, v225\n"
  "v_fmac_f32 v218, s76, v226\n"
  "v_mfma_f32_16x16x32_f16 a[212:215], v[84:87], v[120:123], a[212:215]\n"
  "v_fmac_f32 v222, s76, v226\n"
  "v_fmac_f32 v219, s76, v227\n"
  "v_fmac_f32 v223, s76, v227\n"
  "v_cvt_pk_bf16_f32 v174, v216, v217\n"
  "v_cvt_pk_bf16_f32 v175, v218, v219\n"
  "v_mfma_f32_16x16x32_f16 a[216:219], v[56:59], v[96:99], a[216:219]\n"
  "v_cvt_pk_bf16_f32 v178, v220, v221\n"
  "v_cvt_pk_bf16_f32 v179, v222, v223\n"
  "v_accvgpr_read_b32 v124, a192\n"
  "v_accvgpr_read_b32 v125, a193\n"
  "v_accvgpr_read_b32 v126, a194\n"
  "v_mfma_f32_16x16x32_f16 a[228:231], v[60:63], v[96:99], a[228:231]\n"
  "s_mov_b64 vcc, s[66:67]\n"
  "v_cndmask_b32_dpp v234, v176, v172, vcc quad_perm:[1,0,3,2] row_mask:0xf bank_mask:0xf\n"
  "v_cndmask_b32_dpp v235, v177, v173, vcc quad_perm:[1,0,3,2] row_mask:0xf bank_mask:0xf\n"
  "v_cndmask_b32_dpp v236, v178, v174, vcc quad_perm:[1,0,3,2] row_mask:0xf bank_mask:0xf\n"
  "v_cndmask_b32_dpp v237, v179, v175, vcc quad_perm:[1,0,3,2] row_mask:0xf bank_mask:0xf\n"
  "v_accvgpr_read_b32 v127, a195\n"
  "v_mfma_f32_16x16x32_f16 a[220:223], v[72:75], v[100:103], a[220:223]\n"
  "s_add_i32 s64, s83, 4096\n"
  "buffer_store_dwordx4 v[234:237], v250, s[72:75], s64 offen sc1\n"
  "s_not_b64 vcc, vcc\n"
  "v_cndmask_b32_dpp v238, v172, v176, vcc quad_perm:[1,0,3,2] row_mask:0xf bank_mask:0xf\n"
  "v_cndmask_b32_dpp v239, v173, v177, vcc quad_perm:[1,0,3,2] row_mask:0xf bank_mask:0xf\n"
  "v_cndmask_b32_dpp v240, v174, v178, vcc quad_perm:[1,0,3,2] row_mask:0xf bank_mask:0xf\n"
  "v_cndmask_b32_dpp v241, v175, v179, vcc quad_perm:[1,0,3,2] row_mask:0xf bank_mask:0xf\n"
  "v_mfma_f32_16x16x32_f16 a[232:235], v[76:79], v[100:103], a[232:235]\n"
  "s_add_i32 s65, s83, 69632\n"
  "buffer_store_dwordx4 v[238:241], v250, s[72:75], s65 offen sc1\n"
  "v_accvgpr_read_b32 v128, a196\n"
  "v_accvgpr_read_b32 v129, a197\n"
  "v_accvgpr_read_b32 v130, a198\n"
  "v_accvgpr_read_b32 v131, a199\n"
  "v_mfma_f32_16x16x32_f16 a[224:227], v[88:91], v[120:123], a[224:227]\n"
  "v_accvgpr_read_b32 v132, a200\n"
  "v_accvgpr_read_b32 v133, a201\n"
  "v_accvgpr_read_b32 v134, a202\n"
  "v_accvgpr_read_b32 v135, a203\n"
  "v_pk_fma_f32 v[124:125], v[132:133], s[76:77], v[124:125]\n"
  "v_mfma_f32_16x16x32_f16 a[236:239], v[92:95], v[120:123], a[236:239]\n"
  "v_pk_fma_f32 v[128:129], v[132:133], s[76:77], v[128:129]\n"
  "v_pk_fma_f32 v[126:127], v[134:135], s[76:77], v[126:127]\n"
  "v_pk_fma_f32 v[130:131], v[134:135], s[76:77], v[130:131]\n"
  "v_cvt_pk_bf16_f32 v172, v124, v125\n"
  "v_cvt_pk_bf16_f32 v173, v126, v127\n"
  "v_cvt_pk_bf16_f32 v176, v128, v129\n"
  "v_cvt_pk_bf16_f32 v177, v130, v131\n"
  "v_accvgpr_read_b32 v136, a204\n"
  "v_accvgpr_read_b32 v137, a205\n"
  "v_accvgpr_read_b32 v138, a206\n"
  "v_accvgpr_read_b32 v139, a207\n"
  "v_accvgpr_read_b32 v140, a208\n"
  "v_accvgpr_read_b32 v141, a209\n"
  "v_accvgpr_read_b32 v142, a210\n"
  "v_accvgpr_read_b32 v143, a211\n"
  "v_accvgpr_read_b32 v144, a212\n"
  "v_accvgpr_read_b32 v145, a213\n"
  "v_accvgpr_read_b32 v146, a214\n"
  "v_accvgpr_read_b32 v147, a215\n"
  "v_pk_fma_f32 v[136:137], v[144:145], s[76:77], v[136:137]\n"
  "v_pk_fma_f32 v[140:141], v[144:145], s[76:77], v[140:141]\n"
  "v_pk_fma_f32 v[138:139], v[146:147], s[76:77], v[138:139]\n"
  "v_pk_fma_f32 v[142:143], v[146:147], s[76:77], v[142:143]\n"
  "v_cvt_pk_bf16_f32 v174, v136, v137\n"
  "v_cvt_pk_bf16_f32 v175, v138, v139\n"
  "v_cvt_pk_bf16_f32 v178, v140, v141\n"
  "v_cvt_pk_bf16_f32 v179, v142, v143\n"
  "v_accvgpr_read_b32 v148, a216\n"
  "v_accvgpr_read_b32 v149, a217\n"
  "v_accvgpr_read_b32 v150, a218\n"
  "s_mov_b64 vcc, s[66:67]\n"
  "v_cndmask_b32_dpp v234, v176, v172, vcc quad_perm:[1,0,3,2] row_mask:0xf bank_mask:0xf\n"
  "v_cndmask_b32_dpp v235, v177, v173, vcc quad_perm:[1,0,3,2] row_mask:0xf bank_mask:0xf\n"
  "v_cndmask_b32_dpp v236, v178, v174, vcc quad_perm:[1,0,3,2] row_mask:0xf bank_mask:0xf\n"
  "v_cndmask_b32_dpp v237, v179, v175, vcc quad_perm:[1,0,3,2] row_mask:0xf bank_mask:0xf\n"
  "v_accvgpr_read_b32 v151, a219\n"
  "s_add_i32 s64, s84, 0\n"
  "buffer_store_dwordx4 v[234:237], v250, s[72:75], s64 offen sc1\n"
  "s_not_b64 vcc, vcc\n"
  "v_cndmask_b32_dpp v238, v172, v176, vcc quad_perm:[1,0,3,2] row_mask:0xf bank_mask:0xf\n"
  "v_cndmask_b32_dpp v239, v173, v177, vcc quad_perm:[1,0,3,2] row_mask:0xf bank_mask:0xf\n"
  "v_cndmask_b32_dpp v240, v174, v178, vcc quad_perm:[1,0,3,2] row_mask:0xf bank_mask:0xf\n"
  "v_cndmask_b32_dpp v241, v175, v179, vcc quad_perm:[1,0,3,2] row_mask:0xf bank_mask:0xf\n"
  "s_add_i32 s65, s84, 65536\n"
  "buffer_store_dwordx4 v[238:241], v250, s[72:75], s65 offen sc1\n"
  "v_accvgpr_read_b32 v152, a220\n"
  "v_accvgpr_read_b32 v153, a221\n"
  "v_accvgpr_read_b32 v154, a222\n"
  "v_accvgpr_read_b32 v155, a223\n"
  "v_accvgpr_read_b32 v156, a224\n"
  "v_accvgpr_read_b32 v157, a225\n"
  "v_accvgpr_read_b32 v158, a226\n"
  "v_accvgpr_read_b32 v159, a227\n"
  "v_pk_fma_f32 v[148:149], v[156:157], s[76:77], v[148:149]\n"
  "v_pk_fma_f32 v[152:153], v[156:157], s[76:77], v[152:153]\n"
  "v_pk_fma_f32 v[150:151], v[158:159], s[76:77], v[150:151]\n"
  "v_pk_fma_f32 v[154:155], v[158:159], s[76:77], v[154:155]\n"
  "v_cvt_pk_bf16_f32 v172, v148, v149\n"
  "v_cvt_pk_bf16_f32 v173, v150, v151\n"
  "v_cvt_pk_bf16_f32 v176, v152, v153\n"
  "v_cvt_pk_bf16_f32 v177, v154, v155\n"
  "v_accvgpr_read_b32 v160, a228\n"
  "v_accvgpr_read_b32 v161, a229\n"
  "v_accvgpr_read_b32 v162, a230\n"
  "v_accvgpr_read_b32 v163, a231\n"
  "v_accvgpr_read_b32 v164, a232\n"
  "v_accvgpr_read_b32 v165, a233\n"
  "v_accvgpr_read_b32 v166, a234\n"
  "v_accvgpr_read_b32 v167, a235\n"
  "v_accvgpr_read_b32 v168, a236\n"
  "v_accvgpr_read_b32 v169, a237\n"
  "v_accvgpr_read_b32 v170, a238\n"
  "v_accvgpr_read_b32 v171, a239\n"
  "v_pk_fma_f32 v[160:161], v[168:169], s[76:77], v[160:161]\n"
  "v_pk_fma_f32 v[164:165], v[168:169], s[76:77], v[164:165]\n"
  "v_pk_fma_f32 v[162:163], v[170:171], s[76:77], v[162:163]\n"
  "v_pk_fma_f32 v[166:167], v[170:171], s[76:77], v[166:167]\n"
  "v_cvt_pk_bf16_f32 v174, v160, v161\n"
  "v_cvt_pk_bf16_f32 v175, v162, v163\n"
  "v_cvt_pk_bf16_f32 v178, v164, v165\n"
  "v_cvt_pk_bf16_f32 v179, v166, v167\n"
  "s_mov_b64 vcc, s[66:67]\n"
  "v_cndmask_b32_dpp v234, v176, v172, vcc quad_perm:[1,0,3,2] row_mask:0xf bank_mask:0xf\n"
  "v_cndmask_b32_dpp v235, v177, v173, vcc quad_perm:[1,0,3,2] row_mask:0xf bank_mask:0xf\n"
  "v_cndmask_b32_dpp v236, v178, v174, vcc quad_perm:[1,0,3,2] row_mask:0xf bank_mask:0xf\n"
  "v_cndmask_b32_dpp v237, v179, v175, vcc quad_perm:[1,0,3,2] row_mask:0xf bank_mask:0xf\n"
  "s_add_i32 s64, s84, 4096\n"
  "buffer_store_dwordx4 v[234:237], v250, s[72:75], s64 offen sc1\n"
  "s_not_b64 vcc, vcc\n"
  "v_cndmask_b32_dpp v238, v172, v176, vcc quad_perm:[1,0,3,2] row_mask:0xf bank_mask:0xf\n"
  "v_cndmask_b32_dpp v239, v173, v177, vcc quad_perm:[1,0,3,2] row_mask:0xf bank_mask:0xf\n"
  "v_cndmask_b32_dpp v240, v174, v178, vcc quad_perm:[1,0,3,2] row_mask:0xf bank_mask:0xf\n"
  "v_cndmask_b32_dpp v241, v175, v179, vcc quad_perm:[1,0,3,2] row_mask:0xf bank_mask:0xf\n"
  "s_add_i32 s65, s84, 69632\n"
  "buffer_store_dwordx4 v[238:241], v250, s[72:75], s65 offen sc1\n"
  "s_waitcnt vmcnt(20)\n"
  "s_branch .Lend_%=\n"
  ".Lpace_s0_None_%=:\n"
  "s_memrealtime s[70:71]\n"
  "s_waitcnt lgkmcnt(0)\n"
  "s_sub_i32 s64, s70, s61\n"
  "s_cmp_lt_i32 s64, 0\n"
  "s_cbranch_scc0 .Lpace2_s0_None_%=\n"
  "s_sleep 1\n"
  "s_branch .Lpace_s0_None_%=\n"
  ".Lpace2_s0_None_%=:\n"
  "s_add_i32 s61, s70, s62\n"
  "s_add_i32 s61, s61, 8\n"
  "s_sub_i32 s62, s62, 14\n"
  "s_branch .Lpaced_s0_None_%=\n"
  ".Lpace_s1_None_%=:\n"
  "s_memrealtime s[70:71]\n"
  "s_waitcnt lgkmcnt(0)\n"
  "s_sub_i32 s64, s70, s61\n"
  "s_cmp_lt_i32 s64, 0\n"
  "s_cbranch_scc0 .Lpace2_s1_None_%=\n"
  "s_sleep 1\n"
  "s_branch .Lpace_s1_None_%=\n"
  ".Lpace2_s1_None_%=:\n"
  "s_add_i32 s61, s70, s62\n"
  "s_add_i32 s61, s61, 0\n"
  "s_sub_i32 s62, s62, 14\n"
  "s_branch .Lpaced_s1_None_%=\n"
  ".Lspin_pe_None_%=:\n"
  "s_mov_b32 s64, 0\n"
  ".Lspin2_pe_None_%=:\n"
  "s_sleep 3\n"
  "buffer_load_dword v247, v246, s[40:43], s54 offen sc1\n"
  "s_add_u32 s64, s64, 1\n"
  "s_waitcnt vmcnt(0)\n"
  "s_cmp_gt_u32 s64, 0x200000\n"
  "s_cbranch_scc1 .Lspun_pe_None_%=\n"
  "v_cmp_eq_u32 vcc, 0, v247\n"
  "s_cbranch_vccnz .Lspin2_pe_None_%=\n"
  "s_branch .Lspun_pe_None_%=\n"
  ".Lpace_pe_None_%=:\n"
  "s_memrealtime s[70:71]\n"
  "s_waitcnt lgkmcnt(0)\n"
  "s_sub_i32 s64, s70, s61\n"
  "s_cmp_lt_i32 s64, 0\n"
  "s_cbranch_scc0 .Lpace2_pe_None_%=\n"
  "s_sleep 1\n"
  "s_branch .Lpace_pe_None_%=\n"
  ".Lpace2_pe_None_%=:\n"
  "s_add_i32 s61, s70, s62\n"
  "s_add_i32 s61, s61, 8\n"
  "s_sub_i32 s62, s62, 14\n"
  "s_branch .Lpaced_pe_None_%=\n"
  ".Lpace_po_None_%=:\n"
  "s_memrealtime s[70:71]\n"
  "s_waitcnt lgkmcnt(0)\n"
  "s_sub_i32 s64, s70, s61\n"
  "s_cmp_lt_i32 s64, 0\n"
  "s_cbranch_scc0 .Lpace2_po_None_%=\n"
  "s_sleep 1\n"
  "s_branch .Lpace_po_None_%=\n"
  ".Lpace2_po_None_%=:\n"
  "s_add_i32 s61, s70, s62\n"
  "s_add_i32 s61, s61, 0\n"
  "s_sub_i32 s62, s62, 14\n"
  "s_branch .Lpaced_po_None_%=\n"
  ".Lspin_s22_None_%=:\n"
  "s_mov_b32 s64, 0\n"
  ".Lspin2_s22_None_%=:\n"
  "s_sleep 3\n"
  "buffer_load_dword v247, v246, s[40:43], s54 offen sc1\n"
  "s_add_u32 s64, s64, 1\n"
  "s_waitcnt vmcnt(0)\n"
  "s_cmp_gt_u32 s64, 0x200000\n"
  "s_cbranch_scc1 .Lspun_s22_None_%=\n"
  "v_cmp_eq_u32 vcc, 0, v247\n"
  "s_cbranch_vccnz .Lspin2_s22_None_%=\n"
  "s_branch .Lspun_s22_None_%=\n"
  ".Lspin_ne_None_%=:\n"
  "s_mov_b32 s64, 0\n"
  ".Lspin2_ne_None_%=:\n"
  "s_sleep 3\n"
  "buffer_load_dword v247, v246, s[40:43], s54 offen sc1\n"
  "s_add_u32 s64, s64, 1\n"
  "s_waitcnt vmcnt(0)\n"
  "s_cmp_gt_u32 s64, 0x200000\n"
  "s_cbranch_scc1 .Lspun_ne_None_%=\n"
  "v_cmp_eq_u32 vcc, 0, v247\n"
  "s_cbranch_vccnz .Lspin2_ne_None_%=\n"
  "s_branch .Lspun_ne_None_%=\n"
  ".Lspin_s28_None_%=:\n"
  "s_mov_b32 s64, 0\n"
  ".Lspin2_s28_None_%=:\n"
  "s_sleep 3\n"
  "buffer_load_dword v247, v246, s[40:43], s54 offen sc1\n"
  "s_add_u32 s64, s64, 1\n"
  "s_waitcnt vmcnt(0)\n"
  "s_cmp_gt_u32 s64, 0x200000\n"
  "s_cbranch_scc1 .Lspun_s28_None_%=\n"
  "v_cmp_eq_u32 vcc, 0, v247\n"
  "s_cbranch_vccnz .Lspin2_s28_None_%=\n"
  "s_branch .Lspun_s28_None_%=\n"
  ".Lend_%=:\n"
    : "={v[180:211]}"(o3), "={v[212:227]}"(o4), "={v[176:179]}"(o5), "={v[80:95]}"(o0), "={v[156:163]}"(o1)
    : "{v[180:211]}"(h0), "{v[212:227]}"(h1), "{v[176:179]}"(hd.vb), "{v[80:95]}"(st0), "{v[156:163]}"(st1), "{s[40:43]}"(cx.rs)
    : "v0", "v1", "v2", "v3", "v4", "v5", "v6", "v7", "v8", "v9", "v10", "v11", "v12", "v13", "v14", "v15", "v16", "v17", "v18", "v19", "v20", "v21", "v22", "v23", "v24", "v25", "v26", "v27", "v28", "v29", "v30", "v31", "v32", "v33", "v34", "v35", "v36", "v37", "v38", "v39", "v40", "v41", "v42", "v43", "v44", "v45", "v46", "v47", "v48", "v49", "v50", "v51", "v52", "v53", "v54", "v55", "v56", "v57", "v58", "v59", "v60", "v61", "v62", "v63", "v64", "v65", "v66", "v67", "v68", "v69", "v70", "v71", "v72", "v73", "v74", "v75", "v76", "v77", "v78", "v79", "v96", "v97", "v98", "v99", "v100", "v101", "v102", "v103", "v104", "v105", "v106", "v107", "v108", "v109", "v110", "v111", "v112", "v113", "v114", "v115", "v116", "v117", "v118", "v119", "v120", "v121", "v122", "v123", "v124", "v125", "v126", "v127", "v128", "v129", "v130", "v131", "v132", "v133", "v134", "v135", "v136", "v137", "v138", "v139", "v140", "v141", "v142", "v143", "v144", "v145", "v146", "v147", "v148", "v149", "v150", "v151", "v152", "v153", "v154", "v155", "v164", "v165", "v166", "v167", "v168", "v169", "v170", "v171", "v172", "v173", "v174", "v175", "v228", "v229", "v230", "v231", "v232", "v233", "v234", "v235", "v236", "v237", "v238", "v239", "v240", "v241", "v242", "v243", "v244", "v245", "v246", "v247", "v248", "v249", "v250", "v251", "v252", "v253", "v254", "v255", "a0", "a1", "a2", "a3", "a4", "a5", "a6", "a7", "a8", "a9", "a10", "a11", "a12", "a13", "a14", "a15", "a16", "a17", "a18", "a19", "a20", "a21", "a22", "a23", "a24", "a25", "a26", "a27", "a28", "a29", "a30", "a31", "a32", "a33", "a34", "a35", "a36", "a37", "a38", "a39", "a40", "a41", "a42", "a43", "a44", "a45", "a46", "a47", "a48", "a49", "a50", "a51", "a52", "a53", "a54", "a55", "a56", "a57", "a58", "a59", "a60", "a61", "a62", "a63", "a64", "a65", "a66", "a67", "a68", "a69", "a70", "a71", "a72", "a73", "a74", "a75", "a76", "a77", "a78", "a79", "a80", "a81", "a82", "a83", "a84", "a85", "a86", "a87", "a88", "a89", "a90", "a91", "a92", "a93", "a94", "a95", "a96", "a97", "a98", "a99", "a100", "a101", "a102", "a103", "a104", "a105", "a106", "a107", "a108", "a109", "a110", "a111", "a112", "a113", "a114", "a115", "a116", "a117", "a118", "a119", "a120", "a121", "a122", "a123", "a124", "a125", "a126", "a127", "a128", "a129", "a130", "a131", "a132", "a133", "a134", "a135", "a136", "a137", "a138", "a139", "a140", "a141", "a142", "a143", "a144", "a145", "a146", "a147", "a148", "a149", "a150", "a151", "a152", "a153", "a154", "a155", "a156", "a157", "a158", "a159", "a160", "a161", "a162", "a163", "a164", "a165", "a166", "a167", "a168", "a169", "a170", "a171", "a172", "a173", "a174", "a175", "a176", "a177", "a178", "a179", "a180", "a181", "a182", "a183", "a184", "a185", "a186", "a187", "a188", "a189", "a190", "a191", "a192", "a193", "a194", "a195", "a196", "a197", "a198", "a199", "a200", "a201", "a202", "a203", "a204", "a205", "a206", "a207", "a208", "a209", "a210", "a211", "a212", "a213", "a214", "a215", "a216", "a217", "a218", "a219", "a220", "a221", "a222", "a223", "a224", "a225", "a226", "a227", "a228", "a229", "a230", "a231", "a232", "a233", "a234", "a235", "a236", "a237", "a238", "a239", "a240", "a241", "a242", "a243", "a244", "a245", "a246", "a247", "a248", "a249", "a250", "a251", "a252", "a253", "a254", "a255", "s44", "s45", "s46", "s47", "s48", "s49", "s50", "s51", "s52", "s53", "s54", "s55", "s56", "s57", "s58", "s59", "s60", "s61", "s62", "s63", "s64", "s65", "s66", "s67", "s68", "s69", "s70", "s71", "s72", "s73", "s74", "s75", "s76", "s77", "s78", "s79", "s80", "s81", "s82", "s83", "s84", "s85", "s86", "s87", "s88", "s89", "vcc", "scc", "m0", "memory");
}

__device__ __forceinline__ void imfma0(f32x4& d, const bf16x8& a, const bf16x8& b, const f32x4& c) {
  asm volatile("v_mfma_f32_16x16x32_bf16 %0, %1, %2, %3" : "=&v"(d) : "v"(a), "a"(b), "v"(c));
}
__device__ __forceinline__ void imfma1(f32x4& d, const bf16x8& a, const bf16x8& b) {
  asm volatile("v_mfma_f32_16x16x32_bf16 %0, %1, %2, %0" : "+v"(d) : "v"(a), "a"(b));
}
#define DPPADD(d, s, CTRL, BANKS) asm volatile("v_add_f32_dpp %0, %1, %1 " CTRL " row_mask:0xf bank_mask:" BANKS : "+v"(d) : "v"(s))
template <int CTRL>
__device__ __forceinline__ float dpp_add(float v) {
  asm("" : "+v"(v));
  return v + __builtin_bit_cast(float, __builtin_amdgcn_update_dpp(0, __builtin_bit_cast(int, v), CTRL, 0xf, 0xf, true));
}

extern "C" __global__ __launch_bounds__(256, 1) void conv_fft(char* ws, const unsigned short* __restrict__ x,
                                                   const unsigned short* __restrict__ w,
                                                   const unsigned short* __restrict__ gamma,
                                                   const unsigned short* __restrict__ beta,
                                                   unsigned short* __restrict__ out) {
  __shared__ __attribute__((aligned(16))) char smem[160 * 1024];
  const unsigned lds0 = (unsigned)(uintptr_t)(lds_void*)smem;
  const int wv = __builtin_amdgcn_readfirstlane(threadIdx.x >> 6);
  Ctx cx;

  {
    unsigned len;
    asm volatile("s_getpc_b64 %0\n.Lcf_a:\n\ts_mov_b32 %1, .Lcf_b-.Lcf_a" : "=s"(cx.pc), "=s"(len));
    cx.wv = wv;
    cx.pfo = len;
    unsigned xcc;
    asm volatile("s_getreg_b32 %0, hwreg(HW_REG_XCC_ID, 0, 3)" : "=s"(xcc));
    unsigned id = blockIdx.x;
    asm volatile("" : "+s"(id));
    cx.id = id;
    cx.vb = (int)((id & 248u) | (id & 256u ? (id >> 10) & 7u : xcc));
    const char* pb = ws + WS_PUB;
    u32x4 &sx = cx.sx, &sw = cx.sw;
    cx.xcc = xcc;
    if ((id | wv) == 0) {
      const unsigned l = threadIdx.x;
      const u32x4 z = {0u, 0u, 0u, 0u};
      {
        const unsigned so = l * (31u * 1024u + 16u) + (4u << 20);
        asm volatile("global_load_dwordx4 %0, %2, %3 nt\n\tglobal_load_dwordx4 %1, %4, %5 nt"
                     : "=&v"(sx), "=&v"(sw) : "v"(so + (l & 3u) * (8u << 20)), "s"(x), "v"(so + (l & 1u) * (6u << 20)), "s"(w) : "memory");
      }
      {
#pragma unroll
        for (int k = 0; k < 2; ++k)
          asm volatile("global_store_dwordx4 %0, %1, %2 sc1" :: "v"(32768 + (l & 7) * 16 + (l & 8) * 512 + (l & 16) * 1024 + (l & 32) * 32 + k * 8192), "v"(z), "s"(pb) : "memory");
        asm volatile("global_store_dwordx4 %0, %1, %2 sc1" :: "v"(l * 16 + 1024), "v"(z), "s"(pb) : "memory");
      }
    }
    if (id) code_touch(cx);
    SB0;
  }
  cx.smem = smem;
  cx.ws = ws;
  cx.gamma = gamma;
  cx.beta = beta;
  run12(cx, x, w);
  ws = cx.ws;
  const int bid = cx.vb, idx = bid >> 3;
  const int slot = (bid & 7) * 4 + (idx >> 3), b = (idx >> 2) & 1, nt = idx & 3;
  const int lane = (int)__builtin_amdgcn_mbcnt_hi(~0u, __builtin_amdgcn_mbcnt_lo(~0u, 0u));
  const int c64i = nt * 2 + ((bid >> 2) & 1), rg = (bid & 1) * 8 + (slot & 1) * 4 + (bid & 2) + (idx >> 4);
  const int bs = 10 * rg, hw = wv & 1, par = wv >> 1;
  const int g = lane >> 4, qq = (lane >> 2) & 3, pp = lane & 3, fr = lane & 15;
  const int lend = min(HOP * (bs + 10), L_);
  const u32x4 rsf = make_rsrc(ws + WS_GRP + nt * 256, 16 * 1024);
  unsigned fw = (unsigned)((lane + (lane & 4)) * 1024 + (b * 32 + slot) * 4);
  unsigned fp = (unsigned)(((bid & 7) + (bid & 4)) * 1024 + (b * 32 + ((lane & 31) ^ ((lane & 31) == slot))) * 4);
  f32x4 y[30], ye[2], cin[2], npv[2];
  {
    const int ee = fr & 1 ? cx.es : -2000;
#pragma unroll
    for (int s = 0; s < 2; ++s) {
      cin[s] = *reinterpret_cast<const f32x4*>(smem + CBUF + (par + (fr & 14)) * 256 + hw * 128 + g * 32 + s * 16);
#pragma unroll
      for (int r = 0; r < 4; ++r) cin[s][r] = __builtin_amdgcn_ldexpf(cin[s][r], ee);
    }
  }
  char* my = smem + wv * 20480;
  unsigned src = (unsigned)(uintptr_t)(lds_void*)my + (unsigned)((4 * g + qq) * 64 + ((g & 1) << 5) + pp * 8), src1 = src ^ 32u;
  unsigned vo = (unsigned)((lane >> 2) * 64 + (((lane & 3) ^ ((lane >> 3) & 2)) << 4)), vp = (unsigned)(g * 8);
  const u32x4 rsy = make_rsrc(ws + WS_Y, ROWS * 65536);
  unsigned so[6];
  {
    const unsigned t0 = (unsigned)(b * NBLK * 65536 + (c64i * 2 + hw) * 4096);
#pragma unroll
    for (int i = 0; i < 6; ++i) {
      const unsigned t = __builtin_amdgcn_readfirstlane(i < 5 ? t0 + (unsigned)(bs + par + 2 * i) * 65536 : t0);
      so[i] = t ^ ((t >> 8) & 8192u);
      asm volatile("" : "+s"(so[i]));
    }
  }
  asm volatile("" : "+v"(cin[0]), "+v"(cin[1]), "+v"(src), "+v"(src1), "+v"(vo), "+v"(vp), "+v"(fw), "+v"(fp));
  SB0;
  wait_vm<0>();
  __builtin_amdgcn_s_barrier();
  unsigned fv = 1u;
  if (wv == 1 && lane < 8) asm volatile("buffer_store_dword %0, %1, %2, 0 offen sc1" :: "v"(fv), "v"(fw), "s"(rsf) : "memory");
  if (wv == 0) asm volatile("buffer_load_dword %0, %1, %2, 0 offen sc1" : "=v"(fv) : "v"(fp), "s"(rsf) : "memory");
  bf16x8 gb[16];
  {
    const unsigned l16 = (unsigned)lane * 16u;
    const char* gt = (const char*)&TAB.G[0][0][0][0][0];
#pragma unroll
    for (int i = 0; i < 16; ++i)
      asm volatile("global_load_dwordx4 %0, %1, %2 offset:%3" : "=a"(gb[i]) : "v"(l16), "s"(gt + (i >> 2) * 4096), "i"((i & 3) * 1024) : "memory");
  }
  SB0;
  constexpr float GC = 0.4686686930532063f, GA = 4.91222953926424f, GL = -1.0933596685052493f;
  float gam = GC * bf_lo(*reinterpret_cast<const unsigned short*>(smem + GAM + wv * 32 + fr * 2));
  float bet = GC * bf_lo(*reinterpret_cast<const unsigned short*>(smem + GAM + 128 + wv * 32 + fr * 2));
  float eps = fmaxf(__builtin_amdgcn_ldexpf(1e-5f, 2 * cx.es), 1e-30f);
  const int l0 = HOP * (bs + par) + fr;
  const int le = fr < 10 ? HOP * (bs + par + 2 * (fr >> 1)) + 48 + (fr & 1) : 1 << 20;
  unsigned oo = (unsigned)((l0 * D_ + (c64i * 2 + hw) * 32 + 8 * g) * 2), oe = (unsigned)((le * D_ + (c64i * 2 + hw) * 32 + 8 * g) * 2);
  float v4[4];
#pragma unroll
  for (int tt = 0; tt < 4; ++tt) v4[tt] = (tt < 3 ? l0 + 8 * HOP + 16 * tt : le) < lend ? 1.f : 0.f;
  unsigned long long* stats = reinterpret_cast<unsigned long long*>(ws + WS_STATS);
  const u32x4 rss = make_rsrc(stats + (b * 32 + (c64i & 1) * 4) * 256 + nt * 64, 65536);
  unsigned sp = (unsigned)((g * 256 + 16 * wv + fr) * 8);
  const unsigned red = lds0 + 8 * 12288, aff = red + 4096;
  unsigned rw_ = red + (unsigned)((par * 64 + hw * 32 + 8 * g + (fr & 7)) * 8 + (fr >> 3) * 4), aw_ = aff + (unsigned)(((g & 1) * 64 + 16 * wv + fr) * 4);
  unsigned ar_ = aff + (unsigned)((hw * 32 + 8 * g) * 4);
  asm volatile("" : "+v"(gam), "+v"(bet), "+v"(eps), "+v"(oo), "+v"(oe), "+v"(sp), "+v"(rw_), "+v"(aw_), "+v"(ar_), "+v"(v4[0]), "+v"(v4[1]), "+v"(v4[2]), "+v"(v4[3]));
  SB0;
  if (wv == 0) {
    wait_vm<16>();
    asm volatile("" : "+v"(fv));
    for (unsigned spins = 0; !__all(fv != 0u) && spins < (1u << 21); ++spins)
      asm volatile("s_sleep 1\n\tbuffer_load_dword %0, %1, %2, 0 offen sc1\n\ts_waitcnt vmcnt(0)" : "=v"(fv) : "v"(fp), "s"(rsf) : "memory");
  }
  __builtin_amdgcn_s_barrier();
  u32x2 pv[2];
  asm volatile("buffer_load_dwordx2 %0, %2, %3, %4 offen sc1\n\tbuffer_load_dwordx2 %1, %2, %3, %4 offen offset:32 sc1"
               : "=&v"(pv[0]), "=&v"(pv[1]) : "v"(vp), "s"(rsy), "s"(so[5]) : "memory");
#pragma unroll
  for (int i = 0; i < 5; ++i)
    asm volatile("s_mov_b32 m0, %0\n\ts_nop 0\n\tbuffer_load_dwordx4 %1, %2, %3 offen sc1 lds\n\tbuffer_load_dwordx4 %1, %2, %3 offen offset:1024 sc1 lds\n\t"
                 "buffer_load_dwordx4 %1, %2, %3 offen offset:2048 sc1 lds\n\tbuffer_load_dwordx4 %1, %2, %3 offen offset:3072 sc1 lds"
                 :: "s"(lds0 + wv * 20480 + i * 4096), "v"(vo), "s"(rsy), "s"(so[i]) : "memory");
  float s1[8], s2[8];
  {
    bf16x8 a[2][4];
    auto rd = [&](int i, int q) __attribute__((always_inline)) {
      const lds_s16x4* p = (const lds_s16x4*)(uintptr_t)((q & 2 ? src1 : src) + i * 4096 + (q & 1) * 2048);
      a[i & 1][q] = cat(__builtin_amdgcn_ds_read_tr16_b64_v4i16((lds_s16x4*)p), __builtin_amdgcn_ds_read_tr16_b64_v4i16((lds_s16x4*)p + 128));
    };
    f32x4 t3[2];
    auto sums = [&](int cp, int q) __attribute__((always_inline)) {
      const int i = cp >> 3, s = (cp >> 2) & 1, tt = cp & 3, e = 4 * s + q;
      if (tt == 3) {
        if (i) ye[s][q] = (fr >> 1) == i ? t3[s][q] : ye[s][q];
        asm volatile("" : "+v"(ye[s]));
        return;
      }
      float d = y[6 * i + 3 * s + tt][q];
      if (i == 4) d *= v4[tt];
      if (i == 0 && tt == 0) { s1[e] = d; s2[e] = d * d; }
      else { s1[e] += d; s2[e] = __builtin_fmaf(d, d, s2[e]); }
      asm volatile("" : "+v"(s1[e]), "+v"(s2[e]));
    };
    wait_vm<16>();
    SB0;
#pragma unroll
    for (int q = 0; q < 4; ++q) { rd(0, q); SB0; }
    asm volatile("" : "+v"(pv[0]), "+v"(pv[1]));
#pragma unroll
    for (int s = 0; s < 2; ++s) {
      npv[s] = f32x4{bf_lo(pv[s][0]), bf_hi(pv[s][0]), bf_lo(pv[s][1]), bf_hi(pv[s][1])} * (-1.0f / 64.0f);
      cin[s] += npv[s];
    }
    asm volatile("" : "+v"(npv[0]), "+v"(npv[1]), "+v"(cin[0]), "+v"(cin[1]));
    asm volatile("s_nop 1");
    SB0;
    sfor<0, 42>([&](auto cc) __attribute__((always_inline)) {
      constexpr int c = cc.v, i = c >> 3, is = (c >> 2) & 1, tt = c & 3;
      if constexpr ((c & 7) == 4 && i < 4) wait_vm<12 - 4 * i>();
      sfor<0, 4>([&](auto qq_) __attribute__((always_inline)) {
        constexpr int q = qq_.v;
        if constexpr (c < 40) {
          f32x4& d = tt < 3 ? y[6 * i + 3 * is + (tt < 3 ? tt : 0)] : i ? t3[is] : ye[is];
          if constexpr (q == 0) imfma0(d, a[i & 1][2 * is], gb[4 * tt + 1], tt == 3 ? cin[is] : npv[is]);
          else imfma1(d, a[i & 1][2 * is + (q >> 1)], gb[4 * tt + (q ^ 1)]);
        }
        SB0;
        if constexpr (((c & 7) == 4 || (c & 7) == 5) && i < 4) { if (q & 1) rd(i + 1, (c & 1) * 2 + (q >> 1)); }
        if constexpr (c >= 2) sums(c - 2, q);
        SB0;
      });
    });
    asm volatile("" :: "v"(npv[0]), "v"(npv[1]), "v"(cin[0]), "v"(cin[1]));
#pragma unroll
    for (int e = 0; e < 8; ++e) {
      const float d = v4[3] != 0.f ? ye[e >> 2][e & 3] : 0.f;
      s1[e] += d;
      s2[e] = __builtin_fmaf(d, d, s2[e]);
    }
  }
  {
    float v[8];
#pragma unroll
    for (int j = 0; j < 8; ++j) { v[j] = s1[j]; }
#pragma unroll
    for (int j = 0; j < 8; ++j) DPPADD(v[j], s1[j], "row_ror:8", "0x3");
#pragma unroll
    for (int j = 0; j < 8; ++j) DPPADD(v[j], s2[j], "row_ror:8", "0xc");
    asm volatile("s_nop 0");
#pragma unroll
    for (int j = 0; j < 4; ++j) DPPADD(v[j], v[j], "row_half_mirror", "0x5");
#pragma unroll
    for (int j = 0; j < 4; ++j) DPPADD(v[j], v[j + 4], "row_half_mirror", "0xa");
    asm volatile("s_nop 0");
#pragma unroll
    for (int j = 0; j < 4; ++j) v[j] = dpp_add<0x4E>(v[j]);
    SB0;
#pragma unroll
    for (int j = 0; j < 4; ++j) v[j] = dpp_add<0xB1>(v[j]);
    const float w0 = fr & 1 ? v[1] : v[0], w1 = fr & 1 ? v[3] : v[2];
    *(__attribute__((address_space(3))) float*)(uintptr_t)rw_ = fr & 2 ? w1 : w0;
  }
  asm volatile("s_waitcnt lgkmcnt(0)" ::: "memory");
  __builtin_amdgcn_s_barrier();
  if (wv == 0) {
    typedef __attribute__((address_space(3))) f32x2 lds_f32x2;
    const f32x2 sm = *(lds_f32x2*)(uintptr_t)(red + lane * 8) + *(lds_f32x2*)(uintptr_t)(red + 512 + lane * 8);
    u32x2 pk = {__float_as_uint(sm[0]), __float_as_uint(sm[1])};
    if (pk[0] == ~0u) pk[0] = ~1u;
    asm volatile("buffer_store_dwordx2 %0, %1, %2, %3 offen sc1" :: "v"(pk), "v"(lane * 8), "s"(rss), "s"((rg + (rg & 12)) * 2048) : "memory");
  }
  {
    u32x2 pk[4];
#define PDLY 10
    __builtin_amdgcn_s_sleep(PDLY);
    for (unsigned spins = 0;; ++spins) {
      asm volatile("buffer_load_dwordx2 %0, %4, %5, 0 offen sc1\n\tbuffer_load_dwordx2 %1, %4, %5, %6 offen sc1\n\tbuffer_load_dwordx2 %2, %4, %5, %7 offen sc1\n\t"
                   "buffer_load_dwordx2 %3, %4, %5, %8 offen sc1\n\ts_waitcnt vmcnt(0)"
                   : "=&v"(pk[0]), "=&v"(pk[1]), "=&v"(pk[2]), "=&v"(pk[3]) : "v"(sp), "s"(rss), "s"(16384), "s"(32768), "s"(49152) : "memory");
      if (__all(max(max(pk[0][0], pk[1][0]), max(pk[2][0], pk[3][0])) != ~0u) || spins > (1u << 20)) break;
      __builtin_amdgcn_s_sleep(1);
    }
    float a = 0.f, q = 0.f;
#pragma unroll
    for (int k = 0; k < 4; ++k) { a += __uint_as_float(pk[k][0]); q += __uint_as_float(pk[k][1]); }
    auto r0 = __builtin_amdgcn_permlane16_swap(__float_as_uint(a), __float_as_uint(q), false, false);
    const unsigned c0 = __float_as_uint(__uint_as_float(r0[0]) + __uint_as_float(r0[1]));
    auto r1 = __builtin_amdgcn_permlane32_swap(c0, c0, false, false);
    const unsigned c1 = __float_as_uint(__uint_as_float(r1[0]) + __uint_as_float(r1[1]));
    auto r2 = __builtin_amdgcn_permlane16_swap(c1, c1, false, false);
    const float mean = __uint_as_float(r2[0]) * (1.0f / (float)L_);
    const float var = fmaxf(__builtin_fmaf(-mean, mean, __uint_as_float(r2[1]) * (1.0f / (float)L_)), 0.f);
    const float gg = gam * __builtin_amdgcn_rsqf(var + eps);
    *(__attribute__((address_space(3))) float*)(uintptr_t)aw_ = g & 1 ? __builtin_fmaf(-mean, gg, bet) : gg;
    asm volatile("s_waitcnt lgkmcnt(0)" ::: "memory");
    __builtin_amdgcn_s_barrier();
  }
  {
    const u32x4 ro = make_rsrc(out + (size_t)b * L_ * D_, lend * (D_ * 2));
    f32x4 ga[2], be[2];
#pragma unroll
    for (int s = 0; s < 2; ++s) {
      typedef __attribute__((address_space(3))) f32x4 lds_f32x4;
      ga[s] = *(lds_f32x4*)(uintptr_t)(ar_ + 16 * s);
      be[s] = *(lds_f32x4*)(uintptr_t)(ar_ + 256 + 16 * s);
    }
    f32x4 kk1 = {GA, GA, GA, GA}, kk2 = {GL, GL, GL, GL}, kk3 = {GC, GC, GC, GC};
    asm volatile("" : "+v"(kk1), "+v"(kk2), "+v"(kk3));
#pragma unroll
    for (int n = 0; n < 16; ++n) {
      const int i = n / 3, tt = n % 3;
      f32x4 o[2];
#pragma unroll
      for (int s = 0; s < 2; ++s) {
        const f32x4 v = (n < 15 ? y[(6 * i + 3 * s + tt) % 30] : ye[s]) * ga[s] + be[s];
        const f32x4 a = kk2 - v * (v * v + kk1);
        f32x4 e;
#pragma unroll
        for (int r = 0; r < 4; ++r) e[r] = __builtin_amdgcn_exp2f(a[r]);
        e += kk3;
#pragma unroll
        for (int r = 0; r < 4; ++r) e[r] = __builtin_amdgcn_rcpf(e[r]);
        o[s] = v * e;
      }
      st16(pack8(o[0], o[1]), ro, n < 15 ? oo : oe, n < 15 ? (2 * HOP * i + 16 * tt) * (D_ * 2) : 0);
      SB0;
    }
  }

  asm volatile(".Lcf_b:\n\ts_nop 0" ::: "memory");
}

extern "C" void kernel_launch(void* const* d_in, const int* in_sizes, int n_in,
                              void* d_out, int out_size, void* d_ws, size_t ws_size,
                              hipStream_t stream) {
  const unsigned short* x = (const unsigned short*)d_in[0];
  const unsigned short* w = (const unsigned short*)d_in[1];
  const unsigned short* gamma = (const unsigned short*)d_in[2];
  const unsigned short* beta = (const unsigned short*)d_in[3];
  char* ws = (char*)(((uintptr_t)d_ws + (2u << 20) - 1) & ~(uintptr_t)((2u << 20) - 1));
  conv_fft<<<256, 256, 0, stream>>>(ws, x, w, gamma, beta, (unsigned short*)d_out);
}
```

```cpp
#include <hip/hip_runtime.h>
#include <stdint.h>

typedef __attribute__((ext_vector_type(8))) short bf16x8;
typedef __attribute__((ext_vector_type(4))) short s16x4;
typedef __attribute__((ext_vector_type(4))) float f32x4;
typedef __attribute__((ext_vector_type(2))) float f32x2;
typedef __attribute__((ext_vector_type(2))) __bf16 bf16x2v;
typedef __attribute__((ext_vector_type(4))) unsigned int u32x4;
typedef __attribute__((ext_vector_type(2))) unsigned int u32x2;
constexpr int NH = 6, NST = 3;
typedef __attribute__((ext_vector_type(2 * NH))) unsigned int u32xh;
typedef __attribute__((ext_vector_type(8))) _Float16 f16x8;
typedef __attribute__((ext_vector_type(2))) _Float16 f16x2;
typedef __attribute__((ext_vector_type(2))) unsigned short u16x2;
typedef __attribute__((address_space(1))) unsigned long long gu64;
typedef __attribute__((address_space(1))) unsigned gu32;
typedef __attribute__((address_space(3))) void lds_void;
typedef __attribute__((address_space(3))) s16x4 lds_s16x4;

constexpr int T_ = 16000, D_ = 512, L_ = 7985;
constexpr int HOP = 50, NBLK = 160, ROWS = 2 * NBLK;

constexpr unsigned WS_S = 0;
constexpr unsigned SBLK = 4u << 20;
constexpr unsigned sbase(unsigned sl) { return sl * 131072 + (sl >> 4) * 8192; }
constexpr unsigned FLG = 32u * SBLK;
constexpr unsigned WS_GRP = WS_S + FLG + 16 * 8192;
constexpr unsigned WS_D = WS_S + FLG + 20 * 8192;
constexpr unsigned WS_L = WS_D + ROWS * 1024;
constexpr unsigned WS_Y = 40u << 20;
constexpr unsigned WS_STATS = 129u << 20;
constexpr unsigned WS_FA = WS_STATS + (1u << 17);
constexpr unsigned WS_PUB = WS_STATS + (1u << 19);
static_assert(WS_L + D_ * D_ * 2 <= WS_STATS && FLG % (16 * 8192) == 0, "workspace layout");

#define RLX_AGENT __ATOMIC_RELAXED, __HIP_MEMORY_SCOPE_AGENT

constexpr double COS64[17] = {1.0, 0.99518472667219688624, 0.98078528040323044913, 0.95694033573220886494,
    0.92387953251128675613, 0.88192126434835502971, 0.83146961230254523708, 0.77301045336273696081,
    0.70710678118654752440, 0.63439328416364549822, 0.55557023301960222474, 0.47139673682599764856,
    0.38268343236508977173, 0.29028467725446236764, 0.19509032201612826785, 0.09801714032956060199, 0.0};
constexpr double c64(int k) { k &= 63; if (k > 32) k = 64 - k; return k <= 16 ? COS64[k] : -COS64[32 - k]; }
constexpr double s64(int k) { return c64(k + 48); }
constexpr unsigned short bf_rne(double v) {
  const unsigned u = __builtin_bit_cast(unsigned, (float)v);
  return (unsigned short)((u + 0x7fffu + ((u >> 16) & 1u)) >> 16);
}
constexpr double bf_val(unsigned short h) { return (double)__builtin_bit_cast(float, (unsigned)h << 16); }
constexpr unsigned short hilo(double v, int lo) { return lo ? bf_rne(v - bf_val(bf_rne(v))) : bf_rne(v); }
constexpr unsigned short nz(unsigned short h) { return h & 0x7fff ? h : (unsigned short)(97 << 7); }
constexpr double R3 = 0.86602540378443864676;
constexpr double dft(int c, int t, double sgn) {
  return (c >> 1) == 0 ? ((c & t) & 1 ? -1.0 : 1.0)
                       : ((c & 1) ? -0.5 * c64((c >> 1) * t) - R3 * sgn * s64((c >> 1) * t) : c64((c >> 1) * t));
}
constexpr double idft(int c, int t) {
  return (c >> 1) == 0 ? ((c & t) & 1 ? -1.0 : 1.0) / 64.0
                       : ((c & 1) ? -s64((c >> 1) * t) / R3 - c64((c >> 1) * t) * (2.0 / 3.0) : c64((c >> 1) * t) * (4.0 / 3.0)) / 32.0;
}
constexpr double UP = 1.0 + 0.72134752 / 256.0;
constexpr int kidx(int g, int j) { return j < 4 ? 4 * g + j : 12 + 4 * g + j; }
struct Tables {
  unsigned short F[4][2][2][64][8];
  unsigned short V[2][4][64][8];
  unsigned short G[4][2][2][64][8];
};
constexpr Tables make_tables() {
  Tables tb{};
  for (int l = 0; l < 64; ++l)
    for (int j = 0; j < 8; ++j) {
      const int g = l >> 4, n = l & 15;
      for (int ct = 0; ct < 4; ++ct) {
        for (int ks = 0; ks < 2; ++ks)
          for (int h = 0; h < 2; ++h) tb.F[ct][ks][h][l][j] = nz(hilo(UP * dft(16 * ct + n, 32 * ks + kidx(g, j), -1.0), h));
        for (int p = 0; p < 2; ++p) {
          const int tap = 4 * g + (j & 3);
          tb.V[p][ct][l][j] = (p == 1 && tap == 15) ? (unsigned short)0 : nz(hilo(UP * dft(16 * ct + n, tap, 1.0), j >> 2));
        }
      }
      for (int tt = 0; tt < 4; ++tt)
        for (int ks = 0; ks < 2; ++ks)
          for (int h = 0; h < 2; ++h) {
            const int c = 32 * ks + kidx(g, j);
            tb.G[tt][ks][h][l][j] = hilo(idft(c, 16 * tt + (tt < 3 ? n : n & 1)), h);
          }
    }
  return tb;
}
__device__ const __attribute__((aligned(16))) Tables TAB = make_tables();

__device__ __forceinline__ unsigned pk_bf16(float lo, float hi) {
  return __builtin_bit_cast(unsigned, __builtin_convertvector(f32x2{lo, hi}, bf16x2v));
}
__device__ __forceinline__ float bf_lo(unsigned v) { return __uint_as_float(v << 16); }
__device__ __forceinline__ float bf_hi(unsigned v) { return __uint_as_float(v & 0xffff0000u); }
__device__ __forceinline__ u32x4 pack8(f32x4 a, f32x4 b) {
  return u32x4{pk_bf16(a[0], a[1]), pk_bf16(a[2], a[3]), pk_bf16(b[0], b[1]), pk_bf16(b[2], b[3])};
}
__device__ __forceinline__ unsigned pk_f16(float lo, float hi) {
  return (__builtin_bit_cast(unsigned, __builtin_amdgcn_cvt_pkrtz(lo, hi)) & 0xfff8fff8u);
}
__device__ __forceinline__ u32x4 pack8h(f32x4 a, f32x4 b) {
  return u32x4{pk_f16(a[0], a[1]), pk_f16(a[2], a[3]), pk_f16(b[0], b[1]), pk_f16(b[2], b[3])};
}
__device__ __forceinline__ unsigned add_h(const bf16x8& a, const bf16x8& b, int d) {
  const unsigned x = __builtin_bit_cast(u32x4, a)[d], y = __builtin_bit_cast(u32x4, b)[d];
  unsigned r = __builtin_bit_cast(unsigned, __builtin_bit_cast(f16x2, x) + __builtin_bit_cast(f16x2, y));
  return r;
}
__device__ __forceinline__ unsigned amax(unsigned m, unsigned v) {
  return __builtin_bit_cast(unsigned, __builtin_elementwise_max(__builtin_bit_cast(u16x2, m), __builtin_bit_cast(u16x2, v & 0x7fff7fffu)));
}
struct Rng { f16x2 hi = {0, 0}, lo = {0, 0}; };
__device__ __forceinline__ void widen(Rng& r, unsigned a, unsigned b) {
  const f16x2 x = __builtin_bit_cast(f16x2, a), y = __builtin_bit_cast(f16x2, b);
  r.hi = __builtin_elementwise_maximum(__builtin_elementwise_maximum(r.hi, x), y);
  r.lo = __builtin_elementwise_minimum(__builtin_elementwise_minimum(r.lo, x), y);
}
__device__ __forceinline__ unsigned amax(const Rng& r) { return amax(amax(0, __builtin_bit_cast(unsigned, r.hi)), __builtin_bit_cast(unsigned, r.lo)); }
__device__ __forceinline__ unsigned expo(unsigned m) { return max(m & 0xffffu, m >> 16) >> 7; }
__device__ __forceinline__ unsigned wave_max(unsigned v) {
  unsigned m = 0;
#pragma unroll 1
  for (int l = 0; l < 64; ++l) m = max(m, (unsigned)__builtin_amdgcn_readlane((int)v, l));
  return m;
}
__device__ __forceinline__ void scale8(bf16x8& v, int e) {
  u32x4 u = __builtin_bit_cast(u32x4, v);
#pragma unroll
  for (int d = 0; d < 4; ++d)
    u[d] = __builtin_amdgcn_perm(__float_as_uint(__builtin_amdgcn_ldexpf(bf_hi(u[d]), e)), __float_as_uint(__builtin_amdgcn_ldexpf(bf_lo(u[d]), e)), 0x07060302u);
  v = __builtin_bit_cast(bf16x8, u);
}
__device__ __forceinline__ void restart(unsigned id) {
  asm volatile("s_waitcnt vmcnt(0)\n\ts_barrier\n\ts_branch conv_fft" :: "{s[0:1]}"(__builtin_amdgcn_kernarg_segment_ptr()), "{s2}"(id), "{v0}"(threadIdx.x) : "memory");
}
__device__ __forceinline__ s16x4 tr_read(const char* p) {
  return __builtin_amdgcn_ds_read_tr16_b64_v4i16((lds_s16x4*)p);
}
__device__ __forceinline__ bf16x8 cat(s16x4 a, s16x4 b) {
  const u32x2 x = __builtin_bit_cast(u32x2, a), y = __builtin_bit_cast(u32x2, b);
  return __builtin_bit_cast(bf16x8, u32x4{x[0], x[1], y[0], y[1]});
}
#define MFMA(a, b, c) __builtin_amdgcn_mfma_f32_16x16x32_bf16(a, b, c, 0, 0, 0)
#define MFMH(a, b, c) __builtin_amdgcn_mfma_f32_16x16x32_f16(__builtin_bit_cast(f16x8, a), __builtin_bit_cast(f16x8, b), c, 0, 0, 0)
#define SB0 __builtin_amdgcn_sched_barrier(0)
#define ZERO4 (f32x4{0.f, 0.f, 0.f, 0.f})
#define DMA16(POL, rs, lds, voff, soff) \
  asm volatile("s_mov_b32 m0, %0\n\ts_nop 0\n\tbuffer_load_dwordx4 %1, %2, %3 offen " POL " lds" \
               :: "s"(lds), "v"(voff), "s"(rs), "s"(soff) : "memory")
__device__ __forceinline__ void st16(const u32x4& v, const u32x4& rs, unsigned voff, unsigned soff) {
  asm volatile("buffer_store_dwordx4 %0, %1, %2, %3 offen sc1\n\ts_nop 1" :: "v"(v), "v"(voff), "s"(rs), "s"(soff) : "memory");
}
__device__ __forceinline__ void ld16(bf16x8& v, const void* p) {
  asm volatile("global_load_dwordx4 %0, %1, off" : "=v"(v) : "v"(p) : "memory");
}
template <int N> __device__ __forceinline__ void wait_vm() { asm volatile("s_waitcnt vmcnt(%0)" :: "i"(N) : "memory"); }
__device__ __forceinline__ u32x4 make_rsrc(const void* p, unsigned bytes) {
  const unsigned long long a = (unsigned long long)p;
  return u32x4{(unsigned)a, (unsigned)(a >> 32) & 0xffffu, bytes, 0x00020000u};
}
__device__ __forceinline__ void spin_flags(const char* p) {
  for (unsigned spins = 0; !__all(__hip_atomic_load((const gu32*)p, RLX_AGENT) != 0u) && spins < (1u << 21); ++spins)
    __builtin_amdgcn_s_sleep(3);
}

typedef __attribute__((ext_vector_type(8))) unsigned int u32x8;
typedef __attribute__((ext_vector_type(16))) unsigned int u32x16;
typedef __attribute__((ext_vector_type(32))) unsigned int u32x32;
constexpr int GW = 10;
constexpr int NQ1 = GW / 2, NQW = 16 - NQ1;
constexpr int STG = 36 * 1024;
constexpr int RAWB = 67 * 1024, XT1 = RAWB + NQ1 * 4096, SINK = XT1 + RAWB;
constexpr int RAWL = 3 * STG;
constexpr int RAW0 = RAWL + 12 * 4096;
constexpr int GAM = RAWL + 36864, CBUF = RAWL + 40960;

struct Ctx {

  char* smem;
  char* ws;
  const unsigned short *gamma, *beta;
  u32x4 rs;
  unsigned lds0;
  unsigned voffw, voffx;
  unsigned poff, xo;
  unsigned id;
  unsigned tag[2];
  int es;
  int wv, vb;
  unsigned long long pc;
  unsigned pfo;
  u32x4 sx, sw;
  unsigned xcc;
};
__device__ __forceinline__ void code_touch(const Ctx& cx) {
  if ((cx.wv ^ (cx.vb >> 3)) & 3) return;
  const unsigned l = __builtin_amdgcn_mbcnt_hi(~0u, __builtin_amdgcn_mbcnt_lo(~0u, cx.wv * 64u));
#pragma unroll
  for (int k = 0; k < 2; ++k)
    asm volatile("s_mov_b32 m0, %0\n\ts_nop 0\n\tglobal_load_lds_dword %1, %2" :: "s"(SINK + cx.wv * 512 + k * 256), "v"(min((l + k * 256) * 128u, cx.pfo)), "s"(cx.pc) : "memory");
}
__device__ __forceinline__ void stage_issue(const Ctx& cx, int kc) {
  const unsigned slot = (unsigned)((kc + 1) % 3) * STG;
#pragma unroll
  for (int i = 0; i < 9; ++i) {
    const int j = i < 4 ? i : i - 4;
    const bool ahead = i < 4 || j == 0 || j == 3;
    const unsigned lo = i < 4 ? j * 4096 : j < 3 ? 16384 + j * 4096 : j == 3 ? 16384 + 10240 : 16384 + 14336;
    const unsigned go = i < 4 ? j * 4096 : (j >> 1) * 16384 + (j & 1) * 4096;
    if (kc >= 0 || ahead)
      DMA16("sc1", cx.rs, cx.lds0 + slot + lo + (i == 6 ? (cx.wv & 2) * 4096 : 0), i < 4 ? cx.voffw : cx.voffx, (unsigned)(kc + ahead) * SBLK + go);
  }
}
__device__ __forceinline__ unsigned x_place(unsigned kb, unsigned blk) { return kb + (blk + (blk & 192u)) * 128u; }
template <int V> struct IC { static constexpr int v = V; constexpr operator int() const { return V; } };
template <int A, int B, class F> __device__ __forceinline__ void sfor(F f) {
  if constexpr (A < B) { f(IC<A>{}); sfor<A + 1, B>(f); }
}
__device__ __forceinline__ unsigned dpp_swap1(unsigned v) {
  return (unsigned)__builtin_amdgcn_update_dpp(0, (int)v, 0xB1, 0xf, 0xf, false);
}

__device__ __forceinline__ int run_bs(int vb, int t) { return 5 * ((vb >> 4) + 16 * ((vb ^ t) & 1)); }
__device__ __forceinline__ int run_q(int vb, int t) { return (((vb >> 1) & 1) + ((vb >> 2) & 2) + 2 * t) & 3; }
__device__ __forceinline__ unsigned x_addr(int vb, int t, int wv) {
  const int lane = threadIdx.x & 63, q = run_q(vb, t);
  const unsigned c = (unsigned)((lane & 15) ^ (((4 * wv + (lane >> 4)) & 7) << 1));
  return (unsigned)(2 * (HOP * run_bs(vb, t) + 4 * wv + (lane >> 4)) + ((t ^ q ^ (q >> 1)) & 1)) * 1024u + c * 16 + q * 256;
}
__device__ __forceinline__ u32x4 x_rsrc(const unsigned short* x, int vb) { return make_rsrc(x + (size_t)((vb >> 2) & 1) * T_ * D_, T_ * D_ * 2); }
__device__ __forceinline__ void issue_x(int t, int wv, unsigned a, const u32x4& rx, unsigned lds0, int j0 = 0, int j1 = 17) {
#pragma unroll
  for (int jj = 0; jj < 17; ++jj)
    if (jj >= j0 && jj < j1 && (jj < 16 || wv < 3)) DMA16("nt", rx, lds0 + t * XT1 + wv * 1024 + jj * 4096, a, jj * 32768u);
}
#define CKP(i) CK(i)
#define CKQ(p)
#define CKE(i)
#define CKI(i)
__device__ __forceinline__ void mfma0(f32x4& d, const bf16x8& a, const bf16x8& b) {
  asm volatile("v_mfma_f32_16x16x32_bf16 %0, %1, %2, 0" : "=&v"(d) : "v"(a), "v"(b));
}
__device__ __forceinline__ void mfma1(f32x4& d, const bf16x8& a, const bf16x8& b) {
  asm volatile("v_mfma_f32_16x16x32_bf16 %0, %1, %2, %0" : "+v"(d) : "v"(a), "v"(b));
}
__device__ __forceinline__ bf16x8 dup(s16x4 a) {
  u32x2 lo = __builtin_bit_cast(u32x2, a), hi;
  asm volatile("v_mov_b64 %0, %1" : "=v"(hi) : "v"(lo));
  return __builtin_bit_cast(bf16x8, u32x4{lo[0], lo[1], hi[0], hi[1]});
}
__device__ __forceinline__ void scale8p(bf16x8& v, int e) {
  typedef __attribute__((ext_vector_type(8))) unsigned short u16x8;
  const unsigned short h = (unsigned short)(e << 7);
  v = __builtin_bit_cast(bf16x8, (u16x8)(__builtin_bit_cast(u16x8, v) + h));
}
__device__ __forceinline__ void pin(u32x4& v) { asm volatile("" : "+v"(v)); }
struct XR { s16x4 t[8]; };
#define P0_ 16
#define SLP_ 3
#define GPO_ 101u
#define GPE_ 149u
#define CT_ 0
#define PRK_ 16
#define TE_ 6
#define LDN_ 4
#define NA_ 14
#define SX0_ 9
#define SCN_ 8
#define PG_ 42
#define PS_ 42
#define NFA_ 2
constexpr int P0 = P0_, P1 = 46;
constexpr int PG = PG_, NFA = NFA_;
constexpr int TE = TE_;
constexpr int NLD = 17 - TE + 1 + NQW + 1;
constexpr int ld_pos(int l) { return 2 * l / LDN_; }
constexpr int ld_slot(int l) { int o = 0; for (int k = 0; k < l; ++k) o += ld_pos(k) == ld_pos(l); return 1 + 2 * o; }
constexpr int NIT = 46, NS = 40, NA = NA_, NB = 26;
static_assert(NH == 6 && NST == 3, "x blocks stored in the loop");
constexpr int ord(int p) { return p == 0 ? 0 : p < 6 ? p + 2 : p < 8 ? p - 5 : p; }
constexpr int st_w(int j) { return j < 2 ? j : j < 7 ? -1 : j < 15 ? j - 5 : -1; }
constexpr int st_x(int j) { return j < 7 ? j - 2 : j - 10; }
constexpr int st_pos(int j) { return st_w(j) >= 0 ? (j < 9 ? 0 : j < 13 ? 6 : 7) : j < 7 ? j - 1 : j - 7; }
#define NBP_ 4
constexpr int NBP = NBP_;
constexpr int st_first(int h) {
  int J = h <= P0 ? 0 : h >= P1 ? NS : h <= P0 + NBP ? (h - P0) * (NA + NFA) / NBP : NA + NFA + (h - P0 - NBP) * (NS - NA - NFA) / (P1 - P0 - NBP);
  for (int k = 0; k < J; ++k)
    if (2 * st_pos(k >> 1) + (k & 1) + 2 > h - 1) return k;
  return J;
}
constexpr int sc_pos(int k) { return k < 17 ? k * SX0_ / 17 : SX0_ + (k - 17) * (P0 - SX0_) / 17; }
constexpr int sc_raw(int k) { return k < NQ1 ? k : P0 + (k - NQ1) * (NBP - 1) / NQW; }
constexpr int sc_cnt(int h) { int c = 0; for (int k = 0; k < 34; ++k) c += sc_pos(k) == h; return c; }
constexpr bool sc_ok() { for (int h = 0; h < NIT; ++h) if (sc_cnt(h) > 3) return false; return true; }
constexpr int sc_wait(int h) {
  int j = -1;
  for (int k = 17; k < 34; ++k) if (sc_pos(k) == h) j = k - 17;
  if (j < 0) return -1;
  int c = j < TE ? TE - 1 - j : 0;
  for (int l = 0; l < NLD; ++l)
    if (ld_pos(l) < h && l > j - TE) c += l != 16 - TE;
  return c;
}
constexpr int XBAR = st_first(25) > NA + NFA - 1 ? -1 : 24;
static_assert(ld_pos(NLD - 1) < P0 && P0 < 24 && LDN_ <= 8, "loads before the first store");
static_assert(sc_ok() && st_first(P0 + NBP) == NA + NFA && st_first(P0 + NBP - 1) <= NA + NFA - 1 && SX0_ < P0, "range check before the first hand-off");
struct Hand {
  u32x4 held[2 * NH], st[2 * NST];
  bf16x8 vb;
};
constexpr int A_RAW = 144, A_WL = 184, A_VEC = 188, A_SCA = 214, A_XA = 240;
template <int I> __device__ __forceinline__ void stash(unsigned v) { asm volatile("v_accvgpr_write_b32 a%c0, %1" :: "i"(I), "v"(v)); }
template <int I> __device__ __forceinline__ unsigned unstash() {
  unsigned v;
  asm volatile("v_accvgpr_read_b32 %0, a%c1" : "=v"(v) : "i"(I));
  return v;
}
__device__ __forceinline__ void stash4(int a, const u32x4& v) {
  asm volatile("v_accvgpr_write_b32 a%c0, %4\n\tv_accvgpr_write_b32 a%c1, %5\n\tv_accvgpr_write_b32 a%c2, %6\n\tv_accvgpr_write_b32 a%c3, %7"
               :: "i"(a), "i"(a + 1), "i"(a + 2), "i"(a + 3), "v"(v[0]), "v"(v[1]), "v"(v[2]), "v"(v[3]));
}
constexpr int hw_reg(int mg, int q, int half) { return ((mg * 5 + q) * 2 + half) * 4; }
template <int A> __device__ __forceinline__ void load_acc(unsigned vo, const u32x4& rs, unsigned so) {
  asm volatile("buffer_load_dwordx4 a[%c3:%c4], %0, %1, %2 offen nt" :: "v"(vo), "s"(rs), "s"(so), "i"(A), "i"(A + 3) : "memory");
}
__device__ __forceinline__ void ld8(u32x2& v, const char* p, unsigned o) {
  asm volatile("global_load_dwordx2 %0, %1, %2 sc1" : "=v"(v) : "v"(o), "s"(p) : "memory");
}
__device__ __forceinline__ void loop_setup(const Ctx& cx, unsigned pwr, unsigned prd, unsigned hst) {
  const int bid = cx.vb, idx = bid >> 3, wv = cx.wv, lane = threadIdx.x & 63;
  const int slot = (bid & 7) * 4 + (idx >> 3), b = (idx >> 2) & 1, nt = idx & 3, xb = (bid >> 2) & 1;
  const int mg = wv >> 1, nh = wv & 1, g = lane >> 4, qq = (lane >> 2) & 3, pp = lane & 3, rt = 4 * g + qq;
  const int rg = (bid & 1) * 8 + (slot & 1) * 4 + (bid & 2) + (idx >> 4);
  sfor<0, 4>([&](auto c) __attribute__((always_inline)) {
    stash<A_VEC + c.v>((unsigned)(rt * 512 + (((nh * 4 + c.v) ^ (rt & 7)) << 5) + pp * 8));
    stash<A_VEC + 5 + c.v>((RAW0 + prd) ^ (unsigned)(c.v << 5));
  });
  { const int r = lane & 15; stash<A_VEC + 4>((unsigned)(16384 + mg * 5 * 2048 + (r >> 3) * 1024 + (r & 7) * 64 + ((g ^ ((r >> 3) << 1)) << 4))); }
  stash<A_VEC + 9>(cx.voffw);
  stash<A_VEC + 10>(cx.voffx);
  {
    const int kr8 = lane >> 3;
    stash<A_VEC + 11>((unsigned)(WS_L - WS_S + (8 * wv + kr8) * 1024 + (nt * 2 + xb) * 128 + (((lane & 7) ^ ((kr8 & 2) | (wv & 1) << 2)) << 4)));
  }
  stash<A_VEC + 12>((unsigned)(WS_D - WS_S + (b * NBLK + 10 * rg + min(lane & 15, 9)) * 1024 + g * 16));
  stash<A_VEC + 13>((unsigned)(RAWL + (8 * g + qq) * 128 + ((wv ^ ((qq >> 1) | (g & 1) << 1)) << 5) + pp * 8));
  stash<A_VEC + 14>(cx.poff);
  stash<A_VEC + 15>(cx.xo);
  stash<A_VEC + 16>(hst);
  stash<A_VEC + 17>(wv == 0 && lane < 8 ? (unsigned)(lane * 1024 + (bid >> 2) * 4) : 0x80000000u);
  stash<A_VEC + 18>((unsigned)((bid & 7) * 1024 + wv * 256 + lane * 4));
  {
    const char* gp = (const char*)(lane < 32 ? cx.gamma : cx.beta) + (nt * 2 + xb) * 128 + (lane & 31) * 4;
    stash<A_VEC + 20>((unsigned)(uintptr_t)gp);
    stash<A_VEC + 21>((unsigned)((uintptr_t)gp >> 32));
  }
  stash<A_VEC + 22>((unsigned)((b * NBLK + mg * 80 + (lane & 14)) * 65536 + nt * 16384 + g * 16 + (2 * slot + (lane & 1)) * 64));
  {
    unsigned len;
    asm volatile("s_mov_b32 %0, .Lcf_b-.Lcf_t-64" : "=s"(len));
    stash<A_VEC + 23>(min((unsigned)(wv * 64 + lane) * 128u, len));
  }
  stash<A_VEC + 24>(RAWL + pwr);
  stash<A_VEC + 25>((unsigned)(CBUF + (lane & 15) * 256 + wv * 64 + g * 16));
  const unsigned long long gt = (unsigned long long)&TAB.G[0][0][0][0][0];
  unsigned sc[15] = {cx.lds0, (unsigned)(wv & 2) * 4096u, FLG + 2 * 8192 + (bid & 3) * 256, wv == nt ? 0x3ffcu : 0x444u,
                     NQ1 * 2 * SBLK + ((bid + NQ1) & 3) * 16384, wv == 3 ? (bid & 1 ? GPO_ : GPE_) - 14u : 0u, slot ? 0xbf800000u : 0u,
                     0, 0, 0, 0, 0, ((wv ^ idx) & 3) == 0 ? RAWL + 32768 + wv * 256u : 0u, (unsigned)gt, (unsigned)(gt >> 32)};
#pragma unroll
  for (int m = 0; m < 5; ++m) {
    const int xt = mg ? (m + 1) % 5 : m;
    sc[7 + m] = (unsigned)(xt * 16 * 65536 + ((b * 10 + mg * 5 + 2 * nh + xt) & 2) * 4096);
  }
  unsigned sv = 0;
#pragma unroll
  for (int i = 0; i < 15; ++i)
    asm volatile("v_writelane_b32 %0, %1, %2" : "+v"(sv) : "s"(__builtin_amdgcn_readfirstlane((int)sc[i])), "i"(i));
  stash<A_SCA>(sv);
}
__device__ __forceinline__ void phase1(Ctx& cx, const unsigned short* __restrict__ x, const unsigned short* __restrict__ w, Hand& hd) {
  u32x4 rq0;
  static_assert(NQ1 == 5, "phase-1 wait counts assume five quanta");
  char* smem = cx.smem;
  const unsigned lds0 = (unsigned)(uintptr_t)(lds_void*)smem;
  const int idx = cx.vb >> 3, wv = cx.wv, lane = threadIdx.x & 63;
  const u32x4 rw = make_rsrc(w, 31 * D_ * D_ * 2);
  const int tap = threadIdx.x >> 4, pc = threadIdx.x & 15;
  const unsigned roff = (unsigned)(tap * (2 * D_ * D_ * 2) + pc * 16);
  const int ng = wv;
  const int g = lane >> 4, qq = (lane >> 2) & 3, pp = lane & 3;
  u32x4 r1[NQ1];
  bf16x8 fb[2][2];
  unsigned long long tag;
  const char* rec = cx.ws + WS_PUB + ((cx.id & 7) * 8 + wv) * 16;
  u32x4 pr;
  unsigned pwr, prd, pvo, pww, xb0[4], pq[4], xa[2];
  const int kr = (cx.vb >> 7) * 32 + (cx.vb & 4) * 4 + (idx & 15);
  auto raw_so = [&](int q) {
    return (unsigned)(kr + 64 * (2 * (q & 3) + ((q >> 2) & 1)) + 512 * (((q >> 3) ^ q ^ (q >> 1)) & 1)) * 1024u + (unsigned)((cx.vb + q) & 3) * 256u;
  };
  auto raw_load = [&](u32x4& r, int q) {
    asm volatile("buffer_load_dwordx4 %0, %1, %2, %3 offen nt" : "=v"(r) : "v"(roff), "s"(rw), "s"(raw_so(q)) : "memory");
  };
  const unsigned xa1 = x_addr(cx.vb, 1, wv);
  {
    const int vb = cx.vb;
    auto setup = [&]() __attribute__((always_inline)) {
    {
      const int bid = vb, xb = (bid >> 2) & 1, krow = kr;
      const int slot = (bid & 7) * 4 + (idx >> 3), b = (idx >> 2) & 1, nt = idx & 3;
      const int rt = 4 * g + qq;
      pvo = (unsigned)((bid & 7) * 1024 + wv * 256 + lane * 4);
      pwr = (unsigned)((pc >> 3) * 2048 + tap * 128 + ((((pc >> 1) ^ (tap >> 1)) & 3) << 5) + (pc & 1) * 16);
      prd = (unsigned)(rt * 128 + (((rt >> 1) & 3) << 5) + pp * 8);
      cx.poff = (unsigned)(sbase(8 * ng + ((lane >> 1) & 7)) + (krow >> 5) * SBLK + (krow & 31) * 512 + (lane & 1) * 64 + g * 16);
      cx.xo = (unsigned)(sbase(8 * ng + ((lane >> 1) & 7)) + 65536 + xb * 8192 + (lane & 1) * 64 + g * 16);
      cx.lds0 = lds0 + wv * 1024;
#pragma unroll
      for (int k = 0; k < 4; ++k) {
        xb0[k] = (unsigned)((4 * g + qq) * 256 + (((4 * g + qq + 2 * k) & 7) << 5) + pp * 8);
        pq[k] = (prd + RAWB) ^ (unsigned)(k << 5);
      }
      pww = pwr + RAWB;
      asm volatile("" : "+v"(xb0[0]), "+v"(xb0[1]), "+v"(xb0[2]), "+v"(xb0[3]), "+v"(pq[0]), "+v"(pq[1]), "+v"(pq[2]), "+v"(pq[3]), "+v"(pww));
      {
        const int row = 2 * wv + (lane >> 5), p = ((lane & 31) >> 1) ^ (row & 7);
        const int chunk = ((p >> 2) & 1) * 8 + ((p >> 1) & 1) * 4 + ((p >> 3) & 1) * 2 + (p & 1);
        cx.voffw = (unsigned)(sbase(slot) + (nt * 32 + row) * 512 + chunk * 32 + (lane & 1) * 16);
        cx.voffx = (unsigned)(sbase(slot) + 65536 + b * 8192 + (8 * wv + ((lane >> 2) & 7)) * 128 + (lane >> 5) * 64 + (((lane & 3) ^ ((wv & 1) << 1)) << 4));
      }
    }
    asm volatile("" : "+v"(pvo), "+v"(cx.poff), "+v"(cx.xo), "+v"(cx.voffw), "+v"(cx.voffx));
    {
      const unsigned so1 = (unsigned)(16 + 2 * run_q(vb, 1)) * SBLK;
      const int e = lane & 31, n = (lane < 32 ? 20 - NH : 10) + (e >> 1);
      unsigned hst = e < (lane < 32 ? 2 * NH : 8) ? x_place(so1 + (n >= 15 ? 8 * SBLK : 0) + (e & 1) * SBLK, (unsigned)(run_bs(vb, 1) + n % 5)) : 0xf0000000u;
      if ((lane >> 1) == 20 && run_q(vb, 0)) hst = x_place((unsigned)(8 + 2 * run_q(vb, 0) + (e & 1)) * SBLK, (unsigned)(run_bs(vb, 0) + 4));
      loop_setup(cx, pwr, prd, hst);
    }
      };
    if (cx.id | wv) issue_x(0, wv, x_addr(vb, 0, wv), x_rsrc(x, vb), lds0);
    if (cx.id == 0 && wv) {
#pragma unroll
      for (int k = 0; k < 6; ++k)
        if (k < 5 || wv < 3) DMA16("nt", x_rsrc(x, vb), lds0 + (3 * k + wv - 1) * 4096, x_addr(vb, 0, 0), (3 * k + wv - 1) * 32768u);
    }
    SB0;
#pragma unroll
    for (int k = 0; k < NQ1; ++k) raw_load(r1[k], k);
#pragma unroll
    for (int i = 0; i < 4; ++i) ld16(fb[i >> 1][i & 1], TAB.F[ng][i >> 1][i & 1][lane]);
    ld16(hd.vb, TAB.V[0][ng][lane]);
    if (cx.id | wv) asm volatile("global_load_dwordx4 %0, %1, %2 sc1" : "=v"(pr) : "v"(0u), "s"(rec) : "memory");
    SB0;
    setup();
    SB0;
    if ((cx.id | wv) == 0) {
      wait_vm<3 + NQ1 + 5>();
      u32x4 &sx = cx.sx, &sw = cx.sw;
      asm volatile("" : "+v"(sx), "+v"(sw));
      unsigned m = expo(amax(amax(amax(amax(0, sx[0]), sx[1]), sx[2]), sx[3])) | expo(amax(amax(amax(amax(0, sw[0]), sw[1]), sw[2]), sw[3])) << 16;
      m = amax(m, (unsigned)__builtin_amdgcn_update_dpp(0, (int)m, 0x128, 0xf, 0xf, false));
      m = amax(m, (unsigned)__builtin_amdgcn_update_dpp(0, (int)m, 0x124, 0xf, 0xf, false));
      m = amax(m, (unsigned)__builtin_amdgcn_update_dpp(0, (int)m, 0x122, 0xf, 0xf, false));
      m = amax(m, (unsigned)__builtin_amdgcn_update_dpp(0, (int)m, 0x121, 0xf, 0xf, false));
      unsigned ex = 28, ew = 28;
#pragma unroll
      for (int l = 0; l < 64; l += 16) {
        const unsigned v = (unsigned)__builtin_amdgcn_readlane((int)m, l);
        ex = max(ex + 2, v & 255u) - 2;
        ew = max(ew + 3, v >> 16) - 3;
      }
      const unsigned long long tg = __builtin_amdgcn_s_memrealtime();
      const unsigned lo = (unsigned)tg << 4 | cx.xcc << 1, hi = ((unsigned)(tg >> 28) & 0xffffu) | min(ex, 220u) << 16 | min(ew, 220u) << 24;
      asm volatile("global_store_dwordx4 %0, %1, %2 sc1\n\ts_nop 1" :: "v"(threadIdx.x * 16), "v"(u32x4{lo, hi, ~lo, ~hi}), "s"(cx.ws + WS_PUB) : "memory");
      pr = u32x4{lo, hi, ~lo, ~hi};
      wait_vm<NQ1 + 5 + 1>();
      unsigned g[2] = {~0u, ~0u};
#pragma unroll 1
      for (int k = -1; k < PRK_ && ((k & 1) || min(g[0], g[1]) > 440u); ++k) {
        unsigned long long t0, t1;
        unsigned d;
        asm volatile("s_memtime %0\n\ts_waitcnt lgkmcnt(0)\n\ts_load_dword %2, %3, 0x0\n\ts_waitcnt lgkmcnt(0)\n\ts_memtime %1\n\ts_waitcnt lgkmcnt(0)"
                     : "=&s"(t0), "=&s"(t1), "=&s"(d) : "s"(rec + 32768 + (k < 0 ? 128 : (k & 1) * 8192 + (k & 2) * 2048 + (k & 4) * 4096 + (k & 8) * 128)) : "memory");
        if (k >= 0) { if (k & 1) g[1] = min(g[1], (unsigned)(t1 - t0)); else g[0] = min(g[0], (unsigned)(t1 - t0)); }
      }
      asm volatile("global_store_dwordx4 %0, %1, %2 sc1\n\ts_nop 1" :: "v"(lane * 16 + 2048), "v"(u32x4{lo | (((g[0] < g[1] ? 0u : 1u) ^ (cx.vb >> 2)) & 1u), hi, lo | (((g[0] < g[1] ? 0u : 1u) ^ (cx.vb >> 2)) & 1u), hi}), "s"(rec) : "memory");
    }
    issue_x(1, wv, xa1, x_rsrc(x, vb), lds0, 0, TE);
  }
  Rng gx, gw;
  wait_vm<6 + TE>();
#pragma unroll
  for (int q = 0; q < NQ1; ++q) {
    asm volatile("" : "+v"(r1[q]));
    *reinterpret_cast<u32x4*>(smem + (pww + q * 4096)) = r1[q];
  }
  SB0;
  unsigned near;
  {
    wait_vm<TE>();
    unsigned d0, d1;
    for (unsigned spins = 0;; ++spins) {
      asm volatile("" : "+v"(pr));
      d0 = __builtin_amdgcn_readfirstlane(pr[0]);
      d1 = __builtin_amdgcn_readfirstlane(pr[1]);
      const unsigned d2 = __builtin_amdgcn_readfirstlane(pr[2]), d3 = __builtin_amdgcn_readfirstlane(pr[3]);
      if (__builtin_expect((d0 == ~d2 && d1 == ~d3) || spins > (1u << 21), 1)) break;
      __builtin_amdgcn_s_sleep(2);
      asm volatile("global_load_dwordx4 %0, %1, %2 sc1\n\ts_waitcnt vmcnt(0)" : "=v"(pr) : "v"(0u), "s"(rec) : "memory");
    }
    tag = d0 | (unsigned long long)d1 << 32;
    const unsigned vn = (cx.id + (d0 >> 1)) & 7u;
    if (__builtin_expect(vn != (cx.vb & 7u), 0)) restart(cx.id | 256u | vn << 10);
  }
  const unsigned ee = (cx.id & 512u ? cx.id : (unsigned)(tag >> 32)) >> 16, ex = ee & 255u, ew = ee >> 8;
  cx.es = 256 - (int)(ex + ew);
  char* ws;
  const int bid = cx.vb;
  bf16x8& vb = hd.vb;
  u32x4 f0, f1;
  const u32x4* fa;
  u32x2 ov;
  unsigned pollv;
  const int rb[2] = {run_bs(bid, 0), run_bs(bid, 1)};
  asm volatile("" : "+v"(fb[0][0]), "+v"(fb[0][1]), "+v"(fb[1][0]), "+v"(fb[1][1]), "+v"(vb));
  scale8p(vb, 128 - (int)ew);
#pragma unroll
  for (int i = 0; i < 4; ++i) scale8p(fb[i >> 1][i & 1], 128 - (int)ex);
  asm volatile("" : "+v"(fb[0][0]), "+v"(fb[0][1]), "+v"(fb[1][0]), "+v"(fb[1][1]), "+v"(vb));
  asm volatile("s_waitcnt lgkmcnt(0)" ::: "memory");
  __builtin_amdgcn_s_barrier();
  SB0;
  u32x4 hx[2][20], hwl;
  f32x4 pend[2];
  auto rd = [&](int h, int i) __attribute__((always_inline)) {
    const int it = ord(h >> 1), mg = h & 1;
    if (it < 3) return tr_read(smem + (pq[i & 3] + mg * 2048 + (2 * it + (i >> 2)) * 4096));
    const int n = it - 3;
    const int c = 4 * ((n / 5) & 1) + 2 * mg + (i & 1);
    return tr_read(smem + ((c ? xa[i & 1] : xb0[n % 5 & 3]) + n % 5 * (HOP * 256) + (i >> 1) * (16 * 256)));
  };
  auto xaddr = [&](int h) __attribute__((always_inline)) {
    const int n = ord(h >> 1) - 3, mg = h & 1;
    if (h >= NIT || n < 0) return;
    if (h == 26) {
#pragma unroll
      for (int k = 0; k < 4; ++k) xb0[k] += XT1;
      asm volatile("" : "+v"(xb0[0]), "+v"(xb0[1]), "+v"(xb0[2]), "+v"(xb0[3]));
    }
#pragma unroll
    for (int i = 0; i < 2; ++i) {
      const int c = 4 * ((n / 5) & 1) + 2 * mg + i;
      if (c) { xa[i] = xb0[n % 5 & 3] ^ (unsigned)(c << 5); asm volatile("" : "+v"(xa[i])); }
    }
  };
  auto dst = [&](int h) -> u32x4& { const int it = ord(h >> 1); return it < 3 || it == 12 || it == 16 ? hwl : hx[h & 1][it - 3]; };
  auto item = [&](int h, XR& xr, auto side) __attribute__((always_inline)) {
    const XR c = xr;
    const int it = ord(h >> 1), mg = h & 1;
    const int nm = it == 2 ? 4 : 8, nr = h + 1 < NIT && ord((h + 1) >> 1) == 2 ? 4 : 8;
    const int pn = h > 0 && ord((h - 1) >> 1) == 2 ? 4 : 8;
    f32x4 r[8];
    bf16x8 dq[3];
    if (it < 3) { dq[0] = dup(c.t[0]); dq[1] = dup(c.t[1]); asm volatile("s_nop 0"); }
#pragma unroll
    for (int i = 0; i < 8; ++i) {
      if (i >= nm) break;
      const int s2 = i & 1, ks = (i >> 1) & 1;
      if (it < 3) mfma0(r[i], dq[i % 3], vb);
      else if (i < 2) mfma0(r[6 + i], cat(c.t[4 * ks + s2], c.t[4 * ks + 2 + s2]), fb[ks][1]);
      else mfma1(r[6 + s2], cat(c.t[4 * ks + s2], c.t[4 * ks + 2 + s2]), fb[ks][i < 4]);
      SB0;
      if (it < 3 && i + 2 < nm) dq[(i + 2) % 3] = dup(c.t[i + 2]);
      if (h + 1 < NIT && i < 4) {
        const bool nq = ord((h + 1) >> 1) < 3;
#pragma unroll
        for (int u = 0; u < 2; ++u) {
          const int k = 2 * i + u, j = nq ? k : (k & 4) + ((k >> 1) & 1) + (k & 1) * 2;
          if (j < nr) xr.t[j] = rd(h + 1, j);
        }
      }
      if (i == (nm == 4 ? 3 : 4)) xaddr(h + 2);
      if (h > 0 && (nm == 4 ? i >= 2 : i >= 4)) {
        u32x4& d = dst(h - 1);
#pragma unroll
        for (int e = 0; e < 4; ++e)
          if ((nm == 4 ? 2 + (e >> 1) : 4 + e) == i) d[e] = pk_f16(pend[e >> 1][(e & 1) * 2], pend[e >> 1][(e & 1) * 2 + 1]);
        if (i == nm - 1) {
          const int pi = ord((h - 1) >> 1);
          if (pi < 3) stash4(hw_reg((h - 1) & 1, pi == 2 ? 4 : 2 * pi + 1, 1), d);
          else if (pi == 12 || pi == 16) stash4(A_XA + (pi == 12 ? 8 : 0) + ((h - 1) & 1) * 4, d);
          else asm volatile("" : "+v"(d));
        }
      }
      if (it < 3 && i >= 3 && (i & 1)) {
        stash4(hw_reg(mg, 2 * it + ((i - 3) >> 2), ((i - 3) >> 1) & 1), pack8h(r[i - 3], r[i - 2]));
      }
      side(i * (8 / nm));
      if (nm == 4) side(2 * i + 1);
      SB0;
    }
    (void)pn;
    pend[0] = r[it < 3 ? nm - 2 : 6];
    pend[1] = r[it < 3 ? nm - 1 : 7];
  };
  const unsigned so0 = (unsigned)(2 * run_q(bid, 0)) * SBLK, so1 = (unsigned)(16 + 2 * run_q(bid, 1)) * SBLK;
  auto store = [&](int J) __attribute__((always_inline)) {
    const int j = J >> 1, mg = J & 1;
    if (J == 0) {
      wait_vm<0>();
      for (unsigned spins = 0;; ++spins) {
        asm volatile("" : "+v"(ov));
        near = __builtin_amdgcn_readfirstlane(ov[0]) ^ (unsigned)tag;
        if (__builtin_expect((near < 2u && __builtin_amdgcn_readfirstlane(ov[1]) == (unsigned)(tag >> 32)) || spins > (1u << 21), 1)) break;
        __builtin_amdgcn_s_sleep(2);
        asm volatile("global_load_dwordx2 %0, %1, %2 sc1\n\ts_waitcnt vmcnt(0)" : "=v"(ov) : "v"(2048u), "s"(rec) : "memory");
      }
      ws = cx.ws += (near & 1u) * 8192;
      cx.rs = make_rsrc(ws + WS_S, WS_L - WS_S + D_ * D_ * 2);
    }
    if (J == 1) {
      if (wv == 1) {
        const unsigned o = (lane < 32 ? WS_STATS : WS_S + FLG + 16384 - 512) + bid * 512 + lane * 16;
        const unsigned v = lane < 32 ? ~0u : 0u;
        asm volatile("global_store_dwordx4 %0, %1, %2 sc1\n\ts_nop 1" :: "v"(o), "v"(u32x4{v, v, v, v}), "s"(ws) : "memory");
        if (lane < 8) __hip_atomic_store((gu32*)(ws + WS_S + FLG + lane * 1024 + ((bid + 2) & 3) * 256 + (bid >> 2) * 4), 0u, RLX_AGENT);
      }
    }
    if (J == NB + NFA) {
      if (((bid >> 1) ^ (bid >> 3) ^ 1) & 1 && threadIdx.x < 160) {
        const int t = threadIdx.x >= 80, blk = (threadIdx.x >> 4) - 5 * t;
        const unsigned o = (unsigned)(t * XT1 + blk * (HOP * 256) + (((lane & 15) ^ ((blk & 3) << 2)) << 4));
        const u32x4 lo = *reinterpret_cast<const u32x4*>(smem + o), hi = *reinterpret_cast<const u32x4*>(smem + o + 64 * 256);
        u32x4 d;
#pragma unroll
        for (int e = 0; e < 4; ++e) d[e] = pk_bf16(bf_lo(hi[e]) - bf_lo(lo[e]), bf_hi(hi[e]) - bf_hi(lo[e]));
        st16(d, cx.rs, (unsigned)((((bid >> 2) & 1) * NBLK + (t ? rb[1] : rb[0]) + blk) * 1024 + run_q(bid, t) * 256 + (lane & 15) * 16), WS_D - WS_S);
      }
      asm volatile("buffer_store_dwordx4 a[%c3:%c4], %0, %1, %2 offen sc1" :: "v"(wv >= 2 ? (threadIdx.x & 127) * 16 : 0x80000000u), "s"(cx.rs), "s"(WS_L - WS_S + bid * 2048), "i"(A_WL), "i"(A_WL + 3) : "memory");
    }
    const int wq = st_w(j), xn = st_x(j);
    if (wq >= 0)
      asm volatile("buffer_store_dwordx4 a[%c3:%c4], %0, %1, %2 offen sc1" :: "v"(cx.poff), "s"(cx.rs),
                   "s"((unsigned)(wq >> 1) * (2 * SBLK) + (unsigned)((bid + (wq >> 1)) & 3) * 16384u + (wq & 1) * 128u + mg * 256u),
                   "i"(hw_reg(mg, wq >> 1, wq & 1)), "i"(hw_reg(mg, wq >> 1, wq & 1) + 3) : "memory");
    else if (xn == 9)
      asm volatile("buffer_store_dwordx4 a[%c3:%c4], %0, %1, %2 offen sc1" :: "v"(cx.xo), "s"(cx.rs),
                   "s"(run_q(bid, 0) ? 0xf0000000u : x_place(so0 + 8 * SBLK + mg * SBLK, (unsigned)(rb[0] + 4))), "i"(A_XA + 8 + mg * 4), "i"(A_XA + 11 + mg * 4) : "memory");
    else st16(hx[mg][xn], cx.rs, cx.xo, x_place((xn < 10 ? so0 : so1) + ((xn / 5) & 1) * (8 * SBLK) + mg * SBLK, (unsigned)(rb[xn / 10] + xn % 5)));
    if (J == NA + NFA - 1) {
      const unsigned mx = amax(gx), mw = amax(gw);
      if (__builtin_expect(__any(expo(mx) > ex + 7 || expo(mw) > ew + 9) && !(cx.id & 512u), 0)) {
        const u32x2 m = {wave_max(expo(mx)), wave_max(expo(mw))};
        asm volatile("global_atomic_umax %0, %1, %3 sc1\n\tglobal_atomic_umax %0, %2, %3 offset:4 sc1\n\ts_waitcnt vmcnt(0)"
                     :: "v"(lane * 16 + 1024), "v"(m[0]), "v"(m[1]), "s"(rec - ((cx.id & 7) * 8 + wv) * 16) : "memory");
      }
    }
    if (J == NA + NFA - 1 || J == NB + NFA - 1) {
      if (J < NB) {
      } else {
      }
      wait_vm<NFA>();
      if (J < NB) {
      } else {
      }
      __builtin_amdgcn_s_barrier();
      if (J < NB) {
      } else {
      }
      if (wv == 0 && lane < 8) {
        if (J < NB) __hip_atomic_store((gu64*)(ws + WS_FA + (cx.id & 512u) * 32 + lane * 2048) + bid, tag, RLX_AGENT);
        else __hip_atomic_store((gu32*)(ws + WS_S + FLG + lane * 1024 + ((bid + 2) & 3) * 256 + (bid >> 2) * 4), 1u, RLX_AGENT);
      }
    }
    SB0;
  };
  u32x4 sv[3];
  auto check = [&](auto hc, int s) __attribute__((always_inline)) {
    constexpr int h = hc.v;
    if constexpr (sc_wait(h) >= 0) { if (s == 0) wait_vm<sc_wait(h) < 0 ? 0 : sc_wait(h)>(); }
#pragma unroll
    for (int k = 0; k < 34; ++k) {
      if (sc_pos(k) != h) continue;
      int o = 0;
#pragma unroll
      for (int k2 = 0; k2 < k; ++k2) o += sc_pos(k2) == h;
      const int t = k / 17, j = k % 17;
      if (s == o) sv[o] = *reinterpret_cast<const u32x4*>(smem + (t * XT1 + (j < 16 || wv < 3 ? j : 15) * 4096 + threadIdx.x * 16));
      if (s == 5 + o) {
        widen(gx, sv[o][0], sv[o][1]);
        widen(gx, sv[o][2], sv[o][3]);
      }
    }
    if (s == 4)
      sfor<0, 16>([&](auto k) __attribute__((always_inline)) {
        if (sc_raw(k.v) != h) return;
        u32x4 r;
        if constexpr (k.v <= NQ1) {
          u32x4& q = k.v < NQ1 ? r1[k.v < NQ1 ? k.v : 0] : rq0;
          pin(q);
          r = q;
        } else {
          constexpr int a = A_RAW + 4 * (k.v - NQ1 - 1);
          r = u32x4{unstash<a>(), unstash<a + 1>(), unstash<a + 2>(), unstash<a + 3>()};
        }
        widen(gw, r[0], r[1]);
        widen(gw, r[2], r[3]);
      });
    if (s == 1 && h <= P0 + NBP) asm volatile("" : "+v"(gx.hi), "+v"(gx.lo), "+v"(gw.hi), "+v"(gw.lo));
  };
  auto loads = [&](auto p, int s) __attribute__((always_inline)) {
    sfor<0, NLD>([&](auto l) __attribute__((always_inline)) {
      if (ld_pos(l.v) != p.v || ld_slot(l.v) != s) return;
      constexpr int n = l.v - (17 - TE);
      if constexpr (n < 0) {
        issue_x(1, wv, xa1, x_rsrc(x, bid), lds0, TE + l.v, TE + l.v + 1);
      } else if constexpr (n == 0) {
        load_acc<A_WL>(wv >= 2 ? (threadIdx.x & 127) * 16 : 0x80000000u, rw, 30 * D_ * D_ * 2 + bid * 2048);
      } else if constexpr (n == 1) {
        raw_load(rq0, NQ1);
      } else if constexpr (n <= NQW) {
        load_acc<A_RAW + 4 * (n - 2)>(roff, rw, raw_so(NQ1 + n - 1));
      } else {
        ld8(ov, rec, 2048u);
      }
    });
  };
  XR xr;
#pragma unroll
  for (int j = 0; j < 8; ++j) xr.t[j] = rd(0, j);
  static_assert(ord(0) < 3 && ord(1) >= 3 && ord(0) != 2, "positions 0, 1: weights");
  SB0;
  static_assert(st_first(NIT) == NS && PG >= 28 && st_first(PG - 4) > NB + NFA && NA + NFA <= NB && NFA >= 1 && NH <= 7, "order of the hand-off steps");
  sfor<0, NIT>([&](auto p) __attribute__((always_inline)) {
    constexpr int j0 = st_first(p.v), j1 = st_first(p.v + 1);
    if (SLP_ && p >= 24 && p < PG && !(p & 1)) { if (!(bid & 1) && cx.id) __builtin_amdgcn_s_sleep(SLP_); }
    if (p == XBAR) __builtin_amdgcn_s_barrier();
    if (p == PG - 4) {
      code_touch(cx);
      fa = reinterpret_cast<const u32x4*>(ws + WS_FA + (cx.id & 512u) * 32 + (bid & 7) * 2048) + lane;
      if (wv == 0)
        asm volatile("global_load_dwordx4 %0, %2, off sc1\n\tglobal_load_dwordx4 %1, %2, off offset:1024 sc1" : "=&v"(f0), "=&v"(f1) : "v"(fa) : "memory");
    }
    if (p == PG) {
      if (wv == 0) {
        wait_vm<st_first(PG) - st_first(PG - 4)>();
        for (unsigned spins = 0; spins < (1u << 21); ++spins) {
          asm volatile("" : "+v"(f0), "+v"(f1));
          const unsigned t0 = (unsigned)tag, t1 = (unsigned)(tag >> 32);
          if (__all(f0[0] == t0 && f0[1] == t1 && f0[2] == t0 && f0[3] == t1 && f1[0] == t0 && f1[1] == t1 && f1[2] == t0 && f1[3] == t1)) break;
          __builtin_amdgcn_s_sleep(2);
          asm volatile("global_load_dwordx4 %0, %2, off sc1\n\tglobal_load_dwordx4 %1, %2, off offset:1024 sc1\n\ts_waitcnt vmcnt(0)"
                       : "=&v"(f0), "=&v"(f1) : "v"(fa) : "memory");
        }
      }
      __builtin_amdgcn_s_barrier();
      stage_issue(cx, -1);
      asm volatile("global_load_dwordx2 %0, %1, %2 sc1" : "=v"(ov) : "v"(1024u), "s"(rec) : "memory");
      stage_issue(cx, 0);
      SB0;
    }
    if (p == NIT - 2)
      asm volatile("buffer_load_dword %0, %1, %2, %3 offen sc1" : "=v"(pollv) : "v"(pvo), "s"(cx.rs), "s"(FLG) : "memory");
    item(p, xr, [&](int i) __attribute__((always_inline)) {
      if ((i * (j1 - j0) + 7) / 8 < ((i + 1) * (j1 - j0) + 7) / 8) store(j0 + (i * (j1 - j0) + 7) / 8);
      check(p, i);
      loads(p, i);
    });
  });
  hx[1][19] = pack8h(pend[0], pend[1]);
#pragma unroll
  for (int e = 0; e < 2 * NH; ++e) hd.held[e] = hx[e & 1][20 - NH + (e >> 1)];
#pragma unroll
  for (int e = 0; e < 2 * NST; ++e) hd.st[e] = hx[e & 1][10 + (e >> 1)];
  *reinterpret_cast<u32x4*>(smem + RAW0 + pwr) = rq0;
  wait_vm<NS - st_first(NIT - 2)>();
  {
    asm volatile("" : "+v"(ov));
    const unsigned ox = __builtin_amdgcn_readfirstlane(ov[0]), ow = __builtin_amdgcn_readfirstlane(ov[1]);
    if (__builtin_expect((ox | ow) && !(cx.id & 512u), 0))
      restart((cx.id & 255u) | 768u | (bid & 7u) << 10 | min(max(ex + 7, ox) - 7, 220u) << 16 | min(max(ew + 9, ow) - 9, 220u) << 24);
    if ((cx.id & 255u) == 0 && wv == 0)
      asm volatile("global_store_dwordx4 %0, %1, %2 sc1\n\ts_nop 1" :: "v"(lane * 16), "v"(u32x4{0u, 0u, 0u, 0u}), "s"(rec) : "memory");
  }
  asm volatile("" : "+v"(pollv));
  if (!__all(pollv != 0u)) spin_flags(ws + WS_S + FLG + pvo);
  asm volatile("s_waitcnt lgkmcnt(0)" ::: "memory");
  __builtin_amdgcn_s_barrier();
  SB0;
}

__device__ __forceinline__ void run12(Ctx& cx, const unsigned short* __restrict__ x, const unsigned short* __restrict__ w) {
  Hand hd;
  phase1(cx, x, w, hd);
  u32x16 h1, st0;
  u32x8 st1;
  u32x32 h0;
#pragma unroll
  for (int i = 0; i < 32; ++i) {
    h0[i] = hd.held[i >> 2][i & 3];
    if (i < 16) { h1[i] = hd.held[8 + (i >> 2)][i & 3]; st0[i] = hd.st[i >> 2][i & 3]; }
    if (i < 8) st1[i] = hd.st[4 + (i >> 2)][i & 3];
  }
  u32x16 o0, o4;
  u32x8 o1;
  u32x4 o5;
  u32x32 o3;
  asm volatile(
  "v_accvgpr_read_b32 v228, a188\n"
  "v_accvgpr_read_b32 v229, a189\n"
  "v_accvgpr_read_b32 v230, a190\n"
  "v_accvgpr_read_b32 v231, a191\n"
  "v_accvgpr_read_b32 v232, a192\n"
  "ds_read_b64_tr_b16 v[0:1], v228 offset:0\n"
  "ds_read_b64_tr_b16 v[2:3], v228 offset:8192\n"
  "ds_read_b64_tr_b16 v[16:17], v228 offset:256\n"
  "ds_read_b64_tr_b16 v[18:19], v228 offset:8448\n"
  "ds_read_b64_tr_b16 v[4:5], v229 offset:0\n"
  "ds_read_b64_tr_b16 v[6:7], v229 offset:8192\n"
  "ds_read_b64_tr_b16 v[20:21], v229 offset:256\n"
  "ds_read_b64_tr_b16 v[22:23], v229 offset:8448\n"
  "ds_read_b64_tr_b16 v[8:9], v230 offset:0\n"
  "ds_read_b64_tr_b16 v[10:11], v230 offset:8192\n"
  "ds_read_b64_tr_b16 v[24:25], v230 offset:256\n"
  "ds_read_b64_tr_b16 v[26:27], v230 offset:8448\n"
  "ds_read_b64_tr_b16 v[12:13], v231 offset:0\n"
  "ds_read_b64_tr_b16 v[14:15], v231 offset:8192\n"
  "ds_read_b64_tr_b16 v[28:29], v231 offset:256\n"
  "ds_read_b64_tr_b16 v[30:31], v231 offset:8448\n"
  "ds_read_b128 v[96:99], v232 offset:0\n"
  "ds_read_b128 v[100:103], v232 offset:512\n"
  "ds_read_b128 v[104:107], v232 offset:2048\n"
  "ds_read_b128 v[108:111], v232 offset:2560\n"
  "v_accvgpr_read_b32 v255, a214\n"
  "v_accvgpr_read_b32 v237, a197\n"
  "v_accvgpr_read_b32 v238, a198\n"
  "v_readlane_b32 s44, v255, 0\n"
  "v_readlane_b32 s45, v255, 1\n"
  "v_readlane_b32 s62, v255, 5\n"
  "v_accvgpr_read_b32 v233, a193\n"
  "v_accvgpr_read_b32 v234, a194\n"
  "v_accvgpr_read_b32 v235, a195\n"
  "v_accvgpr_read_b32 v236, a196\n"
  "v_accvgpr_read_b32 v239, a199\n"
  "v_accvgpr_read_b32 v240, a200\n"
  "v_accvgpr_read_b32 v241, a201\n"
  "v_accvgpr_read_b32 v242, a202\n"
  "v_accvgpr_read_b32 v243, a203\n"
  "v_accvgpr_read_b32 v244, a204\n"
  "v_accvgpr_read_b32 v245, a205\n"
  "v_accvgpr_read_b32 v246, a206\n"
  "v_accvgpr_read_b32 v248, a208\n"
  "v_accvgpr_read_b32 v249, a209\n"
  "v_accvgpr_read_b32 v250, a210\n"
  "v_accvgpr_read_b32 v251, a211\n"
  "v_accvgpr_read_b32 v252, a212\n"
  "v_accvgpr_read_b32 v253, a213\n"
  "s_add_i32 s67, s44, s45\n"
  "s_add_i32 m0, s44, 73728\n"
  "s_mov_b32 s64, 8388608\n"
  "buffer_load_dwordx4 v237, s[40:43], s64 offen sc1 lds\n"
  "s_add_i32 m0, s44, 77824\n"
  "s_mov_b32 s64, 8392704\n"
  "buffer_load_dwordx4 v237, s[40:43], s64 offen sc1 lds\n"
  "s_add_i32 m0, s44, 81920\n"
  "s_mov_b32 s64, 8396800\n"
  "buffer_load_dwordx4 v237, s[40:43], s64 offen sc1 lds\n"
  "s_add_i32 m0, s44, 86016\n"
  "s_mov_b32 s64, 8400896\n"
  "buffer_load_dwordx4 v237, s[40:43], s64 offen sc1 lds\n"
  "s_add_i32 m0, s44, 90112\n"
  "s_mov_b32 s64, 8388608\n"
  "buffer_load_dwordx4 v238, s[40:43], s64 offen sc1 lds\n"
  "s_add_i32 m0, s44, 94208\n"
  "s_mov_b32 s64, 4198400\n"
  "buffer_load_dwordx4 v238, s[40:43], s64 offen sc1 lds\n"
  "s_add_i32 m0, s67, 98304\n"
  "s_mov_b32 s64, 4210688\n"
  "buffer_load_dwordx4 v238, s[40:43], s64 offen sc1 lds\n"
  "s_add_i32 m0, s44, 100352\n"
  "s_mov_b32 s64, 8409088\n"
  "buffer_load_dwordx4 v238, s[40:43], s64 offen sc1 lds\n"
  "s_add_i32 m0, s44, 104448\n"
  "s_mov_b32 s64, 4227072\n"
  "buffer_load_dwordx4 v238, s[40:43], s64 offen sc1 lds\n"
  "s_memrealtime s[70:71]\n"
  "v_readlane_b32 s51, v255, 2\n"
  "v_readlane_b32 s52, v255, 3\n"
  "v_readlane_b32 s55, v255, 4\n"
  "v_readlane_b32 s56, v255, 4\n"
  "v_readlane_b32 s76, v255, 6\n"
  "v_readlane_b32 s77, v255, 6\n"
  "v_readlane_b32 s80, v255, 7\n"
  "v_readlane_b32 s81, v255, 8\n"
  "v_readlane_b32 s82, v255, 9\n"
  "v_readlane_b32 s83, v255, 10\n"
  "v_readlane_b32 s84, v255, 11\n"
  "v_readlane_b32 s85, v255, 12\n"
  "v_readlane_b32 s88, v255, 13\n"
  "v_readlane_b32 s89, v255, 14\n"
  "v_mov_b32 v172, 0\n"
  "v_mov_b32 v173, 0\n"
  "v_mov_b32 v174, 0\n"
  "v_mov_b32 v175, 0\n"
  "v_mov_b32 v247, 1\n"
  "s_mov_b32 s46, 36864\n"
  "s_mov_b32 s47, s44\n"
  "s_mov_b32 s48, s67\n"
  "s_mov_b32 s49, 36864\n"
  "s_mov_b32 s50, 8388608\n"
  "s_mov_b32 s53, 134217728\n"
  "s_mov_b32 s54, 134217728\n"
  "s_mov_b32 s57, -131072\n"
  "s_mov_b32 s58, 0xf0000000\n"
  "s_mov_b32 s59, 0xf0000000\n"
  "s_mov_b32 s60, -1\n"
  "s_mov_b32 s68, -73728\n"
  "s_mov_b32 s69, 36864\n"
  "s_add_u32 s72, s40, 41943040\n"
  "s_addc_u32 s73, s41, 0\n"
  "s_mov_b32 s74, 20971520\n"
  "s_mov_b32 s75, 0x20000\n"
  "s_waitcnt lgkmcnt(0)\n"
  "s_add_i32 s61, s70, s62\n"
  "s_add_i32 s61, s61, 14\n"
  "v_pk_add_f16 v32, v0, v16\n"
  "v_pk_add_f16 v33, v1, v17\n"
  "v_pk_add_f16 v34, v2, v18\n"
  "v_pk_add_f16 v35, v3, v19\n"
  "v_pk_add_f16 v36, v4, v20\n"
  "v_pk_add_f16 v37, v5, v21\n"
  "v_pk_add_f16 v38, v6, v22\n"
  "v_pk_add_f16 v39, v7, v23\n"
  "v_pk_add_f16 v40, v8, v24\n"
  "v_pk_add_f16 v41, v9, v25\n"
  "v_pk_add_f16 v42, v10, v26\n"
  "v_pk_add_f16 v43, v11, v27\n"
  "v_pk_add_f16 v44, v12, v28\n"
  "v_pk_add_f16 v45, v13, v29\n"
  "v_pk_add_f16 v46, v14, v30\n"
  "v_pk_add_f16 v47, v15, v31\n"
  "v_add_u32 v228, 36864, v228\n"
  "v_add_u32 v229, 36864, v229\n"
  "v_add_u32 v230, 36864, v230\n"
  "v_add_u32 v231, 36864, v231\n"
  "v_add_u32 v232, 36864, v232\n"
  "s_barrier\n"
  "v_mfma_f32_16x16x32_f16 a[0:3], v[0:3], v[96:99], 0\n"
  "v_readlane_b32 s65, v244, 32\n"
  "v_pk_add_f16 v120, v96, v100\n"
  "v_mfma_f32_16x16x32_f16 a[12:15], v[4:7], v[96:99], 0\n"
  "v_pk_add_f16 v121, v97, v101\n"
  "v_pk_add_f16 v122, v98, v102\n"
  "v_mfma_f32_16x16x32_f16 a[24:27], v[8:11], v[96:99], 0\n"
  "s_add_i32 m0, s47, 0\n"
  "ds_read_b64_tr_b16 v[48:49], v228 offset:0\n"
  "v_pk_add_f16 v123, v99, v103\n"
  "v_mfma_f32_16x16x32_f16 a[36:39], v[12:15], v[96:99], 0\n"
  "s_add_i32 s64, s50, 4194304\n"
  "buffer_load_dwordx4 v237, s[40:43], s64 offen sc1 lds\n"
  "v_readlane_b32 s66, v244, 33\n"
  "v_mfma_f32_16x16x32_f16 a[4:7], v[16:19], v[100:103], 0\n"
  "s_cmp_eq_u32 s46, 73728\n"
  "s_cselect_b32 s49, s68, s69\n"
  "ds_read_b64_tr_b16 v[124:125], v233 offset:0\n"
  "v_mfma_f32_16x16x32_f16 a[16:19], v[20:23], v[100:103], 0\n"
  "ds_read_b64_tr_b16 v[128:129], v234 offset:0\n"
  "ds_read_b64_tr_b16 v[50:51], v228 offset:8192\n"
  "v_mfma_f32_16x16x32_f16 a[28:31], v[24:27], v[100:103], 0\n"
  "buffer_store_dwordx4 v[80:83], v243, s[40:43], s65 offen sc1\n"
  "ds_read_b128 v[96:99], v232 offset:4096\n"
  "v_mfma_f32_16x16x32_f16 a[40:43], v[28:31], v[100:103], 0\n"
  "ds_read_b64_tr_b16 v[132:133], v235 offset:0\n"
  "v_readlane_b32 s65, v244, 34\n"
  "v_mfma_f32_16x16x32_f16 a[8:11], v[32:35], v[120:123], 0\n"
  "s_add_i32 m0, s47, 4096\n"
  "ds_read_b64_tr_b16 v[64:65], v228 offset:256\n"
  "ds_read_b128 v[100:103], v232 offset:4608\n"
  "v_mfma_f32_16x16x32_f16 a[20:23], v[36:39], v[120:123], 0\n"
  "s_add_i32 s64, s50, 4198400\n"
  "buffer_load_dwordx4 v237, s[40:43], s64 offen sc1 lds\n"
  "ds_read_b64_tr_b16 v[136:137], v236 offset:0\n"
  "v_mfma_f32_16x16x32_f16 a[32:35], v[40:43], v[120:123], 0\n"
  "ds_read_b64_tr_b16 v[66:67], v228 offset:8448\n"
  "v_add_u32 v228, s49, v228\n"
  "v_mfma_f32_16x16x32_f16 a[44:47], v[44:47], v[120:123], 0\n"
  "ds_read_b64_tr_b16 v[52:53], v229 offset:0\n"
  "ds_read_b64_tr_b16 v[54:55], v229 offset:8192\n"
  "v_mfma_f32_16x16x32_f16 a[48:51], v[0:3], v[104:107], 0\n"
  "buffer_store_dwordx4 v[84:87], v243, s[40:43], s66 offen sc1\n"
  "v_pk_add_f16 v120, v104, v108\n"
  "v_mfma_f32_16x16x32_f16 a[60:63], v[4:7], v[104:107], 0\n"
  "v_pk_add_f16 v121, v105, v109\n"
  "v_pk_add_f16 v122, v106, v110\n"
  "v_mfma_f32_16x16x32_f16 a[72:75], v[8:11], v[104:107], 0\n"
  "s_add_i32 m0, s47, 8192\n"
  "ds_read_b64_tr_b16 v[68:69], v229 offset:256\n"
  "v_pk_add_f16 v123, v107, v111\n"
  "v_mfma_f32_16x16x32_f16 a[84:87], v[12:15], v[104:107], 0\n"
  "s_add_i32 s64, s50, 4202496\n"
  "buffer_load_dwordx4 v237, s[40:43], s64 offen sc1 lds\n"
  "v_readlane_b32 s66, v244, 35\n"
  "v_mfma_f32_16x16x32_f16 a[52:55], v[16:19], v[108:111], 0\n"
  "s_waitcnt lgkmcnt(4)\n"
  "ds_read_b64_tr_b16 v[70:71], v229 offset:8448\n"
  "v_mov_b64 v[126:127], v[124:125]\n"
  "v_mfma_f32_16x16x32_f16 a[64:67], v[20:23], v[108:111], 0\n"
  "s_waitcnt lgkmcnt(4)\n"
  "ds_read_b128 v[104:107], v232 offset:6144\n"
  "v_mov_b64 v[130:131], v[128:129]\n"
  "v_mfma_f32_16x16x32_f16 a[76:79], v[24:27], v[108:111], 0\n"
  "buffer_store_dwordx4 v[88:91], v243, s[40:43], s65 offen sc1\n"
  "v_mov_b64 v[134:135], v[132:133]\n"
  "v_mfma_f32_16x16x32_f16 a[88:91], v[28:31], v[108:111], 0\n"
  "ds_read_b64_tr_b16 v[56:57], v230 offset:0\n"
  "v_readlane_b32 s65, v244, 36\n"
  "v_mfma_f32_16x16x32_f16 a[56:59], v[32:35], v[120:123], 0\n"
  "s_add_i32 m0, s47, 12288\n"
  "ds_read_b128 v[108:111], v232 offset:6656\n"
  "v_mov_b64 v[138:139], v[136:137]\n"
  "v_mfma_f32_16x16x32_f16 a[68:71], v[36:39], v[120:123], 0\n"
  "s_add_i32 s64, s50, 4206592\n"
  "buffer_load_dwordx4 v237, s[40:43], s64 offen sc1 lds\n"
  "v_mfma_f32_16x16x32_f16 a[80:83], v[40:43], v[120:123], 0\n"
  "ds_read_b64_tr_b16 v[58:59], v230 offset:8192\n"
  "v_pk_add_f16 v80, v48, v64\n"
  "v_mfma_f32_16x16x32_f16 a[92:95], v[44:47], v[120:123], 0\n"
  "s_waitcnt lgkmcnt(4)\n"
  "ds_read_b64_tr_b16 v[72:73], v230 offset:256\n"
  "v_pk_add_f16 v81, v49, v65\n"
  "v_mfma_f32_16x16x32_f16 a[96:99], v[0:3], v[96:99], 0\n"
  "buffer_store_dwordx4 v[92:95], v243, s[40:43], s66 offen sc1\n"
  "v_pk_add_f16 v120, v96, v100\n"
  "v_mfma_f32_16x16x32_bf16 v[140:143], v[124:127], v[176:179], 0\n"
  "ds_read_b64_tr_b16 v[74:75], v230 offset:8448\n"
  "v_pk_add_f16 v121, v97, v101\n"
  "v_mfma_f32_16x16x32_f16 a[108:111], v[4:7], v[96:99], 0\n"
  "s_add_i32 m0, s47, 16384\n"
  "v_pk_add_f16 v122, v98, v102\n"
  "v_pk_add_f16 v123, v99, v103\n"
  "v_mfma_f32_16x16x32_f16 a[120:123], v[8:11], v[96:99], 0\n"
  "s_add_i32 s64, s50, 4194304\n"
  "buffer_load_dwordx4 v238, s[40:43], s64 offen sc1 lds\n"
  "v_readlane_b32 s66, v244, 37\n"
  "v_mfma_f32_16x16x32_f16 a[132:135], v[12:15], v[96:99], 0\n"
  "ds_read_b64_tr_b16 v[60:61], v231 offset:0\n"
  "v_cvt_pkrtz_f16_f32 v124, v140, v141\n"
  "v_mfma_f32_16x16x32_bf16 v[144:147], v[128:131], v[176:179], 0\n"
  "ds_read_b64_tr_b16 v[62:63], v231 offset:8192\n"
  "v_cvt_pkrtz_f16_f32 v125, v142, v143\n"
  "v_mfma_f32_16x16x32_f16 a[100:103], v[16:19], v[100:103], 0\n"
  "buffer_store_dwordx4 v[156:159], v243, s[40:43], s65 offen sc1\n"
  "v_and_b32 v124, 0xfff8fff8, v124\n"
  "v_mfma_f32_16x16x32_f16 a[112:115], v[20:23], v[100:103], 0\n"
  "ds_read_b128 v[96:99], v232 offset:0\n"
  "v_and_b32 v125, 0xfff8fff8, v125\n"
  "v_mfma_f32_16x16x32_f16 a[124:127], v[24:27], v[100:103], 0\n"
  "s_add_i32 m0, s47, 20480\n"
  "ds_read_b128 v[112:115], v232 offset:8192\n"
  "v_cvt_pkrtz_f16_f32 v126, v144, v145\n"
  "v_mfma_f32_16x16x32_bf16 v[148:151], v[132:135], v[176:179], 0\n"
  "s_add_i32 s64, s50, 4096\n"
  "buffer_load_dwordx4 v238, s[40:43], s64 offen sc1 lds\n"
  "v_cvt_pkrtz_f16_f32 v127, v146, v147\n"
  "v_mfma_f32_16x16x32_f16 a[136:139], v[28:31], v[100:103], 0\n"
  "s_waitcnt lgkmcnt(4)\n"
  "ds_write_b128 v252, a[144:147] offset:4096\n"
  "v_and_b32 v126, 0xfff8fff8, v126\n"
  "v_mfma_f32_16x16x32_f16 a[104:107], v[32:35], v[120:123], 0\n"
  "ds_read_b64_tr_b16 v[76:77], v231 offset:256\n"
  "v_and_b32 v127, 0xfff8fff8, v127\n"
  "v_mfma_f32_16x16x32_f16 a[116:119], v[36:39], v[120:123], 0\n"
  "buffer_store_dwordx4 v[160:163], v243, s[40:43], s66 offen sc1\n"
  "v_cvt_pkrtz_f16_f32 v128, v148, v149\n"
  "v_mfma_f32_16x16x32_bf16 v[152:155], v[136:139], v[176:179], 0\n"
  "ds_write_b128 v252, a[156:159] offset:16384\n"
  "v_cvt_pkrtz_f16_f32 v129, v150, v151\n"
  "v_mfma_f32_16x16x32_f16 a[128:131], v[40:43], v[120:123], 0\n"
  "s_add_i32 m0, s48, 24576\n"
  "ds_read_b128 v[100:103], v232 offset:512\n"
  "v_and_b32 v128, 0xfff8fff8, v128\n"
  "v_mfma_f32_16x16x32_f16 a[140:143], v[44:47], v[120:123], 0\n"
  "s_add_i32 s64, s50, 16384\n"
  "buffer_load_dwordx4 v238, s[40:43], s64 offen sc1 lds\n"
  "v_and_b32 v129, 0xfff8fff8, v129\n"
  "v_mfma_f32_16x16x32_f16 a[144:147], v[0:3], v[104:107], 0\n"
  "ds_read_b128 v[116:119], v232 offset:8704\n"
  "v_pk_add_f16 v120, v104, v108\n"
  "v_mfma_f32_16x16x32_f16 a[156:159], v[4:7], v[104:107], 0\n"
  "ds_write_b128 v252, a[168:171] offset:28672\n"
  "v_pk_add_f16 v121, v105, v109\n"
  "v_mfma_f32_16x16x32_f16 a[168:171], v[8:11], v[104:107], 0\n"
  "buffer_store_dwordx4 v[124:127], v242, s[40:43], s56 offen offset:0 sc1\n"
  "ds_read_b64_tr_b16 v[78:79], v231 offset:8448\n"
  "ds_write_b128 v252, a[180:183] offset:40960\n"
  "v_mfma_f32_16x16x32_f16 a[180:183], v[12:15], v[104:107], 0\n"
  "ds_write_b128 v252, a[148:151] offset:8192\n"
  "v_pk_add_f16 v122, v106, v110\n"
  "v_mfma_f32_16x16x32_f16 a[148:151], v[16:19], v[108:111], 0\n"
  "s_add_i32 m0, s47, 26624\n"
  "ds_write_b128 v252, a[160:163] offset:20480\n"
  "v_pk_add_f16 v123, v107, v111\n"
  "v_mfma_f32_16x16x32_f16 a[160:163], v[20:23], v[108:111], 0\n"
  "s_add_i32 s64, s50, 4214784\n"
  "buffer_load_dwordx4 v238, s[40:43], s64 offen sc1 lds\n"
  "ds_write_b128 v252, a[172:175] offset:32768\n"
  "v_mfma_f32_16x16x32_f16 a[172:175], v[24:27], v[108:111], 0\n"
  "ds_write_b128 v252, a[152:155] offset:12288\n"
  "v_cvt_pkrtz_f16_f32 v130, v152, v153\n"
  "v_mfma_f32_16x16x32_f16 a[184:187], v[28:31], v[108:111], 0\n"
  "s_add_i32 m0, s47, 30720\n"
  "ds_write_b128 v252, a[164:167] offset:24576\n"
  "v_cvt_pkrtz_f16_f32 v131, v154, v155\n"
  "v_mfma_f32_16x16x32_f16 a[152:155], v[32:35], v[120:123], 0\n"
  "s_add_i32 s64, s50, 32768\n"
  "buffer_load_dwordx4 v238, s[40:43], s64 offen sc1 lds\n"
  "ds_write_b128 v252, a[176:179] offset:36864\n"
  "v_mfma_f32_16x16x32_f16 a[164:167], v[36:39], v[120:123], 0\n"
  "s_waitcnt lgkmcnt(9)\n"
  "v_and_b32 v130, 0xfff8fff8, v130\n"
  "v_and_b32 v131, 0xfff8fff8, v131\n"
  "v_mfma_f32_16x16x32_f16 a[176:179], v[40:43], v[120:123], 0\n"
  "s_add_i32 s47, s44, s46\n"
  "s_waitcnt lgkmcnt(7)\n"
  "ds_read_b128 v[104:107], v232 offset:2048\n"
  "v_mfma_f32_16x16x32_f16 a[188:191], v[44:47], v[120:123], 0\n"
  "s_add_i32 s48, s47, s45\n"
  "buffer_store_dwordx4 v[128:131], v242, s[40:43], s56 offen offset:128 sc1\n"
  "v_pk_add_f16 v82, v50, v66\n"
  "v_mfma_f32_16x16x32_f16 a[192:195], v[0:3], v[112:115], 0\n"
  "s_add_i32 s46, s46, s49\n"
  "ds_read_b128 v[108:111], v232 offset:2560\n"
  "v_pk_add_f16 v120, v112, v116\n"
  "v_mfma_f32_16x16x32_f16 a[204:207], v[4:7], v[112:115], 0\n"
  "s_add_i32 s50, s50, 4194304\n"
  "v_pk_add_f16 v121, v113, v117\n"
  "v_pk_add_f16 v122, v114, v118\n"
  "v_mfma_f32_16x16x32_f16 a[216:219], v[8:11], v[112:115], 0\n"
  "v_pk_add_f16 v123, v115, v119\n"
  "v_pk_add_f16 v83, v51, v67\n"
  "v_mfma_f32_16x16x32_f16 a[228:231], v[12:15], v[112:115], 0\n"
  "v_pk_add_f16 v84, v52, v68\n"
  "v_pk_add_f16 v85, v53, v69\n"
  "v_mfma_f32_16x16x32_f16 a[196:199], v[16:19], v[116:119], 0\n"
  "v_pk_add_f16 v86, v54, v70\n"
  "v_pk_add_f16 v87, v55, v71\n"
  "v_mfma_f32_16x16x32_f16 a[208:211], v[20:23], v[116:119], 0\n"
  "v_pk_add_f16 v88, v56, v72\n"
  "v_pk_add_f16 v89, v57, v73\n"
  "v_mfma_f32_16x16x32_f16 a[220:223], v[24:27], v[116:119], 0\n"
  "v_pk_add_f16 v90, v58, v74\n"
  "v_pk_add_f16 v91, v59, v75\n"
  "v_mfma_f32_16x16x32_f16 a[232:235], v[28:31], v[116:119], 0\n"
  "v_pk_add_f16 v92, v60, v76\n"
  "v_pk_add_f16 v93, v61, v77\n"
  "v_mfma_f32_16x16x32_f16 a[200:203], v[32:35], v[120:123], 0\n"
  "v_pk_add_f16 v94, v62, v78\n"
  "v_pk_add_f16 v95, v63, v79\n"
  "s_waitcnt vmcnt(17) lgkmcnt(0)\n"
  "v_mfma_f32_16x16x32_f16 a[212:215], v[36:39], v[120:123], 0\n"
  "s_cmp_gt_u32 s62, 88\n"
  "s_cbranch_scc1 .Lpace_s0_None_%=\n"
  ".Lpaced_s0_None_%=:\n"
  "v_mfma_f32_16x16x32_f16 a[224:227], v[40:43], v[120:123], 0\n"
  "v_add_u32 v229, s49, v229\n"
  "v_add_u32 v230, s49, v230\n"
  "s_barrier\n"
  "v_mfma_f32_16x16x32_f16 a[236:239], v[44:47], v[120:123], 0\n"
  "v_add_u32 v231, s49, v231\n"
  "v_add_u32 v232, s49, v232\n"
  "v_mfma_f32_16x16x32_f16 a[0:3], v[48:51], v[96:99], a[0:3]\n"
  "v_pk_add_f16 v120, v96, v100\n"
  "v_pk_add_f16 v121, v97, v101\n"
  "v_mfma_f32_16x16x32_f16 a[12:15], v[52:55], v[96:99], a[12:15]\n"
  "v_pk_add_f16 v122, v98, v102\n"
  "v_pk_add_f16 v123, v99, v103\n"
  "v_mfma_f32_16x16x32_f16 a[24:27], v[56:59], v[96:99], a[24:27]\n"
  "s_and_b32 s65, s51, 0xffffe300\n"
  "ds_read_b64_tr_b16 v[0:1], v228 offset:0\n"
  "v_readlane_b32 s66, v244, 38\n"
  "v_mfma_f32_16x16x32_f16 a[36:39], v[60:63], v[96:99], a[36:39]\n"
  "buffer_load_dwordx4 v[164:167], v240, s[40:43], s59 offen sc1\n"
  "v_readlane_b32 s67, v244, 39\n"
  "v_mfma_f32_16x16x32_f16 a[4:7], v[64:67], v[100:103], a[4:7]\n"
  "s_cmp_eq_u32 s46, 73728\n"
  "s_cselect_b32 s49, s68, s69\n"
  "ds_read_b64_tr_b16 v[124:125], v233 offset:2048\n"
  "v_mfma_f32_16x16x32_f16 a[16:19], v[68:71], v[100:103], a[16:19]\n"
  "s_add_i32 m0, s44, 155648\n"
  "ds_read_b64_tr_b16 v[128:129], v234 offset:2048\n"
  "v_add_u32 v233, -45056, v233\n"
  "v_mfma_f32_16x16x32_f16 a[28:31], v[72:75], v[100:103], a[28:31]\n"
  "buffer_load_dwordx4 v239, s[40:43], s58 offen sc1 lds\n"
  "v_add_u32 v234, -45056, v234\n"
  "v_mfma_f32_16x16x32_f16 a[40:43], v[76:79], v[100:103], a[40:43]\n"
  "s_add_i32 s57, s57, 32768\n"
  "ds_read_b64_tr_b16 v[2:3], v228 offset:8192\n"
  "ds_read_b128 v[96:99], v232 offset:4096\n"
  "v_mfma_f32_16x16x32_f16 a[8:11], v[80:83], v[120:123], a[8:11]\n"
  "s_add_i32 m0, s47, 0\n"
  "s_lshr_b32 s59, s57, 9\n"
  "ds_read_b64_tr_b16 v[132:133], v235 offset:2048\n"
  "v_mfma_f32_16x16x32_f16 a[20:23], v[84:87], v[120:123], a[20:23]\n"
  "s_add_i32 s64, s50, 4194304\n"
  "buffer_load_dwordx4 v237, s[40:43], s64 offen sc1 lds\n"
  "v_add_u32 v235, -45056, v235\n"
  "v_mfma_f32_16x16x32_f16 a[32:35], v[88:91], v[120:123], a[32:35]\n"
  "s_add_i32 s53, s53, 8192\n"
  "s_lshr_b32 s52, s52, 1\n"
  "ds_read_b64_tr_b16 v[16:17], v228 offset:256\n"
  "v_mfma_f32_16x16x32_f16 a[44:47], v[92:95], v[120:123], a[44:47]\n"
  "s_bitcmp1_b32 s52, 0\n"
  "s_cselect_b32 s54, s53, 134217728\n"
  "ds_read_b128 v[100:103], v232 offset:4608\n"
  "v_mfma_f32_16x16x32_f16 a[48:51], v[48:51], v[104:107], a[48:51]\n"
  "s_add_i32 s51, s51, 8448\n"
  "buffer_store_dword v232, v245, s[40:43], s65 offen sc1\n"
  "v_pk_add_f16 v120, v104, v108\n"
  "v_mfma_f32_16x16x32_f16 a[60:63], v[52:55], v[104:107], a[60:63]\n"
  "ds_read_b64_tr_b16 v[136:137], v236 offset:2048\n"
  "v_pk_add_f16 v121, v105, v109\n"
  "v_mfma_f32_16x16x32_f16 a[72:75], v[56:59], v[104:107], a[72:75]\n"
  "ds_read_b64_tr_b16 v[18:19], v228 offset:8448\n"
  "v_pk_add_f16 v122, v106, v110\n"
  "v_mfma_f32_16x16x32_f16 a[84:87], v[60:63], v[104:107], a[84:87]\n"
  "buffer_store_dwordx4 a[240:243], v243, s[40:43], s66 offen sc1\n"
  "v_pk_add_f16 v123, v107, v111\n"
  "v_mfma_f32_16x16x32_f16 a[52:55], v[64:67], v[108:111], a[52:55]\n"
  "ds_read_b64_tr_b16 v[4:5], v229 offset:0\n"
  "v_readlane_b32 s66, v244, 40\n"
  "v_mfma_f32_16x16x32_f16 a[64:67], v[68:71], v[108:111], a[64:67]\n"
  "s_add_i32 m0, s47, 4096\n"
  "ds_read_b64_tr_b16 v[6:7], v229 offset:8192\n"
  "v_add_u32 v228, s49, v228\n"
  "v_mfma_f32_16x16x32_f16 a[76:79], v[72:75], v[108:111], a[76:79]\n"
  "s_add_i32 s64, s50, 4198400\n"
  "buffer_load_dwordx4 v237, s[40:43], s64 offen sc1 lds\n"
  "v_add_u32 v236, -45056, v236\n"
  "v_mfma_f32_16x16x32_f16 a[88:91], v[76:79], v[108:111], a[88:91]\n"
  "ds_read_b64_tr_b16 v[20:21], v229 offset:256\n"
  "ds_read_b64_tr_b16 v[22:23], v229 offset:8448\n"
  "v_mfma_f32_16x16x32_f16 a[56:59], v[80:83], v[120:123], a[56:59]\n"
  "s_waitcnt lgkmcnt(5)\n"
  "ds_read_b128 v[104:107], v232 offset:6144\n"
  "v_mov_b64 v[126:127], v[124:125]\n"
  "v_mfma_f32_16x16x32_f16 a[68:71], v[84:87], v[120:123], a[68:71]\n"
  "buffer_store_dwordx4 a[244:247], v243, s[40:43], s67 offen sc1\n"
  "v_mov_b64 v[130:131], v[128:129]\n"
  "v_mfma_f32_16x16x32_f16 a[80:83], v[88:91], v[120:123], a[80:83]\n"
  "s_waitcnt lgkmcnt(5)\n"
  "ds_read_b64_tr_b16 v[8:9], v230 offset:0\n"
  "v_readlane_b32 s67, v244, 41\n"
  "v_mfma_f32_16x16x32_f16 a[92:95], v[92:95], v[120:123], a[92:95]\n"
  "s_add_i32 m0, s47, 8192\n"
  "ds_read_b128 v[108:111], v232 offset:6656\n"
  "v_mov_b64 v[134:135], v[132:133]\n"
  "v_mfma_f32_16x16x32_f16 a[96:99], v[48:51], v[96:99], a[96:99]\n"
  "s_add_i32 s64, s50, 4202496\n"
  "buffer_load_dwordx4 v237, s[40:43], s64 offen sc1 lds\n"
  "v_pk_add_f16 v120, v96, v100\n"
  "v_mfma_f32_16x16x32_bf16 v[140:143], v[124:127], v[176:179], 0\n"
  "ds_read_b64_tr_b16 v[10:11], v230 offset:8192\n"
  "v_pk_add_f16 v121, v97, v101\n"
  "v_mfma_f32_16x16x32_f16 a[108:111], v[52:55], v[96:99], a[108:111]\n"
  "s_waitcnt lgkmcnt(4)\n"
  "ds_read_b64_tr_b16 v[24:25], v230 offset:256\n"
  "v_pk_add_f16 v122, v98, v102\n"
  "v_mfma_f32_16x16x32_f16 a[120:123], v[56:59], v[96:99], a[120:123]\n"
  "buffer_store_dwordx4 a[248:251], v243, s[40:43], s66 offen sc1\n"
  "v_pk_add_f16 v123, v99, v103\n"
  "v_mfma_f32_16x16x32_f16 a[132:135], v[60:63], v[96:99], a[132:135]\n"
  "ds_read_b64_tr_b16 v[26:27], v230 offset:8448\n"
  "v_mov_b64 v[138:139], v[136:137]\n"
  "v_mfma_f32_16x16x32_bf16 v[144:147], v[128:131], v[176:179], 0\n"
  "s_add_i32 m0, s47, 12288\n"
  "ds_read_b64_tr_b16 v[12:13], v231 offset:0\n"
  "v_cvt_pkrtz_f16_f32 v124, v140, v141\n"
  "v_mfma_f32_16x16x32_f16 a[100:103], v[64:67], v[100:103], a[100:103]\n"
  "s_add_i32 s64, s50, 4206592\n"
  "buffer_load_dwordx4 v237, s[40:43], s64 offen sc1 lds\n"
  "v_cvt_pkrtz_f16_f32 v125, v142, v143\n"
  "v_mfma_f32_16x16x32_f16 a[112:115], v[68:71], v[100:103], a[112:115]\n"
  "ds_read_b64_tr_b16 v[14:15], v231 offset:8192\n"
  "v_and_b32 v124, 0xfff8fff8, v124\n"
  "v_mfma_f32_16x16x32_f16 a[124:127], v[72:75], v[100:103], a[124:127]\n"
  "ds_read_b128 v[96:99], v232 offset:0\n"
  "v_cvt_pkrtz_f16_f32 v126, v144, v145\n"
  "v_mfma_f32_16x16x32_bf16 v[148:151], v[132:135], v[176:179], 0\n"
  "buffer_store_dwordx4 a[252:255], v243, s[40:43], s67 offen sc1\n"
  "v_cvt_pkrtz_f16_f32 v127, v146, v147\n"
  "v_mfma_f32_16x16x32_f16 a[136:139], v[76:79], v[100:103], a[136:139]\n"
  "ds_read_b128 v[112:115], v232 offset:8192\n"
  "v_and_b32 v125, 0xfff8fff8, v125\n"
  "v_mfma_f32_16x16x32_f16 a[104:107], v[80:83], v[120:123], a[104:107]\n"
  "s_add_i32 m0, s47, 16384\n"
  "ds_read_b64_tr_b16 v[28:29], v231 offset:256\n"
  "v_and_b32 v126, 0xfff8fff8, v126\n"
  "v_mfma_f32_16x16x32_f16 a[116:119], v[84:87], v[120:123], a[116:119]\n"
  "s_waitcnt lgkmcnt(8)\n"
  "s_add_i32 s64, s50, 4194304\n"
  "buffer_load_dwordx4 v238, s[40:43], s64 offen sc1 lds\n"
  "v_mfma_f32_16x16x32_bf16 v[152:155], v[136:139], v[176:179], 0\n"
  "s_waitcnt lgkmcnt(5)\n"
  "ds_read_b128 v[100:103], v232 offset:512\n"
  "v_and_b32 v127, 0xfff8fff8, v127\n"
  "v_mfma_f32_16x16x32_f16 a[128:131], v[88:91], v[120:123], a[128:131]\n"
  "s_add_i32 m0, s47, 20480\n"
  "ds_read_b128 v[116:119], v232 offset:8704\n"
  "v_cvt_pkrtz_f16_f32 v128, v148, v149\n"
  "v_mfma_f32_16x16x32_f16 a[140:143], v[92:95], v[120:123], a[140:143]\n"
  "s_add_i32 s64, s50, 4096\n"
  "buffer_load_dwordx4 v238, s[40:43], s64 offen sc1 lds\n"
  "v_cvt_pkrtz_f16_f32 v129, v150, v151\n"
  "v_mfma_f32_16x16x32_f16 a[144:147], v[48:51], v[104:107], a[144:147]\n"
  "ds_read_b64_tr_b16 v[30:31], v231 offset:8448\n"
  "v_pk_add_f16 v120, v104, v108\n"
  "v_mfma_f32_16x16x32_f16 a[156:159], v[52:55], v[104:107], a[156:159]\n"
  "s_add_i32 m0, s48, 24576\n"
  "v_pk_add_f16 v121, v105, v109\n"
  "v_pk_add_f16 v122, v106, v110\n"
  "v_mfma_f32_16x16x32_f16 a[168:171], v[56:59], v[104:107], a[168:171]\n"
  "s_add_i32 s64, s50, 16384\n"
  "buffer_load_dwordx4 v238, s[40:43], s64 offen sc1 lds\n"
  "v_pk_add_f16 v123, v107, v111\n"
  "v_mfma_f32_16x16x32_f16 a[180:183], v[60:63], v[104:107], a[180:183]\n"
  "ds_read_b128 v[104:107], v232 offset:2048\n"
  "v_cvt_pkrtz_f16_f32 v130, v152, v153\n"
  "v_mfma_f32_16x16x32_f16 a[148:151], v[64:67], v[108:111], a[148:151]\n"
  "v_cvt_pkrtz_f16_f32 v131, v154, v155\n"
  "v_and_b32 v128, 0xfff8fff8, v128\n"
  "v_mfma_f32_16x16x32_f16 a[160:163], v[68:71], v[108:111], a[160:163]\n"
  "buffer_store_dwordx4 v[124:127], v242, s[40:43], s56 offen offset:256 sc1\n"
  "v_and_b32 v129, 0xfff8fff8, v129\n"
  "v_mfma_f32_16x16x32_f16 a[172:175], v[72:75], v[108:111], a[172:175]\n"
  "v_and_b32 v130, 0xfff8fff8, v130\n"
  "v_and_b32 v131, 0xfff8fff8, v131\n"
  "v_mfma_f32_16x16x32_f16 a[184:187], v[76:79], v[108:111], a[184:187]\n"
  "s_add_i32 m0, s47, 26624\n"
  "s_waitcnt lgkmcnt(1)\n"
  "ds_read_b128 v[108:111], v232 offset:2560\n"
  "v_mfma_f32_16x16x32_f16 a[152:155], v[80:83], v[120:123], a[152:155]\n"
  "s_add_i32 s64, s50, 4214784\n"
  "buffer_load_dwordx4 v238, s[40:43], s64 offen sc1 lds\n"
  "v_pk_add_f16 v32, v0, v16\n"
  "v_mfma_f32_16x16x32_f16 a[164:167], v[84:87], v[120:123], a[164:167]\n"
  "v_pk_add_f16 v33, v1, v17\n"
  "v_pk_add_f16 v34, v2, v18\n"
  "v_mfma_f32_16x16x32_f16 a[176:179], v[88:91], v[120:123], a[176:179]\n"
  "s_add_i32 m0, s47, 30720\n"
  "v_pk_add_f16 v35, v3, v19\n"
  "v_pk_add_f16 v36, v4, v20\n"
  "v_mfma_f32_16x16x32_f16 a[188:191], v[92:95], v[120:123], a[188:191]\n"
  "s_add_i32 s64, s50, 32768\n"
  "buffer_load_dwordx4 v238, s[40:43], s64 offen sc1 lds\n"
  "v_pk_add_f16 v37, v5, v21\n"
  "v_mfma_f32_16x16x32_f16 a[192:195], v[48:51], v[112:115], a[192:195]\n"
  "s_add_i32 s47, s44, s46\n"
  "v_pk_add_f16 v120, v112, v116\n"
  "v_pk_add_f16 v121, v113, v117\n"
  "v_mfma_f32_16x16x32_f16 a[204:207], v[52:55], v[112:115], a[204:207]\n"
  "s_add_i32 s48, s47, s45\n"
  "v_pk_add_f16 v122, v114, v118\n"
  "v_pk_add_f16 v123, v115, v119\n"
  "v_mfma_f32_16x16x32_f16 a[216:219], v[56:59], v[112:115], a[216:219]\n"
  "s_add_i32 s46, s46, s49\n"
  "buffer_store_dwordx4 v[128:131], v242, s[40:43], s56 offen offset:384 sc1\n"
  "v_pk_add_f16 v38, v6, v22\n"
  "v_mfma_f32_16x16x32_f16 a[228:231], v[60:63], v[112:115], a[228:231]\n"
  "s_cmp_lt_u32 s57, 0x80000\n"
  "s_cselect_b32 s58, s57, 0xf0000000\n"
  "s_cselect_b32 s59, s59, 0xf0000000\n"
  "v_mfma_f32_16x16x32_f16 a[196:199], v[64:67], v[116:119], a[196:199]\n"
  "s_add_i32 s55, s55, 8404992\n"
  "v_pk_add_f16 v39, v7, v23\n"
  "v_pk_add_f16 v40, v8, v24\n"
  "v_mfma_f32_16x16x32_f16 a[208:211], v[68:71], v[116:119], a[208:211]\n"
  "s_add_i32 s50, s50, 4194304\n"
  "v_pk_add_f16 v41, v9, v25\n"
  "v_pk_add_f16 v42, v10, v26\n"
  "v_mfma_f32_16x16x32_f16 a[220:223], v[72:75], v[116:119], a[220:223]\n"
  "s_and_b32 s56, s55, 0xff80c000\n"
  "v_pk_add_f16 v43, v11, v27\n"
  "v_pk_add_f16 v44, v12, v28\n"
  "v_mfma_f32_16x16x32_f16 a[232:235], v[76:79], v[116:119], a[232:235]\n"
  "v_pk_add_f16 v45, v13, v29\n"
  "v_pk_add_f16 v46, v14, v30\n"
  "v_mfma_f32_16x16x32_f16 a[200:203], v[80:83], v[120:123], a[200:203]\n"
  "v_pk_add_f16 v47, v15, v31\n"
  "v_add_u32 v229, s49, v229\n"
  "s_waitcnt vmcnt(16) lgkmcnt(0)\n"
  "v_mfma_f32_16x16x32_f16 a[212:215], v[84:87], v[120:123], a[212:215]\n"
  "s_cmp_gt_u32 s62, 88\n"
  "s_cbranch_scc1 .Lpace_s1_None_%=\n"
  ".Lpaced_s1_None_%=:\n"
  "v_mfma_f32_16x16x32_f16 a[224:227], v[88:91], v[120:123], a[224:227]\n"
  "v_add_u32 v230, s49, v230\n"
  "v_add_u32 v231, s49, v231\n"
  "s_barrier\n"
  "v_mfma_f32_16x16x32_f16 a[236:239], v[92:95], v[120:123], a[236:239]\n"
  "v_add_u32 v232, s49, v232\n"
  "s_mov_b32 s63, 10\n"
  ".p2align 6\n"
  ".LloopP_%=:\n"
  "v_mfma_f32_16x16x32_f16 a[0:3], v[0:3], v[96:99], a[0:3]\n"
  "buffer_load_dword v247, v246, s[40:43], s54 offen sc1\n"
  "v_pk_add_f16 v120, v96, v100\n"
  "v_mfma_f32_16x16x32_f16 a[12:15], v[4:7], v[96:99], a[12:15]\n"
  "v_pk_add_f16 v121, v97, v101\n"
  "v_pk_add_f16 v122, v98, v102\n"
  "v_mfma_f32_16x16x32_f16 a[24:27], v[8:11], v[96:99], a[24:27]\n"
  "s_min_u32 s67, s60, 11\n"
  "s_lshl_b32 s67, s67, 2\n"
  "v_pk_add_f16 v123, v99, v103\n"
  "v_mfma_f32_16x16x32_f16 a[36:39], v[12:15], v[96:99], a[36:39]\n"
  "ds_read_b64_tr_b16 v[48:49], v228 offset:0\n"
  "v_readlane_b32 s66, v244, s60\n"
  "v_mfma_f32_16x16x32_f16 a[4:7], v[16:19], v[100:103], a[4:7]\n"
  "s_cmp_eq_u32 s46, 73728\n"
  "s_cselect_b32 s49, s68, s69\n"
  "buffer_load_dwordx4 v[168:171], v240, s[40:43], s59 offen sc1\n"
  "v_mfma_f32_16x16x32_f16 a[16:19], v[20:23], v[100:103], a[16:19]\n"
  "ds_read_b64_tr_b16 v[124:125], v233 offset:0\n"
  "ds_read_b64_tr_b16 v[128:129], v234 offset:0\n"
  "v_mfma_f32_16x16x32_f16 a[28:31], v[24:27], v[100:103], a[28:31]\n"
  "ds_read_b64_tr_b16 v[50:51], v228 offset:8192\n"
  "ds_read_b128 v[96:99], v232 offset:4096\n"
  "v_mfma_f32_16x16x32_f16 a[40:43], v[28:31], v[100:103], a[40:43]\n"
  "s_add_i32 m0, s44, 110592\n"
  "ds_read_b64_tr_b16 v[160:161], v241 offset:45056\n"
  "ds_read_b64_tr_b16 v[162:163], v241 offset:45568\n"
  "v_mfma_f32_16x16x32_f16 a[8:11], v[32:35], v[120:123], a[8:11]\n"
  "buffer_load_dwordx4 v239, s[40:43], s58 offen sc1 lds\n"
  "ds_read_b64_tr_b16 v[132:133], v235 offset:0\n"
  "v_mfma_f32_16x16x32_f16 a[20:23], v[36:39], v[120:123], a[20:23]\n"
  "s_add_i32 s57, s57, 32768\n"
  "ds_read_b64_tr_b16 v[64:65], v228 offset:256\n"
  "ds_read_b128 v[100:103], v232 offset:4608\n"
  "v_mfma_f32_16x16x32_f16 a[32:35], v[40:43], v[120:123], a[32:35]\n"
  "s_lshr_b32 s59, s57, 9\n"
  "s_add_i32 s60, s60, 1\n"
  "ds_read_b64_tr_b16 v[136:137], v236 offset:0\n"
  "v_mfma_f32_16x16x32_f16 a[44:47], v[44:47], v[120:123], a[44:47]\n"
  "s_add_i32 m0, s47, 0\n"
  "ds_read_b64_tr_b16 v[66:67], v228 offset:8448\n"
  "v_add_u32 v228, s49, v228\n"
  "v_mfma_f32_16x16x32_f16 a[48:51], v[0:3], v[104:107], a[48:51]\n"
  "s_add_i32 s64, s50, 4194304\n"
  "buffer_load_dwordx4 v237, s[40:43], s64 offen sc1 lds\n"
  "v_pk_add_f16 v120, v104, v108\n"
  "v_mfma_f32_16x16x32_f16 a[60:63], v[4:7], v[104:107], a[60:63]\n"
  "s_set_gpr_idx_on s67, gpr_idx(SRC0)\n"
  "v_mov_b64 v[156:157], v[180:181]\n"
  "v_mov_b64 v[158:159], v[182:183]\n"
  "s_set_gpr_idx_off\n"
  "v_mfma_f32_16x16x32_f16 a[72:75], v[8:11], v[104:107], a[72:75]\n"
  "s_waitcnt lgkmcnt(5)\n"
  "ds_read_b64_tr_b16 v[52:53], v229 offset:0\n"
  "v_pk_add_f16 v121, v105, v109\n"
  "v_mfma_f32_16x16x32_f16 a[84:87], v[12:15], v[104:107], a[84:87]\n"
  "v_pk_add_f16 v122, v106, v110\n"
  "v_pk_add_f16 v123, v107, v111\n"
  "v_mfma_f32_16x16x32_f16 a[52:55], v[16:19], v[108:111], a[52:55]\n"
  "buffer_store_dwordx4 v[156:159], v243, s[40:43], s66 offen sc1\n"
  "ds_read_b64_tr_b16 v[54:55], v229 offset:8192\n"
  "v_mfma_f32_16x16x32_f16 a[64:67], v[20:23], v[108:111], a[64:67]\n"
  "s_waitcnt lgkmcnt(3)\n"
  "ds_read_b64_tr_b16 v[68:69], v229 offset:256\n"
  "v_mov_b64 v[126:127], v[124:125]\n"
  "v_mfma_f32_16x16x32_f16 a[76:79], v[24:27], v[108:111], a[76:79]\n"
  "s_waitcnt lgkmcnt(3)\n"
  "ds_read_b64_tr_b16 v[70:71], v229 offset:8448\n"
  "v_mov_b64 v[130:131], v[128:129]\n"
  "v_mfma_f32_16x16x32_bf16 v[172:175], v[160:163], v[164:167], v[172:175]\n"
  "s_add_i32 m0, s47, 4096\n"
  "ds_read_b128 v[104:107], v232 offset:6144\n"
  "v_mov_b64 v[134:135], v[132:133]\n"
  "v_mfma_f32_16x16x32_f16 a[88:91], v[28:31], v[108:111], a[88:91]\n"
  "s_add_i32 s64, s50, 4198400\n"
  "buffer_load_dwordx4 v237, s[40:43], s64 offen sc1 lds\n"
  "v_mov_b64 v[138:139], v[136:137]\n"
  "v_mfma_f32_16x16x32_f16 a[56:59], v[32:35], v[120:123], a[56:59]\n"
  "ds_read_b64_tr_b16 v[56:57], v230 offset:0\n"
  "v_pk_add_f16 v80, v48, v64\n"
  "v_mfma_f32_16x16x32_f16 a[68:71], v[36:39], v[120:123], a[68:71]\n"
  "ds_read_b128 v[108:111], v232 offset:6656\n"
  "v_pk_add_f16 v81, v49, v65\n"
  "v_mfma_f32_16x16x32_f16 a[80:83], v[40:43], v[120:123], a[80:83]\n"
  "s_add_i32 m0, s47, 8192\n"
  "ds_read_b64_tr_b16 v[58:59], v230 offset:8192\n"
  "v_pk_add_f16 v82, v50, v66\n"
  "v_mfma_f32_16x16x32_f16 a[92:95], v[44:47], v[120:123], a[92:95]\n"
  "s_add_i32 s64, s50, 4202496\n"
  "buffer_load_dwordx4 v237, s[40:43], s64 offen sc1 lds\n"
  "v_pk_add_f16 v83, v51, v67\n"
  "v_mfma_f32_16x16x32_f16 a[96:99], v[0:3], v[96:99], a[96:99]\n"
  "s_waitcnt lgkmcnt(4)\n"
  "v_pk_add_f16 v120, v96, v100\n"
  "v_pk_add_f16 v121, v97, v101\n"
  "v_mfma_f32_16x16x32_bf16 v[140:143], v[124:127], v[176:179], 0\n"
  "ds_read_b64_tr_b16 v[72:73], v230 offset:256\n"
  "v_pk_add_f16 v84, v52, v68\n"
  "v_mfma_f32_16x16x32_f16 a[108:111], v[4:7], v[96:99], a[108:111]\n"
  "s_add_i32 m0, s47, 12288\n"
  "v_pk_add_f16 v122, v98, v102\n"
  "v_pk_add_f16 v123, v99, v103\n"
  "v_mfma_f32_16x16x32_f16 a[120:123], v[8:11], v[96:99], a[120:123]\n"
  "s_add_i32 s64, s50, 4206592\n"
  "buffer_load_dwordx4 v237, s[40:43], s64 offen sc1 lds\n"
  "v_pk_add_f16 v85, v53, v69\n"
  "v_mfma_f32_16x16x32_f16 a[132:135], v[12:15], v[96:99], a[132:135]\n"
  "ds_read_b64_tr_b16 v[74:75], v230 offset:8448\n"
  "v_cvt_pkrtz_f16_f32 v124, v140, v141\n"
  "v_mfma_f32_16x16x32_bf16 v[144:147], v[128:131], v[176:179], 0\n"
  "ds_read_b64_tr_b16 v[60:61], v231 offset:0\n"
  "v_cvt_pkrtz_f16_f32 v125, v142, v143\n"
  "v_mfma_f32_16x16x32_f16 a[100:103], v[16:19], v[100:103], a[100:103]\n"
  "s_add_i32 m0, s47, 16384\n"
  "ds_read_b64_tr_b16 v[62:63], v231 offset:8192\n"
  "v_and_b32 v124, 0xfff8fff8, v124\n"
  "v_mfma_f32_16x16x32_f16 a[112:115], v[20:23], v[100:103], a[112:115]\n"
  "s_add_i32 s64, s50, 4194304\n"
  "buffer_load_dwordx4 v238, s[40:43], s64 offen sc1 lds\n"
  "v_and_b32 v125, 0xfff8fff8, v125\n"
  "v_mfma_f32_16x16x32_f16 a[124:127], v[24:27], v[100:103], a[124:127]\n"
  "ds_read_b128 v[96:99], v232 offset:0\n"
  "v_cvt_pkrtz_f16_f32 v126, v144, v145\n"
  "v_mfma_f32_16x16x32_bf16 v[148:151], v[132:135], v[176:179], 0\n"
  "ds_read_b128 v[112:115], v232 offset:8192\n"
  "v_cvt_pkrtz_f16_f32 v127, v146, v147\n"
  "v_mfma_f32_16x16x32_f16 a[136:139], v[28:31], v[100:103], a[136:139]\n"
  "s_add_i32 m0, s47, 20480\n"
  "ds_read_b64_tr_b16 v[76:77], v231 offset:256\n"
  "v_and_b32 v126, 0xfff8fff8, v126\n"
  "v_mfma_f32_16x16x32_f16 a[104:107], v[32:35], v[120:123], a[104:107]\n"
  "s_add_i32 s64, s50, 4096\n"
  "buffer_load_dwordx4 v238, s[40:43], s64 offen sc1 lds\n"
  "v_and_b32 v127, 0xfff8fff8, v127\n"
  "v_mfma_f32_16x16x32_f16 a[116:119], v[36:39], v[120:123], a[116:119]\n"
  "s_waitcnt lgkmcnt(8)\n"
  "ds_read_b128 v[100:103], v232 offset:512\n"
  "v_cvt_pkrtz_f16_f32 v128, v148, v149\n"
  "v_mfma_f32_16x16x32_bf16 v[152:155], v[136:139], v[176:179], 0\n"
  "s_waitcnt lgkmcnt(6)\n"
  "ds_read_b128 v[116:119], v232 offset:8704\n"
  "v_cvt_pkrtz_f16_f32 v129, v150, v151\n"
  "v_mfma_f32_16x16x32_f16 a[128:131], v[40:43], v[120:123], a[128:131]\n"
  "s_add_i32 m0, s48, 24576\n"
  "ds_read_b64_tr_b16 v[78:79], v231 offset:8448\n"
  "v_and_b32 v128, 0xfff8fff8, v128\n"
  "v_mfma_f32_16x16x32_f16 a[140:143], v[44:47], v[120:123], a[140:143]\n"
  "s_add_i32 s64, s50, 16384\n"
  "buffer_load_dwordx4 v238, s[40:43], s64 offen sc1 lds\n"
  "v_and_b32 v129, 0xfff8fff8, v129\n"
  "v_mfma_f32_16x16x32_f16 a[144:147], v[0:3], v[104:107], a[144:147]\n"
  "v_pk_add_f16 v120, v104, v108\n"
  "v_pk_add_f16 v121, v105, v109\n"
  "v_mfma_f32_16x16x32_f16 a[156:159], v[4:7], v[104:107], a[156:159]\n"
  "v_pk_add_f16 v122, v106, v110\n"
  "v_pk_add_f16 v123, v107, v111\n"
  "v_mfma_f32_16x16x32_f16 a[168:171], v[8:11], v[104:107], a[168:171]\n"
  "v_cvt_pkrtz_f16_f32 v130, v152, v153\n"
  "v_cvt_pkrtz_f16_f32 v131, v154, v155\n"
  "v_mfma_f32_16x16x32_f16 a[180:183], v[12:15], v[104:107], a[180:183]\n"
  "buffer_store_dwordx4 v[124:127], v242, s[40:43], s56 offen offset:0 sc1\n"
  "v_and_b32 v130, 0xfff8fff8, v130\n"
  "v_mfma_f32_16x16x32_f16 a[148:151], v[16:19], v[108:111], a[148:151]\n"
  "ds_read_b128 v[104:107], v232 offset:2048\n"
  "v_and_b32 v131, 0xfff8fff8, v131\n"
  "v_mfma_f32_16x16x32_f16 a[160:163], v[20:23], v[108:111], a[160:163]\n"
  "s_cmp_lt_u32 s57, 0x80000\n"
  "s_cselect_b32 s58, s57, 0xf0000000\n"
  "s_cselect_b32 s59, s59, 0xf0000000\n"
  "v_mfma_f32_16x16x32_f16 a[172:175], v[24:27], v[108:111], a[172:175]\n"
  "s_add_i32 m0, s47, 26624\n"
  "s_waitcnt lgkmcnt(1)\n"
  "v_pk_add_f16 v86, v54, v70\n"
  "v_mfma_f32_16x16x32_f16 a[184:187], v[28:31], v[108:111], a[184:187]\n"
  "s_add_i32 s64, s50, 4214784\n"
  "buffer_load_dwordx4 v238, s[40:43], s64 offen sc1 lds\n"
  "v_pk_add_f16 v87, v55, v71\n"
  "v_mfma_f32_16x16x32_f16 a[152:155], v[32:35], v[120:123], a[152:155]\n"
  "ds_read_b128 v[108:111], v232 offset:2560\n"
  "v_pk_add_f16 v88, v56, v72\n"
  "v_mfma_f32_16x16x32_f16 a[164:167], v[36:39], v[120:123], a[164:167]\n"
  "v_pk_add_f16 v89, v57, v73\n"
  "v_pk_add_f16 v90, v58, v74\n"
  "v_mfma_f32_16x16x32_f16 a[176:179], v[40:43], v[120:123], a[176:179]\n"
  "s_add_i32 m0, s47, 30720\n"
  "v_pk_add_f16 v91, v59, v75\n"
  "v_pk_add_f16 v92, v60, v76\n"
  "v_mfma_f32_16x16x32_f16 a[188:191], v[44:47], v[120:123], a[188:191]\n"
  "s_add_i32 s64, s50, 32768\n"
  "buffer_load_dwordx4 v238, s[40:43], s64 offen sc1 lds\n"
  "v_pk_add_f16 v93, v61, v77\n"
  "v_mfma_f32_16x16x32_f16 a[192:195], v[0:3], v[112:115], a[192:195]\n"
  "s_add_i32 s47, s44, s46\n"
  "v_pk_add_f16 v120, v112, v116\n"
  "v_pk_add_f16 v121, v113, v117\n"
  "v_mfma_f32_16x16x32_f16 a[204:207], v[4:7], v[112:115], a[204:207]\n"
  "s_add_i32 s48, s47, s45\n"
  "v_pk_add_f16 v122, v114, v118\n"
  "v_pk_add_f16 v123, v115, v119\n"
  "v_mfma_f32_16x16x32_f16 a[216:219], v[8:11], v[112:115], a[216:219]\n"
  "s_add_i32 s46, s46, s49\n"
  "v_pk_add_f16 v94, v62, v78\n"
  "v_pk_add_f16 v95, v63, v79\n"
  "v_mfma_f32_16x16x32_f16 a[228:231], v[12:15], v[112:115], a[228:231]\n"
  "s_add_i32 s50, s50, 4194304\n"
  "buffer_store_dwordx4 v[128:131], v242, s[40:43], s56 offen offset:128 sc1\n"
  "v_add_u32 v229, s49, v229\n"
  "v_mfma_f32_16x16x32_f16 a[196:199], v[16:19], v[116:119], a[196:199]\n"
  "v_add_u32 v230, s49, v230\n"
  "v_add_u32 v231, s49, v231\n"
  "v_mfma_f32_16x16x32_f16 a[208:211], v[20:23], v[116:119], a[208:211]\n"
  "v_add_u32 v232, s49, v232\n"
  "v_mfma_f32_16x16x32_f16 a[220:223], v[24:27], v[116:119], a[220:223]\n"
  "v_mfma_f32_16x16x32_f16 a[232:235], v[28:31], v[116:119], a[232:235]\n"
  "v_mfma_f32_16x16x32_f16 a[200:203], v[32:35], v[120:123], a[200:203]\n"
  "s_waitcnt vmcnt(12) lgkmcnt(0)\n"
  "v_mfma_f32_16x16x32_f16 a[212:215], v[36:39], v[120:123], a[212:215]\n"
  "v_cmp_eq_u32 vcc, 0, v247\n"
  "s_cbranch_vccnz .Lspin_pe_None_%=\n"
  ".Lspun_pe_None_%=:\n"
  "s_cmp_gt_u32 s62, 88\n"
  "s_cbranch_scc1 .Lpace_pe_None_%=\n"
  ".Lpaced_pe_None_%=:\n"
  "v_mfma_f32_16x16x32_f16 a[224:227], v[40:43], v[120:123], a[224:227]\n"
  "s_barrier\n"
  "v_mfma_f32_16x16x32_f16 a[236:239], v[44:47], v[120:123], a[236:239]\n"
  "v_mfma_f32_16x16x32_f16 a[0:3], v[48:51], v[96:99], a[0:3]\n"
  "buffer_load_dwordx4 v[164:167], v240, s[40:43], s59 offen sc1\n"
  "v_pk_add_f16 v120, v96, v100\n"
  "v_mfma_f32_16x16x32_f16 a[12:15], v[52:55], v[96:99], a[12:15]\n"
  "v_pk_add_f16 v121, v97, v101\n"
  "v_pk_add_f16 v122, v98, v102\n"
  "v_mfma_f32_16x16x32_f16 a[24:27], v[56:59], v[96:99], a[24:27]\n"
  "s_min_u32 s67, s60, 11\n"
  "s_lshl_b32 s67, s67, 2\n"
  "v_pk_add_f16 v123, v99, v103\n"
  "v_mfma_f32_16x16x32_f16 a[36:39], v[60:63], v[96:99], a[36:39]\n"
  "s_add_i32 m0, s44, 155648\n"
  "s_and_b32 s65, s51, 0xffffe300\n"
  "ds_read_b64_tr_b16 v[0:1], v228 offset:0\n"
  "v_mfma_f32_16x16x32_f16 a[4:7], v[64:67], v[100:103], a[4:7]\n"
  "buffer_load_dwordx4 v239, s[40:43], s58 offen sc1 lds\n"
  "v_readlane_b32 s66, v244, s60\n"
  "v_mfma_f32_16x16x32_f16 a[16:19], v[68:71], v[100:103], a[16:19]\n"
  "s_cmp_eq_u32 s46, 73728\n"
  "s_cselect_b32 s49, s68, s69\n"
  "ds_read_b64_tr_b16 v[124:125], v233 offset:2048\n"
  "v_mfma_f32_16x16x32_f16 a[28:31], v[72:75], v[100:103], a[28:31]\n"
  "s_add_i32 s57, s57, 32768\n"
  "ds_read_b64_tr_b16 v[128:129], v234 offset:2048\n"
  "v_add_u32 v233, 4096, v233\n"
  "v_mfma_f32_16x16x32_f16 a[40:43], v[76:79], v[100:103], a[40:43]\n"
  "s_add_i32 m0, s47, 0\n"
  "s_lshr_b32 s59, s57, 9\n"
  "ds_read_b64_tr_b16 v[2:3], v228 offset:8192\n"
  "v_mfma_f32_16x16x32_f16 a[8:11], v[80:83], v[120:123], a[8:11]\n"
  "s_add_i32 s64, s50, 4194304\n"
  "buffer_load_dwordx4 v237, s[40:43], s64 offen sc1 lds\n"
  "v_add_u32 v234, 4096, v234\n"
  "v_mfma_f32_16x16x32_f16 a[20:23], v[84:87], v[120:123], a[20:23]\n"
  "ds_read_b128 v[96:99], v232 offset:4096\n"
  "ds_read_b64_tr_b16 v[160:161], v241 offset:0\n"
  "v_mfma_f32_16x16x32_f16 a[32:35], v[88:91], v[120:123], a[32:35]\n"
  "s_add_i32 s53, s53, 8192\n"
  "s_lshr_b32 s52, s52, 1\n"
  "ds_read_b64_tr_b16 v[162:163], v241 offset:512\n"
  "v_mfma_f32_16x16x32_f16 a[44:47], v[92:95], v[120:123], a[44:47]\n"
  "s_add_i32 s60, s60, 1\n"
  "ds_read_b64_tr_b16 v[132:133], v235 offset:2048\n"
  "v_add_u32 v235, 4096, v235\n"
  "v_mfma_f32_16x16x32_f16 a[48:51], v[48:51], v[104:107], a[48:51]\n"
  "s_add_i32 s51, s51, 8448\n"
  "buffer_store_dword v232, v245, s[40:43], s65 offen sc1\n"
  "ds_read_b64_tr_b16 v[16:17], v228 offset:256\n"
  "v_mfma_f32_16x16x32_f16 a[60:63], v[52:55], v[104:107], a[60:63]\n"
  "ds_read_b128 v[100:103], v232 offset:4608\n"
  "v_pk_add_f16 v120, v104, v108\n"
  "v_mfma_f32_16x16x32_f16 a[72:75], v[56:59], v[104:107], a[72:75]\n"
  "ds_read_b64_tr_b16 v[136:137], v236 offset:2048\n"
  "ds_read_b64_tr_b16 v[18:19], v228 offset:8448\n"
  "s_set_gpr_idx_on s67, gpr_idx(SRC0)\n"
  "v_mov_b64 v[156:157], v[180:181]\n"
  "v_mov_b64 v[158:159], v[182:183]\n"
  "s_set_gpr_idx_off\n"
  "v_mfma_f32_16x16x32_f16 a[84:87], v[60:63], v[104:107], a[84:87]\n"
  "ds_read_b64_tr_b16 v[4:5], v229 offset:0\n"
  "v_pk_add_f16 v121, v105, v109\n"
  "v_mfma_f32_16x16x32_f16 a[52:55], v[64:67], v[108:111], a[52:55]\n"
  "buffer_store_dwordx4 v[156:159], v243, s[40:43], s66 offen sc1\n"
  "v_pk_add_f16 v122, v106, v110\n"
  "v_mfma_f32_16x16x32_f16 a[64:67], v[68:71], v[108:111], a[64:67]\n"
  "s_waitcnt lgkmcnt(6)\n"
  "ds_read_b64_tr_b16 v[6:7], v229 offset:8192\n"
  "v_pk_add_f16 v123, v107, v111\n"
  "v_mfma_f32_16x16x32_f16 a[76:79], v[72:75], v[108:111], a[76:79]\n"
  "s_bitcmp1_b32 s52, 0\n"
  "s_cselect_b32 s54, s53, 134217728\n"
  "ds_read_b64_tr_b16 v[20:21], v229 offset:256\n"
  "v_mfma_f32_16x16x32_bf16 v[172:175], v[160:163], v[168:171], v[172:175]\n"
  "s_add_i32 m0, s47, 4096\n"
  "ds_read_b64_tr_b16 v[22:23], v229 offset:8448\n"
  "v_add_u32 v228, s49, v228\n"
  "v_mfma_f32_16x16x32_f16 a[88:91], v[76:79], v[108:111], a[88:91]\n"
  "s_add_i32 s64, s50, 4198400\n"
  "buffer_load_dwordx4 v237, s[40:43], s64 offen sc1 lds\n"
  "v_add_u32 v229, s49, v229\n"
  "v_mfma_f32_16x16x32_f16 a[56:59], v[80:83], v[120:123], a[56:59]\n"
  "s_waitcnt lgkmcnt(5)\n"
  "ds_read_b128 v[104:107], v232 offset:6144\n"
  "v_mov_b64 v[126:127], v[124:125]\n"
  "v_mfma_f32_16x16x32_f16 a[68:71], v[84:87], v[120:123], a[68:71]\n"
  "ds_read_b64_tr_b16 v[8:9], v230 offset:0\n"
  "v_mov_b64 v[130:131], v[128:129]\n"
  "v_mfma_f32_16x16x32_f16 a[80:83], v[88:91], v[120:123], a[80:83]\n"
  "s_add_i32 m0, s47, 8192\n"
  "ds_read_b128 v[108:111], v232 offset:6656\n"
  "v_mov_b64 v[134:135], v[132:133]\n"
  "v_mfma_f32_16x16x32_f16 a[92:95], v[92:95], v[120:123], a[92:95]\n"
  "s_add_i32 s64, s50, 4202496\n"
  "buffer_load_dwordx4 v237, s[40:43], s64 offen sc1 lds\n"
  "v_mov_b64 v[138:139], v[136:137]\n"
  "v_mfma_f32_16x16x32_f16 a[96:99], v[48:51], v[96:99], a[96:99]\n"
  "s_waitcnt lgkmcnt(7)\n"
  "ds_read_b64_tr_b16 v[10:11], v230 offset:8192\n"
  "v_pk_add_f16 v120, v96, v100\n"
  "v_mfma_f32_16x16x32_bf16 v[140:143], v[124:127], v[176:179], 0\n"
  "s_waitcnt lgkmcnt(4)\n"
  "ds_read_b64_tr_b16 v[24:25], v230 offset:256\n"
  "v_pk_add_f16 v121, v97, v101\n"
  "v_mfma_f32_16x16x32_f16 a[108:111], v[52:55], v[96:99], a[108:111]\n"
  "s_add_i32 m0, s47, 12288\n"
  "v_pk_add_f16 v122, v98, v102\n"
  "v_pk_add_f16 v123, v99, v103\n"
  "v_mfma_f32_16x16x32_f16 a[120:123], v[56:59], v[96:99], a[120:123]\n"
  "s_add_i32 s64, s50, 4206592\n"
  "buffer_load_dwordx4 v237, s[40:43], s64 offen sc1 lds\n"
  "v_pk_add_f16 v32, v0, v16\n"
  "v_mfma_f32_16x16x32_f16 a[132:135], v[60:63], v[96:99], a[132:135]\n"
  "ds_read_b64_tr_b16 v[26:27], v230 offset:8448\n"
  "v_cvt_pkrtz_f16_f32 v124, v140, v141\n"
  "v_mfma_f32_16x16x32_bf16 v[144:147], v[128:131], v[176:179], 0\n"
  "ds_read_b64_tr_b16 v[12:13], v231 offset:0\n"
  "v_cvt_pkrtz_f16_f32 v125, v142, v143\n"
  "v_mfma_f32_16x16x32_f16 a[100:103], v[64:67], v[100:103], a[100:103]\n"
  "s_add_i32 m0, s47, 16384\n"
  "ds_read_b64_tr_b16 v[14:15], v231 offset:8192\n"
  "v_and_b32 v124, 0xfff8fff8, v124\n"
  "v_mfma_f32_16x16x32_f16 a[112:115], v[68:71], v[100:103], a[112:115]\n"
  "s_add_i32 s64, s50, 4194304\n"
  "buffer_load_dwordx4 v238, s[40:43], s64 offen sc1 lds\n"
  "v_and_b32 v125, 0xfff8fff8, v125\n"
  "v_mfma_f32_16x16x32_f16 a[124:127], v[72:75], v[100:103], a[124:127]\n"
  "ds_read_b128 v[96:99], v232 offset:0\n"
  "v_cvt_pkrtz_f16_f32 v126, v144, v145\n"
  "v_mfma_f32_16x16x32_bf16 v[148:151], v[132:135], v[176:179], 0\n"
  "ds_read_b128 v[112:115], v232 offset:8192\n"
  "v_cvt_pkrtz_f16_f32 v127, v146, v147\n"
  "v_mfma_f32_16x16x32_f16 a[136:139], v[76:79], v[100:103], a[136:139]\n"
  "s_add_i32 m0, s47, 20480\n"
  "ds_read_b64_tr_b16 v[28:29], v231 offset:256\n"
  "v_and_b32 v126, 0xfff8fff8, v126\n"
  "v_mfma_f32_16x16x32_f16 a[104:107], v[80:83], v[120:123], a[104:107]\n"
  "s_add_i32 s64, s50, 4096\n"
  "buffer_load_dwordx4 v238, s[40:43], s64 offen sc1 lds\n"
  "v_and_b32 v127, 0xfff8fff8, v127\n"
  "v_mfma_f32_16x16x32_f16 a[116:119], v[84:87], v[120:123], a[116:119]\n"
  "s_waitcnt lgkmcnt(8)\n"
  "ds_read_b128 v[100:103], v232 offset:512\n"
  "v_cvt_pkrtz_f16_f32 v128, v148, v149\n"
  "v_mfma_f32_16x16x32_bf16 v[152:155], v[136:139], v[176:179], 0\n"
  "s_waitcnt lgkmcnt(6)\n"
  "ds_read_b128 v[116:119], v232 offset:8704\n"
  "v_cvt_pkrtz_f16_f32 v129, v150, v151\n"
  "v_mfma_f32_16x16x32_f16 a[128:131], v[88:91], v[120:123], a[128:131]\n"
  "s_add_i32 m0, s48, 24576\n"
  "ds_read_b64_tr_b16 v[30:31], v231 offset:8448\n"
  "v_and_b32 v128, 0xfff8fff8, v128\n"
  "v_mfma_f32_16x16x32_f16 a[140:143], v[92:95], v[120:123], a[140:143]\n"
  "s_add_i32 s64, s50, 16384\n"
  "buffer_load_dwordx4 v238, s[40:43], s64 offen sc1 lds\n"
  "v_and_b32 v129, 0xfff8fff8, v129\n"
  "v_mfma_f32_16x16x32_f16 a[144:147], v[48:51], v[104:107], a[144:147]\n"
  "v_pk_add_f16 v120, v104, v108\n"
  "v_pk_add_f16 v121, v105, v109\n"
  "v_mfma_f32_16x16x32_f16 a[156:159], v[52:55], v[104:107], a[156:159]\n"
  "v_pk_add_f16 v122, v106, v110\n"
  "v_pk_add_f16 v123, v107, v111\n"
  "v_mfma_f32_16x16x32_f16 a[168:171], v[56:59], v[104:107], a[168:171]\n"
  "v_cvt_pkrtz_f16_f32 v130, v152, v153\n"
  "v_cvt_pkrtz_f16_f32 v131, v154, v155\n"
  "v_mfma_f32_16x16x32_f16 a[180:183], v[60:63], v[104:107], a[180:183]\n"
  "buffer_store_dwordx4 v[124:127], v242, s[40:43], s56 offen offset:256 sc1\n"
  "v_and_b32 v130, 0xfff8fff8, v130\n"
  "v_mfma_f32_16x16x32_f16 a[148:151], v[64:67], v[108:111], a[148:151]\n"
  "ds_read_b128 v[104:107], v232 offset:2048\n"
  "v_and_b32 v131, 0xfff8fff8, v131\n"
  "v_mfma_f32_16x16x32_f16 a[160:163], v[68:71], v[108:111], a[160:163]\n"
  "s_cmp_lt_u32 s57, 0x80000\n"
  "s_cselect_b32 s58, s57, 0xf0000000\n"
  "s_cselect_b32 s59, s59, 0xf0000000\n"
  "v_mfma_f32_16x16x32_f16 a[172:175], v[72:75], v[108:111], a[172:175]\n"
  "s_add_i32 m0, s47, 26624\n"
  "s_waitcnt lgkmcnt(1)\n"
  "v_pk_add_f16 v33, v1, v17\n"
  "v_mfma_f32_16x16x32_f16 a[184:187], v[76:79], v[108:111], a[184:187]\n"
  "s_add_i32 s64, s50, 4214784\n"
  "buffer_load_dwordx4 v238, s[40:43], s64 offen sc1 lds\n"
  "v_pk_add_f16 v34, v2, v18\n"
  "v_mfma_f32_16x16x32_f16 a[152:155], v[80:83], v[120:123], a[152:155]\n"
  "ds_read_b128 v[108:111], v232 offset:2560\n"
  "v_pk_add_f16 v35, v3, v19\n"
  "v_mfma_f32_16x16x32_f16 a[164:167], v[84:87], v[120:123], a[164:167]\n"
  "v_pk_add_f16 v36, v4, v20\n"
  "v_pk_add_f16 v37, v5, v21\n"
  "v_mfma_f32_16x16x32_f16 a[176:179], v[88:91], v[120:123], a[176:179]\n"
  "s_add_i32 m0, s47, 30720\n"
  "v_pk_add_f16 v38, v6, v22\n"
  "v_pk_add_f16 v39, v7, v23\n"
  "v_mfma_f32_16x16x32_f16 a[188:191], v[92:95], v[120:123], a[188:191]\n"
  "s_add_i32 s64, s50, 32768\n"
  "buffer_load_dwordx4 v238, s[40:43], s64 offen sc1 lds\n"
  "v_pk_add_f16 v40, v8, v24\n"
  "v_mfma_f32_16x16x32_f16 a[192:195], v[48:51], v[112:115], a[192:195]\n"
  "s_add_i32 s47, s44, s46\n"
  "v_pk_add_f16 v120, v112, v116\n"
  "v_pk_add_f16 v121, v113, v117\n"
  "v_mfma_f32_16x16x32_f16 a[204:207], v[52:55], v[112:115], a[204:207]\n"
  "s_add_i32 s48, s47, s45\n"
  "v_pk_add_f16 v122, v114, v118\n"
  "v_pk_add_f16 v123, v115, v119\n"
  "v_mfma_f32_16x16x32_f16 a[216:219], v[56:59], v[112:115], a[216:219]\n"
  "s_add_i32 s46, s46, s49\n"
  "v_pk_add_f16 v41, v9, v25\n"
  "v_pk_add_f16 v42, v10, v26\n"
  "v_mfma_f32_16x16x32_f16 a[228:231], v[60:63], v[112:115], a[228:231]\n"
  "s_add_i32 s50, s50, 4194304\n"
  "buffer_store_dwordx4 v[128:131], v242, s[40:43], s56 offen offset:384 sc1\n"
  "v_pk_add_f16 v43, v11, v27\n"
  "v_mfma_f32_16x16x32_f16 a[196:199], v[64:67], v[116:119], a[196:199]\n"
  "s_add_i32 s55, s55, 8404992\n"
  "v_pk_add_f16 v44, v12, v28\n"
  "v_pk_add_f16 v45, v13, v29\n"
  "v_mfma_f32_16x16x32_f16 a[208:211], v[68:71], v[116:119], a[208:211]\n"
  "s_and_b32 s56, s55, 0xff80c000\n"
  "v_pk_add_f16 v46, v14, v30\n"
  "v_pk_add_f16 v47, v15, v31\n"
  "v_mfma_f32_16x16x32_f16 a[220:223], v[72:75], v[116:119], a[220:223]\n"
  "v_add_u32 v230, s49, v230\n"
  "v_add_u32 v231, s49, v231\n"
  "v_mfma_f32_16x16x32_f16 a[232:235], v[76:79], v[116:119], a[232:235]\n"
  "v_add_u32 v232, s49, v232\n"
  "v_add_u32 v236, 4096, v236\n"
  "v_mfma_f32_16x16x32_f16 a[200:203], v[80:83], v[120:123], a[200:203]\n"
  "s_waitcnt vmcnt(13) lgkmcnt(0)\n"
  "v_mfma_f32_16x16x32_f16 a[212:215], v[84:87], v[120:123], a[212:215]\n"
  "s_cmp_gt_u32 s62, 88\n"
  "s_cbranch_scc1 .Lpace_po_None_%=\n"
  ".Lpaced_po_None_%=:\n"
  "v_mfma_f32_16x16x32_f16 a[224:227], v[88:91], v[120:123], a[224:227]\n"
  "s_barrier\n"
  "v_mfma_f32_16x16x32_f16 a[236:239], v[92:95], v[120:123], a[236:239]\n"
  "s_sub_i32 s63, s63, 1\n"
  "s_cmp_lg_u32 s63, 0\n"
  "s_cbranch_scc1 .LloopP_%=\n"
  ".Lcf_t:\n"
  "s_cmp_eq_u32 s85, 0\n"
  "s_cbranch_scc1 .Lnt_%=\n"
  "s_getpc_b64 s[86:87]\n"
  "s_mov_b32 m0, s85\n"
  "s_nop 0\n"
  "global_load_lds_dword v251, s[86:87]\n"
  ".Lnt_%=:\n"
  "v_mfma_f32_16x16x32_f16 a[0:3], v[0:3], v[96:99], a[0:3]\n"
  "buffer_load_dword v247, v246, s[40:43], s54 offen sc1\n"
  "v_pk_add_f16 v120, v96, v100\n"
  "v_mfma_f32_16x16x32_f16 a[12:15], v[4:7], v[96:99], a[12:15]\n"
  "v_pk_add_f16 v121, v97, v101\n"
  "v_pk_add_f16 v122, v98, v102\n"
  "v_mfma_f32_16x16x32_f16 a[24:27], v[8:11], v[96:99], a[24:27]\n"
  "ds_read_b64_tr_b16 v[48:49], v228 offset:0\n"
  "v_pk_add_f16 v123, v99, v103\n"
  "v_mfma_f32_16x16x32_f16 a[36:39], v[12:15], v[96:99], a[36:39]\n"
  "s_add_i32 m0, s47, 0\n"
  "ds_read_b64_tr_b16 v[50:51], v228 offset:8192\n"
  "v_accvgpr_read_b32 v124, a0\n"
  "v_mfma_f32_16x16x32_f16 a[4:7], v[16:19], v[100:103], a[4:7]\n"
  "s_add_i32 s64, s50, 4194304\n"
  "buffer_load_dwordx4 v237, s[40:43], s64 offen sc1 lds\n"
  "v_accvgpr_read_b32 v125, a1\n"
  "v_mfma_f32_16x16x32_f16 a[16:19], v[20:23], v[100:103], a[16:19]\n"
  "s_cmp_eq_u32 s46, 73728\n"
  "s_cselect_b32 s49, s68, s69\n"
  "ds_read_b128 v[96:99], v232 offset:4096\n"
  "v_mfma_f32_16x16x32_f16 a[28:31], v[24:27], v[100:103], a[28:31]\n"
  "ds_read_b64_tr_b16 v[64:65], v228 offset:256\n"
  "v_accvgpr_read_b32 v126, a2\n"
  "v_mfma_f32_16x16x32_f16 a[40:43], v[28:31], v[100:103], a[40:43]\n"
  "s_add_i32 m0, s47, 4096\n"
  "ds_read_b128 v[100:103], v232 offset:4608\n"
  "v_accvgpr_read_b32 v127, a3\n"
  "v_mfma_f32_16x16x32_f16 a[8:11], v[32:35], v[120:123], a[8:11]\n"
  "s_add_i32 s64, s50, 4198400\n"
  "buffer_load_dwordx4 v237, s[40:43], s64 offen sc1 lds\n"
  "v_accvgpr_read_b32 v128, a4\n"
  "v_mfma_f32_16x16x32_f16 a[20:23], v[36:39], v[120:123], a[20:23]\n"
  "ds_read_b64_tr_b16 v[66:67], v228 offset:8448\n"
  "v_accvgpr_read_b32 v129, a5\n"
  "v_mfma_f32_16x16x32_f16 a[32:35], v[40:43], v[120:123], a[32:35]\n"
  "ds_read_b64_tr_b16 v[52:53], v229 offset:0\n"
  "v_accvgpr_read_b32 v130, a6\n"
  "v_mfma_f32_16x16x32_f16 a[44:47], v[44:47], v[120:123], a[44:47]\n"
  "s_add_i32 m0, s47, 8192\n"
  "ds_read_b64_tr_b16 v[54:55], v229 offset:8192\n"
  "v_accvgpr_read_b32 v131, a7\n"
  "v_mfma_f32_16x16x32_f16 a[48:51], v[0:3], v[104:107], a[48:51]\n"
  "s_add_i32 s64, s50, 4202496\n"
  "buffer_load_dwordx4 v237, s[40:43], s64 offen sc1 lds\n"
  "v_pk_add_f16 v120, v104, v108\n"
  "v_mfma_f32_16x16x32_f16 a[60:63], v[4:7], v[104:107], a[60:63]\n"
  "v_pk_add_f16 v121, v105, v109\n"
  "v_pk_add_f16 v122, v106, v110\n"
  "v_mfma_f32_16x16x32_f16 a[72:75], v[8:11], v[104:107], a[72:75]\n"
  "ds_read_b64_tr_b16 v[68:69], v229 offset:256\n"
  "v_pk_add_f16 v123, v107, v111\n"
  "v_mfma_f32_16x16x32_f16 a[84:87], v[12:15], v[104:107], a[84:87]\n"
  "s_add_i32 m0, s47, 12288\n"
  "ds_read_b64_tr_b16 v[70:71], v229 offset:8448\n"
  "v_accvgpr_read_b32 v132, a8\n"
  "v_mfma_f32_16x16x32_f16 a[52:55], v[16:19], v[108:111], a[52:55]\n"
  "s_add_i32 s64, s50, 4206592\n"
  "buffer_load_dwordx4 v237, s[40:43], s64 offen sc1 lds\n"
  "v_accvgpr_read_b32 v133, a9\n"
  "v_mfma_f32_16x16x32_f16 a[64:67], v[20:23], v[108:111], a[64:67]\n"
  "s_waitcnt lgkmcnt(4)\n"
  "ds_read_b128 v[104:107], v232 offset:6144\n"
  "v_accvgpr_read_b32 v134, a10\n"
  "v_mfma_f32_16x16x32_f16 a[76:79], v[24:27], v[108:111], a[76:79]\n"
  "ds_read_b64_tr_b16 v[56:57], v230 offset:0\n"
  "v_accvgpr_read_b32 v135, a11\n"
  "v_mfma_f32_16x16x32_f16 a[88:91], v[28:31], v[108:111], a[88:91]\n"
  "s_add_i32 m0, s47, 16384\n"
  "ds_read_b128 v[108:111], v232 offset:6656\n"
  "v_accvgpr_read_b32 v136, a12\n"
  "v_mfma_f32_16x16x32_f16 a[56:59], v[32:35], v[120:123], a[56:59]\n"
  "s_add_i32 s64, s50, 4194304\n"
  "buffer_load_dwordx4 v238, s[40:43], s64 offen sc1 lds\n"
  "v_accvgpr_read_b32 v137, a13\n"
  "v_mfma_f32_16x16x32_f16 a[68:71], v[36:39], v[120:123], a[68:71]\n"
  "ds_read_b64_tr_b16 v[58:59], v230 offset:8192\n"
  "v_accvgpr_read_b32 v138, a14\n"
  "v_mfma_f32_16x16x32_f16 a[80:83], v[40:43], v[120:123], a[80:83]\n"
  "s_waitcnt lgkmcnt(4)\n"
  "ds_read_b64_tr_b16 v[72:73], v230 offset:256\n"
  "v_accvgpr_read_b32 v139, a15\n"
  "v_mfma_f32_16x16x32_f16 a[92:95], v[44:47], v[120:123], a[92:95]\n"
  "s_add_i32 m0, s47, 20480\n"
  "ds_read_b64_tr_b16 v[74:75], v230 offset:8448\n"
  "v_accvgpr_read_b32 v140, a16\n"
  "v_mfma_f32_16x16x32_f16 a[96:99], v[0:3], v[96:99], a[96:99]\n"
  "s_add_i32 s64, s50, 4096\n"
  "buffer_load_dwordx4 v238, s[40:43], s64 offen sc1 lds\n"
  "v_pk_add_f16 v120, v96, v100\n"
  "v_mfma_f32_16x16x32_f16 a[108:111], v[4:7], v[96:99], a[108:111]\n"
  "v_pk_add_f16 v121, v97, v101\n"
  "v_pk_add_f16 v122, v98, v102\n"
  "v_mfma_f32_16x16x32_f16 a[120:123], v[8:11], v[96:99], a[120:123]\n"
  "ds_read_b64_tr_b16 v[60:61], v231 offset:0\n"
  "v_pk_add_f16 v123, v99, v103\n"
  "v_mfma_f32_16x16x32_f16 a[132:135], v[12:15], v[96:99], a[132:135]\n"
  "s_add_i32 m0, s48, 24576\n"
  "ds_read_b64_tr_b16 v[62:63], v231 offset:8192\n"
  "v_accvgpr_read_b32 v141, a17\n"
  "v_mfma_f32_16x16x32_f16 a[100:103], v[16:19], v[100:103], a[100:103]\n"
  "s_add_i32 s64, s50, 16384\n"
  "buffer_load_dwordx4 v238, s[40:43], s64 offen sc1 lds\n"
  "v_accvgpr_read_b32 v142, a18\n"
  "v_mfma_f32_16x16x32_f16 a[112:115], v[20:23], v[100:103], a[112:115]\n"
  "ds_read_b128 v[96:99], v232 offset:0\n"
  "v_accvgpr_read_b32 v143, a19\n"
  "v_mfma_f32_16x16x32_f16 a[124:127], v[24:27], v[100:103], a[124:127]\n"
  "s_waitcnt lgkmcnt(3)\n"
  "ds_read_b128 v[112:115], v232 offset:8192\n"
  "v_accvgpr_read_b32 v144, a20\n"
  "v_mfma_f32_16x16x32_f16 a[136:139], v[28:31], v[100:103], a[136:139]\n"
  "s_add_i32 m0, s47, 26624\n"
  "ds_read_b64_tr_b16 v[76:77], v231 offset:256\n"
  "v_accvgpr_read_b32 v145, a21\n"
  "v_mfma_f32_16x16x32_f16 a[104:107], v[32:35], v[120:123], a[104:107]\n"
  "s_add_i32 s64, s50, 4214784\n"
  "buffer_load_dwordx4 v238, s[40:43], s64 offen sc1 lds\n"
  "v_accvgpr_read_b32 v146, a22\n"
  "v_mfma_f32_16x16x32_f16 a[116:119], v[36:39], v[120:123], a[116:119]\n"
  "ds_read_b128 v[100:103], v232 offset:512\n"
  "v_accvgpr_read_b32 v147, a23\n"
  "v_mfma_f32_16x16x32_f16 a[128:131], v[40:43], v[120:123], a[128:131]\n"
  "ds_read_b128 v[116:119], v232 offset:8704\n"
  "v_accvgpr_read_b32 v148, a24\n"
  "v_mfma_f32_16x16x32_f16 a[140:143], v[44:47], v[120:123], a[140:143]\n"
  "s_add_i32 m0, s47, 30720\n"
  "ds_read_b64_tr_b16 v[78:79], v231 offset:8448\n"
  "v_accvgpr_read_b32 v149, a25\n"
  "v_mfma_f32_16x16x32_f16 a[144:147], v[0:3], v[104:107], a[144:147]\n"
  "s_add_i32 s64, s50, 32768\n"
  "buffer_load_dwordx4 v238, s[40:43], s64 offen sc1 lds\n"
  "v_pk_add_f16 v120, v104, v108\n"
  "v_mfma_f32_16x16x32_f16 a[156:159], v[4:7], v[104:107], a[156:159]\n"
  "v_pk_add_f16 v121, v105, v109\n"
  "v_pk_add_f16 v122, v106, v110\n"
  "v_mfma_f32_16x16x32_f16 a[168:171], v[8:11], v[104:107], a[168:171]\n"
  "v_pk_add_f16 v123, v107, v111\n"
  "v_accvgpr_read_b32 v150, a26\n"
  "v_mfma_f32_16x16x32_f16 a[180:183], v[12:15], v[104:107], a[180:183]\n"
  "ds_read_b128 v[104:107], v232 offset:2048\n"
  "v_accvgpr_read_b32 v151, a27\n"
  "v_mfma_f32_16x16x32_f16 a[148:151], v[16:19], v[108:111], a[148:151]\n"
  "s_add_i32 s47, s44, s46\n"
  "v_accvgpr_read_b32 v152, a28\n"
  "v_accvgpr_read_b32 v153, a29\n"
  "v_mfma_f32_16x16x32_f16 a[160:163], v[20:23], v[108:111], a[160:163]\n"
  "s_add_i32 s48, s47, s45\n"
  "v_accvgpr_read_b32 v154, a30\n"
  "v_accvgpr_read_b32 v155, a31\n"
  "v_mfma_f32_16x16x32_f16 a[172:175], v[24:27], v[108:111], a[172:175]\n"
  "s_waitcnt lgkmcnt(1)\n"
  "v_accvgpr_read_b32 v156, a32\n"
  "v_accvgpr_read_b32 v157, a33\n"
  "v_mfma_f32_16x16x32_f16 a[184:187], v[28:31], v[108:111], a[184:187]\n"
  "s_add_i32 s46, s46, s49\n"
  "ds_read_b128 v[108:111], v232 offset:2560\n"
  "v_accvgpr_read_b32 v158, a34\n"
  "v_mfma_f32_16x16x32_f16 a[152:155], v[32:35], v[120:123], a[152:155]\n"
  "s_add_i32 s50, s50, 4194304\n"
  "v_accvgpr_read_b32 v159, a35\n"
  "v_accvgpr_read_b32 v160, a36\n"
  "v_mfma_f32_16x16x32_f16 a[164:167], v[36:39], v[120:123], a[164:167]\n"
  "v_accvgpr_read_b32 v161, a37\n"
  "v_accvgpr_read_b32 v162, a38\n"
  "v_mfma_f32_16x16x32_f16 a[176:179], v[40:43], v[120:123], a[176:179]\n"
  "v_accvgpr_read_b32 v163, a39\n"
  "v_accvgpr_read_b32 v164, a40\n"
  "v_mfma_f32_16x16x32_f16 a[188:191], v[44:47], v[120:123], a[188:191]\n"
  "v_accvgpr_read_b32 v165, a41\n"
  "v_accvgpr_read_b32 v166, a42\n"
  "v_mfma_f32_16x16x32_f16 a[192:195], v[0:3], v[112:115], a[192:195]\n"
  "v_pk_add_f16 v120, v112, v116\n"
  "v_pk_add_f16 v121, v113, v117\n"
  "v_mfma_f32_16x16x32_f16 a[204:207], v[4:7], v[112:115], a[204:207]\n"
  "v_pk_add_f16 v122, v114, v118\n"
  "v_pk_add_f16 v123, v115, v119\n"
  "v_mfma_f32_16x16x32_f16 a[216:219], v[8:11], v[112:115], a[216:219]\n"
  "v_accvgpr_read_b32 v167, a43\n"
  "v_accvgpr_read_b32 v168, a44\n"
  "v_mfma_f32_16x16x32_f16 a[228:231], v[12:15], v[112:115], a[228:231]\n"
  "v_accvgpr_read_b32 v169, a45\n"
  "v_accvgpr_read_b32 v170, a46\n"
  "v_mfma_f32_16x16x32_f16 a[196:199], v[16:19], v[116:119], a[196:199]\n"
  "v_accvgpr_read_b32 v171, a47\n"
  "v_pk_add_f16 v80, v48, v64\n"
  "v_mfma_f32_16x16x32_f16 a[208:211], v[20:23], v[116:119], a[208:211]\n"
  "v_pk_add_f16 v81, v49, v65\n"
  "v_pk_add_f16 v82, v50, v66\n"
  "v_mfma_f32_16x16x32_f16 a[220:223], v[24:27], v[116:119], a[220:223]\n"
  "v_pk_add_f16 v83, v51, v67\n"
  "v_pk_add_f16 v84, v52, v68\n"
  "v_mfma_f32_16x16x32_f16 a[232:235], v[28:31], v[116:119], a[232:235]\n"
  "v_pk_add_f16 v85, v53, v69\n"
  "v_pk_add_f16 v86, v54, v70\n"
  "v_mfma_f32_16x16x32_f16 a[200:203], v[32:35], v[120:123], a[200:203]\n"
  "v_pk_add_f16 v87, v55, v71\n"
  "v_pk_add_f16 v88, v56, v72\n"
  "s_waitcnt vmcnt(9) lgkmcnt(0)\n"
  "v_mfma_f32_16x16x32_f16 a[212:215], v[36:39], v[120:123], a[212:215]\n"
  "v_cmp_eq_u32 vcc, 0, v247\n"
  "s_cbranch_vccnz .Lspin_s22_None_%=\n"
  ".Lspun_s22_None_%=:\n"
  "v_mfma_f32_16x16x32_f16 a[224:227], v[40:43], v[120:123], a[224:227]\n"
  "v_pk_add_f16 v89, v57, v73\n"
  "v_pk_add_f16 v90, v58, v74\n"
  "s_barrier\n"
  "v_mfma_f32_16x16x32_f16 a[236:239], v[44:47], v[120:123], a[236:239]\n"
  "v_pk_add_f16 v91, v59, v75\n"
  "v_pk_add_f16 v92, v60, v76\n"
  "v_pk_add_f16 v93, v61, v77\n"
  "v_pk_add_f16 v94, v62, v78\n"
  "v_pk_add_f16 v95, v63, v79\n"
  "v_add_u32 v228, s49, v228\n"
  "v_add_u32 v229, s49, v229\n"
  "v_add_u32 v230, s49, v230\n"
  "v_add_u32 v231, s49, v231\n"
  "v_add_u32 v232, s49, v232\n"
  "v_mfma_f32_16x16x32_f16 v[124:127], v[48:51], v[96:99], v[124:127]\n"
  "v_pk_add_f16 v120, v96, v100\n"
  "v_pk_add_f16 v121, v97, v101\n"
  "v_mfma_f32_16x16x32_f16 v[136:139], v[52:55], v[96:99], v[136:139]\n"
  "v_pk_add_f16 v122, v98, v102\n"
  "v_pk_add_f16 v123, v99, v103\n"
  "v_mfma_f32_16x16x32_f16 v[148:151], v[56:59], v[96:99], v[148:151]\n"
  "s_and_b32 s65, s51, 0xffffe300\n"
  "ds_read_b64_tr_b16 v[0:1], v228 offset:0\n"
  "ds_read_b64_tr_b16 v[2:3], v228 offset:8192\n"
  "v_mfma_f32_16x16x32_f16 v[160:163], v[60:63], v[96:99], v[160:163]\n"
  "s_add_i32 m0, s47, 0\n"
  "ds_read_b128 v[96:99], v232 offset:4096\n"
  "ds_read_b64_tr_b16 v[16:17], v228 offset:256\n"
  "v_mfma_f32_16x16x32_f16 v[128:131], v[64:67], v[100:103], v[128:131]\n"
  "s_add_i32 s64, s50, 4194304\n"
  "buffer_load_dwordx4 v237, s[40:43], s64 offen sc1 lds\n"
  "ds_read_b64_tr_b16 v[18:19], v228 offset:8448\n"
  "v_mfma_f32_16x16x32_f16 v[140:143], v[68:71], v[100:103], v[140:143]\n"
  "s_cmp_eq_u32 s46, 73728\n"
  "s_cselect_b32 s49, s68, s69\n"
  "ds_read_b64_tr_b16 v[4:5], v229 offset:0\n"
  "v_mfma_f32_16x16x32_f16 v[152:155], v[72:75], v[100:103], v[152:155]\n"
  "ds_read_b64_tr_b16 v[6:7], v229 offset:8192\n"
  "v_add_u32 v228, s49, v228\n"
  "v_mfma_f32_16x16x32_f16 v[164:167], v[76:79], v[100:103], v[164:167]\n"
  "ds_read_b128 v[100:103], v232 offset:4608\n"
  "ds_read_b64_tr_b16 v[20:21], v229 offset:256\n"
  "v_mfma_f32_16x16x32_f16 v[132:135], v[80:83], v[120:123], v[132:135]\n"
  "buffer_store_dword v232, v245, s[40:43], s65 offen sc1\n"
  "ds_read_b64_tr_b16 v[22:23], v229 offset:8448\n"
  "v_mfma_f32_16x16x32_f16 v[144:147], v[84:87], v[120:123], v[144:147]\n"
  "ds_read_b64_tr_b16 v[8:9], v230 offset:0\n"
  "v_add_u32 v229, s49, v229\n"
  "v_mfma_f32_16x16x32_f16 v[156:159], v[88:91], v[120:123], v[156:159]\n"
  "s_add_i32 s53, s53, 8192\n"
  "s_lshr_b32 s52, s52, 1\n"
  "ds_read_b64_tr_b16 v[10:11], v230 offset:8192\n"
  "v_mfma_f32_16x16x32_f16 v[168:171], v[92:95], v[120:123], v[168:171]\n"
  "s_add_i32 m0, s47, 4096\n"
  "s_waitcnt lgkmcnt(7)\n"
  "ds_read_b64_tr_b16 v[24:25], v230 offset:256\n"
  "v_mfma_f32_16x16x32_f16 a[48:51], v[48:51], v[104:107], a[48:51]\n"
  "s_add_i32 s64, s50, 4198400\n"
  "buffer_load_dwordx4 v237, s[40:43], s64 offen sc1 lds\n"
  "v_pk_add_f16 v120, v104, v108\n"
  "v_mfma_f32_16x16x32_f16 a[60:63], v[52:55], v[104:107], a[60:63]\n"
  "s_add_i32 s51, s51, 8448\n"
  "v_pk_add_f16 v121, v105, v109\n"
  "v_pk_add_f16 v122, v106, v110\n"
  "v_mfma_f32_16x16x32_f16 a[72:75], v[56:59], v[104:107], a[72:75]\n"
  "ds_read_b64_tr_b16 v[26:27], v230 offset:8448\n"
  "v_pk_add_f16 v123, v107, v111\n"
  "v_mfma_f32_16x16x32_f16 a[84:87], v[60:63], v[104:107], a[84:87]\n"
  "s_add_i32 m0, s47, 8192\n"
  "ds_read_b128 v[104:107], v232 offset:6144\n"
  "v_accvgpr_read_b32 v180, a48\n"
  "v_mfma_f32_16x16x32_f16 a[52:55], v[64:67], v[108:111], a[52:55]\n"
  "s_add_i32 s64, s50, 4202496\n"
  "buffer_load_dwordx4 v237, s[40:43], s64 offen sc1 lds\n"
  "v_accvgpr_read_b32 v181, a49\n"
  "v_mfma_f32_16x16x32_f16 a[64:67], v[68:71], v[108:111], a[64:67]\n"
  "s_bitcmp1_b32 s52, 0\n"
  "s_cselect_b32 s54, s53, 134217728\n"
  "ds_read_b64_tr_b16 v[12:13], v231 offset:0\n"
  "v_mfma_f32_16x16x32_f16 a[76:79], v[72:75], v[108:111], a[76:79]\n"
  "s_waitcnt lgkmcnt(6)\n"
  "ds_read_b64_tr_b16 v[14:15], v231 offset:8192\n"
  "v_accvgpr_read_b32 v182, a50\n"
  "v_mfma_f32_16x16x32_f16 a[88:91], v[76:79], v[108:111], a[88:91]\n"
  "s_add_i32 m0, s47, 12288\n"
  "ds_read_b128 v[108:111], v232 offset:6656\n"
  "v_accvgpr_read_b32 v183, a51\n"
  "v_mfma_f32_16x16x32_f16 a[56:59], v[80:83], v[120:123], a[56:59]\n"
  "s_add_i32 s64, s50, 4206592\n"
  "buffer_load_dwordx4 v237, s[40:43], s64 offen sc1 lds\n"
  "v_accvgpr_read_b32 v184, a52\n"
  "v_mfma_f32_16x16x32_f16 a[68:71], v[84:87], v[120:123], a[68:71]\n"
  "ds_read_b128 v[112:115], v232 offset:8192\n"
  "v_accvgpr_read_b32 v185, a53\n"
  "v_mfma_f32_16x16x32_f16 a[80:83], v[88:91], v[120:123], a[80:83]\n"
  "s_waitcnt lgkmcnt(5)\n"
  "ds_read_b64_tr_b16 v[28:29], v231 offset:256\n"
  "v_accvgpr_read_b32 v186, a54\n"
  "v_mfma_f32_16x16x32_f16 a[92:95], v[92:95], v[120:123], a[92:95]\n"
  "s_add_i32 m0, s47, 16384\n"
  "ds_read_b128 v[116:119], v232 offset:8704\n"
  "v_accvgpr_read_b32 v187, a55\n"
  "v_mfma_f32_16x16x32_f16 a[96:99], v[48:51], v[96:99], a[96:99]\n"
  "s_add_i32 s64, s50, 4194304\n"
  "buffer_load_dwordx4 v238, s[40:43], s64 offen sc1 lds\n"
  "v_pk_add_f16 v120, v96, v100\n"
  "v_mfma_f32_16x16x32_f16 a[108:111], v[52:55], v[96:99], a[108:111]\n"
  "v_pk_add_f16 v121, v97, v101\n"
  "v_pk_add_f16 v122, v98, v102\n"
  "v_mfma_f32_16x16x32_f16 a[120:123], v[56:59], v[96:99], a[120:123]\n"
  "ds_read_b64_tr_b16 v[30:31], v231 offset:8448\n"
  "v_pk_add_f16 v123, v99, v103\n"
  "v_mfma_f32_16x16x32_f16 a[132:135], v[60:63], v[96:99], a[132:135]\n"
  "s_add_i32 m0, s47, 20480\n"
  "ds_read_b128 v[96:99], v232 offset:0\n"
  "v_accvgpr_read_b32 v188, a56\n"
  "v_mfma_f32_16x16x32_f16 a[100:103], v[64:67], v[100:103], a[100:103]\n"
  "s_add_i32 s64, s50, 4096\n"
  "buffer_load_dwordx4 v238, s[40:43], s64 offen sc1 lds\n"
  "v_accvgpr_read_b32 v189, a57\n"
  "v_mfma_f32_16x16x32_f16 a[112:115], v[68:71], v[100:103], a[112:115]\n"
  "v_accvgpr_read_b32 v190, a58\n"
  "v_accvgpr_read_b32 v191, a59\n"
  "v_mfma_f32_16x16x32_f16 a[124:127], v[72:75], v[100:103], a[124:127]\n"
  "v_accvgpr_read_b32 v192, a60\n"
  "v_accvgpr_read_b32 v193, a61\n"
  "v_mfma_f32_16x16x32_f16 a[136:139], v[76:79], v[100:103], a[136:139]\n"
  "s_add_i32 m0, s48, 24576\n"
  "ds_read_b128 v[100:103], v232 offset:512\n"
  "v_accvgpr_read_b32 v194, a62\n"
  "v_mfma_f32_16x16x32_f16 a[104:107], v[80:83], v[120:123], a[104:107]\n"
  "s_add_i32 s64, s50, 16384\n"
  "buffer_load_dwordx4 v238, s[40:43], s64 offen sc1 lds\n"
  "v_accvgpr_read_b32 v195, a63\n"
  "v_mfma_f32_16x16x32_f16 a[116:119], v[84:87], v[120:123], a[116:119]\n"
  "s_waitcnt lgkmcnt(6)\n"
  "v_accvgpr_read_b32 v196, a64\n"
  "v_accvgpr_read_b32 v197, a65\n"
  "v_mfma_f32_16x16x32_f16 a[128:131], v[88:91], v[120:123], a[128:131]\n"
  "s_waitcnt lgkmcnt(2)\n"
  "v_accvgpr_read_b32 v198, a66\n"
  "v_accvgpr_read_b32 v199, a67\n"
  "v_mfma_f32_16x16x32_f16 a[140:143], v[92:95], v[120:123], a[140:143]\n"
  "s_add_i32 m0, s47, 26624\n"
  "v_accvgpr_read_b32 v200, a68\n"
  "v_accvgpr_read_b32 v201, a69\n"
  "v_mfma_f32_16x16x32_f16 a[144:147], v[48:51], v[104:107], a[144:147]\n"
  "s_add_i32 s64, s50, 4214784\n"
  "buffer_load_dwordx4 v238, s[40:43], s64 offen sc1 lds\n"
  "v_pk_add_f16 v120, v104, v108\n"
  "v_mfma_f32_16x16x32_f16 a[156:159], v[52:55], v[104:107], a[156:159]\n"
  "v_pk_add_f16 v121, v105, v109\n"
  "v_pk_add_f16 v122, v106, v110\n"
  "v_mfma_f32_16x16x32_f16 a[168:171], v[56:59], v[104:107], a[168:171]\n"
  "v_pk_add_f16 v123, v107, v111\n"
  "v_accvgpr_read_b32 v202, a70\n"
  "v_mfma_f32_16x16x32_f16 a[180:183], v[60:63], v[104:107], a[180:183]\n"
  "s_add_i32 m0, s47, 30720\n"
  "ds_read_b128 v[104:107], v232 offset:2048\n"
  "v_accvgpr_read_b32 v203, a71\n"
  "v_mfma_f32_16x16x32_f16 a[148:151], v[64:67], v[108:111], a[148:151]\n"
  "s_add_i32 s64, s50, 32768\n"
  "buffer_load_dwordx4 v238, s[40:43], s64 offen sc1 lds\n"
  "v_accvgpr_read_b32 v204, a72\n"
  "v_mfma_f32_16x16x32_f16 a[160:163], v[68:71], v[108:111], a[160:163]\n"
  "s_add_i32 s47, s44, s46\n"
  "v_accvgpr_read_b32 v205, a73\n"
  "v_accvgpr_read_b32 v206, a74\n"
  "v_mfma_f32_16x16x32_f16 a[172:175], v[72:75], v[108:111], a[172:175]\n"
  "s_add_i32 s48, s47, s45\n"
  "v_accvgpr_read_b32 v207, a75\n"
  "v_accvgpr_read_b32 v208, a76\n"
  "v_mfma_f32_16x16x32_f16 a[184:187], v[76:79], v[108:111], a[184:187]\n"
  "s_add_i32 s46, s46, s49\n"
  "ds_read_b128 v[108:111], v232 offset:2560\n"
  "v_accvgpr_read_b32 v209, a77\n"
  "v_mfma_f32_16x16x32_f16 a[152:155], v[80:83], v[120:123], a[152:155]\n"
  "s_add_i32 s50, s50, 4194304\n"
  "v_accvgpr_read_b32 v210, a78\n"
  "v_accvgpr_read_b32 v211, a79\n"
  "v_mfma_f32_16x16x32_f16 a[164:167], v[84:87], v[120:123], a[164:167]\n"
  "v_accvgpr_read_b32 v212, a80\n"
  "v_accvgpr_read_b32 v213, a81\n"
  "v_mfma_f32_16x16x32_f16 a[176:179], v[88:91], v[120:123], a[176:179]\n"
  "v_accvgpr_read_b32 v214, a82\n"
  "v_accvgpr_read_b32 v215, a83\n"
  "v_mfma_f32_16x16x32_f16 a[188:191], v[92:95], v[120:123], a[188:191]\n"
  "v_accvgpr_read_b32 v216, a84\n"
  "v_accvgpr_read_b32 v217, a85\n"
  "v_mfma_f32_16x16x32_f16 a[192:195], v[48:51], v[112:115], a[192:195]\n"
  "v_pk_add_f16 v120, v112, v116\n"
  "v_pk_add_f16 v121, v113, v117\n"
  "v_mfma_f32_16x16x32_f16 a[204:207], v[52:55], v[112:115], a[204:207]\n"
  "v_pk_add_f16 v122, v114, v118\n"
  "v_pk_add_f16 v123, v115, v119\n"
  "v_mfma_f32_16x16x32_f16 a[216:219], v[56:59], v[112:115], a[216:219]\n"
  "v_accvgpr_read_b32 v218, a86\n"
  "v_accvgpr_read_b32 v219, a87\n"
  "v_mfma_f32_16x16x32_f16 a[228:231], v[60:63], v[112:115], a[228:231]\n"
  "v_accvgpr_read_b32 v220, a88\n"
  "v_accvgpr_read_b32 v221, a89\n"
  "v_mfma_f32_16x16x32_f16 a[196:199], v[64:67], v[116:119], a[196:199]\n"
  "v_accvgpr_read_b32 v222, a90\n"
  "v_accvgpr_read_b32 v223, a91\n"
  "v_mfma_f32_16x16x32_f16 a[208:211], v[68:71], v[116:119], a[208:211]\n"
  "v_accvgpr_read_b32 v224, a92\n"
  "v_accvgpr_read_b32 v225, a93\n"
  "v_mfma_f32_16x16x32_f16 a[220:223], v[72:75], v[116:119], a[220:223]\n"
  "v_accvgpr_read_b32 v226, a94\n"
  "v_accvgpr_read_b32 v227, a95\n"
  "v_mfma_f32_16x16x32_f16 a[232:235], v[76:79], v[116:119], a[232:235]\n"
  "v_pk_add_f16 v32, v0, v16\n"
  "v_pk_add_f16 v33, v1, v17\n"
  "v_mfma_f32_16x16x32_f16 a[200:203], v[80:83], v[120:123], a[200:203]\n"
  "v_pk_add_f16 v34, v2, v18\n"
  "v_pk_add_f16 v35, v3, v19\n"
  "s_waitcnt vmcnt(10) lgkmcnt(0)\n"
  "v_mfma_f32_16x16x32_f16 a[212:215], v[84:87], v[120:123], a[212:215]\n"
  "v_pk_add_f16 v36, v4, v20\n"
  "v_pk_add_f16 v37, v5, v21\n"
  "v_mfma_f32_16x16x32_f16 a[224:227], v[88:91], v[120:123], a[224:227]\n"
  "v_pk_add_f16 v38, v6, v22\n"
  "v_pk_add_f16 v39, v7, v23\n"
  "s_barrier\n"
  "v_mfma_f32_16x16x32_f16 a[236:239], v[92:95], v[120:123], a[236:239]\n"
  "v_pk_add_f16 v40, v8, v24\n"
  "v_pk_add_f16 v41, v9, v25\n"
  "v_pk_add_f16 v42, v10, v26\n"
  "v_pk_add_f16 v43, v11, v27\n"
  "v_pk_add_f16 v44, v12, v28\n"
  "v_pk_add_f16 v45, v13, v29\n"
  "v_pk_add_f16 v46, v14, v30\n"
  "v_pk_add_f16 v47, v15, v31\n"
  "v_add_u32 v230, s49, v230\n"
  "v_add_u32 v231, s49, v231\n"
  "v_add_u32 v232, s49, v232\n"
  "s_mov_b32 s63, 2\n"
  ".p2align 6\n"
  ".LloopN_%=:\n"
  "v_mfma_f32_16x16x32_f16 v[124:127], v[0:3], v[96:99], v[124:127]\n"
  "buffer_load_dword v247, v246, s[40:43], s54 offen sc1\n"
  "v_pk_add_f16 v120, v96, v100\n"
  "v_mfma_f32_16x16x32_f16 v[136:139], v[4:7], v[96:99], v[136:139]\n"
  "v_pk_add_f16 v121, v97, v101\n"
  "v_pk_add_f16 v122, v98, v102\n"
  "v_mfma_f32_16x16x32_f16 v[148:151], v[8:11], v[96:99], v[148:151]\n"
  "ds_read_b64_tr_b16 v[48:49], v228 offset:0\n"
  "v_pk_add_f16 v123, v99, v103\n"
  "v_mfma_f32_16x16x32_f16 v[160:163], v[12:15], v[96:99], v[160:163]\n"
  "s_add_i32 m0, s47, 0\n"
  "ds_read_b64_tr_b16 v[50:51], v228 offset:8192\n"
  "ds_read_b128 v[96:99], v232 offset:4096\n"
  "v_mfma_f32_16x16x32_f16 v[128:131], v[16:19], v[100:103], v[128:131]\n"
  "s_add_i32 s64, s50, 4194304\n"
  "buffer_load_dwordx4 v237, s[40:43], s64 offen sc1 lds\n"
  "ds_read_b64_tr_b16 v[64:65], v228 offset:256\n"
  "v_mfma_f32_16x16x32_f16 v[140:143], v[20:23], v[100:103], v[140:143]\n"
  "s_cmp_eq_u32 s46, 73728\n"
  "s_cselect_b32 s49, s68, s69\n"
  "ds_read_b64_tr_b16 v[66:67], v228 offset:8448\n"
  "v_mfma_f32_16x16x32_f16 v[152:155], v[24:27], v[100:103], v[152:155]\n"
  "ds_read_b64_tr_b16 v[52:53], v229 offset:0\n"
  "v_add_u32 v228, s49, v228\n"
  "v_mfma_f32_16x16x32_f16 v[164:167], v[28:31], v[100:103], v[164:167]\n"
  "s_add_i32 m0, s47, 4096\n"
  "ds_read_b128 v[100:103], v232 offset:4608\n"
  "ds_read_b64_tr_b16 v[54:55], v229 offset:8192\n"
  "v_mfma_f32_16x16x32_f16 v[132:135], v[32:35], v[120:123], v[132:135]\n"
  "s_add_i32 s64, s50, 4198400\n"
  "buffer_load_dwordx4 v237, s[40:43], s64 offen sc1 lds\n"
  "ds_read_b64_tr_b16 v[68:69], v229 offset:256\n"
  "v_mfma_f32_16x16x32_f16 v[144:147], v[36:39], v[120:123], v[144:147]\n"
  "ds_read_b64_tr_b16 v[70:71], v229 offset:8448\n"
  "v_add_u32 v229, s49, v229\n"
  "v_mfma_f32_16x16x32_f16 v[156:159], v[40:43], v[120:123], v[156:159]\n"
  "ds_read_b64_tr_b16 v[56:57], v230 offset:0\n"
  "ds_read_b64_tr_b16 v[58:59], v230 offset:8192\n"
  "v_mfma_f32_16x16x32_f16 v[168:171], v[44:47], v[120:123], v[168:171]\n"
  "s_add_i32 m0, s47, 8192\n"
  "ds_read_b64_tr_b16 v[72:73], v230 offset:256\n"
  "ds_read_b64_tr_b16 v[74:75], v230 offset:8448\n"
  "v_mfma_f32_16x16x32_f16 v[180:183], v[0:3], v[104:107], v[180:183]\n"
  "s_add_i32 s64, s50, 4202496\n"
  "buffer_load_dwordx4 v237, s[40:43], s64 offen sc1 lds\n"
  "v_pk_add_f16 v120, v104, v108\n"
  "v_mfma_f32_16x16x32_f16 v[192:195], v[4:7], v[104:107], v[192:195]\n"
  "s_waitcnt lgkmcnt(9)\n"
  "v_pk_add_f16 v121, v105, v109\n"
  "v_pk_add_f16 v122, v106, v110\n"
  "v_mfma_f32_16x16x32_f16 v[204:207], v[8:11], v[104:107], v[204:207]\n"
  "ds_read_b64_tr_b16 v[60:61], v231 offset:0\n"
  "v_pk_add_f16 v123, v107, v111\n"
  "v_mfma_f32_16x16x32_f16 v[216:219], v[12:15], v[104:107], v[216:219]\n"
  "s_add_i32 m0, s47, 12288\n"
  "ds_read_b128 v[104:107], v232 offset:6144\n"
  "v_pk_add_f16 v80, v48, v64\n"
  "v_mfma_f32_16x16x32_f16 v[184:187], v[16:19], v[108:111], v[184:187]\n"
  "s_waitcnt lgkmcnt(6)\n"
  "s_add_i32 s64, s50, 4206592\n"
  "buffer_load_dwordx4 v237, s[40:43], s64 offen sc1 lds\n"
  "v_mfma_f32_16x16x32_f16 v[196:199], v[20:23], v[108:111], v[196:199]\n"
  "ds_read_b64_tr_b16 v[62:63], v231 offset:8192\n"
  "v_pk_add_f16 v81, v49, v65\n"
  "v_mfma_f32_16x16x32_f16 v[208:211], v[24:27], v[108:111], v[208:211]\n"
  "s_waitcnt lgkmcnt(3)\n"
  "ds_read_b128 v[112:115], v232 offset:8192\n"
  "v_pk_add_f16 v82, v50, v66\n"
  "v_mfma_f32_16x16x32_f16 v[220:223], v[28:31], v[108:111], v[220:223]\n"
  "s_add_i32 m0, s47, 16384\n"
  "ds_read_b128 v[108:111], v232 offset:6656\n"
  "v_pk_add_f16 v83, v51, v67\n"
  "v_mfma_f32_16x16x32_f16 v[188:191], v[32:35], v[120:123], v[188:191]\n"
  "s_add_i32 s64, s50, 4194304\n"
  "buffer_load_dwordx4 v238, s[40:43], s64 offen sc1 lds\n"
  "v_pk_add_f16 v84, v52, v68\n"
  "v_mfma_f32_16x16x32_f16 v[200:203], v[36:39], v[120:123], v[200:203]\n"
  "ds_read_b64_tr_b16 v[76:77], v231 offset:256\n"
  "v_pk_add_f16 v85, v53, v69\n"
  "v_mfma_f32_16x16x32_f16 v[212:215], v[40:43], v[120:123], v[212:215]\n"
  "ds_read_b128 v[116:119], v232 offset:8704\n"
  "v_pk_add_f16 v86, v54, v70\n"
  "v_mfma_f32_16x16x32_f16 v[224:227], v[44:47], v[120:123], v[224:227]\n"
  "s_add_i32 m0, s47, 20480\n"
  "ds_read_b64_tr_b16 v[78:79], v231 offset:8448\n"
  "v_pk_add_f16 v87, v55, v71\n"
  "v_mfma_f32_16x16x32_f16 a[96:99], v[0:3], v[96:99], a[96:99]\n"
  "s_add_i32 s64, s50, 4096\n"
  "buffer_load_dwordx4 v238, s[40:43], s64 offen sc1 lds\n"
  "v_pk_add_f16 v120, v96, v100\n"
  "v_mfma_f32_16x16x32_f16 a[108:111], v[4:7], v[96:99], a[108:111]\n"
  "v_pk_add_f16 v121, v97, v101\n"
  "v_pk_add_f16 v122, v98, v102\n"
  "v_mfma_f32_16x16x32_f16 a[120:123], v[8:11], v[96:99], a[120:123]\n"
  "v_pk_add_f16 v123, v99, v103\n"
  "v_pk_add_f16 v88, v56, v72\n"
  "v_mfma_f32_16x16x32_f16 a[132:135], v[12:15], v[96:99], a[132:135]\n"
  "s_add_i32 m0, s48, 24576\n"
  "ds_read_b128 v[96:99], v232 offset:0\n"
  "v_pk_add_f16 v89, v57, v73\n"
  "v_mfma_f32_16x16x32_f16 a[100:103], v[16:19], v[100:103], a[100:103]\n"
  "s_add_i32 s64, s50, 16384\n"
  "buffer_load_dwordx4 v238, s[40:43], s64 offen sc1 lds\n"
  "v_pk_add_f16 v90, v58, v74\n"
  "v_mfma_f32_16x16x32_f16 a[112:115], v[20:23], v[100:103], a[112:115]\n"
  "v_pk_add_f16 v91, v59, v75\n"
  "v_add_u32 v230, s49, v230\n"
  "v_mfma_f32_16x16x32_f16 a[124:127], v[24:27], v[100:103], a[124:127]\n"
  "s_waitcnt lgkmcnt(1)\n"
  "v_pk_add_f16 v92, v60, v76\n"
  "v_pk_add_f16 v93, v61, v77\n"
  "v_mfma_f32_16x16x32_f16 a[136:139], v[28:31], v[100:103], a[136:139]\n"
  "s_add_i32 m0, s47, 26624\n"
  "ds_read_b128 v[100:103], v232 offset:512\n"
  "v_pk_add_f16 v94, v62, v78\n"
  "v_mfma_f32_16x16x32_f16 a[104:107], v[32:35], v[120:123], a[104:107]\n"
  "s_add_i32 s64, s50, 4214784\n"
  "buffer_load_dwordx4 v238, s[40:43], s64 offen sc1 lds\n"
  "v_pk_add_f16 v95, v63, v79\n"
  "v_mfma_f32_16x16x32_f16 a[116:119], v[36:39], v[120:123], a[116:119]\n"
  "v_add_u32 v231, s49, v231\n"
  "v_mfma_f32_16x16x32_f16 a[128:131], v[40:43], v[120:123], a[128:131]\n"
  "v_mfma_f32_16x16x32_f16 a[140:143], v[44:47], v[120:123], a[140:143]\n"
  "s_add_i32 m0, s47, 30720\n"
  "v_mfma_f32_16x16x32_f16 a[144:147], v[0:3], v[104:107], a[144:147]\n"
  "s_add_i32 s64, s50, 32768\n"
  "buffer_load_dwordx4 v238, s[40:43], s64 offen sc1 lds\n"
  "v_pk_add_f16 v120, v104, v108\n"
  "v_mfma_f32_16x16x32_f16 a[156:159], v[4:7], v[104:107], a[156:159]\n"
  "v_pk_add_f16 v121, v105, v109\n"
  "v_pk_add_f16 v122, v106, v110\n"
  "v_mfma_f32_16x16x32_f16 a[168:171], v[8:11], v[104:107], a[168:171]\n"
  "v_pk_add_f16 v123, v107, v111\n"
  "v_mfma_f32_16x16x32_f16 a[180:183], v[12:15], v[104:107], a[180:183]\n"
  "ds_read_b128 v[104:107], v232 offset:2048\n"
  "v_mfma_f32_16x16x32_f16 a[148:151], v[16:19], v[108:111], a[148:151]\n"
  "s_add_i32 s47, s44, s46\n"
  "s_add_i32 s50, s50, 4194304\n"
  "v_mfma_f32_16x16x32_f16 a[160:163], v[20:23], v[108:111], a[160:163]\n"
  "s_add_i32 s48, s47, s45\n"
  "s_add_i32 s46, s46, s49\n"
  "v_mfma_f32_16x16x32_f16 a[172:175], v[24:27], v[108:111], a[172:175]\n"
  "v_mfma_f32_16x16x32_f16 a[184:187], v[28:31], v[108:111], a[184:187]\n"
  "ds_read_b128 v[108:111], v232 offset:2560\n"
  "v_add_u32 v232, s49, v232\n"
  "v_mfma_f32_16x16x32_f16 a[152:155], v[32:35], v[120:123], a[152:155]\n"
  "v_mfma_f32_16x16x32_f16 a[164:167], v[36:39], v[120:123], a[164:167]\n"
  "v_mfma_f32_16x16x32_f16 a[176:179], v[40:43], v[120:123], a[176:179]\n"
  "v_mfma_f32_16x16x32_f16 a[188:191], v[44:47], v[120:123], a[188:191]\n"
  "v_mfma_f32_16x16x32_f16 a[192:195], v[0:3], v[112:115], a[192:195]\n"
  "v_pk_add_f16 v120, v112, v116\n"
  "v_pk_add_f16 v121, v113, v117\n"
  "v_mfma_f32_16x16x32_f16 a[204:207], v[4:7], v[112:115], a[204:207]\n"
  "v_pk_add_f16 v122, v114, v118\n"
  "v_pk_add_f16 v123, v115, v119\n"
  "v_mfma_f32_16x16x32_f16 a[216:219], v[8:11], v[112:115], a[216:219]\n"
  "v_mfma_f32_16x16x32_f16 a[228:231], v[12:15], v[112:115], a[228:231]\n"
  "v_mfma_f32_16x16x32_f16 a[196:199], v[16:19], v[116:119], a[196:199]\n"
  "v_mfma_f32_16x16x32_f16 a[208:211], v[20:23], v[116:119], a[208:211]\n"
  "v_mfma_f32_16x16x32_f16 a[220:223], v[24:27], v[116:119], a[220:223]\n"
  "v_mfma_f32_16x16x32_f16 a[232:235], v[28:31], v[116:119], a[232:235]\n"
  "v_mfma_f32_16x16x32_f16 a[200:203], v[32:35], v[120:123], a[200:203]\n"
  "s_waitcnt vmcnt(9) lgkmcnt(0)\n"
  "v_mfma_f32_16x16x32_f16 a[212:215], v[36:39], v[120:123], a[212:215]\n"
  "v_cmp_eq_u32 vcc, 0, v247\n"
  "s_cbranch_vccnz .Lspin_ne_None_%=\n"
  ".Lspun_ne_None_%=:\n"
  "v_mfma_f32_16x16x32_f16 a[224:227], v[40:43], v[120:123], a[224:227]\n"
  "s_barrier\n"
  "v_mfma_f32_16x16x32_f16 a[236:239], v[44:47], v[120:123], a[236:239]\n"
  "v_mfma_f32_16x16x32_f16 v[124:127], v[48:51], v[96:99], v[124:127]\n"
  "v_pk_add_f16 v120, v96, v100\n"
  "v_pk_add_f16 v121, v97, v101\n"
  "v_mfma_f32_16x16x32_f16 v[136:139], v[52:55], v[96:99], v[136:139]\n"
  "v_pk_add_f16 v122, v98, v102\n"
  "v_pk_add_f16 v123, v99, v103\n"
  "v_mfma_f32_16x16x32_f16 v[148:151], v[56:59], v[96:99], v[148:151]\n"
  "s_and_b32 s65, s51, 0xffffe300\n"
  "ds_read_b64_tr_b16 v[0:1], v228 offset:0\n"
  "ds_read_b64_tr_b16 v[2:3], v228 offset:8192\n"
  "v_mfma_f32_16x16x32_f16 v[160:163], v[60:63], v[96:99], v[160:163]\n"
  "s_add_i32 m0, s47, 0\n"
  "ds_read_b128 v[96:99], v232 offset:4096\n"
  "ds_read_b64_tr_b16 v[16:17], v228 offset:256\n"
  "v_mfma_f32_16x16x32_f16 v[128:131], v[64:67], v[100:103], v[128:131]\n"
  "s_add_i32 s64, s50, 4194304\n"
  "buffer_load_dwordx4 v237, s[40:43], s64 offen sc1 lds\n"
  "ds_read_b64_tr_b16 v[18:19], v228 offset:8448\n"
  "v_mfma_f32_16x16x32_f16 v[140:143], v[68:71], v[100:103], v[140:143]\n"
  "s_cmp_eq_u32 s46, 73728\n"
  "s_cselect_b32 s49, s68, s69\n"
  "ds_read_b64_tr_b16 v[4:5], v229 offset:0\n"
  "v_mfma_f32_16x16x32_f16 v[152:155], v[72:75], v[100:103], v[152:155]\n"
  "ds_read_b64_tr_b16 v[6:7], v229 offset:8192\n"
  "v_add_u32 v228, s49, v228\n"
  "v_mfma_f32_16x16x32_f16 v[164:167], v[76:79], v[100:103], v[164:167]\n"
  "ds_read_b128 v[100:103], v232 offset:4608\n"
  "ds_read_b64_tr_b16 v[20:21], v229 offset:256\n"
  "v_mfma_f32_16x16x32_f16 v[132:135], v[80:83], v[120:123], v[132:135]\n"
  "buffer_store_dword v232, v245, s[40:43], s65 offen sc1\n"
  "ds_read_b64_tr_b16 v[22:23], v229 offset:8448\n"
  "v_mfma_f32_16x16x32_f16 v[144:147], v[84:87], v[120:123], v[144:147]\n"
  "ds_read_b64_tr_b16 v[8:9], v230 offset:0\n"
  "v_add_u32 v229, s49, v229\n"
  "v_mfma_f32_16x16x32_f16 v[156:159], v[88:91], v[120:123], v[156:159]\n"
  "s_add_i32 s53, s53, 8192\n"
  "s_lshr_b32 s52, s52, 1\n"
  "ds_read_b64_tr_b16 v[10:11], v230 offset:8192\n"
  "v_mfma_f32_16x16x32_f16 v[168:171], v[92:95], v[120:123], v[168:171]\n"
  "s_add_i32 m0, s47, 4096\n"
  "s_waitcnt lgkmcnt(7)\n"
  "ds_read_b64_tr_b16 v[24:25], v230 offset:256\n"
  "v_mfma_f32_16x16x32_f16 v[180:183], v[48:51], v[104:107], v[180:183]\n"
  "s_add_i32 s64, s50, 4198400\n"
  "buffer_load_dwordx4 v237, s[40:43], s64 offen sc1 lds\n"
  "v_pk_add_f16 v120, v104, v108\n"
  "v_mfma_f32_16x16x32_f16 v[192:195], v[52:55], v[104:107], v[192:195]\n"
  "s_add_i32 s51, s51, 8448\n"
  "v_pk_add_f16 v121, v105, v109\n"
  "v_pk_add_f16 v122, v106, v110\n"
  "v_mfma_f32_16x16x32_f16 v[204:207], v[56:59], v[104:107], v[204:207]\n"
  "ds_read_b64_tr_b16 v[26:27], v230 offset:8448\n"
  "v_pk_add_f16 v123, v107, v111\n"
  "v_mfma_f32_16x16x32_f16 v[216:219], v[60:63], v[104:107], v[216:219]\n"
  "s_add_i32 m0, s47, 8192\n"
  "s_waitcnt lgkmcnt(4)\n"
  "ds_read_b128 v[104:107], v232 offset:6144\n"
  "v_mfma_f32_16x16x32_f16 v[184:187], v[64:67], v[108:111], v[184:187]\n"
  "s_add_i32 s64, s50, 4202496\n"
  "buffer_load_dwordx4 v237, s[40:43], s64 offen sc1 lds\n"
  "v_pk_add_f16 v32, v0, v16\n"
  "v_mfma_f32_16x16x32_f16 v[196:199], v[68:71], v[108:111], v[196:199]\n"
  "s_bitcmp1_b32 s52, 0\n"
  "s_cselect_b32 s54, s53, 134217728\n"
  "ds_read_b64_tr_b16 v[12:13], v231 offset:0\n"
  "v_mfma_f32_16x16x32_f16 v[208:211], v[72:75], v[108:111], v[208:211]\n"
  "ds_read_b64_tr_b16 v[14:15], v231 offset:8192\n"
  "v_pk_add_f16 v33, v1, v17\n"
  "v_mfma_f32_16x16x32_f16 v[220:223], v[76:79], v[108:111], v[220:223]\n"
  "s_add_i32 m0, s47, 12288\n"
  "ds_read_b128 v[108:111], v232 offset:6656\n"
  "v_pk_add_f16 v34, v2, v18\n"
  "v_mfma_f32_16x16x32_f16 v[188:191], v[80:83], v[120:123], v[188:191]\n"
  "s_add_i32 s64, s50, 4206592\n"
  "buffer_load_dwordx4 v237, s[40:43], s64 offen sc1 lds\n"
  "v_pk_add_f16 v35, v3, v19\n"
  "v_mfma_f32_16x16x32_f16 v[200:203], v[84:87], v[120:123], v[200:203]\n"
  "s_waitcnt lgkmcnt(4)\n"
  "ds_read_b128 v[112:115], v232 offset:8192\n"
  "v_mfma_f32_16x16x32_f16 v[212:215], v[88:91], v[120:123], v[212:215]\n"
  "ds_read_b64_tr_b16 v[28:29], v231 offset:256\n"
  "v_pk_add_f16 v36, v4, v20\n"
  "v_mfma_f32_16x16x32_f16 v[224:227], v[92:95], v[120:123], v[224:227]\n"
  "s_add_i32 m0, s47, 16384\n"
  "ds_read_b128 v[116:119], v232 offset:8704\n"
  "v_pk_add_f16 v37, v5, v21\n"
  "v_mfma_f32_16x16x32_f16 a[96:99], v[48:51], v[96:99], a[96:99]\n"
  "s_add_i32 s64, s50, 4194304\n"
  "buffer_load_dwordx4 v238, s[40:43], s64 offen sc1 lds\n"
  "v_pk_add_f16 v120, v96, v100\n"
  "v_mfma_f32_16x16x32_f16 a[108:111], v[52:55], v[96:99], a[108:111]\n"
  "v_pk_add_f16 v121, v97, v101\n"
  "v_pk_add_f16 v122, v98, v102\n"
  "v_mfma_f32_16x16x32_f16 a[120:123], v[56:59], v[96:99], a[120:123]\n"
  "ds_read_b64_tr_b16 v[30:31], v231 offset:8448\n"
  "v_pk_add_f16 v123, v99, v103\n"
  "v_mfma_f32_16x16x32_f16 a[132:135], v[60:63], v[96:99], a[132:135]\n"
  "s_add_i32 m0, s47, 20480\n"
  "ds_read_b128 v[96:99], v232 offset:0\n"
  "v_pk_add_f16 v38, v6, v22\n"
  "v_mfma_f32_16x16x32_f16 a[100:103], v[64:67], v[100:103], a[100:103]\n"
  "s_add_i32 s64, s50, 4096\n"
  "buffer_load_dwordx4 v238, s[40:43], s64 offen sc1 lds\n"
  "v_pk_add_f16 v39, v7, v23\n"
  "v_mfma_f32_16x16x32_f16 a[112:115], v[68:71], v[100:103], a[112:115]\n"
  "v_pk_add_f16 v40, v8, v24\n"
  "v_pk_add_f16 v41, v9, v25\n"
  "v_mfma_f32_16x16x32_f16 a[124:127], v[72:75], v[100:103], a[124:127]\n"
  "v_pk_add_f16 v42, v10, v26\n"
  "v_pk_add_f16 v43, v11, v27\n"
  "v_mfma_f32_16x16x32_f16 a[136:139], v[76:79], v[100:103], a[136:139]\n"
  "s_add_i32 m0, s48, 24576\n"
  "ds_read_b128 v[100:103], v232 offset:512\n"
  "v_add_u32 v230, s49, v230\n"
  "v_mfma_f32_16x16x32_f16 a[104:107], v[80:83], v[120:123], a[104:107]\n"
  "s_add_i32 s64, s50, 16384\n"
  "buffer_load_dwordx4 v238, s[40:43], s64 offen sc1 lds\n"
  "v_add_u32 v231, s49, v231\n"
  "v_mfma_f32_16x16x32_f16 a[116:119], v[84:87], v[120:123], a[116:119]\n"
  "s_waitcnt lgkmcnt(6)\n"
  "s_waitcnt lgkmcnt(2)\n"
  "v_pk_add_f16 v44, v12, v28\n"
  "v_mfma_f32_16x16x32_f16 a[128:131], v[88:91], v[120:123], a[128:131]\n"
  "v_pk_add_f16 v45, v13, v29\n"
  "v_pk_add_f16 v46, v14, v30\n"
  "v_mfma_f32_16x16x32_f16 a[140:143], v[92:95], v[120:123], a[140:143]\n"
  "s_add_i32 m0, s47, 26624\n"
  "v_pk_add_f16 v47, v15, v31\n"
  "v_mfma_f32_16x16x32_f16 a[144:147], v[48:51], v[104:107], a[144:147]\n"
  "s_add_i32 s64, s50, 4214784\n"
  "buffer_load_dwordx4 v238, s[40:43], s64 offen sc1 lds\n"
  "v_pk_add_f16 v120, v104, v108\n"
  "v_mfma_f32_16x16x32_f16 a[156:159], v[52:55], v[104:107], a[156:159]\n"
  "v_pk_add_f16 v121, v105, v109\n"
  "v_pk_add_f16 v122, v106, v110\n"
  "v_mfma_f32_16x16x32_f16 a[168:171], v[56:59], v[104:107], a[168:171]\n"
  "v_pk_add_f16 v123, v107, v111\n"
  "v_mfma_f32_16x16x32_f16 a[180:183], v[60:63], v[104:107], a[180:183]\n"
  "s_add_i32 m0, s47, 30720\n"
  "ds_read_b128 v[104:107], v232 offset:2048\n"
  "v_mfma_f32_16x16x32_f16 a[148:151], v[64:67], v[108:111], a[148:151]\n"
  "s_add_i32 s64, s50, 32768\n"
  "buffer_load_dwordx4 v238, s[40:43], s64 offen sc1 lds\n"
  "v_mfma_f32_16x16x32_f16 a[160:163], v[68:71], v[108:111], a[160:163]\n"
  "s_add_i32 s47, s44, s46\n"
  "s_add_i32 s50, s50, 4194304\n"
  "v_mfma_f32_16x16x32_f16 a[172:175], v[72:75], v[108:111], a[172:175]\n"
  "s_add_i32 s48, s47, s45\n"
  "s_add_i32 s46, s46, s49\n"
  "v_mfma_f32_16x16x32_f16 a[184:187], v[76:79], v[108:111], a[184:187]\n"
  "ds_read_b128 v[108:111], v232 offset:2560\n"
  "v_add_u32 v232, s49, v232\n"
  "v_mfma_f32_16x16x32_f16 a[152:155], v[80:83], v[120:123], a[152:155]\n"
  "v_mfma_f32_16x16x32_f16 a[164:167], v[84:87], v[120:123], a[164:167]\n"
  "v_mfma_f32_16x16x32_f16 a[176:179], v[88:91], v[120:123], a[176:179]\n"
  "v_mfma_f32_16x16x32_f16 a[188:191], v[92:95], v[120:123], a[188:191]\n"
  "v_mfma_f32_16x16x32_f16 a[192:195], v[48:51], v[112:115], a[192:195]\n"
  "v_pk_add_f16 v120, v112, v116\n"
  "v_pk_add_f16 v121, v113, v117\n"
  "v_mfma_f32_16x16x32_f16 a[204:207], v[52:55], v[112:115], a[204:207]\n"
  "v_pk_add_f16 v122, v114, v118\n"
  "v_pk_add_f16 v123, v115, v119\n"
  "v_mfma_f32_16x16x32_f16 a[216:219], v[56:59], v[112:115], a[216:219]\n"
  "v_mfma_f32_16x16x32_f16 a[228:231], v[60:63], v[112:115], a[228:231]\n"
  "v_mfma_f32_16x16x32_f16 a[196:199], v[64:67], v[116:119], a[196:199]\n"
  "v_mfma_f32_16x16x32_f16 a[208:211], v[68:71], v[116:119], a[208:211]\n"
  "v_mfma_f32_16x16x32_f16 a[220:223], v[72:75], v[116:119], a[220:223]\n"
  "v_mfma_f32_16x16x32_f16 a[232:235], v[76:79], v[116:119], a[232:235]\n"
  "v_mfma_f32_16x16x32_f16 a[200:203], v[80:83], v[120:123], a[200:203]\n"
  "s_waitcnt vmcnt(10) lgkmcnt(0)\n"
  "v_mfma_f32_16x16x32_f16 a[212:215], v[84:87], v[120:123], a[212:215]\n"
  "v_mfma_f32_16x16x32_f16 a[224:227], v[88:91], v[120:123], a[224:227]\n"
  "s_barrier\n"
  "v_mfma_f32_16x16x32_f16 a[236:239], v[92:95], v[120:123], a[236:239]\n"
  "s_sub_i32 s63, s63, 1\n"
  "s_cmp_lg_u32 s63, 0\n"
  "s_cbranch_scc1 .LloopN_%=\n"
  "v_mfma_f32_16x16x32_f16 v[124:127], v[0:3], v[96:99], v[124:127]\n"
  "s_add_i32 s67, s44, s45\n"
  "buffer_load_dword v247, v246, s[40:43], s54 offen sc1\n"
  "v_pk_add_f16 v120, v96, v100\n"
  "v_mfma_f32_16x16x32_f16 v[136:139], v[4:7], v[96:99], v[136:139]\n"
  "v_pk_add_f16 v121, v97, v101\n"
  "v_pk_add_f16 v122, v98, v102\n"
  "v_mfma_f32_16x16x32_f16 v[148:151], v[8:11], v[96:99], v[148:151]\n"
  "ds_read_b64_tr_b16 v[48:49], v228 offset:0\n"
  "v_pk_add_f16 v123, v99, v103\n"
  "v_mfma_f32_16x16x32_f16 v[160:163], v[12:15], v[96:99], v[160:163]\n"
  "s_add_i32 m0, s47, 0\n"
  "ds_read_b64_tr_b16 v[50:51], v228 offset:8192\n"
  "ds_read_b128 v[96:99], v232 offset:4096\n"
  "v_mfma_f32_16x16x32_f16 v[128:131], v[16:19], v[100:103], v[128:131]\n"
  "s_add_i32 s64, s50, 4194304\n"
  "buffer_load_dwordx4 v237, s[40:43], s64 offen sc1 lds\n"
  "ds_read_b64_tr_b16 v[64:65], v228 offset:256\n"
  "v_mfma_f32_16x16x32_f16 v[140:143], v[20:23], v[100:103], v[140:143]\n"
  "s_cmp_eq_u32 s46, 73728\n"
  "s_cselect_b32 s49, s68, s69\n"
  "ds_read_b64_tr_b16 v[66:67], v228 offset:8448\n"
  "v_mfma_f32_16x16x32_f16 v[152:155], v[24:27], v[100:103], v[152:155]\n"
  "ds_read_b64_tr_b16 v[52:53], v229 offset:0\n"
  "v_add_u32 v228, s49, v228\n"
  "v_mfma_f32_16x16x32_f16 v[164:167], v[28:31], v[100:103], v[164:167]\n"
  "s_add_i32 m0, s47, 4096\n"
  "ds_read_b128 v[100:103], v232 offset:4608\n"
  "ds_read_b64_tr_b16 v[54:55], v229 offset:8192\n"
  "v_mfma_f32_16x16x32_f16 v[132:135], v[32:35], v[120:123], v[132:135]\n"
  "s_add_i32 s64, s50, 4198400\n"
  "buffer_load_dwordx4 v237, s[40:43], s64 offen sc1 lds\n"
  "ds_read_b64_tr_b16 v[68:69], v229 offset:256\n"
  "v_mfma_f32_16x16x32_f16 v[144:147], v[36:39], v[120:123], v[144:147]\n"
  "ds_read_b64_tr_b16 v[70:71], v229 offset:8448\n"
  "v_add_u32 v229, s49, v229\n"
  "v_mfma_f32_16x16x32_f16 v[156:159], v[40:43], v[120:123], v[156:159]\n"
  "ds_read_b64_tr_b16 v[56:57], v230 offset:0\n"
  "ds_read_b64_tr_b16 v[58:59], v230 offset:8192\n"
  "v_mfma_f32_16x16x32_f16 v[168:171], v[44:47], v[120:123], v[168:171]\n"
  "s_add_i32 m0, s47, 8192\n"
  "ds_read_b64_tr_b16 v[72:73], v230 offset:256\n"
  "ds_read_b64_tr_b16 v[74:75], v230 offset:8448\n"
  "v_mfma_f32_16x16x32_f16 v[180:183], v[0:3], v[104:107], v[180:183]\n"
  "s_add_i32 s64, s50, 4202496\n"
  "buffer_load_dwordx4 v237, s[40:43], s64 offen sc1 lds\n"
  "v_pk_add_f16 v120, v104, v108\n"
  "v_mfma_f32_16x16x32_f16 v[192:195], v[4:7], v[104:107], v[192:195]\n"
  "s_waitcnt lgkmcnt(9)\n"
  "v_pk_add_f16 v121, v105, v109\n"
  "v_pk_add_f16 v122, v106, v110\n"
  "v_mfma_f32_16x16x32_f16 v[204:207], v[8:11], v[104:107], v[204:207]\n"
  "ds_read_b64_tr_b16 v[60:61], v231 offset:0\n"
  "v_pk_add_f16 v123, v107, v111\n"
  "v_mfma_f32_16x16x32_f16 v[216:219], v[12:15], v[104:107], v[216:219]\n"
  "s_add_i32 m0, s47, 12288\n"
  "ds_read_b128 v[104:107], v232 offset:6144\n"
  "v_pk_add_f16 v80, v48, v64\n"
  "v_mfma_f32_16x16x32_f16 v[184:187], v[16:19], v[108:111], v[184:187]\n"
  "s_waitcnt lgkmcnt(6)\n"
  "s_add_i32 s64, s50, 4206592\n"
  "buffer_load_dwordx4 v237, s[40:43], s64 offen sc1 lds\n"
  "v_mfma_f32_16x16x32_f16 v[196:199], v[20:23], v[108:111], v[196:199]\n"
  "ds_read_b64_tr_b16 v[62:63], v231 offset:8192\n"
  "v_pk_add_f16 v81, v49, v65\n"
  "v_mfma_f32_16x16x32_f16 v[208:211], v[24:27], v[108:111], v[208:211]\n"
  "s_waitcnt lgkmcnt(3)\n"
  "ds_read_b128 v[112:115], v232 offset:8192\n"
  "v_pk_add_f16 v82, v50, v66\n"
  "v_mfma_f32_16x16x32_f16 v[220:223], v[28:31], v[108:111], v[220:223]\n"
  "s_add_i32 m0, s47, 16384\n"
  "ds_read_b128 v[108:111], v232 offset:6656\n"
  "v_pk_add_f16 v83, v51, v67\n"
  "v_mfma_f32_16x16x32_f16 v[188:191], v[32:35], v[120:123], v[188:191]\n"
  "s_add_i32 s64, s50, 4194304\n"
  "buffer_load_dwordx4 v238, s[40:43], s64 offen sc1 lds\n"
  "v_pk_add_f16 v84, v52, v68\n"
  "v_mfma_f32_16x16x32_f16 v[200:203], v[36:39], v[120:123], v[200:203]\n"
  "ds_read_b64_tr_b16 v[76:77], v231 offset:256\n"
  "v_pk_add_f16 v85, v53, v69\n"
  "v_mfma_f32_16x16x32_f16 v[212:215], v[40:43], v[120:123], v[212:215]\n"
  "ds_read_b128 v[116:119], v232 offset:8704\n"
  "v_pk_add_f16 v86, v54, v70\n"
  "v_mfma_f32_16x16x32_f16 v[224:227], v[44:47], v[120:123], v[224:227]\n"
  "s_add_i32 m0, s47, 20480\n"
  "ds_read_b64_tr_b16 v[78:79], v231 offset:8448\n"
  "v_pk_add_f16 v87, v55, v71\n"
  "v_mfma_f32_16x16x32_f16 a[96:99], v[0:3], v[96:99], a[96:99]\n"
  "s_add_i32 s64, s50, 4096\n"
  "buffer_load_dwordx4 v238, s[40:43], s64 offen sc1 lds\n"
  "v_pk_add_f16 v120, v96, v100\n"
  "v_mfma_f32_16x16x32_f16 a[108:111], v[4:7], v[96:99], a[108:111]\n"
  "v_pk_add_f16 v121, v97, v101\n"
  "v_pk_add_f16 v122, v98, v102\n"
  "v_mfma_f32_16x16x32_f16 a[120:123], v[8:11], v[96:99], a[120:123]\n"
  "ds_write_b128 v253, v[172:175]\n"
  "v_pk_add_f16 v123, v99, v103\n"
  "v_mfma_f32_16x16x32_f16 a[132:135], v[12:15], v[96:99], a[132:135]\n"
  "s_add_i32 m0, s48, 24576\n"
  "ds_read_b128 v[96:99], v232 offset:0\n"
  "v_pk_add_f16 v88, v56, v72\n"
  "v_mfma_f32_16x16x32_f16 a[100:103], v[16:19], v[100:103], a[100:103]\n"
  "s_add_i32 s64, s50, 16384\n"
  "buffer_load_dwordx4 v238, s[40:43], s64 offen sc1 lds\n"
  "v_pk_add_f16 v89, v57, v73\n"
  "v_mfma_f32_16x16x32_f16 a[112:115], v[20:23], v[100:103], a[112:115]\n"
  "v_pk_add_f16 v90, v58, v74\n"
  "v_pk_add_f16 v91, v59, v75\n"
  "v_mfma_f32_16x16x32_f16 a[124:127], v[24:27], v[100:103], a[124:127]\n"
  "s_waitcnt lgkmcnt(2)\n"
  "v_pk_add_f16 v92, v60, v76\n"
  "v_pk_add_f16 v93, v61, v77\n"
  "v_mfma_f32_16x16x32_f16 a[136:139], v[28:31], v[100:103], a[136:139]\n"
  "s_add_i32 m0, s47, 26624\n"
  "ds_read_b128 v[100:103], v232 offset:512\n"
  "v_pk_add_f16 v94, v62, v78\n"
  "v_mfma_f32_16x16x32_f16 a[104:107], v[32:35], v[120:123], a[104:107]\n"
  "s_add_i32 s64, s50, 4214784\n"
  "buffer_load_dwordx4 v238, s[40:43], s64 offen sc1 lds\n"
  "v_pk_add_f16 v95, v63, v79\n"
  "v_mfma_f32_16x16x32_f16 a[116:119], v[36:39], v[120:123], a[116:119]\n"
  "v_add_u32 v230, s49, v230\n"
  "v_add_u32 v231, s49, v231\n"
  "v_mfma_f32_16x16x32_f16 a[128:131], v[40:43], v[120:123], a[128:131]\n"
  "v_mfma_f32_16x16x32_f16 a[140:143], v[44:47], v[120:123], a[140:143]\n"
  "s_add_i32 m0, s47, 30720\n"
  "v_mfma_f32_16x16x32_f16 a[144:147], v[0:3], v[104:107], a[144:147]\n"
  "s_add_i32 s64, s50, 32768\n"
  "buffer_load_dwordx4 v238, s[40:43], s64 offen sc1 lds\n"
  "v_pk_add_f16 v120, v104, v108\n"
  "v_mfma_f32_16x16x32_f16 a[156:159], v[4:7], v[104:107], a[156:159]\n"
  "v_pk_add_f16 v121, v105, v109\n"
  "v_pk_add_f16 v122, v106, v110\n"
  "v_mfma_f32_16x16x32_f16 a[168:171], v[8:11], v[104:107], a[168:171]\n"
  "v_pk_add_f16 v123, v107, v111\n"
  "v_mfma_f32_16x16x32_f16 a[180:183], v[12:15], v[104:107], a[180:183]\n"
  "s_add_i32 m0, s44, 114688\n"
  "ds_read_b128 v[104:107], v232 offset:2048\n"
  "v_mfma_f32_16x16x32_f16 a[148:151], v[16:19], v[108:111], a[148:151]\n"
  "s_add_i32 s47, s44, s46\n"
  "s_add_i32 s64, s50, 4198400\n"
  "buffer_load_dwordx4 v238, s[40:43], s64 offen sc1 lds\n"
  "v_mfma_f32_16x16x32_f16 a[160:163], v[20:23], v[108:111], a[160:163]\n"
  "s_add_i32 s48, s47, s45\n"
  "s_add_i32 s46, s46, s49\n"
  "v_mfma_f32_16x16x32_f16 a[172:175], v[24:27], v[108:111], a[172:175]\n"
  "v_mfma_f32_16x16x32_f16 a[184:187], v[28:31], v[108:111], a[184:187]\n"
  "s_add_i32 m0, s67, 118784\n"
  "ds_read_b128 v[108:111], v232 offset:2560\n"
  "v_add_u32 v232, s49, v232\n"
  "v_mfma_f32_16x16x32_f16 a[152:155], v[32:35], v[120:123], a[152:155]\n"
  "s_add_i32 s64, s50, 4210688\n"
  "buffer_load_dwordx4 v238, s[40:43], s64 offen sc1 lds\n"
  "v_mfma_f32_16x16x32_f16 a[164:167], v[36:39], v[120:123], a[164:167]\n"
  "v_mfma_f32_16x16x32_f16 a[176:179], v[40:43], v[120:123], a[176:179]\n"
  "v_mfma_f32_16x16x32_f16 a[188:191], v[44:47], v[120:123], a[188:191]\n"
  "s_add_i32 m0, s44, 124928\n"
  "v_mfma_f32_16x16x32_f16 a[192:195], v[0:3], v[112:115], a[192:195]\n"
  "s_add_i32 s64, s50, 4227072\n"
  "buffer_load_dwordx4 v238, s[40:43], s64 offen sc1 lds\n"
  "v_pk_add_f16 v120, v112, v116\n"
  "v_mfma_f32_16x16x32_f16 a[204:207], v[4:7], v[112:115], a[204:207]\n"
  "s_add_i32 s50, s50, 4194304\n"
  "v_pk_add_f16 v121, v113, v117\n"
  "v_pk_add_f16 v122, v114, v118\n"
  "v_mfma_f32_16x16x32_f16 a[216:219], v[8:11], v[112:115], a[216:219]\n"
  "v_pk_add_f16 v123, v115, v119\n"
  "v_mfma_f32_16x16x32_f16 a[228:231], v[12:15], v[112:115], a[228:231]\n"
  "v_mfma_f32_16x16x32_f16 a[196:199], v[16:19], v[116:119], a[196:199]\n"
  "v_mfma_f32_16x16x32_f16 a[208:211], v[20:23], v[116:119], a[208:211]\n"
  "v_mfma_f32_16x16x32_f16 a[220:223], v[24:27], v[116:119], a[220:223]\n"
  "v_mfma_f32_16x16x32_f16 a[232:235], v[28:31], v[116:119], a[232:235]\n"
  "v_mfma_f32_16x16x32_f16 a[200:203], v[32:35], v[120:123], a[200:203]\n"
  "s_waitcnt vmcnt(12) lgkmcnt(0)\n"
  "v_mfma_f32_16x16x32_f16 a[212:215], v[36:39], v[120:123], a[212:215]\n"
  "v_cmp_eq_u32 vcc, 0, v247\n"
  "s_cbranch_vccnz .Lspin_s28_None_%=\n"
  ".Lspun_s28_None_%=:\n"
  "v_mfma_f32_16x16x32_f16 a[224:227], v[40:43], v[120:123], a[224:227]\n"
  "s_barrier\n"
  "v_mfma_f32_16x16x32_f16 a[236:239], v[44:47], v[120:123], a[236:239]\n"
  "v_mfma_f32_16x16x32_f16 v[124:127], v[48:51], v[96:99], v[124:127]\n"
  "v_mbcnt_lo_u32_b32 v252, -1, 0\n"
  "v_mbcnt_hi_u32_b32 v252, -1, v252\n"
  "v_mfma_f32_16x16x32_f16 v[136:139], v[52:55], v[96:99], v[136:139]\n"
  "v_pk_add_f16 v120, v96, v100\n"
  "v_pk_add_f16 v121, v97, v101\n"
  "v_mfma_f32_16x16x32_f16 v[148:151], v[56:59], v[96:99], v[148:151]\n"
  "v_lshlrev_b32 v252, 7, v252\n"
  "v_pk_add_f16 v122, v98, v102\n"
  "v_mfma_f32_16x16x32_f16 v[160:163], v[60:63], v[96:99], v[160:163]\n"
  "s_mov_b32 m0, 147456\n"
  "v_pk_add_f16 v123, v99, v103\n"
  "v_add_u32 v252, s45, v252\n"
  "v_mfma_f32_16x16x32_f16 v[128:131], v[64:67], v[100:103], v[128:131]\n"
  "s_cmp_eq_u32 s46, 73728\n"
  "s_cselect_b32 s49, s68, s69\n"
  "global_load_lds_dword v[248:249], off\n"
  "v_mfma_f32_16x16x32_f16 v[140:143], v[68:71], v[100:103], v[140:143]\n"
  "ds_read_b64_tr_b16 v[0:1], v228 offset:0\n"
  "ds_read_b64_tr_b16 v[2:3], v228 offset:8192\n"
  "v_mfma_f32_16x16x32_f16 v[152:155], v[72:75], v[100:103], v[152:155]\n"
  "ds_read_b128 v[96:99], v232 offset:4096\n"
  "ds_read_b64_tr_b16 v[16:17], v228 offset:256\n"
  "v_mfma_f32_16x16x32_f16 v[164:167], v[76:79], v[100:103], v[164:167]\n"
  "ds_read_b128 v[100:103], v232 offset:4608\n"
  "ds_read_b64_tr_b16 v[18:19], v228 offset:8448\n"
  "v_mfma_f32_16x16x32_f16 v[132:135], v[80:83], v[120:123], v[132:135]\n"
  "ds_read_b64_tr_b16 v[4:5], v229 offset:0\n"
  "v_add_u32 v228, s49, v228\n"
  "v_mfma_f32_16x16x32_f16 v[144:147], v[84:87], v[120:123], v[144:147]\n"
  "ds_read_b64_tr_b16 v[6:7], v229 offset:8192\n"
  "ds_read_b64_tr_b16 v[20:21], v229 offset:256\n"
  "v_mfma_f32_16x16x32_f16 v[156:159], v[88:91], v[120:123], v[156:159]\n"
  "ds_read_b64_tr_b16 v[22:23], v229 offset:8448\n"
  "v_add_u32 v229, s49, v229\n"
  "v_mfma_f32_16x16x32_f16 v[168:171], v[92:95], v[120:123], v[168:171]\n"
  "s_mov_b32 m0, 144384\n"
  "ds_read_b64_tr_b16 v[8:9], v230 offset:0\n"
  "ds_read_b64_tr_b16 v[10:11], v230 offset:8192\n"
  "v_mfma_f32_16x16x32_f16 v[180:183], v[48:51], v[104:107], v[180:183]\n"
  "global_load_lds_dword v252, s[88:89]\n"
  "v_pk_add_f16 v120, v104, v108\n"
  "v_mfma_f32_16x16x32_f16 v[192:195], v[52:55], v[104:107], v[192:195]\n"
  "v_pk_add_f16 v121, v105, v109\n"
  "v_pk_add_f16 v122, v106, v110\n"
  "v_mfma_f32_16x16x32_f16 v[204:207], v[56:59], v[104:107], v[204:207]\n"
  "s_waitcnt lgkmcnt(6)\n"
  "ds_read_b64_tr_b16 v[24:25], v230 offset:256\n"
  "v_pk_add_f16 v123, v107, v111\n"
  "v_mfma_f32_16x16x32_f16 v[216:219], v[60:63], v[104:107], v[216:219]\n"
  "ds_read_b128 v[104:107], v232 offset:6144\n"
  "v_pk_add_f16 v32, v0, v16\n"
  "v_mfma_f32_16x16x32_f16 v[184:187], v[64:67], v[108:111], v[184:187]\n"
  "ds_read_b64_tr_b16 v[26:27], v230 offset:8448\n"
  "v_pk_add_f16 v33, v1, v17\n"
  "v_mfma_f32_16x16x32_f16 v[196:199], v[68:71], v[108:111], v[196:199]\n"
  "s_waitcnt lgkmcnt(5)\n"
  "ds_read_b64_tr_b16 v[12:13], v231 offset:0\n"
  "v_pk_add_f16 v34, v2, v18\n"
  "v_mfma_f32_16x16x32_f16 v[208:211], v[72:75], v[108:111], v[208:211]\n"
  "ds_read_b64_tr_b16 v[14:15], v231 offset:8192\n"
  "v_pk_add_f16 v35, v3, v19\n"
  "v_mfma_f32_16x16x32_f16 v[220:223], v[76:79], v[108:111], v[220:223]\n"
  "ds_read_b128 v[108:111], v232 offset:6656\n"
  "v_pk_add_f16 v36, v4, v20\n"
  "v_mfma_f32_16x16x32_f16 v[188:191], v[80:83], v[120:123], v[188:191]\n"
  "ds_read_b128 v[112:115], v232 offset:8192\n"
  "v_pk_add_f16 v37, v5, v21\n"
  "v_mfma_f32_16x16x32_f16 v[200:203], v[84:87], v[120:123], v[200:203]\n"
  "ds_read_b64_tr_b16 v[28:29], v231 offset:256\n"
  "v_pk_add_f16 v38, v6, v22\n"
  "v_mfma_f32_16x16x32_f16 v[212:215], v[88:91], v[120:123], v[212:215]\n"
  "ds_read_b128 v[116:119], v232 offset:8704\n"
  "v_pk_add_f16 v39, v7, v23\n"
  "v_mfma_f32_16x16x32_f16 v[224:227], v[92:95], v[120:123], v[224:227]\n"
  "s_waitcnt lgkmcnt(6)\n"
  "ds_read_b64_tr_b16 v[30:31], v231 offset:8448\n"
  "v_pk_add_f16 v40, v8, v24\n"
  "v_mfma_f32_16x16x32_f16 a[96:99], v[48:51], v[96:99], a[96:99]\n"
  "v_pk_add_f16 v120, v96, v100\n"
  "v_pk_add_f16 v121, v97, v101\n"
  "v_mfma_f32_16x16x32_f16 a[108:111], v[52:55], v[96:99], a[108:111]\n"
  "v_pk_add_f16 v122, v98, v102\n"
  "v_pk_add_f16 v123, v99, v103\n"
  "v_mfma_f32_16x16x32_f16 a[120:123], v[56:59], v[96:99], a[120:123]\n"
  "v_pk_add_f16 v41, v9, v25\n"
  "v_pk_add_f16 v42, v10, v26\n"
  "v_mfma_f32_16x16x32_f16 a[132:135], v[60:63], v[96:99], a[132:135]\n"
  "ds_read_b128 v[96:99], v232 offset:0\n"
  "v_pk_add_f16 v43, v11, v27\n"
  "v_mfma_f32_16x16x32_f16 a[100:103], v[64:67], v[100:103], a[100:103]\n"
  "v_add_u32 v230, s49, v230\n"
  "v_add_u32 v231, s49, v231\n"
  "v_mfma_f32_16x16x32_f16 a[112:115], v[68:71], v[100:103], a[112:115]\n"
  "v_mfma_f32_16x16x32_f16 a[124:127], v[72:75], v[100:103], a[124:127]\n"
  "s_waitcnt lgkmcnt(1)\n"
  "v_pk_add_f16 v44, v12, v28\n"
  "v_pk_add_f16 v45, v13, v29\n"
  "v_mfma_f32_16x16x32_f16 a[136:139], v[76:79], v[100:103], a[136:139]\n"
  "ds_read_b128 v[100:103], v232 offset:512\n"
  "v_pk_add_f16 v46, v14, v30\n"
  "v_mfma_f32_16x16x32_f16 a[104:107], v[80:83], v[120:123], a[104:107]\n"
  "v_pk_add_f16 v47, v15, v31\n"
  "v_mfma_f32_16x16x32_f16 a[116:119], v[84:87], v[120:123], a[116:119]\n"
  "v_mfma_f32_16x16x32_f16 a[128:131], v[88:91], v[120:123], a[128:131]\n"
  "v_mfma_f32_16x16x32_f16 a[140:143], v[92:95], v[120:123], a[140:143]\n"
  "v_mfma_f32_16x16x32_f16 a[144:147], v[48:51], v[104:107], a[144:147]\n"
  "v_pk_add_f16 v120, v104, v108\n"
  "v_pk_add_f16 v121, v105, v109\n"
  "v_mfma_f32_16x16x32_f16 a[156:159], v[52:55], v[104:107], a[156:159]\n"
  "v_pk_add_f16 v122, v106, v110\n"
  "v_pk_add_f16 v123, v107, v111\n"
  "v_mfma_f32_16x16x32_f16 a[168:171], v[56:59], v[104:107], a[168:171]\n"
  "v_mfma_f32_16x16x32_f16 a[180:183], v[60:63], v[104:107], a[180:183]\n"
  "ds_read_b128 v[104:107], v232 offset:2048\n"
  "v_mfma_f32_16x16x32_f16 a[148:151], v[64:67], v[108:111], a[148:151]\n"
  "s_add_i32 s47, s44, s46\n"
  "s_add_i32 s50, s50, 4194304\n"
  "v_mfma_f32_16x16x32_f16 a[160:163], v[68:71], v[108:111], a[160:163]\n"
  "s_add_i32 s48, s47, s45\n"
  "s_add_i32 s46, s46, s49\n"
  "v_mfma_f32_16x16x32_f16 a[172:175], v[72:75], v[108:111], a[172:175]\n"
  "v_mfma_f32_16x16x32_f16 a[184:187], v[76:79], v[108:111], a[184:187]\n"
  "ds_read_b128 v[108:111], v232 offset:2560\n"
  "v_add_u32 v232, s49, v232\n"
  "v_mfma_f32_16x16x32_f16 a[152:155], v[80:83], v[120:123], a[152:155]\n"
  "v_mfma_f32_16x16x32_f16 a[164:167], v[84:87], v[120:123], a[164:167]\n"
  "v_mfma_f32_16x16x32_f16 a[176:179], v[88:91], v[120:123], a[176:179]\n"
  "v_mfma_f32_16x16x32_f16 a[188:191], v[92:95], v[120:123], a[188:191]\n"
  "v_mfma_f32_16x16x32_f16 a[192:195], v[48:51], v[112:115], a[192:195]\n"
  "v_pk_add_f16 v120, v112, v116\n"
  "v_pk_add_f16 v121, v113, v117\n"
  "v_mfma_f32_16x16x32_f16 a[204:207], v[52:55], v[112:115], a[204:207]\n"
  "v_pk_add_f16 v122, v114, v118\n"
  "v_pk_add_f16 v123, v115, v119\n"
  "v_mfma_f32_16x16x32_f16 a[216:219], v[56:59], v[112:115], a[216:219]\n"
  "v_mfma_f32_16x16x32_f16 a[228:231], v[60:63], v[112:115], a[228:231]\n"
  "v_mfma_f32_16x16x32_f16 a[196:199], v[64:67], v[116:119], a[196:199]\n"
  "v_mfma_f32_16x16x32_f16 a[208:211], v[68:71], v[116:119], a[208:211]\n"
  "v_mfma_f32_16x16x32_f16 a[220:223], v[72:75], v[116:119], a[220:223]\n"
  "v_mfma_f32_16x16x32_f16 a[232:235], v[76:79], v[116:119], a[232:235]\n"
  "v_mfma_f32_16x16x32_f16 a[200:203], v[80:83], v[120:123], a[200:203]\n"
  "s_waitcnt vmcnt(2) lgkmcnt(0)\n"
  "v_mfma_f32_16x16x32_f16 a[212:215], v[84:87], v[120:123], a[212:215]\n"
  "v_mfma_f32_16x16x32_f16 a[224:227], v[88:91], v[120:123], a[224:227]\n"
  "s_barrier\n"
  "v_mfma_f32_16x16x32_f16 a[236:239], v[92:95], v[120:123], a[236:239]\n"
  "v_mfma_f32_16x16x32_f16 v[124:127], v[0:3], v[96:99], v[124:127]\n"
  "v_pk_add_f16 v120, v96, v100\n"
  "v_pk_add_f16 v121, v97, v101\n"
  "v_mfma_f32_16x16x32_f16 v[136:139], v[4:7], v[96:99], v[136:139]\n"
  "v_pk_add_f16 v122, v98, v102\n"
  "v_pk_add_f16 v123, v99, v103\n"
  "v_mfma_f32_16x16x32_f16 v[148:151], v[8:11], v[96:99], v[148:151]\n"
  "ds_read_b128 v[112:115], v232 offset:0\n"
  "v_add_u32 v242, 57344, v232\n"
  "v_mfma_f32_16x16x32_f16 v[160:163], v[12:15], v[96:99], v[160:163]\n"
  "s_mov_b32 s66, 0x55555555\n"
  "s_mov_b32 s67, 0x55555555\n"
  "ds_read_b128 v[116:119], v232 offset:512\n"
  "v_mfma_f32_16x16x32_f16 v[128:131], v[16:19], v[100:103], v[128:131]\n"
  "ds_read_b64_tr_b16 v[48:49], v228 offset:0\n"
  "ds_read_b64_tr_b16 v[50:51], v228 offset:8192\n"
  "v_mfma_f32_16x16x32_f16 v[140:143], v[20:23], v[100:103], v[140:143]\n"
  "ds_read_b64_tr_b16 v[64:65], v228 offset:256\n"
  "ds_read_b64_tr_b16 v[66:67], v228 offset:8448\n"
  "v_mfma_f32_16x16x32_f16 v[152:155], v[24:27], v[100:103], v[152:155]\n"
  "ds_read_b64_tr_b16 v[52:53], v229 offset:0\n"
  "ds_read_b64_tr_b16 v[54:55], v229 offset:8192\n"
  "v_mfma_f32_16x16x32_f16 v[164:167], v[28:31], v[100:103], v[164:167]\n"
  "ds_read_b64_tr_b16 v[68:69], v229 offset:256\n"
  "ds_read_b64_tr_b16 v[70:71], v229 offset:8448\n"
  "v_mfma_f32_16x16x32_f16 v[132:135], v[32:35], v[120:123], v[132:135]\n"
  "ds_read_b64_tr_b16 v[56:57], v230 offset:0\n"
  "ds_read_b64_tr_b16 v[58:59], v230 offset:8192\n"
  "v_mfma_f32_16x16x32_f16 v[144:147], v[36:39], v[120:123], v[144:147]\n"
  "ds_read_b64_tr_b16 v[72:73], v230 offset:256\n"
  "ds_read_b64_tr_b16 v[74:75], v230 offset:8448\n"
  "v_mfma_f32_16x16x32_f16 v[156:159], v[40:43], v[120:123], v[156:159]\n"
  "ds_read_b64_tr_b16 v[60:61], v231 offset:0\n"
  "ds_read_b64_tr_b16 v[62:63], v231 offset:8192\n"
  "v_mfma_f32_16x16x32_f16 v[168:171], v[44:47], v[120:123], v[168:171]\n"
  "ds_read_b64_tr_b16 v[76:77], v231 offset:256\n"
  "ds_read_b64_tr_b16 v[78:79], v231 offset:8448\n"
  "v_mfma_f32_16x16x32_f16 v[180:183], v[0:3], v[104:107], v[180:183]\n"
  "s_waitcnt lgkmcnt(12)\n"
  "v_pk_add_f16 v120, v104, v108\n"
  "v_pk_add_f16 v121, v105, v109\n"
  "v_mfma_f32_16x16x32_f16 v[192:195], v[4:7], v[104:107], v[192:195]\n"
  "v_pk_add_f16 v122, v106, v110\n"
  "v_pk_add_f16 v123, v107, v111\n"
  "v_mfma_f32_16x16x32_f16 v[204:207], v[8:11], v[104:107], v[204:207]\n"
  "s_waitcnt lgkmcnt(8)\n"
  "v_pk_add_f16 v80, v48, v64\n"
  "v_pk_add_f16 v81, v49, v65\n"
  "v_mfma_f32_16x16x32_f16 v[216:219], v[12:15], v[104:107], v[216:219]\n"
  "v_pk_add_f16 v82, v50, v66\n"
  "v_pk_add_f16 v83, v51, v67\n"
  "v_mfma_f32_16x16x32_f16 v[184:187], v[16:19], v[108:111], v[184:187]\n"
  "s_waitcnt lgkmcnt(4)\n"
  "ds_read_b128 v[96:99], v232 offset:2048\n"
  "v_pk_add_f16 v84, v52, v68\n"
  "v_mfma_f32_16x16x32_f16 v[196:199], v[20:23], v[108:111], v[196:199]\n"
  "ds_read_b128 v[100:103], v232 offset:2560\n"
  "v_pk_add_f16 v85, v53, v69\n"
  "v_mfma_f32_16x16x32_f16 v[208:211], v[24:27], v[108:111], v[208:211]\n"
  "s_waitcnt lgkmcnt(2)\n"
  "v_pk_add_f16 v86, v54, v70\n"
  "v_pk_add_f16 v87, v55, v71\n"
  "v_mfma_f32_16x16x32_f16 v[220:223], v[28:31], v[108:111], v[220:223]\n"
  "v_pk_add_f16 v88, v56, v72\n"
  "v_pk_add_f16 v89, v57, v73\n"
  "v_mfma_f32_16x16x32_f16 v[188:191], v[32:35], v[120:123], v[188:191]\n"
  "v_pk_add_f16 v90, v58, v74\n"
  "v_pk_add_f16 v91, v59, v75\n"
  "v_mfma_f32_16x16x32_f16 v[200:203], v[36:39], v[120:123], v[200:203]\n"
  "v_pk_add_f16 v92, v60, v76\n"
  "v_pk_add_f16 v93, v61, v77\n"
  "v_mfma_f32_16x16x32_f16 v[212:215], v[40:43], v[120:123], v[212:215]\n"
  "v_pk_add_f16 v94, v62, v78\n"
  "v_pk_add_f16 v95, v63, v79\n"
  "v_mfma_f32_16x16x32_f16 v[224:227], v[44:47], v[120:123], v[224:227]\n"
  "v_mfma_f32_16x16x32_f16 v[124:127], v[48:51], v[112:115], v[124:127]\n"
  "v_pk_add_f16 v120, v112, v116\n"
  "v_pk_add_f16 v121, v113, v117\n"
  "v_mfma_f32_16x16x32_f16 v[136:139], v[52:55], v[112:115], v[136:139]\n"
  "v_pk_add_f16 v122, v114, v118\n"
  "v_pk_add_f16 v123, v115, v119\n"
  "v_mfma_f32_16x16x32_f16 v[128:131], v[64:67], v[116:119], v[128:131]\n"
  "ds_read_b128 v[104:107], v232 offset:4096\n"
  "ds_read_b128 v[108:111], v232 offset:4608\n"
  "v_mfma_f32_16x16x32_f16 v[140:143], v[68:71], v[116:119], v[140:143]\n"
  "v_mfma_f32_16x16x32_f16 v[132:135], v[80:83], v[120:123], v[132:135]\n"
  "v_mfma_f32_16x16x32_f16 v[144:147], v[84:87], v[120:123], v[144:147]\n"
  "v_mfma_f32_16x16x32_f16 v[148:151], v[56:59], v[112:115], v[148:151]\n"
  "v_mfma_f32_16x16x32_f16 v[160:163], v[60:63], v[112:115], v[160:163]\n"
  "v_fmac_f32 v124, s76, v132\n"
  "v_fmac_f32 v128, s76, v132\n"
  "v_mfma_f32_16x16x32_f16 v[152:155], v[72:75], v[116:119], v[152:155]\n"
  "s_waitcnt lgkmcnt(2)\n"
  "v_fmac_f32 v125, s76, v133\n"
  "v_fmac_f32 v129, s76, v133\n"
  "v_mfma_f32_16x16x32_f16 v[164:167], v[76:79], v[116:119], v[164:167]\n"
  "v_fmac_f32 v126, s76, v134\n"
  "v_fmac_f32 v130, s76, v134\n"
  "v_mfma_f32_16x16x32_f16 v[156:159], v[88:91], v[120:123], v[156:159]\n"
  "v_fmac_f32 v127, s76, v135\n"
  "v_fmac_f32 v131, s76, v135\n"
  "v_mfma_f32_16x16x32_f16 v[168:171], v[92:95], v[120:123], v[168:171]\n"
  "v_cvt_pk_bf16_f32 v172, v124, v125\n"
  "v_cvt_pk_bf16_f32 v173, v126, v127\n"
  "v_mfma_f32_16x16x32_f16 v[180:183], v[48:51], v[96:99], v[180:183]\n"
  "v_pk_add_f16 v120, v96, v100\n"
  "v_pk_add_f16 v121, v97, v101\n"
  "v_mfma_f32_16x16x32_f16 v[192:195], v[52:55], v[96:99], v[192:195]\n"
  "v_pk_add_f16 v122, v98, v102\n"
  "v_pk_add_f16 v123, v99, v103\n"
  "v_mfma_f32_16x16x32_f16 v[184:187], v[64:67], v[100:103], v[184:187]\n"
  "ds_read_b128 v[112:115], v242 offset:4096\n"
  "v_cvt_pk_bf16_f32 v176, v128, v129\n"
  "v_cvt_pk_bf16_f32 v177, v130, v131\n"
  "v_fmac_f32 v136, s76, v144\n"
  "v_fmac_f32 v140, s76, v144\n"
  "v_mfma_f32_16x16x32_f16 v[196:199], v[68:71], v[100:103], v[196:199]\n"
  "ds_read_b128 v[116:119], v242 offset:4608\n"
  "v_fmac_f32 v137, s76, v145\n"
  "v_fmac_f32 v141, s76, v145\n"
  "v_fmac_f32 v138, s76, v146\n"
  "v_fmac_f32 v142, s76, v146\n"
  "v_mfma_f32_16x16x32_f16 v[188:191], v[80:83], v[120:123], v[188:191]\n"
  "v_fmac_f32 v139, s76, v147\n"
  "v_fmac_f32 v143, s76, v147\n"
  "v_cvt_pk_bf16_f32 v174, v136, v137\n"
  "v_cvt_pk_bf16_f32 v175, v138, v139\n"
  "v_cvt_pk_bf16_f32 v178, v140, v141\n"
  "v_mfma_f32_16x16x32_f16 v[200:203], v[84:87], v[120:123], v[200:203]\n"
  "v_cvt_pk_bf16_f32 v179, v142, v143\n"
  "v_fmac_f32 v148, s76, v156\n"
  "v_fmac_f32 v152, s76, v156\n"
  "v_fmac_f32 v149, s76, v157\n"
  "v_fmac_f32 v153, s76, v157\n"
  "v_mfma_f32_16x16x32_f16 v[204:207], v[56:59], v[96:99], v[204:207]\n"
  "s_mov_b64 vcc, s[66:67]\n"
  "v_cndmask_b32_dpp v234, v176, v172, vcc quad_perm:[1,0,3,2] row_mask:0xf bank_mask:0xf\n"
  "v_cndmask_b32_dpp v235, v177, v173, vcc quad_perm:[1,0,3,2] row_mask:0xf bank_mask:0xf\n"
  "v_cndmask_b32_dpp v236, v178, v174, vcc quad_perm:[1,0,3,2] row_mask:0xf bank_mask:0xf\n"
  "v_cndmask_b32_dpp v237, v179, v175, vcc quad_perm:[1,0,3,2] row_mask:0xf bank_mask:0xf\n"
  "v_fmac_f32 v150, s76, v158\n"
  "v_mfma_f32_16x16x32_f16 v[216:219], v[60:63], v[96:99], v[216:219]\n"
  "s_add_i32 s64, s80, 0\n"
  "buffer_store_dwordx4 v[234:237], v250, s[72:75], s64 offen sc1\n"
  "s_not_b64 vcc, vcc\n"
  "v_cndmask_b32_dpp v238, v172, v176, vcc quad_perm:[1,0,3,2] row_mask:0xf bank_mask:0xf\n"
  "v_cndmask_b32_dpp v239, v173, v177, vcc quad_perm:[1,0,3,2] row_mask:0xf bank_mask:0xf\n"
  "v_cndmask_b32_dpp v240, v174, v178, vcc quad_perm:[1,0,3,2] row_mask:0xf bank_mask:0xf\n"
  "v_cndmask_b32_dpp v241, v175, v179, vcc quad_perm:[1,0,3,2] row_mask:0xf bank_mask:0xf\n"
  "v_mfma_f32_16x16x32_f16 v[208:211], v[72:75], v[100:103], v[208:211]\n"
  "s_waitcnt lgkmcnt(2)\n"
  "s_add_i32 s65, s80, 65536\n"
  "buffer_store_dwordx4 v[238:241], v250, s[72:75], s65 offen sc1\n"
  "v_fmac_f32 v154, s76, v158\n"
  "v_fmac_f32 v151, s76, v159\n"
  "v_fmac_f32 v155, s76, v159\n"
  "v_cvt_pk_bf16_f32 v172, v148, v149\n"
  "v_mfma_f32_16x16x32_f16 v[220:223], v[76:79], v[100:103], v[220:223]\n"
  "v_cvt_pk_bf16_f32 v173, v150, v151\n"
  "v_cvt_pk_bf16_f32 v176, v152, v153\n"
  "v_cvt_pk_bf16_f32 v177, v154, v155\n"
  "v_fmac_f32 v160, s76, v168\n"
  "v_fmac_f32 v164, s76, v168\n"
  "v_mfma_f32_16x16x32_f16 v[212:215], v[88:91], v[120:123], v[212:215]\n"
  "v_fmac_f32 v161, s76, v169\n"
  "v_fmac_f32 v165, s76, v169\n"
  "v_fmac_f32 v162, s76, v170\n"
  "v_fmac_f32 v166, s76, v170\n"
  "v_fmac_f32 v163, s76, v171\n"
  "v_mfma_f32_16x16x32_f16 v[224:227], v[92:95], v[120:123], v[224:227]\n"
  "v_fmac_f32 v167, s76, v171\n"
  "v_cvt_pk_bf16_f32 v174, v160, v161\n"
  "v_cvt_pk_bf16_f32 v175, v162, v163\n"
  "v_cvt_pk_bf16_f32 v178, v164, v165\n"
  "v_cvt_pk_bf16_f32 v179, v166, v167\n"
  "v_mfma_f32_16x16x32_f16 a[96:99], v[0:3], v[104:107], a[96:99]\n"
  "ds_read_b128 v[96:99], v232 offset:6144\n"
  "v_pk_add_f16 v120, v104, v108\n"
  "v_pk_add_f16 v121, v105, v109\n"
  "v_pk_add_f16 v122, v106, v110\n"
  "v_pk_add_f16 v123, v107, v111\n"
  "v_mfma_f32_16x16x32_f16 a[108:111], v[4:7], v[104:107], a[108:111]\n"
  "ds_read_b128 v[100:103], v232 offset:6656\n"
  "s_mov_b64 vcc, s[66:67]\n"
  "v_cndmask_b32_dpp v234, v176, v172, vcc quad_perm:[1,0,3,2] row_mask:0xf bank_mask:0xf\n"
  "v_cndmask_b32_dpp v235, v177, v173, vcc quad_perm:[1,0,3,2] row_mask:0xf bank_mask:0xf\n"
  "v_cndmask_b32_dpp v236, v178, v174, vcc quad_perm:[1,0,3,2] row_mask:0xf bank_mask:0xf\n"
  "v_cndmask_b32_dpp v237, v179, v175, vcc quad_perm:[1,0,3,2] row_mask:0xf bank_mask:0xf\n"
  "v_mfma_f32_16x16x32_f16 a[120:123], v[8:11], v[104:107], a[120:123]\n"
  "s_add_i32 s64, s80, 4096\n"
  "buffer_store_dwordx4 v[234:237], v250, s[72:75], s64 offen sc1\n"
  "s_not_b64 vcc, vcc\n"
  "v_cndmask_b32_dpp v238, v172, v176, vcc quad_perm:[1,0,3,2] row_mask:0xf bank_mask:0xf\n"
  "v_cndmask_b32_dpp v239, v173, v177, vcc quad_perm:[1,0,3,2] row_mask:0xf bank_mask:0xf\n"
  "v_cndmask_b32_dpp v240, v174, v178, vcc quad_perm:[1,0,3,2] row_mask:0xf bank_mask:0xf\n"
  "v_cndmask_b32_dpp v241, v175, v179, vcc quad_perm:[1,0,3,2] row_mask:0xf bank_mask:0xf\n"
  "v_mfma_f32_16x16x32_f16 a[132:135], v[12:15], v[104:107], a[132:135]\n"
  "s_add_i32 s65, s80, 69632\n"
  "buffer_store_dwordx4 v[238:241], v250, s[72:75], s65 offen sc1\n"
  "v_fmac_f32 v180, s76, v188\n"
  "v_fmac_f32 v184, s76, v188\n"
  "v_fmac_f32 v181, s76, v189\n"
  "v_fmac_f32 v185, s76, v189\n"
  "v_mfma_f32_16x16x32_f16 a[100:103], v[16:19], v[108:111], a[100:103]\n"
  "v_fmac_f32 v182, s76, v190\n"
  "v_fmac_f32 v186, s76, v190\n"
  "v_fmac_f32 v183, s76, v191\n"
  "v_fmac_f32 v187, s76, v191\n"
  "v_cvt_pk_bf16_f32 v172, v180, v181\n"
  "v_mfma_f32_16x16x32_f16 a[112:115], v[20:23], v[108:111], a[112:115]\n"
  "v_cvt_pk_bf16_f32 v173, v182, v183\n"
  "v_cvt_pk_bf16_f32 v176, v184, v185\n"
  "v_cvt_pk_bf16_f32 v177, v186, v187\n"
  "v_fmac_f32 v192, s76, v200\n"
  "v_fmac_f32 v196, s76, v200\n"
  "v_mfma_f32_16x16x32_f16 a[124:127], v[24:27], v[108:111], a[124:127]\n"
  "v_fmac_f32 v193, s76, v201\n"
  "v_fmac_f32 v197, s76, v201\n"
  "v_fmac_f32 v194, s76, v202\n"
  "v_fmac_f32 v198, s76, v202\n"
  "v_fmac_f32 v195, s76, v203\n"
  "v_mfma_f32_16x16x32_f16 a[136:139], v[28:31], v[108:111], a[136:139]\n"
  "v_fmac_f32 v199, s76, v203\n"
  "v_cvt_pk_bf16_f32 v174, v192, v193\n"
  "v_cvt_pk_bf16_f32 v175, v194, v195\n"
  "v_cvt_pk_bf16_f32 v178, v196, v197\n"
  "v_cvt_pk_bf16_f32 v179, v198, v199\n"
  "v_mfma_f32_16x16x32_f16 a[104:107], v[32:35], v[120:123], a[104:107]\n"
  "s_waitcnt lgkmcnt(2)\n"
  "s_mov_b64 vcc, s[66:67]\n"
  "v_cndmask_b32_dpp v234, v176, v172, vcc quad_perm:[1,0,3,2] row_mask:0xf bank_mask:0xf\n"
  "v_cndmask_b32_dpp v235, v177, v173, vcc quad_perm:[1,0,3,2] row_mask:0xf bank_mask:0xf\n"
  "v_cndmask_b32_dpp v236, v178, v174, vcc quad_perm:[1,0,3,2] row_mask:0xf bank_mask:0xf\n"
  "v_cndmask_b32_dpp v237, v179, v175, vcc quad_perm:[1,0,3,2] row_mask:0xf bank_mask:0xf\n"
  "v_fmac_f32 v204, s76, v212\n"
  "v_mfma_f32_16x16x32_f16 a[116:119], v[36:39], v[120:123], a[116:119]\n"
  "s_add_i32 s64, s81, 0\n"
  "buffer_store_dwordx4 v[234:237], v250, s[72:75], s64 offen sc1\n"
  "s_not_b64 vcc, vcc\n"
  "v_cndmask_b32_dpp v238, v172, v176, vcc quad_perm:[1,0,3,2] row_mask:0xf bank_mask:0xf\n"
  "v_cndmask_b32_dpp v239, v173, v177, vcc quad_perm:[1,0,3,2] row_mask:0xf bank_mask:0xf\n"
  "v_cndmask_b32_dpp v240, v174, v178, vcc quad_perm:[1,0,3,2] row_mask:0xf bank_mask:0xf\n"
  "v_cndmask_b32_dpp v241, v175, v179, vcc quad_perm:[1,0,3,2] row_mask:0xf bank_mask:0xf\n"
  "v_mfma_f32_16x16x32_f16 a[128:131], v[40:43], v[120:123], a[128:131]\n"
  "s_add_i32 s65, s81, 65536\n"
  "buffer_store_dwordx4 v[238:241], v250, s[72:75], s65 offen sc1\n"
  "v_fmac_f32 v208, s76, v212\n"
  "v_fmac_f32 v205, s76, v213\n"
  "v_fmac_f32 v209, s76, v213\n"
  "v_fmac_f32 v206, s76, v214\n"
  "v_mfma_f32_16x16x32_f16 a[140:143], v[44:47], v[120:123], a[140:143]\n"
  "v_fmac_f32 v210, s76, v214\n"
  "v_fmac_f32 v207, s76, v215\n"
  "v_fmac_f32 v211, s76, v215\n"
  "v_cvt_pk_bf16_f32 v172, v204, v205\n"
  "v_cvt_pk_bf16_f32 v173, v206, v207\n"
  "v_mfma_f32_16x16x32_f16 a[96:99], v[48:51], v[112:115], a[96:99]\n"
  "ds_read_b128 v[104:107], v242 offset:6144\n"
  "v_pk_add_f16 v120, v112, v116\n"
  "v_pk_add_f16 v121, v113, v117\n"
  "v_pk_add_f16 v122, v114, v118\n"
  "v_pk_add_f16 v123, v115, v119\n"
  "v_mfma_f32_16x16x32_f16 a[108:111], v[52:55], v[112:115], a[108:111]\n"
  "ds_read_b128 v[108:111], v242 offset:6656\n"
  "v_cvt_pk_bf16_f32 v176, v208, v209\n"
  "v_cvt_pk_bf16_f32 v177, v210, v211\n"
  "v_fmac_f32 v216, s76, v224\n"
  "v_fmac_f32 v220, s76, v224\n"
  "v_mfma_f32_16x16x32_f16 a[100:103], v[64:67], v[116:119], a[100:103]\n"
  "v_fmac_f32 v217, s76, v225\n"
  "v_fmac_f32 v221, s76, v225\n"
  "v_fmac_f32 v218, s76, v226\n"
  "v_fmac_f32 v222, s76, v226\n"
  "v_fmac_f32 v219, s76, v227\n"
  "v_mfma_f32_16x16x32_f16 a[112:115], v[68:71], v[116:119], a[112:115]\n"
  "v_fmac_f32 v223, s76, v227\n"
  "v_cvt_pk_bf16_f32 v174, v216, v217\n"
  "v_cvt_pk_bf16_f32 v175, v218, v219\n"
  "v_cvt_pk_bf16_f32 v178, v220, v221\n"
  "v_cvt_pk_bf16_f32 v179, v222, v223\n"
  "v_mfma_f32_16x16x32_f16 a[104:107], v[80:83], v[120:123], a[104:107]\n"
  "s_mov_b64 vcc, s[66:67]\n"
  "v_cndmask_b32_dpp v234, v176, v172, vcc quad_perm:[1,0,3,2] row_mask:0xf bank_mask:0xf\n"
  "v_cndmask_b32_dpp v235, v177, v173, vcc quad_perm:[1,0,3,2] row_mask:0xf bank_mask:0xf\n"
  "v_cndmask_b32_dpp v236, v178, v174, vcc quad_perm:[1,0,3,2] row_mask:0xf bank_mask:0xf\n"
  "v_cndmask_b32_dpp v237, v179, v175, vcc quad_perm:[1,0,3,2] row_mask:0xf bank_mask:0xf\n"
  "v_accvgpr_read_b32 v124, a96\n"
  "v_mfma_f32_16x16x32_f16 a[116:119], v[84:87], v[120:123], a[116:119]\n"
  "s_add_i32 s64, s81, 4096\n"
  "buffer_store_dwordx4 v[234:237], v250, s[72:75], s64 offen sc1\n"
  "s_not_b64 vcc, vcc\n"
  "v_cndmask_b32_dpp v238, v172, v176, vcc quad_perm:[1,0,3,2] row_mask:0xf bank_mask:0xf\n"
  "v_cndmask_b32_dpp v239, v173, v177, vcc quad_perm:[1,0,3,2] row_mask:0xf bank_mask:0xf\n"
  "v_cndmask_b32_dpp v240, v174, v178, vcc quad_perm:[1,0,3,2] row_mask:0xf bank_mask:0xf\n"
  "v_cndmask_b32_dpp v241, v175, v179, vcc quad_perm:[1,0,3,2] row_mask:0xf bank_mask:0xf\n"
  "v_mfma_f32_16x16x32_f16 a[120:123], v[56:59], v[112:115], a[120:123]\n"
  "s_add_i32 s65, s81, 69632\n"
  "buffer_store_dwordx4 v[238:241], v250, s[72:75], s65 offen sc1\n"
  "v_accvgpr_read_b32 v125, a97\n"
  "v_accvgpr_read_b32 v126, a98\n"
  "v_accvgpr_read_b32 v127, a99\n"
  "v_accvgpr_read_b32 v128, a100\n"
  "v_mfma_f32_16x16x32_f16 a[132:135], v[60:63], v[112:115], a[132:135]\n"
  "v_accvgpr_read_b32 v129, a101\n"
  "v_accvgpr_read_b32 v130, a102\n"
  "v_accvgpr_read_b32 v131, a103\n"
  "v_accvgpr_read_b32 v132, a104\n"
  "v_accvgpr_read_b32 v133, a105\n"
  "v_mfma_f32_16x16x32_f16 a[124:127], v[72:75], v[116:119], a[124:127]\n"
  "s_waitcnt lgkmcnt(2)\n"
  "v_accvgpr_read_b32 v134, a106\n"
  "v_accvgpr_read_b32 v135, a107\n"
  "v_fmac_f32 v124, s76, v132\n"
  "v_fmac_f32 v128, s76, v132\n"
  "v_fmac_f32 v125, s76, v133\n"
  "v_mfma_f32_16x16x32_f16 a[136:139], v[76:79], v[116:119], a[136:139]\n"
  "v_fmac_f32 v129, s76, v133\n"
  "v_fmac_f32 v126, s76, v134\n"
  "v_fmac_f32 v130, s76, v134\n"
  "v_fmac_f32 v127, s76, v135\n"
  "v_fmac_f32 v131, s76, v135\n"
  "v_mfma_f32_16x16x32_f16 a[128:131], v[88:91], v[120:123], a[128:131]\n"
  "v_cvt_pk_bf16_f32 v172, v124, v125\n"
  "v_cvt_pk_bf16_f32 v173, v126, v127\n"
  "v_cvt_pk_bf16_f32 v176, v128, v129\n"
  "v_cvt_pk_bf16_f32 v177, v130, v131\n"
  "v_accvgpr_read_b32 v136, a108\n"
  "v_mfma_f32_16x16x32_f16 a[140:143], v[92:95], v[120:123], a[140:143]\n"
  "v_accvgpr_read_b32 v137, a109\n"
  "v_accvgpr_read_b32 v138, a110\n"
  "v_accvgpr_read_b32 v139, a111\n"
  "v_accvgpr_read_b32 v140, a112\n"
  "v_accvgpr_read_b32 v141, a113\n"
  "v_mfma_f32_16x16x32_f16 a[144:147], v[0:3], v[96:99], a[144:147]\n"
  "ds_read_b128 v[112:115], v232 offset:8192\n"
  "v_pk_add_f16 v120, v96, v100\n"
  "v_pk_add_f16 v121, v97, v101\n"
  "v_pk_add_f16 v122, v98, v102\n"
  "v_pk_add_f16 v123, v99, v103\n"
  "v_mfma_f32_16x16x32_f16 a[156:159], v[4:7], v[96:99], a[156:159]\n"
  "ds_read_b128 v[116:119], v232 offset:8704\n"
  "v_accvgpr_read_b32 v142, a114\n"
  "v_accvgpr_read_b32 v143, a115\n"
  "v_accvgpr_read_b32 v144, a116\n"
  "v_accvgpr_read_b32 v145, a117\n"
  "v_mfma_f32_16x16x32_f16 a[168:171], v[8:11], v[96:99], a[168:171]\n"
  "v_accvgpr_read_b32 v146, a118\n"
  "v_accvgpr_read_b32 v147, a119\n"
  "v_fmac_f32 v136, s76, v144\n"
  "v_fmac_f32 v140, s76, v144\n"
  "v_fmac_f32 v137, s76, v145\n"
  "v_mfma_f32_16x16x32_f16 a[180:183], v[12:15], v[96:99], a[180:183]\n"
  "v_fmac_f32 v141, s76, v145\n"
  "v_fmac_f32 v138, s76, v146\n"
  "v_fmac_f32 v142, s76, v146\n"
  "v_fmac_f32 v139, s76, v147\n"
  "v_fmac_f32 v143, s76, v147\n"
  "v_mfma_f32_16x16x32_f16 a[148:151], v[16:19], v[100:103], a[148:151]\n"
  "v_cvt_pk_bf16_f32 v174, v136, v137\n"
  "v_cvt_pk_bf16_f32 v175, v138, v139\n"
  "v_cvt_pk_bf16_f32 v178, v140, v141\n"
  "v_cvt_pk_bf16_f32 v179, v142, v143\n"
  "v_accvgpr_read_b32 v148, a120\n"
  "v_mfma_f32_16x16x32_f16 a[160:163], v[20:23], v[100:103], a[160:163]\n"
  "s_mov_b64 vcc, s[66:67]\n"
  "v_cndmask_b32_dpp v234, v176, v172, vcc quad_perm:[1,0,3,2] row_mask:0xf bank_mask:0xf\n"
  "v_cndmask_b32_dpp v235, v177, v173, vcc quad_perm:[1,0,3,2] row_mask:0xf bank_mask:0xf\n"
  "v_cndmask_b32_dpp v236, v178, v174, vcc quad_perm:[1,0,3,2] row_mask:0xf bank_mask:0xf\n"
  "v_cndmask_b32_dpp v237, v179, v175, vcc quad_perm:[1,0,3,2] row_mask:0xf bank_mask:0xf\n"
  "v_accvgpr_read_b32 v149, a121\n"
  "v_mfma_f32_16x16x32_f16 a[172:175], v[24:27], v[100:103], a[172:175]\n"
  "s_add_i32 s64, s82, 0\n"
  "buffer_store_dwordx4 v[234:237], v250, s[72:75], s64 offen sc1\n"
  "s_not_b64 vcc, vcc\n"
  "v_cndmask_b32_dpp v238, v172, v176, vcc quad_perm:[1,0,3,2] row_mask:0xf bank_mask:0xf\n"
  "v_cndmask_b32_dpp v239, v173, v177, vcc quad_perm:[1,0,3,2] row_mask:0xf bank_mask:0xf\n"
  "v_cndmask_b32_dpp v240, v174, v178, vcc quad_perm:[1,0,3,2] row_mask:0xf bank_mask:0xf\n"
  "v_cndmask_b32_dpp v241, v175, v179, vcc quad_perm:[1,0,3,2] row_mask:0xf bank_mask:0xf\n"
  "v_mfma_f32_16x16x32_f16 a[184:187], v[28:31], v[100:103], a[184:187]\n"
  "s_add_i32 s65, s82, 65536\n"
  "buffer_store_dwordx4 v[238:241], v250, s[72:75], s65 offen sc1\n"
  "v_accvgpr_read_b32 v150, a122\n"
  "v_accvgpr_read_b32 v151, a123\n"
  "v_accvgpr_read_b32 v152, a124\n"
  "v_accvgpr_read_b32 v153, a125\n"
  "v_mfma_f32_16x16x32_f16 a[152:155], v[32:35], v[120:123], a[152:155]\n"
  "s_waitcnt lgkmcnt(2)\n"
  "v_accvgpr_read_b32 v154, a126\n"
  "v_accvgpr_read_b32 v155, a127\n"
  "v_accvgpr_read_b32 v156, a128\n"
  "v_accvgpr_read_b32 v157, a129\n"
  "v_accvgpr_read_b32 v158, a130\n"
  "v_mfma_f32_16x16x32_f16 a[164:167], v[36:39], v[120:123], a[164:167]\n"
  "v_accvgpr_read_b32 v159, a131\n"
  "v_fmac_f32 v148, s76, v156\n"
  "v_fmac_f32 v152, s76, v156\n"
  "v_fmac_f32 v149, s76, v157\n"
  "v_fmac_f32 v153, s76, v157\n"
  "v_mfma_f32_16x16x32_f16 a[176:179], v[40:43], v[120:123], a[176:179]\n"
  "v_fmac_f32 v150, s76, v158\n"
  "v_fmac_f32 v154, s76, v158\n"
  "v_fmac_f32 v151, s76, v159\n"
  "v_fmac_f32 v155, s76, v159\n"
  "v_cvt_pk_bf16_f32 v172, v148, v149\n"
  "v_mfma_f32_16x16x32_f16 a[188:191], v[44:47], v[120:123], a[188:191]\n"
  "v_cvt_pk_bf16_f32 v173, v150, v151\n"
  "v_cvt_pk_bf16_f32 v176, v152, v153\n"
  "v_cvt_pk_bf16_f32 v177, v154, v155\n"
  "v_accvgpr_read_b32 v160, a132\n"
  "v_accvgpr_read_b32 v161, a133\n"
  "v_mfma_f32_16x16x32_f16 a[144:147], v[48:51], v[104:107], a[144:147]\n"
  "ds_read_b128 v[96:99], v242 offset:8192\n"
  "v_pk_add_f16 v120, v104, v108\n"
  "v_pk_add_f16 v121, v105, v109\n"
  "v_pk_add_f16 v122, v106, v110\n"
  "v_pk_add_f16 v123, v107, v111\n"
  "v_mfma_f32_16x16x32_f16 a[156:159], v[52:55], v[104:107], a[156:159]\n"
  "ds_read_b128 v[100:103], v242 offset:8704\n"
  "v_accvgpr_read_b32 v162, a134\n"
  "v_accvgpr_read_b32 v163, a135\n"
  "v_accvgpr_read_b32 v164, a136\n"
  "v_accvgpr_read_b32 v165, a137\n"
  "v_mfma_f32_16x16x32_f16 a[148:151], v[64:67], v[108:111], a[148:151]\n"
  "v_accvgpr_read_b32 v166, a138\n"
  "v_accvgpr_read_b32 v167, a139\n"
  "v_accvgpr_read_b32 v168, a140\n"
  "v_accvgpr_read_b32 v169, a141\n"
  "v_accvgpr_read_b32 v170, a142\n"
  "v_mfma_f32_16x16x32_f16 a[160:163], v[68:71], v[108:111], a[160:163]\n"
  "v_accvgpr_read_b32 v171, a143\n"
  "v_fmac_f32 v160, s76, v168\n"
  "v_fmac_f32 v164, s76, v168\n"
  "v_fmac_f32 v161, s76, v169\n"
  "v_fmac_f32 v165, s76, v169\n"
  "v_mfma_f32_16x16x32_f16 a[152:155], v[80:83], v[120:123], a[152:155]\n"
  "v_fmac_f32 v162, s76, v170\n"
  "v_fmac_f32 v166, s76, v170\n"
  "v_fmac_f32 v163, s76, v171\n"
  "v_fmac_f32 v167, s76, v171\n"
  "v_cvt_pk_bf16_f32 v174, v160, v161\n"
  "v_mfma_f32_16x16x32_f16 a[164:167], v[84:87], v[120:123], a[164:167]\n"
  "v_cvt_pk_bf16_f32 v175, v162, v163\n"
  "v_cvt_pk_bf16_f32 v178, v164, v165\n"
  "v_cvt_pk_bf16_f32 v179, v166, v167\n"
  "v_accvgpr_read_b32 v180, a144\n"
  "v_accvgpr_read_b32 v181, a145\n"
  "v_mfma_f32_16x16x32_f16 a[168:171], v[56:59], v[104:107], a[168:171]\n"
  "s_mov_b64 vcc, s[66:67]\n"
  "v_cndmask_b32_dpp v234, v176, v172, vcc quad_perm:[1,0,3,2] row_mask:0xf bank_mask:0xf\n"
  "v_cndmask_b32_dpp v235, v177, v173, vcc quad_perm:[1,0,3,2] row_mask:0xf bank_mask:0xf\n"
  "v_cndmask_b32_dpp v236, v178, v174, vcc quad_perm:[1,0,3,2] row_mask:0xf bank_mask:0xf\n"
  "v_cndmask_b32_dpp v237, v179, v175, vcc quad_perm:[1,0,3,2] row_mask:0xf bank_mask:0xf\n"
  "v_accvgpr_read_b32 v182, a146\n"
  "v_mfma_f32_16x16x32_f16 a[180:183], v[60:63], v[104:107], a[180:183]\n"
  "s_add_i32 s64, s82, 4096\n"
  "buffer_store_dwordx4 v[234:237], v250, s[72:75], s64 offen sc1\n"
  "s_not_b64 vcc, vcc\n"
  "v_cndmask_b32_dpp v238, v172, v176, vcc quad_perm:[1,0,3,2] row_mask:0xf bank_mask:0xf\n"
  "v_cndmask_b32_dpp v239, v173, v177, vcc quad_perm:[1,0,3,2] row_mask:0xf bank_mask:0xf\n"
  "v_cndmask_b32_dpp v240, v174, v178, vcc quad_perm:[1,0,3,2] row_mask:0xf bank_mask:0xf\n"
  "v_cndmask_b32_dpp v241, v175, v179, vcc quad_perm:[1,0,3,2] row_mask:0xf bank_mask:0xf\n"
  "v_mfma_f32_16x16x32_f16 a[172:175], v[72:75], v[108:111], a[172:175]\n"
  "s_waitcnt lgkmcnt(2)\n"
  "s_add_i32 s65, s82, 69632\n"
  "buffer_store_dwordx4 v[238:241], v250, s[72:75], s65 offen sc1\n"
  "v_accvgpr_read_b32 v183, a147\n"
  "v_accvgpr_read_b32 v184, a148\n"
  "v_accvgpr_read_b32 v185, a149\n"
  "v_accvgpr_read_b32 v186, a150\n"
  "v_mfma_f32_16x16x32_f16 a[184:187], v[76:79], v[108:111], a[184:187]\n"
  "v_accvgpr_read_b32 v187, a151\n"
  "v_accvgpr_read_b32 v188, a152\n"
  "v_accvgpr_read_b32 v189, a153\n"
  "v_accvgpr_read_b32 v190, a154\n"
  "v_accvgpr_read_b32 v191, a155\n"
  "v_mfma_f32_16x16x32_f16 a[176:179], v[88:91], v[120:123], a[176:179]\n"
  "v_fmac_f32 v180, s76, v188\n"
  "v_fmac_f32 v184, s76, v188\n"
  "v_fmac_f32 v181, s76, v189\n"
  "v_fmac_f32 v185, s76, v189\n"
  "v_fmac_f32 v182, s76, v190\n"
  "v_mfma_f32_16x16x32_f16 a[188:191], v[92:95], v[120:123], a[188:191]\n"
  "v_fmac_f32 v186, s76, v190\n"
  "v_fmac_f32 v183, s76, v191\n"
  "v_fmac_f32 v187, s76, v191\n"
  "v_cvt_pk_bf16_f32 v172, v180, v181\n"
  "v_cvt_pk_bf16_f32 v173, v182, v183\n"
  "v_mfma_f32_16x16x32_f16 a[192:195], v[0:3], v[112:115], a[192:195]\n"
  "v_pk_add_f16 v120, v112, v116\n"
  "v_pk_add_f16 v121, v113, v117\n"
  "v_pk_add_f16 v122, v114, v118\n"
  "v_pk_add_f16 v123, v115, v119\n"
  "v_cvt_pk_bf16_f32 v176, v184, v185\n"
  "v_mfma_f32_16x16x32_f16 a[204:207], v[4:7], v[112:115], a[204:207]\n"
  "v_cvt_pk_bf16_f32 v177, v186, v187\n"
  "v_accvgpr_read_b32 v192, a156\n"
  "v_accvgpr_read_b32 v193, a157\n"
  "v_accvgpr_read_b32 v194, a158\n"
  "v_accvgpr_read_b32 v195, a159\n"
  "v_mfma_f32_16x16x32_f16 a[216:219], v[8:11], v[112:115], a[216:219]\n"
  "v_accvgpr_read_b32 v196, a160\n"
  "v_accvgpr_read_b32 v197, a161\n"
  "v_accvgpr_read_b32 v198, a162\n"
  "v_accvgpr_read_b32 v199, a163\n"
  "v_accvgpr_read_b32 v200, a164\n"
  "v_mfma_f32_16x16x32_f16 a[228:231], v[12:15], v[112:115], a[228:231]\n"
  "v_accvgpr_read_b32 v201, a165\n"
  "v_accvgpr_read_b32 v202, a166\n"
  "v_accvgpr_read_b32 v203, a167\n"
  "v_fmac_f32 v192, s76, v200\n"
  "v_fmac_f32 v196, s76, v200\n"
  "v_mfma_f32_16x16x32_f16 a[196:199], v[16:19], v[116:119], a[196:199]\n"
  "v_fmac_f32 v193, s76, v201\n"
  "v_fmac_f32 v197, s76, v201\n"
  "v_fmac_f32 v194, s76, v202\n"
  "v_fmac_f32 v198, s76, v202\n"
  "v_fmac_f32 v195, s76, v203\n"
  "v_mfma_f32_16x16x32_f16 a[208:211], v[20:23], v[116:119], a[208:211]\n"
  "v_fmac_f32 v199, s76, v203\n"
  "v_cvt_pk_bf16_f32 v174, v192, v193\n"
  "v_cvt_pk_bf16_f32 v175, v194, v195\n"
  "v_cvt_pk_bf16_f32 v178, v196, v197\n"
  "v_cvt_pk_bf16_f32 v179, v198, v199\n"
  "v_mfma_f32_16x16x32_f16 a[220:223], v[24:27], v[116:119], a[220:223]\n"
  "s_mov_b64 vcc, s[66:67]\n"
  "v_cndmask_b32_dpp v234, v176, v172, vcc quad_perm:[1,0,3,2] row_mask:0xf bank_mask:0xf\n"
  "v_cndmask_b32_dpp v235, v177, v173, vcc quad_perm:[1,0,3,2] row_mask:0xf bank_mask:0xf\n"
  "v_cndmask_b32_dpp v236, v178, v174, vcc quad_perm:[1,0,3,2] row_mask:0xf bank_mask:0xf\n"
  "v_cndmask_b32_dpp v237, v179, v175, vcc quad_perm:[1,0,3,2] row_mask:0xf bank_mask:0xf\n"
  "v_accvgpr_read_b32 v204, a168\n"
  "v_mfma_f32_16x16x32_f16 a[232:235], v[28:31], v[116:119], a[232:235]\n"
  "s_add_i32 s64, s83, 0\n"
  "buffer_store_dwordx4 v[234:237], v250, s[72:75], s64 offen sc1\n"
  "s_not_b64 vcc, vcc\n"
  "v_cndmask_b32_dpp v238, v172, v176, vcc quad_perm:[1,0,3,2] row_mask:0xf bank_mask:0xf\n"
  "v_cndmask_b32_dpp v239, v173, v177, vcc quad_perm:[1,0,3,2] row_mask:0xf bank_mask:0xf\n"
  "v_cndmask_b32_dpp v240, v174, v178, vcc quad_perm:[1,0,3,2] row_mask:0xf bank_mask:0xf\n"
  "v_cndmask_b32_dpp v241, v175, v179, vcc quad_perm:[1,0,3,2] row_mask:0xf bank_mask:0xf\n"
  "v_mfma_f32_16x16x32_f16 a[200:203], v[32:35], v[120:123], a[200:203]\n"
  "s_waitcnt lgkmcnt(0)\n"
  "s_add_i32 s65, s83, 65536\n"
  "buffer_store_dwordx4 v[238:241], v250, s[72:75], s65 offen sc1\n"
  "v_accvgpr_read_b32 v205, a169\n"
  "v_accvgpr_read_b32 v206, a170\n"
  "v_accvgpr_read_b32 v207, a171\n"
  "v_accvgpr_read_b32 v208, a172\n"
  "v_mfma_f32_16x16x32_f16 a[212:215], v[36:39], v[120:123], a[212:215]\n"
  "v_accvgpr_read_b32 v209, a173\n"
  "v_accvgpr_read_b32 v210, a174\n"
  "v_accvgpr_read_b32 v211, a175\n"
  "v_accvgpr_read_b32 v212, a176\n"
  "v_accvgpr_read_b32 v213, a177\n"
  "v_mfma_f32_16x16x32_f16 a[224:227], v[40:43], v[120:123], a[224:227]\n"
  "v_accvgpr_read_b32 v214, a178\n"
  "v_accvgpr_read_b32 v215, a179\n"
  "v_fmac_f32 v204, s76, v212\n"
  "v_fmac_f32 v208, s76, v212\n"
  "v_fmac_f32 v205, s76, v213\n"
  "v_mfma_f32_16x16x32_f16 a[236:239], v[44:47], v[120:123], a[236:239]\n"
  "v_fmac_f32 v209, s76, v213\n"
  "v_fmac_f32 v206, s76, v214\n"
  "v_fmac_f32 v210, s76, v214\n"
  "v_fmac_f32 v207, s76, v215\n"
  "v_fmac_f32 v211, s76, v215\n"
  "v_mfma_f32_16x16x32_f16 a[192:195], v[48:51], v[96:99], a[192:195]\n"
  "v_pk_add_f16 v120, v96, v100\n"
  "v_pk_add_f16 v121, v97, v101\n"
  "v_pk_add_f16 v122, v98, v102\n"
  "v_pk_add_f16 v123, v99, v103\n"
  "v_cvt_pk_bf16_f32 v172, v204, v205\n"
  "v_mfma_f32_16x16x32_f16 a[204:207], v[52:55], v[96:99], a[204:207]\n"
  "v_cvt_pk_bf16_f32 v173, v206, v207\n"
  "v_cvt_pk_bf16_f32 v176, v208, v209\n"
  "v_cvt_pk_bf16_f32 v177, v210, v211\n"
  "v_accvgpr_read_b32 v216, a180\n"
  "v_accvgpr_read_b32 v217, a181\n"
  "v_mfma_f32_16x16x32_f16 a[196:199], v[64:67], v[100:103], a[196:199]\n"
  "v_accvgpr_read_b32 v218, a182\n"
  "v_accvgpr_read_b32 v219, a183\n"
  "v_accvgpr_read_b32 v220, a184\n"
  "v_accvgpr_read_b32 v221, a185\n"
  "v_accvgpr_read_b32 v222, a186\n"
  "v_mfma_f32_16x16x32_f16 a[208:211], v[68:71], v[100:103], a[208:211]\n"
  "v_accvgpr_read_b32 v223, a187\n"
  "v_accvgpr_read_b32 v224, a188\n"
  "v_accvgpr_read_b32 v225, a189\n"
  "v_accvgpr_read_b32 v226, a190\n"
  "v_accvgpr_read_b32 v227, a191\n"
  "v_mfma_f32_16x16x32_f16 a[200:203], v[80:83], v[120:123], a[200:203]\n"
  "v_fmac_f32 v216, s76, v224\n"
  "v_fmac_f32 v220, s76, v224\n"
  "v_fmac_f32 v217, s76, v225\n"
  "v_fmac_f32 v221, s76, v225\n"
  "v_fmac_f32 v218, s76, v226\n"
  "v_mfma_f32_16x16x32_f16 a[212:215], v[84:87], v[120:123], a[212:215]\n"
  "v_fmac_f32 v222, s76, v226\n"
  "v_fmac_f32 v219, s76, v227\n"
  "v_fmac_f32 v223, s76, v227\n"
  "v_cvt_pk_bf16_f32 v174, v216, v217\n"
  "v_cvt_pk_bf16_f32 v175, v218, v219\n"
  "v_mfma_f32_16x16x32_f16 a[216:219], v[56:59], v[96:99], a[216:219]\n"
  "v_cvt_pk_bf16_f32 v178, v220, v221\n"
  "v_cvt_pk_bf16_f32 v179, v222, v223\n"
  "v_accvgpr_read_b32 v124, a192\n"
  "v_accvgpr_read_b32 v125, a193\n"
  "v_accvgpr_read_b32 v126, a194\n"
  "v_mfma_f32_16x16x32_f16 a[228:231], v[60:63], v[96:99], a[228:231]\n"
  "s_mov_b64 vcc, s[66:67]\n"
  "v_cndmask_b32_dpp v234, v176, v172, vcc quad_perm:[1,0,3,2] row_mask:0xf bank_mask:0xf\n"
  "v_cndmask_b32_dpp v235, v177, v173, vcc quad_perm:[1,0,3,2] row_mask:0xf bank_mask:0xf\n"
  "v_cndmask_b32_dpp v236, v178, v174, vcc quad_perm:[1,0,3,2] row_mask:0xf bank_mask:0xf\n"
  "v_cndmask_b32_dpp v237, v179, v175, vcc quad_perm:[1,0,3,2] row_mask:0xf bank_mask:0xf\n"
  "v_accvgpr_read_b32 v127, a195\n"
  "v_mfma_f32_16x16x32_f16 a[220:223], v[72:75], v[100:103], a[220:223]\n"
  "s_add_i32 s64, s83, 4096\n"
  "buffer_store_dwordx4 v[234:237], v250, s[72:75], s64 offen sc1\n"
  "s_not_b64 vcc, vcc\n"
  "v_cndmask_b32_dpp v238, v172, v176, vcc quad_perm:[1,0,3,2] row_mask:0xf bank_mask:0xf\n"
  "v_cndmask_b32_dpp v239, v173, v177, vcc quad_perm:[1,0,3,2] row_mask:0xf bank_mask:0xf\n"
  "v_cndmask_b32_dpp v240, v174, v178, vcc quad_perm:[1,0,3,2] row_mask:0xf bank_mask:0xf\n"
  "v_cndmask_b32_dpp v241, v175, v179, vcc quad_perm:[1,0,3,2] row_mask:0xf bank_mask:0xf\n"
  "v_mfma_f32_16x16x32_f16 a[232:235], v[76:79], v[100:103], a[232:235]\n"
  "s_add_i32 s65, s83, 69632\n"
  "buffer_store_dwordx4 v[238:241], v250, s[72:75], s65 offen sc1\n"
  "v_accvgpr_read_b32 v128, a196\n"
  "v_accvgpr_read_b32 v129, a197\n"
  "v_accvgpr_read_b32 v130, a198\n"
  "v_accvgpr_read_b32 v131, a199\n"
  "v_mfma_f32_16x16x32_f16 a[224:227], v[88:91], v[120:123], a[224:227]\n"
  "v_accvgpr_read_b32 v132, a200\n"
  "v_accvgpr_read_b32 v133, a201\n"
  "v_accvgpr_read_b32 v134, a202\n"
  "v_accvgpr_read_b32 v135, a203\n"
  "v_pk_fma_f32 v[124:125], v[132:133], s[76:77], v[124:125]\n"
  "v_mfma_f32_16x16x32_f16 a[236:239], v[92:95], v[120:123], a[236:239]\n"
  "v_pk_fma_f32 v[128:129], v[132:133], s[76:77], v[128:129]\n"
  "v_pk_fma_f32 v[126:127], v[134:135], s[76:77], v[126:127]\n"
  "v_pk_fma_f32 v[130:131], v[134:135], s[76:77], v[130:131]\n"
  "v_cvt_pk_bf16_f32 v172, v124, v125\n"
  "v_cvt_pk_bf16_f32 v173, v126, v127\n"
  "v_cvt_pk_bf16_f32 v176, v128, v129\n"
  "v_cvt_pk_bf16_f32 v177, v130, v131\n"
  "v_accvgpr_read_b32 v136, a204\n"
  "v_accvgpr_read_b32 v137, a205\n"
  "v_accvgpr_read_b32 v138, a206\n"
  "v_accvgpr_read_b32 v139, a207\n"
  "v_accvgpr_read_b32 v140, a208\n"
  "v_accvgpr_read_b32 v141, a209\n"
  "v_accvgpr_read_b32 v142, a210\n"
  "v_accvgpr_read_b32 v143, a211\n"
  "v_accvgpr_read_b32 v144, a212\n"
  "v_accvgpr_read_b32 v145, a213\n"
  "v_accvgpr_read_b32 v146, a214\n"
  "v_accvgpr_read_b32 v147, a215\n"
  "v_pk_fma_f32 v[136:137], v[144:145], s[76:77], v[136:137]\n"
  "v_pk_fma_f32 v[140:141], v[144:145], s[76:77], v[140:141]\n"
  "v_pk_fma_f32 v[138:139], v[146:147], s[76:77], v[138:139]\n"
  "v_pk_fma_f32 v[142:143], v[146:147], s[76:77], v[142:143]\n"
  "v_cvt_pk_bf16_f32 v174, v136, v137\n"
  "v_cvt_pk_bf16_f32 v175, v138, v139\n"
  "v_cvt_pk_bf16_f32 v178, v140, v141\n"
  "v_cvt_pk_bf16_f32 v179, v142, v143\n"
  "v_accvgpr_read_b32 v148, a216\n"
  "v_accvgpr_read_b32 v149, a217\n"
  "v_accvgpr_read_b32 v150, a218\n"
  "s_mov_b64 vcc, s[66:67]\n"
  "v_cndmask_b32_dpp v234, v176, v172, vcc quad_perm:[1,0,3,2] row_mask:0xf bank_mask:0xf\n"
  "v_cndmask_b32_dpp v235, v177, v173, vcc quad_perm:[1,0,3,2] row_mask:0xf bank_mask:0xf\n"
  "v_cndmask_b32_dpp v236, v178, v174, vcc quad_perm:[1,0,3,2] row_mask:0xf bank_mask:0xf\n"
  "v_cndmask_b32_dpp v237, v179, v175, vcc quad_perm:[1,0,3,2] row_mask:0xf bank_mask:0xf\n"
  "v_accvgpr_read_b32 v151, a219\n"
  "s_add_i32 s64, s84, 0\n"
  "buffer_store_dwordx4 v[234:237], v250, s[72:75], s64 offen sc1\n"
  "s_not_b64 vcc, vcc\n"
  "v_cndmask_b32_dpp v238, v172, v176, vcc quad_perm:[1,0,3,2] row_mask:0xf bank_mask:0xf\n"
  "v_cndmask_b32_dpp v239, v173, v177, vcc quad_perm:[1,0,3,2] row_mask:0xf bank_mask:0xf\n"
  "v_cndmask_b32_dpp v240, v174, v178, vcc quad_perm:[1,0,3,2] row_mask:0xf bank_mask:0xf\n"
  "v_cndmask_b32_dpp v241, v175, v179, vcc quad_perm:[1,0,3,2] row_mask:0xf bank_mask:0xf\n"
  "s_add_i32 s65, s84, 65536\n"
  "buffer_store_dwordx4 v[238:241], v250, s[72:75], s65 offen sc1\n"
  "v_accvgpr_read_b32 v152, a220\n"
  "v_accvgpr_read_b32 v153, a221\n"
  "v_accvgpr_read_b32 v154, a222\n"
  "v_accvgpr_read_b32 v155, a223\n"
  "v_accvgpr_read_b32 v156, a224\n"
  "v_accvgpr_read_b32 v157, a225\n"
  "v_accvgpr_read_b32 v158, a226\n"
  "v_accvgpr_read_b32 v159, a227\n"
  "v_pk_fma_f32 v[148:149], v[156:157], s[76:77], v[148:149]\n"
  "v_pk_fma_f32 v[152:153], v[156:157], s[76:77], v[152:153]\n"
  "v_pk_fma_f32 v[150:151], v[158:159], s[76:77], v[150:151]\n"
  "v_pk_fma_f32 v[154:155], v[158:159], s[76:77], v[154:155]\n"
  "v_cvt_pk_bf16_f32 v172, v148, v149\n"
  "v_cvt_pk_bf16_f32 v173, v150, v151\n"
  "v_cvt_pk_bf16_f32 v176, v152, v153\n"
  "v_cvt_pk_bf16_f32 v177, v154, v155\n"
  "v_accvgpr_read_b32 v160, a228\n"
  "v_accvgpr_read_b32 v161, a229\n"
  "v_accvgpr_read_b32 v162, a230\n"
  "v_accvgpr_read_b32 v163, a231\n"
  "v_accvgpr_read_b32 v164, a232\n"
  "v_accvgpr_read_b32 v165, a233\n"
  "v_accvgpr_read_b32 v166, a234\n"
  "v_accvgpr_read_b32 v167, a235\n"
  "v_accvgpr_read_b32 v168, a236\n"
  "v_accvgpr_read_b32 v169, a237\n"
  "v_accvgpr_read_b32 v170, a238\n"
  "v_accvgpr_read_b32 v171, a239\n"
  "v_pk_fma_f32 v[160:161], v[168:169], s[76:77], v[160:161]\n"
  "v_pk_fma_f32 v[164:165], v[168:169], s[76:77], v[164:165]\n"
  "v_pk_fma_f32 v[162:163], v[170:171], s[76:77], v[162:163]\n"
  "v_pk_fma_f32 v[166:167], v[170:171], s[76:77], v[166:167]\n"
  "v_cvt_pk_bf16_f32 v174, v160, v161\n"
  "v_cvt_pk_bf16_f32 v175, v162, v163\n"
  "v_cvt_pk_bf16_f32 v178, v164, v165\n"
  "v_cvt_pk_bf16_f32 v179, v166, v167\n"
  "s_mov_b64 vcc, s[66:67]\n"
  "v_cndmask_b32_dpp v234, v176, v172, vcc quad_perm:[1,0,3,2] row_mask:0xf bank_mask:0xf\n"
  "v_cndmask_b32_dpp v235, v177, v173, vcc quad_perm:[1,0,3,2] row_mask:0xf bank_mask:0xf\n"
  "v_cndmask_b32_dpp v236, v178, v174, vcc quad_perm:[1,0,3,2] row_mask:0xf bank_mask:0xf\n"
  "v_cndmask_b32_dpp v237, v179, v175, vcc quad_perm:[1,0,3,2] row_mask:0xf bank_mask:0xf\n"
  "s_add_i32 s64, s84, 4096\n"
  "buffer_store_dwordx4 v[234:237], v250, s[72:75], s64 offen sc1\n"
  "s_not_b64 vcc, vcc\n"
  "v_cndmask_b32_dpp v238, v172, v176, vcc quad_perm:[1,0,3,2] row_mask:0xf bank_mask:0xf\n"
  "v_cndmask_b32_dpp v239, v173, v177, vcc quad_perm:[1,0,3,2] row_mask:0xf bank_mask:0xf\n"
  "v_cndmask_b32_dpp v240, v174, v178, vcc quad_perm:[1,0,3,2] row_mask:0xf bank_mask:0xf\n"
  "v_cndmask_b32_dpp v241, v175, v179, vcc quad_perm:[1,0,3,2] row_mask:0xf bank_mask:0xf\n"
  "s_add_i32 s65, s84, 69632\n"
  "buffer_store_dwordx4 v[238:241], v250, s[72:75], s65 offen sc1\n"
  "s_waitcnt vmcnt(20)\n"
  "s_branch .Lend_%=\n"
  ".Lpace_s0_None_%=:\n"
  "s_memrealtime s[70:71]\n"
  "s_waitcnt lgkmcnt(0)\n"
  "s_sub_i32 s64, s70, s61\n"
  "s_cmp_lt_i32 s64, 0\n"
  "s_cbranch_scc0 .Lpace2_s0_None_%=\n"
  "s_sleep 1\n"
  "s_branch .Lpace_s0_None_%=\n"
  ".Lpace2_s0_None_%=:\n"
  "s_add_i32 s61, s70, s62\n"
  "s_add_i32 s61, s61, 8\n"
  "s_sub_i32 s62, s62, 14\n"
  "s_branch .Lpaced_s0_None_%=\n"
  ".Lpace_s1_None_%=:\n"
  "s_memrealtime s[70:71]\n"
  "s_waitcnt lgkmcnt(0)\n"
  "s_sub_i32 s64, s70, s61\n"
  "s_cmp_lt_i32 s64, 0\n"
  "s_cbranch_scc0 .Lpace2_s1_None_%=\n"
  "s_sleep 1\n"
  "s_branch .Lpace_s1_None_%=\n"
  ".Lpace2_s1_None_%=:\n"
  "s_add_i32 s61, s70, s62\n"
  "s_add_i32 s61, s61, 0\n"
  "s_sub_i32 s62, s62, 14\n"
  "s_branch .Lpaced_s1_None_%=\n"
  ".Lspin_pe_None_%=:\n"
  "s_mov_b32 s64, 0\n"
  ".Lspin2_pe_None_%=:\n"
  "s_sleep 3\n"
  "buffer_load_dword v247, v246, s[40:43], s54 offen sc1\n"
  "s_add_u32 s64, s64, 1\n"
  "s_waitcnt vmcnt(0)\n"
  "s_cmp_gt_u32 s64, 0x200000\n"
  "s_cbranch_scc1 .Lspun_pe_None_%=\n"
  "v_cmp_eq_u32 vcc, 0, v247\n"
  "s_cbranch_vccnz .Lspin2_pe_None_%=\n"
  "s_branch .Lspun_pe_None_%=\n"
  ".Lpace_pe_None_%=:\n"
  "s_memrealtime s[70:71]\n"
  "s_waitcnt lgkmcnt(0)\n"
  "s_sub_i32 s64, s70, s61\n"
  "s_cmp_lt_i32 s64, 0\n"
  "s_cbranch_scc0 .Lpace2_pe_None_%=\n"
  "s_sleep 1\n"
  "s_branch .Lpace_pe_None_%=\n"
  ".Lpace2_pe_None_%=:\n"
  "s_add_i32 s61, s70, s62\n"
  "s_add_i32 s61, s61, 8\n"
  "s_sub_i32 s62, s62, 14\n"
  "s_branch .Lpaced_pe_None_%=\n"
  ".Lpace_po_None_%=:\n"
  "s_memrealtime s[70:71]\n"
  "s_waitcnt lgkmcnt(0)\n"
  "s_sub_i32 s64, s70, s61\n"
  "s_cmp_lt_i32 s64, 0\n"
  "s_cbranch_scc0 .Lpace2_po_None_%=\n"
  "s_sleep 1\n"
  "s_branch .Lpace_po_None_%=\n"
  ".Lpace2_po_None_%=:\n"
  "s_add_i32 s61, s70, s62\n"
  "s_add_i32 s61, s61, 0\n"
  "s_sub_i32 s62, s62, 14\n"
  "s_branch .Lpaced_po_None_%=\n"
  ".Lspin_s22_None_%=:\n"
  "s_mov_b32 s64, 0\n"
  ".Lspin2_s22_None_%=:\n"
  "s_sleep 3\n"
  "buffer_load_dword v247, v246, s[40:43], s54 offen sc1\n"
  "s_add_u32 s64, s64, 1\n"
  "s_waitcnt vmcnt(0)\n"
  "s_cmp_gt_u32 s64, 0x200000\n"
  "s_cbranch_scc1 .Lspun_s22_None_%=\n"
  "v_cmp_eq_u32 vcc, 0, v247\n"
  "s_cbranch_vccnz .Lspin2_s22_None_%=\n"
  "s_branch .Lspun_s22_None_%=\n"
  ".Lspin_ne_None_%=:\n"
  "s_mov_b32 s64, 0\n"
  ".Lspin2_ne_None_%=:\n"
  "s_sleep 3\n"
  "buffer_load_dword v247, v246, s[40:43], s54 offen sc1\n"
  "s_add_u32 s64, s64, 1\n"
  "s_waitcnt vmcnt(0)\n"
  "s_cmp_gt_u32 s64, 0x200000\n"
  "s_cbranch_scc1 .Lspun_ne_None_%=\n"
  "v_cmp_eq_u32 vcc, 0, v247\n"
  "s_cbranch_vccnz .Lspin2_ne_None_%=\n"
  "s_branch .Lspun_ne_None_%=\n"
  ".Lspin_s28_None_%=:\n"
  "s_mov_b32 s64, 0\n"
  ".Lspin2_s28_None_%=:\n"
  "s_sleep 3\n"
  "buffer_load_dword v247, v246, s[40:43], s54 offen sc1\n"
  "s_add_u32 s64, s64, 1\n"
  "s_waitcnt vmcnt(0)\n"
  "s_cmp_gt_u32 s64, 0x200000\n"
  "s_cbranch_scc1 .Lspun_s28_None_%=\n"
  "v_cmp_eq_u32 vcc, 0, v247\n"
  "s_cbranch_vccnz .Lspin2_s28_None_%=\n"
  "s_branch .Lspun_s28_None_%=\n"
  ".Lend_%=:\n"
    : "={v[180:211]}"(o3), "={v[212:227]}"(o4), "={v[176:179]}"(o5), "={v[80:95]}"(o0), "={v[156:163]}"(o1)
    : "{v[180:211]}"(h0), "{v[212:227]}"(h1), "{v[176:179]}"(hd.vb), "{v[80:95]}"(st0), "{v[156:163]}"(st1), "{s[40:43]}"(cx.rs)
    : "v0", "v1", "v2", "v3", "v4", "v5", "v6", "v7", "v8", "v9", "v10", "v11", "v12", "v13", "v14", "v15", "v16", "v17", "v18", "v19", "v20", "v21", "v22", "v23", "v24", "v25", "v26", "v27", "v28", "v29", "v30", "v31", "v32", "v33", "v34", "v35", "v36", "v37", "v38", "v39", "v40", "v41", "v42", "v43", "v44", "v45", "v46", "v47", "v48", "v49", "v50", "v51", "v52", "v53", "v54", "v55", "v56", "v57", "v58", "v59", "v60", "v61", "v62", "v63", "v64", "v65", "v66", "v67", "v68", "v69", "v70", "v71", "v72", "v73", "v74", "v75", "v76", "v77", "v78", "v79", "v96", "v97", "v98", "v99", "v100", "v101", "v102", "v103", "v104", "v105", "v106", "v107", "v108", "v109", "v110", "v111", "v112", "v113", "v114", "v115", "v116", "v117", "v118", "v119", "v120", "v121", "v122", "v123", "v124", "v125", "v126", "v127", "v128", "v129", "v130", "v131", "v132", "v133", "v134", "v135", "v136", "v137", "v138", "v139", "v140", "v141", "v142", "v143", "v144", "v145", "v146", "v147", "v148", "v149", "v150", "v151", "v152", "v153", "v154", "v155", "v164", "v165", "v166", "v167", "v168", "v169", "v170", "v171", "v172", "v173", "v174", "v175", "v228", "v229", "v230", "v231", "v232", "v233", "v234", "v235", "v236", "v237", "v238", "v239", "v240", "v241", "v242", "v243", "v244", "v245", "v246", "v247", "v248", "v249", "v250", "v251", "v252", "v253", "v254", "v255", "a0", "a1", "a2", "a3", "a4", "a5", "a6", "a7", "a8", "a9", "a10", "a11", "a12", "a13", "a14", "a15", "a16", "a17", "a18", "a19", "a20", "a21", "a22", "a23", "a24", "a25", "a26", "a27", "a28", "a29", "a30", "a31", "a32", "a33", "a34", "a35", "a36", "a37", "a38", "a39", "a40", "a41", "a42", "a43", "a44", "a45", "a46", "a47", "a48", "a49", "a50", "a51", "a52", "a53", "a54", "a55", "a56", "a57", "a58", "a59", "a60", "a61", "a62", "a63", "a64", "a65", "a66", "a67", "a68", "a69", "a70", "a71", "a72", "a73", "a74", "a75", "a76", "a77", "a78", "a79", "a80", "a81", "a82", "a83", "a84", "a85", "a86", "a87", "a88", "a89", "a90", "a91", "a92", "a93", "a94", "a95", "a96", "a97", "a98", "a99", "a100", "a101", "a102", "a103", "a104", "a105", "a106", "a107", "a108", "a109", "a110", "a111", "a112", "a113", "a114", "a115", "a116", "a117", "a118", "a119", "a120", "a121", "a122", "a123", "a124", "a125", "a126", "a127", "a128", "a129", "a130", "a131", "a132", "a133", "a134", "a135", "a136", "a137", "a138", "a139", "a140", "a141", "a142", "a143", "a144", "a145", "a146", "a147", "a148", "a149", "a150", "a151", "a152", "a153", "a154", "a155", "a156", "a157", "a158", "a159", "a160", "a161", "a162", "a163", "a164", "a165", "a166", "a167", "a168", "a169", "a170", "a171", "a172", "a173", "a174", "a175", "a176", "a177", "a178", "a179", "a180", "a181", "a182", "a183", "a184", "a185", "a186", "a187", "a188", "a189", "a190", "a191", "a192", "a193", "a194", "a195", "a196", "a197", "a198", "a199", "a200", "a201", "a202", "a203", "a204", "a205", "a206", "a207", "a208", "a209", "a210", "a211", "a212", "a213", "a214", "a215", "a216", "a217", "a218", "a219", "a220", "a221", "a222", "a223", "a224", "a225", "a226", "a227", "a228", "a229", "a230", "a231", "a232", "a233", "a234", "a235", "a236", "a237", "a238", "a239", "a240", "a241", "a242", "a243", "a244", "a245", "a246", "a247", "a248", "a249", "a250", "a251", "a252", "a253", "a254", "a255", "s44", "s45", "s46", "s47", "s48", "s49", "s50", "s51", "s52", "s53", "s54", "s55", "s56", "s57", "s58", "s59", "s60", "s61", "s62", "s63", "s64", "s65", "s66", "s67", "s68", "s69", "s70", "s71", "s72", "s73", "s74", "s75", "s76", "s77", "s78", "s79", "s80", "s81", "s82", "s83", "s84", "s85", "s86", "s87", "s88", "s89", "vcc", "scc", "m0", "memory");
}

__device__ __forceinline__ void imfma0(f32x4& d, const bf16x8& a, const bf16x8& b, const f32x4& c) {
  asm volatile("v_mfma_f32_16x16x32_bf16 %0, %1, %2, %3" : "=&v"(d) : "v"(a), "a"(b), "v"(c));
}
__device__ __forceinline__ void imfma1(f32x4& d, const bf16x8& a, const bf16x8& b) {
  asm volatile("v_mfma_f32_16x16x32_bf16 %0, %1, %2, %0" : "+v"(d) : "v"(a), "a"(b));
}
#define DPPADD(d, s, CTRL, BANKS) asm volatile("v_add_f32_dpp %0, %1, %1 " CTRL " row_mask:0xf bank_mask:" BANKS : "+v"(d) : "v"(s))
template <int CTRL>
__device__ __forceinline__ float dpp_add(float v) {
  asm("" : "+v"(v));
  return v + __builtin_bit_cast(float, __builtin_amdgcn_update_dpp(0, __builtin_bit_cast(int, v), CTRL, 0xf, 0xf, true));
}

extern "C" __global__ __launch_bounds__(256, 1) void conv_fft(char* ws, const unsigned short* __restrict__ x,
                                                   const unsigned short* __restrict__ w,
                                                   const unsigned short* __restrict__ gamma,
                                                   const unsigned short* __restrict__ beta,
                                                   unsigned short* __restrict__ out) {
  __shared__ __attribute__((aligned(16))) char smem[160 * 1024];
  const unsigned lds0 = (unsigned)(uintptr_t)(lds_void*)smem;
  const int wv = __builtin_amdgcn_readfirstlane(threadIdx.x >> 6);
  Ctx cx;

  {
    unsigned len;
    asm volatile("s_getpc_b64 %0\n.Lcf_a:\n\ts_mov_b32 %1, .Lcf_b-.Lcf_a" : "=s"(cx.pc), "=s"(len));
    cx.wv = wv;
    cx.pfo = len;
    unsigned xcc;
    asm volatile("s_getreg_b32 %0, hwreg(HW_REG_XCC_ID, 0, 3)" : "=s"(xcc));
    unsigned id = blockIdx.x;
    asm volatile("" : "+s"(id));
    cx.id = id;
    cx.vb = (int)((id & 248u) | (id & 256u ? (id >> 10) & 7u : xcc));
    const char* pb = ws + WS_PUB;
    u32x4 &sx = cx.sx, &sw = cx.sw;
    cx.xcc = xcc;
    if ((id | wv) == 0) {
      const unsigned l = threadIdx.x;
      const u32x4 z = {0u, 0u, 0u, 0u};
      {
        const unsigned so = l * (31u * 1024u + 16u) + (4u << 20);
        asm volatile("global_load_dwordx4 %0, %2, %3 nt\n\tglobal_load_dwordx4 %1, %4, %5 nt"
                     : "=&v"(sx), "=&v"(sw) : "v"(so + (l & 3u) * (8u << 20)), "s"(x), "v"(so + (l & 1u) * (6u << 20)), "s"(w) : "memory");
      }
      {
#pragma unroll
        for (int k = 0; k < 2; ++k)
          asm volatile("global_store_dwordx4 %0, %1, %2 sc1" :: "v"(32768 + (l & 7) * 16 + (l & 8) * 512 + (l & 16) * 1024 + (l & 32) * 32 + k * 8192), "v"(z), "s"(pb) : "memory");
        asm volatile("global_store_dwordx4 %0, %1, %2 sc1" :: "v"(l * 16 + 1024), "v"(z), "s"(pb) : "memory");
      }
    }
    if (id) code_touch(cx);
    SB0;
  }
  cx.smem = smem;
  cx.ws = ws;
  cx.gamma = gamma;
  cx.beta = beta;
  run12(cx, x, w);
  ws = cx.ws;
  const int bid = cx.vb, idx = bid >> 3;
  const int slot = (bid & 7) * 4 + (idx >> 3), b = (idx >> 2) & 1, nt = idx & 3;
  const int lane = (int)__builtin_amdgcn_mbcnt_hi(~0u, __builtin_amdgcn_mbcnt_lo(~0u, 0u));
  const int c64i = nt * 2 + ((bid >> 2) & 1), rg = (bid & 1) * 8 + (slot & 1) * 4 + (bid & 2) + (idx >> 4);
  const int bs = 10 * rg, hw = wv & 1, par = wv >> 1;
  const int g = lane >> 4, qq = (lane >> 2) & 3, pp = lane & 3, fr = lane & 15;
  const int lend = min(HOP * (bs + 10), L_);
  const u32x4 rsf = make_rsrc(ws + WS_GRP + nt * 256, 16 * 1024);
  unsigned fw = (unsigned)((lane + (lane & 4)) * 1024 + (b * 32 + slot) * 4);
  unsigned fp = (unsigned)(((bid & 7) + (bid & 4)) * 1024 + (b * 32 + ((lane & 31) ^ ((lane & 31) == slot))) * 4);
  f32x4 y[30], ye[2], cin[2], npv[2];
  {
    const int ee = fr & 1 ? cx.es : -2000;
#pragma unroll
    for (int s = 0; s < 2; ++s) {
      cin[s] = *reinterpret_cast<const f32x4*>(smem + CBUF + (par + (fr & 14)) * 256 + hw * 128 + g * 32 + s * 16);
#pragma unroll
      for (int r = 0; r < 4; ++r) cin[s][r] = __builtin_amdgcn_ldexpf(cin[s][r], ee);
    }
  }
  char* my = smem + wv * 20480;
  unsigned src = (unsigned)(uintptr_t)(lds_void*)my + (unsigned)((4 * g + qq) * 64 + ((g & 1) << 5) + pp * 8), src1 = src ^ 32u;
  unsigned vo = (unsigned)((lane >> 2) * 64 + (((lane & 3) ^ ((lane >> 3) & 2)) << 4)), vp = (unsigned)(g * 8);
  const u32x4 rsy = make_rsrc(ws + WS_Y, ROWS * 65536);
  unsigned so[6];
  {
    const unsigned t0 = (unsigned)(b * NBLK * 65536 + (c64i * 2 + hw) * 4096);
#pragma unroll
    for (int i = 0; i < 6; ++i) {
      const unsigned t = __builtin_amdgcn_readfirstlane(i < 5 ? t0 + (unsigned)(bs + par + 2 * i) * 65536 : t0);
      so[i] = t ^ ((t >> 8) & 8192u);
      asm volatile("" : "+s"(so[i]));
    }
  }
  asm volatile("" : "+v"(cin[0]), "+v"(cin[1]), "+v"(src), "+v"(src1), "+v"(vo), "+v"(vp), "+v"(fw), "+v"(fp));
  SB0;
  wait_vm<0>();
  __builtin_amdgcn_s_barrier();
  unsigned fv = 1u;
  if (wv == 1 && lane < 8) asm volatile("buffer_store_dword %0, %1, %2, 0 offen sc1" :: "v"(fv), "v"(fw), "s"(rsf) : "memory");
  if (wv == 0) asm volatile("buffer_load_dword %0, %1, %2, 0 offen sc1" : "=v"(fv) : "v"(fp), "s"(rsf) : "memory");
  bf16x8 gb[16];
  {
    const unsigned l16 = (unsigned)lane * 16u;
    const char* gt = (const char*)&TAB.G[0][0][0][0][0];
#pragma unroll
    for (int i = 0; i < 16; ++i)
      asm volatile("global_load_dwordx4 %0, %1, %2 offset:%3" : "=a"(gb[i]) : "v"(l16), "s"(gt + (i >> 2) * 4096), "i"((i & 3) * 1024) : "memory");
  }
  SB0;
  constexpr float GC = 0.4686686930532063f, GA = 4.91222953926424f, GL = -1.0933596685052493f;
  float gam = GC * bf_lo(*reinterpret_cast<const unsigned short*>(smem + GAM + wv * 32 + fr * 2));
  float bet = GC * bf_lo(*reinterpret_cast<const unsigned short*>(smem + GAM + 128 + wv * 32 + fr * 2));
  float eps = fmaxf(__builtin_amdgcn_ldexpf(1e-5f, 2 * cx.es), 1e-30f);
  const int l0 = HOP * (bs + par) + fr;
  const int le = fr < 10 ? HOP * (bs + par + 2 * (fr >> 1)) + 48 + (fr & 1) : 1 << 20;
  unsigned oo = (unsigned)((l0 * D_ + (c64i * 2 + hw) * 32 + 8 * g) * 2), oe = (unsigned)((le * D_ + (c64i * 2 + hw) * 32 + 8 * g) * 2);
  float v4[4];
#pragma unroll
  for (int tt = 0; tt < 4; ++tt) v4[tt] = (tt < 3 ? l0 + 8 * HOP + 16 * tt : le) < lend ? 1.f : 0.f;
  unsigned long long* stats = reinterpret_cast<unsigned long long*>(ws + WS_STATS);
  const u32x4 rss = make_rsrc(stats + (b * 32 + (c64i & 1) * 4) * 256 + nt * 64, 65536);
  unsigned sp = (unsigned)((g * 256 + 16 * wv + fr) * 8);
  const unsigned red = lds0 + 8 * 12288, aff = red + 4096;
  unsigned rw_ = red + (unsigned)((par * 64 + hw * 32 + 8 * g + (fr & 7)) * 8 + (fr >> 3) * 4), aw_ = aff + (unsigned)(((g & 1) * 64 + 16 * wv + fr) * 4);
  unsigned ar_ = aff + (unsigned)((hw * 32 + 8 * g) * 4);
  asm volatile("" : "+v"(gam), "+v"(bet), "+v"(eps), "+v"(oo), "+v"(oe), "+v"(sp), "+v"(rw_), "+v"(aw_), "+v"(ar_), "+v"(v4[0]), "+v"(v4[1]), "+v"(v4[2]), "+v"(v4[3]));
  SB0;
  if (wv == 0) {
    wait_vm<16>();
    asm volatile("" : "+v"(fv));
    for (unsigned spins = 0; !__all(fv != 0u) && spins < (1u << 21); ++spins)
      asm volatile("s_sleep 1\n\tbuffer_load_dword %0, %1, %2, 0 offen sc1\n\ts_waitcnt vmcnt(0)" : "=v"(fv) : "v"(fp), "s"(rsf) : "memory");
  }
  __builtin_amdgcn_s_barrier();
  u32x2 pv[2];
  asm volatile("buffer_load_dwordx2 %0, %2, %3, %4 offen sc1\n\tbuffer_load_dwordx2 %1, %2, %3, %4 offen offset:32 sc1"
               : "=&v"(pv[0]), "=&v"(pv[1]) : "v"(vp), "s"(rsy), "s"(so[5]) : "memory");
#pragma unroll
  for (int i = 0; i < 5; ++i)
    asm volatile("s_mov_b32 m0, %0\n\ts_nop 0\n\tbuffer_load_dwordx4 %1, %2, %3 offen sc1 lds\n\tbuffer_load_dwordx4 %1, %2, %3 offen offset:1024 sc1 lds\n\t"
                 "buffer_load_dwordx4 %1, %2, %3 offen offset:2048 sc1 lds\n\tbuffer_load_dwordx4 %1, %2, %3 offen offset:3072 sc1 lds"
                 :: "s"(lds0 + wv * 20480 + i * 4096), "v"(vo), "s"(rsy), "s"(so[i]) : "memory");
  float s1[8], s2[8];
  {
    bf16x8 a[2][4];
    auto rd = [&](int i, int q) __attribute__((always_inline)) {
      const lds_s16x4* p = (const lds_s16x4*)(uintptr_t)((q & 2 ? src1 : src) + i * 4096 + (q & 1) * 2048);
      a[i & 1][q] = cat(__builtin_amdgcn_ds_read_tr16_b64_v4i16((lds_s16x4*)p), __builtin_amdgcn_ds_read_tr16_b64_v4i16((lds_s16x4*)p + 128));
    };
    f32x4 t3[2];
    auto sums = [&](int cp, int q) __attribute__((always_inline)) {
      const int i = cp >> 3, s = (cp >> 2) & 1, tt = cp & 3, e = 4 * s + q;
      if (tt == 3) {
        if (i) ye[s][q] = (fr >> 1) == i ? t3[s][q] : ye[s][q];
        asm volatile("" : "+v"(ye[s]));
        return;
      }
      float d = y[6 * i + 3 * s + tt][q];
      if (i == 4) d *= v4[tt];
      if (i == 0 && tt == 0) { s1[e] = d; s2[e] = d * d; }
      else { s1[e] += d; s2[e] = __builtin_fmaf(d, d, s2[e]); }
      asm volatile("" : "+v"(s1[e]), "+v"(s2[e]));
    };
    wait_vm<16>();
    SB0;
#pragma unroll
    for (int q = 0; q < 4; ++q) { rd(0, q); SB0; }
    asm volatile("" : "+v"(pv[0]), "+v"(pv[1]));
#pragma unroll
    for (int s = 0; s < 2; ++s) {
      npv[s] = f32x4{bf_lo(pv[s][0]), bf_hi(pv[s][0]), bf_lo(pv[s][1]), bf_hi(pv[s][1])} * (-1.0f / 64.0f);
      cin[s] += npv[s];
    }
    asm volatile("" : "+v"(npv[0]), "+v"(npv[1]), "+v"(cin[0]), "+v"(cin[1]));
    asm volatile("s_nop 1");
    SB0;
    sfor<0, 42>([&](auto cc) __attribute__((always_inline)) {
      constexpr int c = cc.v, i = c >> 3, is = (c >> 2) & 1, tt = c & 3;
      if constexpr ((c & 7) == 4 && i < 4) wait_vm<12 - 4 * i>();
      sfor<0, 4>([&](auto qq_) __attribute__((always_inline)) {
        constexpr int q = qq_.v;
        if constexpr (c < 40) {
          f32x4& d = tt < 3 ? y[6 * i + 3 * is + (tt < 3 ? tt : 0)] : i ? t3[is] : ye[is];
          if constexpr (q == 0) imfma0(d, a[i & 1][2 * is], gb[4 * tt + 1], tt == 3 ? cin[is] : npv[is]);
          else imfma1(d, a[i & 1][2 * is + (q >> 1)], gb[4 * tt + (q ^ 1)]);
        }
        SB0;
        if constexpr (((c & 7) == 4 || (c & 7) == 5) && i < 4) { if (q & 1) rd(i + 1, (c & 1) * 2 + (q >> 1)); }
        if constexpr (c >= 2) sums(c - 2, q);
        SB0;
      });
    });
    asm volatile("" :: "v"(npv[0]), "v"(npv[1]), "v"(cin[0]), "v"(cin[1]));
#pragma unroll
    for (int e = 0; e < 8; ++e) {
      const float d = v4[3] != 0.f ? ye[e >> 2][e & 3] : 0.f;
      s1[e] += d;
      s2[e] = __builtin_fmaf(d, d, s2[e]);
    }
  }
  {
    float v[8];
#pragma unroll
    for (int j = 0; j < 8; ++j) { v[j] = s1[j]; }
#pragma unroll
    for (int j = 0; j < 8; ++j) DPPADD(v[j], s1[j], "row_ror:8", "0x3");
#pragma unroll
    for (int j = 0; j < 8; ++j) DPPADD(v[j], s2[j], "row_ror:8", "0xc");
    asm volatile("s_nop 0");
#pragma unroll
    for (int j = 0; j < 4; ++j) DPPADD(v[j], v[j], "row_half_mirror", "0x5");
#pragma unroll
    for (int j = 0; j < 4; ++j) DPPADD(v[j], v[j + 4], "row_half_mirror", "0xa");
    asm volatile("s_nop 0");
#pragma unroll
    for (int j = 0; j < 4; ++j) v[j] = dpp_add<0x4E>(v[j]);
    SB0;
#pragma unroll
    for (int j = 0; j < 4; ++j) v[j] = dpp_add<0xB1>(v[j]);
    const float w0 = fr & 1 ? v[1] : v[0], w1 = fr & 1 ? v[3] : v[2];
    *(__attribute__((address_space(3))) float*)(uintptr_t)rw_ = fr & 2 ? w1 : w0;
  }
  asm volatile("s_waitcnt lgkmcnt(0)" ::: "memory");
  __builtin_amdgcn_s_barrier();
  if (wv == 0) {
    typedef __attribute__((address_space(3))) f32x2 lds_f32x2;
    const f32x2 sm = *(lds_f32x2*)(uintptr_t)(red + lane * 8) + *(lds_f32x2*)(uintptr_t)(red + 512 + lane * 8);
    u32x2 pk = {__float_as_uint(sm[0]), __float_as_uint(sm[1])};
    if (pk[0] == ~0u) pk[0] = ~1u;
    asm volatile("buffer_store_dwordx2 %0, %1, %2, %3 offen sc1" :: "v"(pk), "v"(lane * 8), "s"(rss), "s"((rg + (rg & 12)) * 2048) : "memory");
  }
  {
    u32x2 pk[4];
#define PDLY 10
    __builtin_amdgcn_s_sleep(PDLY);
    for (unsigned spins = 0;; ++spins) {
      asm volatile("buffer_load_dwordx2 %0, %4, %5, 0 offen sc1\n\tbuffer_load_dwordx2 %1, %4, %5, %6 offen sc1\n\tbuffer_load_dwordx2 %2, %4, %5, %7 offen sc1\n\t"
                   "buffer_load_dwordx2 %3, %4, %5, %8 offen sc1\n\ts_waitcnt vmcnt(0)"
                   : "=&v"(pk[0]), "=&v"(pk[1]), "=&v"(pk[2]), "=&v"(pk[3]) : "v"(sp), "s"(rss), "s"(16384), "s"(32768), "s"(49152) : "memory");
      if (__all(max(max(pk[0][0], pk[1][0]), max(pk[2][0], pk[3][0])) != ~0u) || spins > (1u << 20)) break;
      __builtin_amdgcn_s_sleep(1);
    }
    float a = 0.f, q = 0.f;
#pragma unroll
    for (int k = 0; k < 4; ++k) { a += __uint_as_float(pk[k][0]); q += __uint_as_float(pk[k][1]); }
    auto r0 = __builtin_amdgcn_permlane16_swap(__float_as_uint(a), __float_as_uint(q), false, false);
    const unsigned c0 = __float_as_uint(__uint_as_float(r0[0]) + __uint_as_float(r0[1]));
    auto r1 = __builtin_amdgcn_permlane32_swap(c0, c0, false, false);
    const unsigned c1 = __float_as_uint(__uint_as_float(r1[0]) + __uint_as_float(r1[1]));
    auto r2 = __builtin_amdgcn_permlane16_swap(c1, c1, false, false);
    const float mean = __uint_as_float(r2[0]) * (1.0f / (float)L_);
    const float var = fmaxf(__builtin_fmaf(-mean, mean, __uint_as_float(r2[1]) * (1.0f / (float)L_)), 0.f);
    const float gg = gam * __builtin_amdgcn_rsqf(var + eps);
    *(__attribute__((address_space(3))) float*)(uintptr_t)aw_ = g & 1 ? __builtin_fmaf(-mean, gg, bet) : gg;
    asm volatile("s_waitcnt lgkmcnt(0)" ::: "memory");
    __builtin_amdgcn_s_barrier();
  }
  {
    const u32x4 ro = make_rsrc(out + (size_t)b * L_ * D_, lend * (D_ * 2));
    f32x4 ga[2], be[2];
#pragma unroll
    for (int s = 0; s < 2; ++s) {
      typedef __attribute__((address_space(3))) f32x4 lds_f32x4;
      ga[s] = *(lds_f32x4*)(uintptr_t)(ar_ + 16 * s);
      be[s] = *(lds_f32x4*)(uintptr_t)(ar_ + 256 + 16 * s);
    }
    f32x4 kk1 = {GA, GA, GA, GA}, kk2 = {GL, GL, GL, GL}, kk3 = {GC, GC, GC, GC};
    asm volatile("" : "+v"(kk1), "+v"(kk2), "+v"(kk3));
#pragma unroll
    for (int n = 0; n < 16; ++n) {
      const int i = n / 3, tt = n % 3;
      f32x4 o[2];
#pragma unroll
      for (int s = 0; s < 2; ++s) {
        const f32x4 v = (n < 15 ? y[(6 * i + 3 * s + tt) % 30] : ye[s]) * ga[s] + be[s];
        const f32x4 a = kk2 - v * (v * v + kk1);
        f32x4 e;
#pragma unroll
        for (int r = 0; r < 4; ++r) e[r] = __builtin_amdgcn_exp2f(a[r]);
        e += kk3;
#pragma unroll
        for (int r = 0; r < 4; ++r) e[r] = __builtin_amdgcn_rcpf(e[r]);
        o[s] = v * e;
      }
      asm volatile("buffer_store_dwordx4 %0, %1, %2, %3 offen sc1 nt\n\ts_nop 1"
                   :: "v"(pack8(o[0], o[1])), "v"(n < 15 ? oo : oe), "s"(ro), "s"(n < 15 ? (2 * HOP * i + 16 * tt) * (D_ * 2) : 0) : "memory");
      SB0;
    }
  }

  asm volatile(".Lcf_b:\n\ts_nop 0" ::: "memory");
}

extern "C" void kernel_launch(void* const* d_in, const int* in_sizes, int n_in,
                              void* d_out, int out_size, void* d_ws, size_t ws_size,
                              hipStream_t stream) {
  const unsigned short* x = (const unsigned short*)d_in[0];
  const unsigned short* w = (const unsigned short*)d_in[1];
  const unsigned short* gamma = (const unsigned short*)d_in[2];
  const unsigned short* beta = (const unsigned short*)d_in[3];
  char* ws = (char*)(((uintptr_t)d_ws + (2u << 20) - 1) & ~(uintptr_t)((2u << 20) - 1));
  conv_fft<<<256, 256, 0, stream>>>(ws, x, w, gamma, beta, (unsigned short*)d_out);
}
```

```cpp
#include <hip/hip_runtime.h>
#include <stdint.h>

typedef __attribute__((ext_vector_type(8))) short bf16x8;
typedef __attribute__((ext_vector_type(4))) short s16x4;
typedef __attribute__((ext_vector_type(4))) float f32x4;
typedef __attribute__((ext_vector_type(2))) float f32x2;
typedef __attribute__((ext_vector_type(2))) __bf16 bf16x2v;
typedef __attribute__((ext_vector_type(4))) unsigned int u32x4;
typedef __attribute__((ext_vector_type(2))) unsigned int u32x2;
constexpr int NH = 6, NST = 3;
typedef __attribute__((ext_vector_type(2 * NH))) unsigned int u32xh;
typedef __attribute__((ext_vector_type(8))) _Float16 f16x8;
typedef __attribute__((ext_vector_type(2))) _Float16 f16x2;
typedef __attribute__((ext_vector_type(2))) unsigned short u16x2;
typedef __attribute__((address_space(1))) unsigned long long gu64;
typedef __attribute__((address_space(1))) unsigned gu32;
typedef __attribute__((address_space(3))) void lds_void;
typedef __attribute__((address_space(3))) s16x4 lds_s16x4;

constexpr int T_ = 16000, D_ = 512, L_ = 7985;
constexpr int HOP = 50, NBLK = 160, ROWS = 2 * NBLK;

constexpr unsigned WS_S = 0;
constexpr unsigned SBLK = 4u << 20;
constexpr unsigned sbase(unsigned sl) { return sl * 131072 + (sl >> 4) * 8192; }
constexpr unsigned FLG = 32u * SBLK;
constexpr unsigned WS_GRP = WS_S + FLG + 16 * 8192;
constexpr unsigned WS_D = WS_S + FLG + 20 * 8192;
constexpr unsigned WS_L = WS_D + ROWS * 1024;
constexpr unsigned WS_Y = 40u << 20;
constexpr unsigned WS_STATS = 129u << 20;
constexpr unsigned WS_FA = WS_STATS + (1u << 17);
constexpr unsigned WS_PUB = WS_STATS + (1u << 19);
static_assert(WS_L + D_ * D_ * 2 <= WS_STATS && FLG % (16 * 8192) == 0, "workspace layout");

#define RLX_AGENT __ATOMIC_RELAXED, __HIP_MEMORY_SCOPE_AGENT

constexpr double COS64[17] = {1.0, 0.99518472667219688624, 0.98078528040323044913, 0.95694033573220886494,
    0.92387953251128675613, 0.88192126434835502971, 0.83146961230254523708, 0.77301045336273696081,
    0.70710678118654752440, 0.63439328416364549822, 0.55557023301960222474, 0.47139673682599764856,
    0.38268343236508977173, 0.29028467725446236764, 0.19509032201612826785, 0.09801714032956060199, 0.0};
constexpr double c64(int k) { k &= 63; if (k > 32) k = 64 - k; return k <= 16 ? COS64[k] : -COS64[32 - k]; }
constexpr double s64(int k) { return c64(k + 48); }
constexpr unsigned short bf_rne(double v) {
  const unsigned u = __builtin_bit_cast(unsigned, (float)v);
  return (unsigned short)((u + 0x7fffu + ((u >> 16) & 1u)) >> 16);
}
constexpr double bf_val(unsigned short h) { return (double)__builtin_bit_cast(float, (unsigned)h << 16); }
constexpr unsigned short hilo(double v, int lo) { return lo ? bf_rne(v - bf_val(bf_rne(v))) : bf_rne(v); }
constexpr unsigned short nz(unsigned short h) { return h & 0x7fff ? h : (unsigned short)(97 << 7); }
constexpr double R3 = 0.86602540378443864676;
constexpr double dft(int c, int t, double sgn) {
  return (c >> 1) == 0 ? ((c & t) & 1 ? -1.0 : 1.0)
                       : ((c & 1) ? -0.5 * c64((c >> 1) * t) - R3 * sgn * s64((c >> 1) * t) : c64((c >> 1) * t));
}
constexpr double idft(int c, int t) {
  return (c >> 1) == 0 ? ((c & t) & 1 ? -1.0 : 1.0) / 64.0
                       : ((c & 1) ? -s64((c >> 1) * t) / R3 - c64((c >> 1) * t) * (2.0 / 3.0) : c64((c >> 1) * t) * (4.0 / 3.0)) / 32.0;
}
constexpr double UP = 1.0 + 0.72134752 / 256.0;
constexpr int kidx(int g, int j) { return j < 4 ? 4 * g + j : 12 + 4 * g + j; }
struct Tables {
  unsigned short F[4][2][2][64][8];
  unsigned short V[2][4][64][8];
  unsigned short G[4][2][2][64][8];
};
constexpr Tables make_tables() {
  Tables tb{};
  for (int l = 0; l < 64; ++l)
    for (int j = 0; j < 8; ++j) {
      const int g = l >> 4, n = l & 15;
      for (int ct = 0; ct < 4; ++ct) {
        for (int ks = 0; ks < 2; ++ks)
          for (int h = 0; h < 2; ++h) tb.F[ct][ks][h][l][j] = nz(hilo(UP * dft(16 * ct + n, 32 * ks + kidx(g, j), -1.0), h));
        for (int p = 0; p < 2; ++p) {
          const int tap = 4 * g + (j & 3);
          tb.V[p][ct][l][j] = (p == 1 && tap == 15) ? (unsigned short)0 : nz(hilo(UP * dft(16 * ct + n, tap, 1.0), j >> 2));
        }
      }
      for (int tt = 0; tt < 4; ++tt)
        for (int ks = 0; ks < 2; ++ks)
          for (int h = 0; h < 2; ++h) {
            const int c = 32 * ks + kidx(g, j);
            tb.G[tt][ks][h][l][j] = hilo(idft(c, 16 * tt + (tt < 3 ? n : n & 1)), h);
          }
    }
  return tb;
}
__device__ const __attribute__((aligned(16))) Tables TAB = make_tables();

__device__ __forceinline__ unsigned pk_bf16(float lo, float hi) {
  return __builtin_bit_cast(unsigned, __builtin_convertvector(f32x2{lo, hi}, bf16x2v));
}
__device__ __forceinline__ float bf_lo(unsigned v) { return __uint_as_float(v << 16); }
__device__ __forceinline__ float bf_hi(unsigned v) { return __uint_as_float(v & 0xffff0000u); }
__device__ __forceinline__ u32x4 pack8(f32x4 a, f32x4 b) {
  return u32x4{pk_bf16(a[0], a[1]), pk_bf16(a[2], a[3]), pk_bf16(b[0], b[1]), pk_bf16(b[2], b[3])};
}
__device__ __forceinline__ unsigned pk_f16(float lo, float hi) {
  return (__builtin_bit_cast(unsigned, __builtin_amdgcn_cvt_pkrtz(lo, hi)) & 0xfff8fff8u);
}
__device__ __forceinline__ u32x4 pack8h(f32x4 a, f32x4 b) {
  return u32x4{pk_f16(a[0], a[1]), pk_f16(a[2], a[3]), pk_f16(b[0], b[1]), pk_f16(b[2], b[3])};
}
__device__ __forceinline__ unsigned add_h(const bf16x8& a, const bf16x8& b, int d) {
  const unsigned x = __builtin_bit_cast(u32x4, a)[d], y = __builtin_bit_cast(u32x4, b)[d];
  unsigned r = __builtin_bit_cast(unsigned, __builtin_bit_cast(f16x2, x) + __builtin_bit_cast(f16x2, y));
  return r;
}
__device__ __forceinline__ unsigned amax(unsigned m, unsigned v) {
  return __builtin_bit_cast(unsigned, __builtin_elementwise_max(__builtin_bit_cast(u16x2, m), __builtin_bit_cast(u16x2, v & 0x7fff7fffu)));
}
struct Rng { f16x2 hi = {0, 0}, lo = {0, 0}; };
__device__ __forceinline__ void widen(Rng& r, unsigned a, unsigned b) {
  const f16x2 x = __builtin_bit_cast(f16x2, a), y = __builtin_bit_cast(f16x2, b);
  r.hi = __builtin_elementwise_maximum(__builtin_elementwise_maximum(r.hi, x), y);
  r.lo = __builtin_elementwise_minimum(__builtin_elementwise_minimum(r.lo, x), y);
}
__device__ __forceinline__ unsigned amax(const Rng& r) { return amax(amax(0, __builtin_bit_cast(unsigned, r.hi)), __builtin_bit_cast(unsigned, r.lo)); }
__device__ __forceinline__ unsigned expo(unsigned m) { return max(m & 0xffffu, m >> 16) >> 7; }
__device__ __forceinline__ unsigned wave_max(unsigned v) {
  unsigned m = 0;
#pragma unroll 1
  for (int l = 0; l < 64; ++l) m = max(m, (unsigned)__builtin_amdgcn_readlane((int)v, l));
  return m;
}
__device__ __forceinline__ void scale8(bf16x8& v, int e) {
  u32x4 u = __builtin_bit_cast(u32x4, v);
#pragma unroll
  for (int d = 0; d < 4; ++d)
    u[d] = __builtin_amdgcn_perm(__float_as_uint(__builtin_amdgcn_ldexpf(bf_hi(u[d]), e)), __float_as_uint(__builtin_amdgcn_ldexpf(bf_lo(u[d]), e)), 0x07060302u);
  v = __builtin_bit_cast(bf16x8, u);
}
__device__ __forceinline__ void restart(unsigned id) {
  asm volatile("s_waitcnt vmcnt(0)\n\ts_barrier\n\ts_branch conv_fft" :: "{s[0:1]}"(__builtin_amdgcn_kernarg_segment_ptr()), "{s2}"(id), "{v0}"(threadIdx.x) : "memory");
}
__device__ __forceinline__ s16x4 tr_read(const char* p) {
  return __builtin_amdgcn_ds_read_tr16_b64_v4i16((lds_s16x4*)p);
}
__device__ __forceinline__ bf16x8 cat(s16x4 a, s16x4 b) {
  const u32x2 x = __builtin_bit_cast(u32x2, a), y = __builtin_bit_cast(u32x2, b);
  return __builtin_bit_cast(bf16x8, u32x4{x[0], x[1], y[0], y[1]});
}
#define MFMA(a, b, c) __builtin_amdgcn_mfma_f32_16x16x32_bf16(a, b, c, 0, 0, 0)
#define MFMH(a, b, c) __builtin_amdgcn_mfma_f32_16x16x32_f16(__builtin_bit_cast(f16x8, a), __builtin_bit_cast(f16x8, b), c, 0, 0, 0)
#define SB0 __builtin_amdgcn_sched_barrier(0)
#define ZERO4 (f32x4{0.f, 0.f, 0.f, 0.f})
#define DMA16(POL, rs, lds, voff, soff) \
  asm volatile("s_mov_b32 m0, %0\n\ts_nop 0\n\tbuffer_load_dwordx4 %1, %2, %3 offen " POL " lds" \
               :: "s"(lds), "v"(voff), "s"(rs), "s"(soff) : "memory")
__device__ __forceinline__ void st16(const u32x4& v, const u32x4& rs, unsigned voff, unsigned soff) {
  asm volatile("buffer_store_dwordx4 %0, %1, %2, %3 offen sc1\n\ts_nop 1" :: "v"(v), "v"(voff), "s"(rs), "s"(soff) : "memory");
}
__device__ __forceinline__ void ld16(bf16x8& v, const void* p) {
  asm volatile("global_load_dwordx4 %0, %1, off" : "=v"(v) : "v"(p) : "memory");
}
template <int N> __device__ __forceinline__ void wait_vm() { asm volatile("s_waitcnt vmcnt(%0)" :: "i"(N) : "memory"); }
__device__ __forceinline__ u32x4 make_rsrc(const void* p, unsigned bytes) {
  const unsigned long long a = (unsigned long long)p;
  return u32x4{(unsigned)a, (unsigned)(a >> 32) & 0xffffu, bytes, 0x00020000u};
}
__device__ __forceinline__ void spin_flags(const char* p) {
  for (unsigned spins = 0; !__all(__hip_atomic_load((const gu32*)p, RLX_AGENT) != 0u) && spins < (1u << 21); ++spins)
    __builtin_amdgcn_s_sleep(3);
}

typedef __attribute__((ext_vector_type(8))) unsigned int u32x8;
typedef __attribute__((ext_vector_type(16))) unsigned int u32x16;
typedef __attribute__((ext_vector_type(32))) unsigned int u32x32;
constexpr int GW = 10;
constexpr int NQ1 = GW / 2, NQW = 16 - NQ1;
constexpr int STG = 36 * 1024;
constexpr int RAWB = 67 * 1024, XT1 = RAWB + NQ1 * 4096, SINK = XT1 + RAWB;
constexpr int RAWL = 3 * STG;
constexpr int RAW0 = RAWL + 12 * 4096;
constexpr int GAM = RAWL + 36864, CBUF = RAWL + 40960;

struct Ctx {

  char* smem;
  char* ws;
  const unsigned short *gamma, *beta;
  u32x4 rs;
  unsigned lds0;
  unsigned voffw, voffx;
  unsigned poff, xo;
  unsigned id;
  unsigned tag[2];
  int es;
  int wv, vb;
  unsigned long long pc;
  unsigned pfo;
  u32x4 sx, sw;
  unsigned xcc;
};
__device__ __forceinline__ void code_touch(const Ctx& cx) {
  if ((cx.wv ^ (cx.vb >> 3)) & 3) return;
  const unsigned l = __builtin_amdgcn_mbcnt_hi(~0u, __builtin_amdgcn_mbcnt_lo(~0u, cx.wv * 64u));
#pragma unroll
  for (int k = 0; k < 2; ++k)
    asm volatile("s_mov_b32 m0, %0\n\ts_nop 0\n\tglobal_load_lds_dword %1, %2" :: "s"(SINK + cx.wv * 512 + k * 256), "v"(min((l + k * 256) * 128u, cx.pfo)), "s"(cx.pc) : "memory");
}
__device__ __forceinline__ void stage_issue(const Ctx& cx, int kc) {
  const unsigned slot = (unsigned)((kc + 1) % 3) * STG;
#pragma unroll
  for (int i = 0; i < 9; ++i) {
    const int j = i < 4 ? i : i - 4;
    const bool ahead = i < 4 || j == 0 || j == 3;
    const unsigned lo = i < 4 ? j * 4096 : j < 3 ? 16384 + j * 4096 : j == 3 ? 16384 + 10240 : 16384 + 14336;
    const unsigned go = i < 4 ? j * 4096 : (j >> 1) * 16384 + (j & 1) * 4096;
    if (kc >= 0 || ahead)
      DMA16("sc1", cx.rs, cx.lds0 + slot + lo + (i == 6 ? (cx.wv & 2) * 4096 : 0), i < 4 ? cx.voffw : cx.voffx, (unsigned)(kc + ahead) * SBLK + go);
  }
}
__device__ __forceinline__ unsigned x_place(unsigned kb, unsigned blk) { return kb + (blk + (blk & 192u)) * 128u; }
template <int V> struct IC { static constexpr int v = V; constexpr operator int() const { return V; } };
template <int A, int B, class F> __device__ __forceinline__ void sfor(F f) {
  if constexpr (A < B) { f(IC<A>{}); sfor<A + 1, B>(f); }
}
__device__ __forceinline__ unsigned dpp_swap1(unsigned v) {
  return (unsigned)__builtin_amdgcn_update_dpp(0, (int)v, 0xB1, 0xf, 0xf, false);
}

__device__ __forceinline__ int run_bs(int vb, int t) { return 5 * ((vb >> 4) + 16 * ((vb ^ t) & 1)); }
__device__ __forceinline__ int run_q(int vb, int t) { return (((vb >> 1) & 1) + ((vb >> 2) & 2) + 2 * t) & 3; }
__device__ __forceinline__ unsigned x_addr(int vb, int t, int wv) {
  const int lane = threadIdx.x & 63, q = run_q(vb, t);
  const unsigned c = (unsigned)((lane & 15) ^ (((4 * wv + (lane >> 4)) & 7) << 1));
  return (unsigned)(2 * (HOP * run_bs(vb, t) + 4 * wv + (lane >> 4)) + ((t ^ q ^ (q >> 1)) & 1)) * 1024u + c * 16 + q * 256;
}
__device__ __forceinline__ u32x4 x_rsrc(const unsigned short* x, int vb) { return make_rsrc(x + (size_t)((vb >> 2) & 1) * T_ * D_, T_ * D_ * 2); }
__device__ __forceinline__ void issue_x(int t, int wv, unsigned a, const u32x4& rx, unsigned lds0, int j0 = 0, int j1 = 17) {
#pragma unroll
  for (int jj = 0; jj < 17; ++jj)
    if (jj >= j0 && jj < j1 && (jj < 16 || wv < 3)) DMA16("nt", rx, lds0 + t * XT1 + wv * 1024 + jj * 4096, a, jj * 32768u);
}
#define CKP(i) CK(i)
#define CKQ(p)
#define CKE(i)
#define CKI(i)
__device__ __forceinline__ void mfma0(f32x4& d, const bf16x8& a, const bf16x8& b) {
  asm volatile("v_mfma_f32_16x16x32_bf16 %0, %1, %2, 0" : "=&v"(d) : "v"(a), "v"(b));
}
__device__ __forceinline__ void mfma1(f32x4& d, const bf16x8& a, const bf16x8& b) {
  asm volatile("v_mfma_f32_16x16x32_bf16 %0, %1, %2, %0" : "+v"(d) : "v"(a), "v"(b));
}
__device__ __forceinline__ bf16x8 dup(s16x4 a) {
  u32x2 lo = __builtin_bit_cast(u32x2, a), hi;
  asm volatile("v_mov_b64 %0, %1" : "=v"(hi) : "v"(lo));
  return __builtin_bit_cast(bf16x8, u32x4{lo[0], lo[1], hi[0], hi[1]});
}
__device__ __forceinline__ void scale8p(bf16x8& v, int e) {
  typedef __attribute__((ext_vector_type(8))) unsigned short u16x8;
  const unsigned short h = (unsigned short)(e << 7);
  v = __builtin_bit_cast(bf16x8, (u16x8)(__builtin_bit_cast(u16x8, v) + h));
}
__device__ __forceinline__ void pin(u32x4& v) { asm volatile("" : "+v"(v)); }
struct XR { s16x4 t[8]; };
#define P0_ 16
#define SLP_ 3
#define GPO_ 101u
#define GPE_ 149u
#define CT_ 0
#define PRK_ 16
#define TE_ 6
#define LDN_ 4
#define NA_ 14
#define SX0_ 9
#define SCN_ 8
#define PG_ 42
#define PS_ 42
#define NFA_ 2
constexpr int P0 = P0_, P1 = 46;
constexpr int PG = PG_, NFA = NFA_;
constexpr int TE = TE_;
constexpr int NLD = 17 - TE + 1 + NQW + 1;
constexpr int ld_pos(int l) { return 2 * l / LDN_; }
constexpr int ld_slot(int l) { int o = 0; for (int k = 0; k < l; ++k) o += ld_pos(k) == ld_pos(l); return 1 + 2 * o; }
constexpr int NIT = 46, NS = 40, NA = NA_, NB = 26;
static_assert(NH == 6 && NST == 3, "x blocks stored in the loop");
constexpr int ord(int p) { return p == 0 ? 0 : p < 6 ? p + 2 : p < 8 ? p - 5 : p; }
constexpr int st_w(int j) { return j < 2 ? j : j < 7 ? -1 : j < 15 ? j - 5 : -1; }
constexpr int st_x(int j) { return j < 7 ? j - 2 : j - 10; }
constexpr int st_pos(int j) { return st_w(j) >= 0 ? (j < 9 ? 0 : j < 13 ? 6 : 7) : j < 7 ? j - 1 : j - 7; }
#define NBP_ 4
constexpr int NBP = NBP_;
constexpr int st_first(int h) {
  int J = h <= P0 ? 0 : h >= P1 ? NS : h <= P0 + NBP ? (h - P0) * (NA + NFA) / NBP : NA + NFA + (h - P0 - NBP) * (NS - NA - NFA) / (P1 - P0 - NBP);
  for (int k = 0; k < J; ++k)
    if (2 * st_pos(k >> 1) + (k & 1) + 2 > h - 1) return k;
  return J;
}
constexpr int sc_pos(int k) { return k < 17 ? k * SX0_ / 17 : SX0_ + (k - 17) * (P0 - SX0_) / 17; }
constexpr int sc_raw(int k) { return k < NQ1 ? k : P0 + (k - NQ1) * (NBP - 1) / NQW; }
constexpr int sc_cnt(int h) { int c = 0; for (int k = 0; k < 34; ++k) c += sc_pos(k) == h; return c; }
constexpr bool sc_ok() { for (int h = 0; h < NIT; ++h) if (sc_cnt(h) > 3) return false; return true; }
constexpr int sc_wait(int h) {
  int j = -1;
  for (int k = 17; k < 34; ++k) if (sc_pos(k) == h) j = k - 17;
  if (j < 0) return -1;
  int c = j < TE ? TE - 1 - j : 0;
  for (int l = 0; l < NLD; ++l)
    if (ld_pos(l) < h && l > j - TE) c += l != 16 - TE;
  return c;
}
constexpr int XBAR = st_first(25) > NA + NFA - 1 ? -1 : 24;
static_assert(ld_pos(NLD - 1) < P0 && P0 < 24 && LDN_ <= 8, "loads before the first store");
static_assert(sc_ok() && st_first(P0 + NBP) == NA + NFA && st_first(P0 + NBP - 1) <= NA + NFA - 1 && SX0_ < P0, "range check before the first hand-off");
struct Hand {
  u32x4 held[2 * NH], st[2 * NST];
  bf16x8 vb;
  u32x2 ov;
  unsigned pollv;
  unsigned ctl;
  const char* rec;
};
constexpr int A_RAW = 144, A_WL = 184, A_VEC = 188, A_SCA = 214, A_XA = 240;
template <int I> __device__ __forceinline__ void stash(unsigned v) { asm volatile("v_accvgpr_write_b32 a%c0, %1" :: "i"(I), "v"(v)); }
template <int I> __device__ __forceinline__ unsigned unstash() {
  unsigned v;
  asm volatile("v_accvgpr_read_b32 %0, a%c1" : "=v"(v) : "i"(I));
  return v;
}
__device__ __forceinline__ void stash4(int a, const u32x4& v) {
  asm volatile("v_accvgpr_write_b32 a%c0, %4\n\tv_accvgpr_write_b32 a%c1, %5\n\tv_accvgpr_write_b32 a%c2, %6\n\tv_accvgpr_write_b32 a%c3, %7"
               :: "i"(a), "i"(a + 1), "i"(a + 2), "i"(a + 3), "v"(v[0]), "v"(v[1]), "v"(v[2]), "v"(v[3]));
}
constexpr int hw_reg(int mg, int q, int half) { return ((mg * 5 + q) * 2 + half) * 4; }
template <int A> __device__ __forceinline__ void load_acc(unsigned vo, const u32x4& rs, unsigned so) {
  asm volatile("buffer_load_dwordx4 a[%c3:%c4], %0, %1, %2 offen nt" :: "v"(vo), "s"(rs), "s"(so), "i"(A), "i"(A + 3) : "memory");
}
__device__ __forceinline__ void ld8(u32x2& v, const char* p, unsigned o) {
  asm volatile("global_load_dwordx2 %0, %1, %2 sc1" : "=v"(v) : "v"(o), "s"(p) : "memory");
}
__device__ __forceinline__ void loop_setup(const Ctx& cx, unsigned pwr, unsigned prd, unsigned hst) {
  const int bid = cx.vb, idx = bid >> 3, wv = cx.wv, lane = threadIdx.x & 63;
  const int slot = (bid & 7) * 4 + (idx >> 3), b = (idx >> 2) & 1, nt = idx & 3, xb = (bid >> 2) & 1;
  const int mg = wv >> 1, nh = wv & 1, g = lane >> 4, qq = (lane >> 2) & 3, pp = lane & 3, rt = 4 * g + qq;
  const int rg = (bid & 1) * 8 + (slot & 1) * 4 + (bid & 2) + (idx >> 4);
  sfor<0, 4>([&](auto c) __attribute__((always_inline)) {
    stash<A_VEC + c.v>((unsigned)(rt * 512 + (((nh * 4 + c.v) ^ (rt & 7)) << 5) + pp * 8));
    stash<A_VEC + 5 + c.v>((RAW0 + prd) ^ (unsigned)(c.v << 5));
  });
  { const int r = lane & 15; stash<A_VEC + 4>((unsigned)(16384 + mg * 5 * 2048 + (r >> 3) * 1024 + (r & 7) * 64 + ((g ^ ((r >> 3) << 1)) << 4))); }
  stash<A_VEC + 9>(cx.voffw);
  stash<A_VEC + 10>(cx.voffx);
  {
    const int kr8 = lane >> 3;
    stash<A_VEC + 11>((unsigned)(WS_L - WS_S + (8 * wv + kr8) * 1024 + (nt * 2 + xb) * 128 + (((lane & 7) ^ ((kr8 & 2) | (wv & 1) << 2)) << 4)));
  }
  stash<A_VEC + 12>((unsigned)(WS_D - WS_S + (b * NBLK + 10 * rg + min(lane & 15, 9)) * 1024 + g * 16));
  stash<A_VEC + 13>((unsigned)(RAWL + (8 * g + qq) * 128 + ((wv ^ ((qq >> 1) | (g & 1) << 1)) << 5) + pp * 8));
  stash<A_VEC + 14>(cx.poff);
  stash<A_VEC + 15>(cx.xo);
  stash<A_VEC + 16>(hst);
  stash<A_VEC + 17>(wv == 0 && lane < 8 ? (unsigned)(lane * 1024 + (bid >> 2) * 4) : 0x80000000u);
  stash<A_VEC + 18>((unsigned)((bid & 7) * 1024 + wv * 256 + lane * 4));
  {
    const char* gp = (const char*)(lane < 32 ? cx.gamma : cx.beta) + (nt * 2 + xb) * 128 + (lane & 31) * 4;
    stash<A_VEC + 20>((unsigned)(uintptr_t)gp);
    stash<A_VEC + 21>((unsigned)((uintptr_t)gp >> 32));
  }
  stash<A_VEC + 22>((unsigned)((b * NBLK + mg * 80 + (lane & 14)) * 65536 + nt * 16384 + g * 16 + (2 * slot + (lane & 1)) * 64));
  {
    unsigned len;
    asm volatile("s_mov_b32 %0, .Lcf_b-.Lcf_t-64" : "=s"(len));
    stash<A_VEC + 23>(min((unsigned)(wv * 64 + lane) * 128u, len));
  }
  stash<A_VEC + 24>(RAWL + pwr);
  stash<A_VEC + 25>((unsigned)(CBUF + (lane & 15) * 256 + wv * 64 + g * 16));
  const unsigned long long gt = (unsigned long long)&TAB.G[0][0][0][0][0];
  unsigned sc[15] = {cx.lds0, (unsigned)(wv & 2) * 4096u, FLG + 2 * 8192 + (bid & 3) * 256, wv == nt ? 0x3ffcu : 0x444u,
                     NQ1 * 2 * SBLK + ((bid + NQ1) & 3) * 16384, wv == 3 ? (bid & 1 ? GPO_ : GPE_) - 14u : 0u, slot ? 0xbf800000u : 0u,
                     0, 0, 0, 0, 0, ((wv ^ idx) & 3) == 0 ? RAWL + 32768 + wv * 256u : 0u, (unsigned)gt, (unsigned)(gt >> 32)};
#pragma unroll
  for (int m = 0; m < 5; ++m) {
    const int xt = mg ? (m + 1) % 5 : m;
    sc[7 + m] = (unsigned)(xt * 16 * 65536 + ((b * 10 + mg * 5 + 2 * nh + xt) & 2) * 4096);
  }
  unsigned sv = 0;
#pragma unroll
  for (int i = 0; i < 15; ++i)
    asm volatile("v_writelane_b32 %0, %1, %2" : "+v"(sv) : "s"(__builtin_amdgcn_readfirstlane((int)sc[i])), "i"(i));
  stash<A_SCA>(sv);
}
__device__ __forceinline__ void phase1(Ctx& cx, const unsigned short* __restrict__ x, const unsigned short* __restrict__ w, Hand& hd) {
  u32x4 rq0;
  static_assert(NQ1 == 5, "phase-1 wait counts assume five quanta");
  char* smem = cx.smem;
  const unsigned lds0 = (unsigned)(uintptr_t)(lds_void*)smem;
  const int idx = cx.vb >> 3, wv = cx.wv, lane = threadIdx.x & 63;
  const u32x4 rw = make_rsrc(w, 31 * D_ * D_ * 2);
  const int tap = threadIdx.x >> 4, pc = threadIdx.x & 15;
  const unsigned roff = (unsigned)(tap * (2 * D_ * D_ * 2) + pc * 16);
  const int ng = wv;
  const int g = lane >> 4, qq = (lane >> 2) & 3, pp = lane & 3;
  u32x4 r1[NQ1];
  bf16x8 fb[2][2];
  unsigned long long tag;
  const char* rec = cx.ws + WS_PUB + ((cx.id & 7) * 8 + wv) * 16;
  u32x4 pr;
  unsigned pwr, prd, pvo, pww, xb0[4], pq[4], xa[2];
  const int kr = (cx.vb >> 7) * 32 + (cx.vb & 4) * 4 + (idx & 15);
  auto raw_so = [&](int q) {
    return (unsigned)(kr + 64 * (2 * (q & 3) + ((q >> 2) & 1)) + 512 * (((q >> 3) ^ q ^ (q >> 1)) & 1)) * 1024u + (unsigned)((cx.vb + q) & 3) * 256u;
  };
  auto raw_load = [&](u32x4& r, int q) {
    asm volatile("buffer_load_dwordx4 %0, %1, %2, %3 offen nt" : "=v"(r) : "v"(roff), "s"(rw), "s"(raw_so(q)) : "memory");
  };
  const unsigned xa1 = x_addr(cx.vb, 1, wv);
  {
    const int vb = cx.vb;
    auto setup = [&]() __attribute__((always_inline)) {
    {
      const int bid = vb, xb = (bid >> 2) & 1, krow = kr;
      const int slot = (bid & 7) * 4 + (idx >> 3), b = (idx >> 2) & 1, nt = idx & 3;
      const int rt = 4 * g + qq;
      pvo = (unsigned)((bid & 7) * 1024 + wv * 256 + lane * 4);
      pwr = (unsigned)((pc >> 3) * 2048 + tap * 128 + ((((pc >> 1) ^ (tap >> 1)) & 3) << 5) + (pc & 1) * 16);
      prd = (unsigned)(rt * 128 + (((rt >> 1) & 3) << 5) + pp * 8);
      cx.poff = (unsigned)(sbase(8 * ng + ((lane >> 1) & 7)) + (krow >> 5) * SBLK + (krow & 31) * 512 + (lane & 1) * 64 + g * 16);
      cx.xo = (unsigned)(sbase(8 * ng + ((lane >> 1) & 7)) + 65536 + xb * 8192 + (lane & 1) * 64 + g * 16);
      cx.lds0 = lds0 + wv * 1024;
#pragma unroll
      for (int k = 0; k < 4; ++k) {
        xb0[k] = (unsigned)((4 * g + qq) * 256 + (((4 * g + qq + 2 * k) & 7) << 5) + pp * 8);
        pq[k] = (prd + RAWB) ^ (unsigned)(k << 5);
      }
      pww = pwr + RAWB;
      asm volatile("" : "+v"(xb0[0]), "+v"(xb0[1]), "+v"(xb0[2]), "+v"(xb0[3]), "+v"(pq[0]), "+v"(pq[1]), "+v"(pq[2]), "+v"(pq[3]), "+v"(pww));
      {
        const int row = 2 * wv + (lane >> 5), p = ((lane & 31) >> 1) ^ (row & 7);
        const int chunk = ((p >> 2) & 1) * 8 + ((p >> 1) & 1) * 4 + ((p >> 3) & 1) * 2 + (p & 1);
        cx.voffw = (unsigned)(sbase(slot) + (nt * 32 + row) * 512 + chunk * 32 + (lane & 1) * 16);
        cx.voffx = (unsigned)(sbase(slot) + 65536 + b * 8192 + (8 * wv + ((lane >> 2) & 7)) * 128 + (lane >> 5) * 64 + (((lane & 3) ^ ((wv & 1) << 1)) << 4));
      }
    }
    asm volatile("" : "+v"(pvo), "+v"(cx.poff), "+v"(cx.xo), "+v"(cx.voffw), "+v"(cx.voffx));
    {
      const unsigned so1 = (unsigned)(16 + 2 * run_q(vb, 1)) * SBLK;
      const int e = lane & 31, n = (lane < 32 ? 20 - NH : 10) + (e >> 1);
      unsigned hst = e < (lane < 32 ? 2 * NH : 8) ? x_place(so1 + (n >= 15 ? 8 * SBLK : 0) + (e & 1) * SBLK, (unsigned)(run_bs(vb, 1) + n % 5)) : 0xf0000000u;
      if ((lane >> 1) == 20 && run_q(vb, 0)) hst = x_place((unsigned)(8 + 2 * run_q(vb, 0) + (e & 1)) * SBLK, (unsigned)(run_bs(vb, 0) + 4));
      loop_setup(cx, pwr, prd, hst);
    }
      };
    if (cx.id | wv) issue_x(0, wv, x_addr(vb, 0, wv), x_rsrc(x, vb), lds0);
    if (cx.id == 0 && wv) {
#pragma unroll
      for (int k = 0; k < 6; ++k)
        if (k < 5 || wv < 3) DMA16("nt", x_rsrc(x, vb), lds0 + (3 * k + wv - 1) * 4096, x_addr(vb, 0, 0), (3 * k + wv - 1) * 32768u);
    }
    SB0;
#pragma unroll
    for (int k = 0; k < NQ1; ++k) raw_load(r1[k], k);
#pragma unroll
    for (int i = 0; i < 4; ++i) ld16(fb[i >> 1][i & 1], TAB.F[ng][i >> 1][i & 1][lane]);
    ld16(hd.vb, TAB.V[0][ng][lane]);
    if (cx.id | wv) asm volatile("global_load_dwordx4 %0, %1, %2 sc1" : "=v"(pr) : "v"(0u), "s"(rec) : "memory");
    SB0;
    setup();
    SB0;
    if ((cx.id | wv) == 0) {
      wait_vm<3 + NQ1 + 5>();
      u32x4 &sx = cx.sx, &sw = cx.sw;
      asm volatile("" : "+v"(sx), "+v"(sw));
      unsigned m = expo(amax(amax(amax(amax(0, sx[0]), sx[1]), sx[2]), sx[3])) | expo(amax(amax(amax(amax(0, sw[0]), sw[1]), sw[2]), sw[3])) << 16;
      m = amax(m, (unsigned)__builtin_amdgcn_update_dpp(0, (int)m, 0x128, 0xf, 0xf, false));
      m = amax(m, (unsigned)__builtin_amdgcn_update_dpp(0, (int)m, 0x124, 0xf, 0xf, false));
      m = amax(m, (unsigned)__builtin_amdgcn_update_dpp(0, (int)m, 0x122, 0xf, 0xf, false));
      m = amax(m, (unsigned)__builtin_amdgcn_update_dpp(0, (int)m, 0x121, 0xf, 0xf, false));
      unsigned ex = 28, ew = 28;
#pragma unroll
      for (int l = 0; l < 64; l += 16) {
        const unsigned v = (unsigned)__builtin_amdgcn_readlane((int)m, l);
        ex = max(ex + 2, v & 255u) - 2;
        ew = max(ew + 3, v >> 16) - 3;
      }
      const unsigned long long tg = __builtin_amdgcn_s_memrealtime();
      const unsigned lo = (unsigned)tg << 4 | cx.xcc << 1, hi = ((unsigned)(tg >> 28) & 0xffffu) | min(ex, 220u) << 16 | min(ew, 220u) << 24;
      asm volatile("global_store_dwordx4 %0, %1, %2 sc1\n\ts_nop 1" :: "v"(threadIdx.x * 16), "v"(u32x4{lo, hi, ~lo, ~hi}), "s"(cx.ws + WS_PUB) : "memory");
      pr = u32x4{lo, hi, ~lo, ~hi};
      wait_vm<NQ1 + 5 + 1>();
      unsigned g[2] = {~0u, ~0u};
#pragma unroll 1
      for (int k = -1; k < PRK_ && ((k & 1) || min(g[0], g[1]) > 440u); ++k) {
        unsigned long long t0, t1;
        unsigned d;
        asm volatile("s_memtime %0\n\ts_waitcnt lgkmcnt(0)\n\ts_load_dword %2, %3, 0x0\n\ts_waitcnt lgkmcnt(0)\n\ts_memtime %1\n\ts_waitcnt lgkmcnt(0)"
                     : "=&s"(t0), "=&s"(t1), "=&s"(d) : "s"(rec + 32768 + (k < 0 ? 128 : (k & 1) * 8192 + (k & 2) * 2048 + (k & 4) * 4096 + (k & 8) * 128)) : "memory");
        if (k >= 0) { if (k & 1) g[1] = min(g[1], (unsigned)(t1 - t0)); else g[0] = min(g[0], (unsigned)(t1 - t0)); }
      }
      asm volatile("global_store_dwordx4 %0, %1, %2 sc1\n\ts_nop 1" :: "v"(lane * 16 + 2048), "v"(u32x4{lo | (((g[0] < g[1] ? 0u : 1u) ^ (cx.vb >> 2)) & 1u), hi, lo | (((g[0] < g[1] ? 0u : 1u) ^ (cx.vb >> 2)) & 1u), hi}), "s"(rec) : "memory");
    }
    issue_x(1, wv, xa1, x_rsrc(x, vb), lds0, 0, TE);
  }
  Rng gx, gw;
  wait_vm<6 + TE>();
#pragma unroll
  for (int q = 0; q < NQ1; ++q) {
    asm volatile("" : "+v"(r1[q]));
    *reinterpret_cast<u32x4*>(smem + (pww + q * 4096)) = r1[q];
  }
  SB0;
  unsigned near;
  {
    wait_vm<TE>();
    unsigned d0, d1;
    for (unsigned spins = 0;; ++spins) {
      asm volatile("" : "+v"(pr));
      d0 = __builtin_amdgcn_readfirstlane(pr[0]);
      d1 = __builtin_amdgcn_readfirstlane(pr[1]);
      const unsigned d2 = __builtin_amdgcn_readfirstlane(pr[2]), d3 = __builtin_amdgcn_readfirstlane(pr[3]);
      if (__builtin_expect((d0 == ~d2 && d1 == ~d3) || spins > (1u << 21), 1)) break;
      __builtin_amdgcn_s_sleep(2);
      asm volatile("global_load_dwordx4 %0, %1, %2 sc1\n\ts_waitcnt vmcnt(0)" : "=v"(pr) : "v"(0u), "s"(rec) : "memory");
    }
    tag = d0 | (unsigned long long)d1 << 32;
    const unsigned vn = (cx.id + (d0 >> 1)) & 7u;
    if (__builtin_expect(vn != (cx.vb & 7u), 0)) restart(cx.id | 256u | vn << 10);
  }
  const unsigned ee = (cx.id & 512u ? cx.id : (unsigned)(tag >> 32)) >> 16, ex = ee & 255u, ew = ee >> 8;
  cx.es = 256 - (int)(ex + ew);
  char* ws;
  const int bid = cx.vb;
  bf16x8& vb = hd.vb;
  u32x4 f0, f1;
  const u32x4* fa;
  u32x2 ov;
  unsigned pollv;
  const int rb[2] = {run_bs(bid, 0), run_bs(bid, 1)};
  asm volatile("" : "+v"(fb[0][0]), "+v"(fb[0][1]), "+v"(fb[1][0]), "+v"(fb[1][1]), "+v"(vb));
  scale8p(vb, 128 - (int)ew);
#pragma unroll
  for (int i = 0; i < 4; ++i) scale8p(fb[i >> 1][i & 1], 128 - (int)ex);
  asm volatile("" : "+v"(fb[0][0]), "+v"(fb[0][1]), "+v"(fb[1][0]), "+v"(fb[1][1]), "+v"(vb));
  asm volatile("s_waitcnt lgkmcnt(0)" ::: "memory");
  __builtin_amdgcn_s_barrier();
  SB0;
  u32x4 hx[2][20], hwl;
  f32x4 pend[2];
  auto rd = [&](int h, int i) __attribute__((always_inline)) {
    const int it = ord(h >> 1), mg = h & 1;
    if (it < 3) return tr_read(smem + (pq[i & 3] + mg * 2048 + (2 * it + (i >> 2)) * 4096));
    const int n = it - 3;
    const int c = 4 * ((n / 5) & 1) + 2 * mg + (i & 1);
    return tr_read(smem + ((c ? xa[i & 1] : xb0[n % 5 & 3]) + n % 5 * (HOP * 256) + (i >> 1) * (16 * 256)));
  };
  auto xaddr = [&](int h) __attribute__((always_inline)) {
    const int n = ord(h >> 1) - 3, mg = h & 1;
    if (h >= NIT || n < 0) return;
    if (h == 26) {
#pragma unroll
      for (int k = 0; k < 4; ++k) xb0[k] += XT1;
      asm volatile("" : "+v"(xb0[0]), "+v"(xb0[1]), "+v"(xb0[2]), "+v"(xb0[3]));
    }
#pragma unroll
    for (int i = 0; i < 2; ++i) {
      const int c = 4 * ((n / 5) & 1) + 2 * mg + i;
      if (c) { xa[i] = xb0[n % 5 & 3] ^ (unsigned)(c << 5); asm volatile("" : "+v"(xa[i])); }
    }
  };
  auto dst = [&](int h) -> u32x4& { const int it = ord(h >> 1); return it < 3 || it == 12 || it == 16 ? hwl : hx[h & 1][it - 3]; };
  auto item = [&](int h, XR& xr, auto side) __attribute__((always_inline)) {
    const XR c = xr;
    const int it = ord(h >> 1), mg = h & 1;
    const int nm = it == 2 ? 4 : 8, nr = h + 1 < NIT && ord((h + 1) >> 1) == 2 ? 4 : 8;
    const int pn = h > 0 && ord((h - 1) >> 1) == 2 ? 4 : 8;
    f32x4 r[8];
    bf16x8 dq[3];
    if (it < 3) { dq[0] = dup(c.t[0]); dq[1] = dup(c.t[1]); asm volatile("s_nop 0"); }
#pragma unroll
    for (int i = 0; i < 8; ++i) {
      if (i >= nm) break;
      const int s2 = i & 1, ks = (i >> 1) & 1;
      if (it < 3) mfma0(r[i], dq[i % 3], vb);
      else if (i < 2) mfma0(r[6 + i], cat(c.t[4 * ks + s2], c.t[4 * ks + 2 + s2]), fb[ks][1]);
      else mfma1(r[6 + s2], cat(c.t[4 * ks + s2], c.t[4 * ks + 2 + s2]), fb[ks][i < 4]);
      SB0;
      if (it < 3 && i + 2 < nm) dq[(i + 2) % 3] = dup(c.t[i + 2]);
      if (h + 1 < NIT && i < 4) {
        const bool nq = ord((h + 1) >> 1) < 3;
#pragma unroll
        for (int u = 0; u < 2; ++u) {
          const int k = 2 * i + u, j = nq ? k : (k & 4) + ((k >> 1) & 1) + (k & 1) * 2;
          if (j < nr) xr.t[j] = rd(h + 1, j);
        }
      }
      if (i == (nm == 4 ? 3 : 4)) xaddr(h + 2);
      if (h > 0 && (nm == 4 ? i >= 2 : i >= 4)) {
        u32x4& d = dst(h - 1);
#pragma unroll
        for (int e = 0; e < 4; ++e)
          if ((nm == 4 ? 2 + (e >> 1) : 4 + e) == i) d[e] = pk_f16(pend[e >> 1][(e & 1) * 2], pend[e >> 1][(e & 1) * 2 + 1]);
        if (i == nm - 1) {
          const int pi = ord((h - 1) >> 1);
          if (pi < 3) stash4(hw_reg((h - 1) & 1, pi == 2 ? 4 : 2 * pi + 1, 1), d);
          else if (pi == 12 || pi == 16) stash4(A_XA + (pi == 12 ? 8 : 0) + ((h - 1) & 1) * 4, d);
          else asm volatile("" : "+v"(d));
        }
      }
      if (it < 3 && i >= 3 && (i & 1)) {
        stash4(hw_reg(mg, 2 * it + ((i - 3) >> 2), ((i - 3) >> 1) & 1), pack8h(r[i - 3], r[i - 2]));
      }
      side(i * (8 / nm));
      if (nm == 4) side(2 * i + 1);
      SB0;
    }
    (void)pn;
    pend[0] = r[it < 3 ? nm - 2 : 6];
    pend[1] = r[it < 3 ? nm - 1 : 7];
  };
  const unsigned so0 = (unsigned)(2 * run_q(bid, 0)) * SBLK, so1 = (unsigned)(16 + 2 * run_q(bid, 1)) * SBLK;
  auto store = [&](int J) __attribute__((always_inline)) {
    const int j = J >> 1, mg = J & 1;
    if (J == 0) {
      wait_vm<0>();
      for (unsigned spins = 0;; ++spins) {
        asm volatile("" : "+v"(ov));
        near = __builtin_amdgcn_readfirstlane(ov[0]) ^ (unsigned)tag;
        if (__builtin_expect((near < 2u && __builtin_amdgcn_readfirstlane(ov[1]) == (unsigned)(tag >> 32)) || spins > (1u << 21), 1)) break;
        __builtin_amdgcn_s_sleep(2);
        asm volatile("global_load_dwordx2 %0, %1, %2 sc1\n\ts_waitcnt vmcnt(0)" : "=v"(ov) : "v"(2048u), "s"(rec) : "memory");
      }
      ws = cx.ws += (near & 1u) * 8192;
      cx.rs = make_rsrc(ws + WS_S, WS_L - WS_S + D_ * D_ * 2);
    }
    if (J == 1) {
      if (wv == 1) {
        const unsigned o = (lane < 32 ? WS_STATS : WS_S + FLG + 16384 - 512) + bid * 512 + lane * 16;
        const unsigned v = lane < 32 ? ~0u : 0u;
        asm volatile("global_store_dwordx4 %0, %1, %2 sc1\n\ts_nop 1" :: "v"(o), "v"(u32x4{v, v, v, v}), "s"(ws) : "memory");
        if (lane < 8) __hip_atomic_store((gu32*)(ws + WS_S + FLG + lane * 1024 + ((bid + 2) & 3) * 256 + (bid >> 2) * 4), 0u, RLX_AGENT);
      }
    }
    if (J == NB + NFA) {
      if (((bid >> 1) ^ (bid >> 3) ^ 1) & 1 && threadIdx.x < 160) {
        const int t = threadIdx.x >= 80, blk = (threadIdx.x >> 4) - 5 * t;
        const unsigned o = (unsigned)(t * XT1 + blk * (HOP * 256) + (((lane & 15) ^ ((blk & 3) << 2)) << 4));
        const u32x4 lo = *reinterpret_cast<const u32x4*>(smem + o), hi = *reinterpret_cast<const u32x4*>(smem + o + 64 * 256);
        u32x4 d;
#pragma unroll
        for (int e = 0; e < 4; ++e) d[e] = pk_bf16(bf_lo(hi[e]) - bf_lo(lo[e]), bf_hi(hi[e]) - bf_hi(lo[e]));
        st16(d, cx.rs, (unsigned)((((bid >> 2) & 1) * NBLK + (t ? rb[1] : rb[0]) + blk) * 1024 + run_q(bid, t) * 256 + (lane & 15) * 16), WS_D - WS_S);
      }
      asm volatile("buffer_store_dwordx4 a[%c3:%c4], %0, %1, %2 offen sc1" :: "v"(wv >= 2 ? (threadIdx.x & 127) * 16 : 0x80000000u), "s"(cx.rs), "s"(WS_L - WS_S + bid * 2048), "i"(A_WL), "i"(A_WL + 3) : "memory");
    }
    const int wq = st_w(j), xn = st_x(j);
    if (wq >= 0)
      asm volatile("buffer_store_dwordx4 a[%c3:%c4], %0, %1, %2 offen sc1" :: "v"(cx.poff), "s"(cx.rs),
                   "s"((unsigned)(wq >> 1) * (2 * SBLK) + (unsigned)((bid + (wq >> 1)) & 3) * 16384u + (wq & 1) * 128u + mg * 256u),
                   "i"(hw_reg(mg, wq >> 1, wq & 1)), "i"(hw_reg(mg, wq >> 1, wq & 1) + 3) : "memory");
    else if (xn == 9)
      asm volatile("buffer_store_dwordx4 a[%c3:%c4], %0, %1, %2 offen sc1" :: "v"(cx.xo), "s"(cx.rs),
                   "s"(run_q(bid, 0) ? 0xf0000000u : x_place(so0 + 8 * SBLK + mg * SBLK, (unsigned)(rb[0] + 4))), "i"(A_XA + 8 + mg * 4), "i"(A_XA + 11 + mg * 4) : "memory");
    else st16(hx[mg][xn], cx.rs, cx.xo, x_place((xn < 10 ? so0 : so1) + ((xn / 5) & 1) * (8 * SBLK) + mg * SBLK, (unsigned)(rb[xn / 10] + xn % 5)));
    if (J == NA + NFA - 1) {
      const unsigned mx = amax(gx), mw = amax(gw);
      if (__builtin_expect(__any(expo(mx) > ex + 7 || expo(mw) > ew + 9) && !(cx.id & 512u), 0)) {
        const u32x2 m = {wave_max(expo(mx)), wave_max(expo(mw))};
        asm volatile("global_atomic_umax %0, %1, %3 sc1\n\tglobal_atomic_umax %0, %2, %3 offset:4 sc1\n\ts_waitcnt vmcnt(0)"
                     :: "v"(lane * 16 + 1024), "v"(m[0]), "v"(m[1]), "s"(rec - ((cx.id & 7) * 8 + wv) * 16) : "memory");
      }
    }
    if (J == NA + NFA - 1 || J == NB + NFA - 1) {
      if (J < NB) {
      } else {
      }
      wait_vm<NFA>();
      if (J < NB) {
      } else {
      }
      __builtin_amdgcn_s_barrier();
      if (J < NB) {
      } else {
      }
      if (wv == 0 && lane < 8) {
        if (J < NB) __hip_atomic_store((gu64*)(ws + WS_FA + (cx.id & 512u) * 32 + lane * 2048) + bid, tag, RLX_AGENT);
        else __hip_atomic_store((gu32*)(ws + WS_S + FLG + lane * 1024 + ((bid + 2) & 3) * 256 + (bid >> 2) * 4), 1u, RLX_AGENT);
      }
    }
    SB0;
  };
  u32x4 sv[3];
  auto check = [&](auto hc, int s) __attribute__((always_inline)) {
    constexpr int h = hc.v;
    if constexpr (sc_wait(h) >= 0) { if (s == 0) wait_vm<sc_wait(h) < 0 ? 0 : sc_wait(h)>(); }
#pragma unroll
    for (int k = 0; k < 34; ++k) {
      if (sc_pos(k) != h) continue;
      int o = 0;
#pragma unroll
      for (int k2 = 0; k2 < k; ++k2) o += sc_pos(k2) == h;
      const int t = k / 17, j = k % 17;
      if (s == o) sv[o] = *reinterpret_cast<const u32x4*>(smem + (t * XT1 + (j < 16 || wv < 3 ? j : 15) * 4096 + threadIdx.x * 16));
      if (s == 5 + o) {
        widen(gx, sv[o][0], sv[o][1]);
        widen(gx, sv[o][2], sv[o][3]);
      }
    }
    if (s == 4)
      sfor<0, 16>([&](auto k) __attribute__((always_inline)) {
        if (sc_raw(k.v) != h) return;
        u32x4 r;
        if constexpr (k.v <= NQ1) {
          u32x4& q = k.v < NQ1 ? r1[k.v < NQ1 ? k.v : 0] : rq0;
          pin(q);
          r = q;
        } else {
          constexpr int a = A_RAW + 4 * (k.v - NQ1 - 1);
          r = u32x4{unstash<a>(), unstash<a + 1>(), unstash<a + 2>(), unstash<a + 3>()};
        }
        widen(gw, r[0], r[1]);
        widen(gw, r[2], r[3]);
      });
    if (s == 1 && h <= P0 + NBP) asm volatile("" : "+v"(gx.hi), "+v"(gx.lo), "+v"(gw.hi), "+v"(gw.lo));
  };
  auto loads = [&](auto p, int s) __attribute__((always_inline)) {
    sfor<0, NLD>([&](auto l) __attribute__((always_inline)) {
      if (ld_pos(l.v) != p.v || ld_slot(l.v) != s) return;
      constexpr int n = l.v - (17 - TE);
      if constexpr (n < 0) {
        issue_x(1, wv, xa1, x_rsrc(x, bid), lds0, TE + l.v, TE + l.v + 1);
      } else if constexpr (n == 0) {
        load_acc<A_WL>(wv >= 2 ? (threadIdx.x & 127) * 16 : 0x80000000u, rw, 30 * D_ * D_ * 2 + bid * 2048);
      } else if constexpr (n == 1) {
        raw_load(rq0, NQ1);
      } else if constexpr (n <= NQW) {
        load_acc<A_RAW + 4 * (n - 2)>(roff, rw, raw_so(NQ1 + n - 1));
      } else {
        ld8(ov, rec, 2048u);
      }
    });
  };
  XR xr;
#pragma unroll
  for (int j = 0; j < 8; ++j) xr.t[j] = rd(0, j);
  static_assert(ord(0) < 3 && ord(1) >= 3 && ord(0) != 2, "positions 0, 1: weights");
  SB0;
  static_assert(st_first(NIT) == NS && PG >= 28 && st_first(PG - 4) > NB + NFA && NA + NFA <= NB && NFA >= 1 && NH <= 7, "order of the hand-off steps");
  sfor<0, NIT>([&](auto p) __attribute__((always_inline)) {
    constexpr int j0 = st_first(p.v), j1 = st_first(p.v + 1);
    if (SLP_ && p >= 24 && p < PG && !(p & 1)) { if (!(bid & 1) && cx.id) __builtin_amdgcn_s_sleep(SLP_); }
    if (p == XBAR) __builtin_amdgcn_s_barrier();
    if (p == PG - 4) {
      code_touch(cx);
      fa = reinterpret_cast<const u32x4*>(ws + WS_FA + (cx.id & 512u) * 32 + (bid & 7) * 2048) + lane;
      if (wv == 0)
        asm volatile("global_load_dwordx4 %0, %2, off sc1\n\tglobal_load_dwordx4 %1, %2, off offset:1024 sc1" : "=&v"(f0), "=&v"(f1) : "v"(fa) : "memory");
    }
    if (p == PG) {
      if (wv == 0) {
        wait_vm<st_first(PG) - st_first(PG - 4)>();
        for (unsigned spins = 0; spins < (1u << 21); ++spins) {
          asm volatile("" : "+v"(f0), "+v"(f1));
          const unsigned t0 = (unsigned)tag, t1 = (unsigned)(tag >> 32);
          if (__all(f0[0] == t0 && f0[1] == t1 && f0[2] == t0 && f0[3] == t1 && f1[0] == t0 && f1[1] == t1 && f1[2] == t0 && f1[3] == t1)) break;
          __builtin_amdgcn_s_sleep(2);
          asm volatile("global_load_dwordx4 %0, %2, off sc1\n\tglobal_load_dwordx4 %1, %2, off offset:1024 sc1\n\ts_waitcnt vmcnt(0)"
                       : "=&v"(f0), "=&v"(f1) : "v"(fa) : "memory");
        }
      }
      __builtin_amdgcn_s_barrier();
      stage_issue(cx, -1);
      asm volatile("global_load_dwordx2 %0, %1, %2 sc1" : "={v[124:125]}"(ov) : "v"(1024u), "s"(rec) : "memory");
      stage_issue(cx, 0);
      SB0;
    }
    if (p == NIT - 2)
      asm volatile("buffer_load_dword %0, %1, %2, %3 offen sc1" : "={v247}"(pollv) : "v"(pvo), "s"(cx.rs), "s"(FLG) : "memory");
    item(p, xr, [&](int i) __attribute__((always_inline)) {
      if ((i * (j1 - j0) + 7) / 8 < ((i + 1) * (j1 - j0) + 7) / 8) store(j0 + (i * (j1 - j0) + 7) / 8);
      check(p, i);
      loads(p, i);
    });
  });
  asm volatile("s_nop 7" : "+v"(pend[0]), "+v"(pend[1]));
  hx[1][19] = pack8h(pend[0], pend[1]);
  asm volatile("" : "+v"(hx[1][19]));
#pragma unroll
  for (int e = 0; e < 2 * NH; ++e) hd.held[e] = hx[e & 1][20 - NH + (e >> 1)];
#pragma unroll
  for (int e = 0; e < 2 * NST; ++e) hd.st[e] = hx[e & 1][10 + (e >> 1)];
  *reinterpret_cast<u32x4*>(smem + RAW0 + pwr) = rq0;
  hd.ov = ov;
  hd.pollv = pollv;
  hd.rec = rec;
  hd.ctl = __builtin_amdgcn_readfirstlane((cx.id & 255u) | (unsigned)(bid & 7) << 8 | (cx.id & 512u) << 2 | ((cx.id & 255u) == 0 && wv == 0 ? 4096u : 0u) | (unsigned)wv << 13 | ex << 16 | ew << 24);
}
constexpr int NW = NS - st_first(NIT - 2);

__device__ __forceinline__ void run12(Ctx& cx, const unsigned short* __restrict__ x, const unsigned short* __restrict__ w) {
  Hand hd;
  phase1(cx, x, w, hd);
  u32x16 h1, st0;
  u32x8 st1;
  u32x32 h0;
#pragma unroll
  for (int i = 0; i < 32; ++i) {
    h0[i] = hd.held[i >> 2][i & 3];
    if (i < 16) { h1[i] = hd.held[8 + (i >> 2)][i & 3]; st0[i] = hd.st[i >> 2][i & 3]; }
    if (i < 8) st1[i] = hd.st[4 + (i >> 2)][i & 3];
  }
  u32x16 o0, o4;
  u32x8 o1;
  u32x4 o5;
  u32x32 o3;
  u32x2 o6;
  unsigned o7;
  asm volatile(
  "v_accvgpr_read_b32 v255, a214\n"
  "v_accvgpr_read_b32 v228, a188\n"
  "v_accvgpr_read_b32 v229, a189\n"
  "v_accvgpr_read_b32 v230, a190\n"
  "v_accvgpr_read_b32 v231, a191\n"
  "v_accvgpr_read_b32 v232, a192\n"
  "v_accvgpr_read_b32 v233, a193\n"
  "v_accvgpr_read_b32 v234, a194\n"
  "v_accvgpr_read_b32 v235, a195\n"
  "v_accvgpr_read_b32 v236, a196\n"
  "v_accvgpr_read_b32 v237, a197\n"
  "v_accvgpr_read_b32 v238, a198\n"
  "v_accvgpr_read_b32 v239, a199\n"
  "v_accvgpr_read_b32 v240, a200\n"
  "v_accvgpr_read_b32 v241, a201\n"
  "v_accvgpr_read_b32 v242, a202\n"
  "v_accvgpr_read_b32 v243, a203\n"
  "v_accvgpr_read_b32 v244, a204\n"
  "v_accvgpr_read_b32 v245, a205\n"
  "v_accvgpr_read_b32 v246, a206\n"
  "v_accvgpr_read_b32 v248, a208\n"
  "v_accvgpr_read_b32 v249, a209\n"
  "v_accvgpr_read_b32 v250, a210\n"
  "v_accvgpr_read_b32 v251, a211\n"
  "v_accvgpr_read_b32 v252, a212\n"
  "v_accvgpr_read_b32 v253, a213\n"
  "v_readlane_b32 s44, v255, 0\n"
  "v_readlane_b32 s45, v255, 1\n"
  "v_readlane_b32 s62, v255, 5\n"
  "v_readlane_b32 s51, v255, 2\n"
  "v_readlane_b32 s52, v255, 3\n"
  "v_readlane_b32 s55, v255, 4\n"
  "v_readlane_b32 s56, v255, 4\n"
  "v_readlane_b32 s76, v255, 6\n"
  "v_readlane_b32 s77, v255, 6\n"
  "v_readlane_b32 s80, v255, 7\n"
  "v_readlane_b32 s81, v255, 8\n"
  "v_readlane_b32 s82, v255, 9\n"
  "v_readlane_b32 s83, v255, 10\n"
  "v_readlane_b32 s84, v255, 11\n"
  "v_readlane_b32 s85, v255, 12\n"
  "v_readlane_b32 s88, v255, 13\n"
  "v_readlane_b32 s89, v255, 14\n"
  "v_mov_b32 v172, 0\n"
  "v_mov_b32 v173, 0\n"
  "v_mov_b32 v174, 0\n"
  "v_mov_b32 v175, 0\n"
  "s_add_i32 s67, s44, s45\n"
  "s_mov_b32 s46, 36864\n"
  "s_mov_b32 s47, s44\n"
  "s_mov_b32 s48, s67\n"
  "s_mov_b32 s49, 36864\n"
  "s_mov_b32 s50, 8388608\n"
  "s_mov_b32 s53, 134217728\n"
  "s_mov_b32 s54, 134217728\n"
  "s_mov_b32 s57, -131072\n"
  "s_mov_b32 s58, 0xf0000000\n"
  "s_mov_b32 s59, 0xf0000000\n"
  "s_mov_b32 s60, -1\n"
  "s_mov_b32 s68, -73728\n"
  "s_mov_b32 s69, 36864\n"
  "s_add_u32 s72, s40, 41943040\n"
  "s_addc_u32 s73, s41, 0\n"
  "s_mov_b32 s74, 20971520\n"
  "s_mov_b32 s75, 0x20000\n"
  "s_waitcnt vmcnt(%c[nw])\n"
  "v_readfirstlane_b32 s70, v124\n"
  "v_readfirstlane_b32 s71, v125\n"
  "v_cmp_eq_u32 vcc, 0, v247\n"
  "s_or_b32 s64, s70, s71\n"
  "s_bitcmp1_b32 %[ctl], 11\n"
  "s_cselect_b32 s66, 0, s64\n"
  "s_cmp_lg_u32 s66, 0\n"
  "s_cbranch_scc1 .Lredo_%=\n"
  "s_bitcmp1_b32 %[ctl], 12\n"
  "s_cbranch_scc0 .Lnoclr_%=\n"
  "v_mbcnt_lo_u32_b32 v126, -1, 0\n"
  "v_mbcnt_hi_u32_b32 v126, -1, v126\n"
  "v_lshlrev_b32 v126, 4, v126\n"
  "global_store_dwordx4 v126, v[172:175], %[rec] sc1\n"
  "s_nop 1\n"
  ".Lnoclr_%=:\n"
  "s_cbranch_vccnz .Lspin_pro_%=\n"
  ".Lspun_pro_%=:\n"
  "s_waitcnt lgkmcnt(0)\n"
  "s_barrier\n"
  "ds_read_b64_tr_b16 v[0:1], v228 offset:0\n"
  "ds_read_b64_tr_b16 v[2:3], v228 offset:8192\n"
  "ds_read_b64_tr_b16 v[16:17], v228 offset:256\n"
  "ds_read_b64_tr_b16 v[18:19], v228 offset:8448\n"
  "ds_read_b64_tr_b16 v[4:5], v229 offset:0\n"
  "ds_read_b64_tr_b16 v[6:7], v229 offset:8192\n"
  "ds_read_b64_tr_b16 v[20:21], v229 offset:256\n"
  "ds_read_b64_tr_b16 v[22:23], v229 offset:8448\n"
  "ds_read_b64_tr_b16 v[8:9], v230 offset:0\n"
  "ds_read_b64_tr_b16 v[10:11], v230 offset:8192\n"
  "ds_read_b64_tr_b16 v[24:25], v230 offset:256\n"
  "ds_read_b64_tr_b16 v[26:27], v230 offset:8448\n"
  "ds_read_b64_tr_b16 v[12:13], v231 offset:0\n"
  "ds_read_b64_tr_b16 v[14:15], v231 offset:8192\n"
  "ds_read_b64_tr_b16 v[28:29], v231 offset:256\n"
  "ds_read_b64_tr_b16 v[30:31], v231 offset:8448\n"
  "ds_read_b128 v[96:99], v232 offset:0\n"
  "ds_read_b128 v[100:103], v232 offset:512\n"
  "ds_read_b128 v[104:107], v232 offset:2048\n"
  "ds_read_b128 v[108:111], v232 offset:2560\n"
  "s_add_i32 m0, s44, 73728\n"
  "s_mov_b32 s64, 8388608\n"
  "buffer_load_dwordx4 v237, s[40:43], s64 offen sc1 lds\n"
  "s_add_i32 m0, s44, 77824\n"
  "s_mov_b32 s64, 8392704\n"
  "buffer_load_dwordx4 v237, s[40:43], s64 offen sc1 lds\n"
  "s_add_i32 m0, s44, 81920\n"
  "s_mov_b32 s64, 8396800\n"
  "buffer_load_dwordx4 v237, s[40:43], s64 offen sc1 lds\n"
  "s_add_i32 m0, s44, 86016\n"
  "s_mov_b32 s64, 8400896\n"
  "buffer_load_dwordx4 v237, s[40:43], s64 offen sc1 lds\n"
  "s_add_i32 m0, s44, 90112\n"
  "s_mov_b32 s64, 8388608\n"
  "buffer_load_dwordx4 v238, s[40:43], s64 offen sc1 lds\n"
  "s_add_i32 m0, s44, 94208\n"
  "s_mov_b32 s64, 4198400\n"
  "buffer_load_dwordx4 v238, s[40:43], s64 offen sc1 lds\n"
  "s_add_i32 m0, s67, 98304\n"
  "s_mov_b32 s64, 4210688\n"
  "buffer_load_dwordx4 v238, s[40:43], s64 offen sc1 lds\n"
  "s_add_i32 m0, s44, 100352\n"
  "s_mov_b32 s64, 8409088\n"
  "buffer_load_dwordx4 v238, s[40:43], s64 offen sc1 lds\n"
  "s_add_i32 m0, s44, 104448\n"
  "s_mov_b32 s64, 4227072\n"
  "buffer_load_dwordx4 v238, s[40:43], s64 offen sc1 lds\n"
  "s_memrealtime s[70:71]\n"
  "v_mov_b32 v247, 1\n"
  "s_waitcnt lgkmcnt(0)\n"
  "s_add_i32 s61, s70, s62\n"
  "s_add_i32 s61, s61, 14\n"
  "v_pk_add_f16 v32, v0, v16\n"
  "v_pk_add_f16 v33, v1, v17\n"
  "v_pk_add_f16 v34, v2, v18\n"
  "v_pk_add_f16 v35, v3, v19\n"
  "v_pk_add_f16 v36, v4, v20\n"
  "v_pk_add_f16 v37, v5, v21\n"
  "v_pk_add_f16 v38, v6, v22\n"
  "v_pk_add_f16 v39, v7, v23\n"
  "v_pk_add_f16 v40, v8, v24\n"
  "v_pk_add_f16 v41, v9, v25\n"
  "v_pk_add_f16 v42, v10, v26\n"
  "v_pk_add_f16 v43, v11, v27\n"
  "v_pk_add_f16 v44, v12, v28\n"
  "v_pk_add_f16 v45, v13, v29\n"
  "v_pk_add_f16 v46, v14, v30\n"
  "v_pk_add_f16 v47, v15, v31\n"
  "v_add_u32 v228, 36864, v228\n"
  "v_add_u32 v229, 36864, v229\n"
  "v_add_u32 v230, 36864, v230\n"
  "v_add_u32 v231, 36864, v231\n"
  "v_add_u32 v232, 36864, v232\n"
  "v_mfma_f32_16x16x32_f16 a[0:3], v[0:3], v[96:99], 0\n"
  "v_readlane_b32 s65, v244, 32\n"
  "v_pk_add_f16 v120, v96, v100\n"
  "v_mfma_f32_16x16x32_f16 a[12:15], v[4:7], v[96:99], 0\n"
  "v_pk_add_f16 v121, v97, v101\n"
  "v_pk_add_f16 v122, v98, v102\n"
  "v_mfma_f32_16x16x32_f16 a[24:27], v[8:11], v[96:99], 0\n"
  "s_add_i32 m0, s47, 0\n"
  "ds_read_b64_tr_b16 v[48:49], v228 offset:0\n"
  "v_pk_add_f16 v123, v99, v103\n"
  "v_mfma_f32_16x16x32_f16 a[36:39], v[12:15], v[96:99], 0\n"
  "s_add_i32 s64, s50, 4194304\n"
  "buffer_load_dwordx4 v237, s[40:43], s64 offen sc1 lds\n"
  "v_readlane_b32 s66, v244, 33\n"
  "v_mfma_f32_16x16x32_f16 a[4:7], v[16:19], v[100:103], 0\n"
  "s_cmp_eq_u32 s46, 73728\n"
  "s_cselect_b32 s49, s68, s69\n"
  "ds_read_b64_tr_b16 v[124:125], v233 offset:0\n"
  "v_mfma_f32_16x16x32_f16 a[16:19], v[20:23], v[100:103], 0\n"
  "ds_read_b64_tr_b16 v[128:129], v234 offset:0\n"
  "ds_read_b64_tr_b16 v[50:51], v228 offset:8192\n"
  "v_mfma_f32_16x16x32_f16 a[28:31], v[24:27], v[100:103], 0\n"
  "buffer_store_dwordx4 v[80:83], v243, s[40:43], s65 offen sc1\n"
  "ds_read_b128 v[96:99], v232 offset:4096\n"
  "v_mfma_f32_16x16x32_f16 a[40:43], v[28:31], v[100:103], 0\n"
  "ds_read_b64_tr_b16 v[132:133], v235 offset:0\n"
  "v_readlane_b32 s65, v244, 34\n"
  "v_mfma_f32_16x16x32_f16 a[8:11], v[32:35], v[120:123], 0\n"
  "s_add_i32 m0, s47, 4096\n"
  "ds_read_b64_tr_b16 v[64:65], v228 offset:256\n"
  "ds_read_b128 v[100:103], v232 offset:4608\n"
  "v_mfma_f32_16x16x32_f16 a[20:23], v[36:39], v[120:123], 0\n"
  "s_add_i32 s64, s50, 4198400\n"
  "buffer_load_dwordx4 v237, s[40:43], s64 offen sc1 lds\n"
  "ds_read_b64_tr_b16 v[136:137], v236 offset:0\n"
  "v_mfma_f32_16x16x32_f16 a[32:35], v[40:43], v[120:123], 0\n"
  "ds_read_b64_tr_b16 v[66:67], v228 offset:8448\n"
  "v_add_u32 v228, s49, v228\n"
  "v_mfma_f32_16x16x32_f16 a[44:47], v[44:47], v[120:123], 0\n"
  "ds_read_b64_tr_b16 v[52:53], v229 offset:0\n"
  "ds_read_b64_tr_b16 v[54:55], v229 offset:8192\n"
  "v_mfma_f32_16x16x32_f16 a[48:51], v[0:3], v[104:107], 0\n"
  "buffer_store_dwordx4 v[84:87], v243, s[40:43], s66 offen sc1\n"
  "v_pk_add_f16 v120, v104, v108\n"
  "v_mfma_f32_16x16x32_f16 a[60:63], v[4:7], v[104:107], 0\n"
  "v_pk_add_f16 v121, v105, v109\n"
  "v_pk_add_f16 v122, v106, v110\n"
  "v_mfma_f32_16x16x32_f16 a[72:75], v[8:11], v[104:107], 0\n"
  "s_add_i32 m0, s47, 8192\n"
  "ds_read_b64_tr_b16 v[68:69], v229 offset:256\n"
  "v_pk_add_f16 v123, v107, v111\n"
  "v_mfma_f32_16x16x32_f16 a[84:87], v[12:15], v[104:107], 0\n"
  "s_add_i32 s64, s50, 4202496\n"
  "buffer_load_dwordx4 v237, s[40:43], s64 offen sc1 lds\n"
  "v_readlane_b32 s66, v244, 35\n"
  "v_mfma_f32_16x16x32_f16 a[52:55], v[16:19], v[108:111], 0\n"
  "s_waitcnt lgkmcnt(4)\n"
  "ds_read_b64_tr_b16 v[70:71], v229 offset:8448\n"
  "v_mov_b64 v[126:127], v[124:125]\n"
  "v_mfma_f32_16x16x32_f16 a[64:67], v[20:23], v[108:111], 0\n"
  "s_waitcnt lgkmcnt(4)\n"
  "ds_read_b128 v[104:107], v232 offset:6144\n"
  "v_mov_b64 v[130:131], v[128:129]\n"
  "v_mfma_f32_16x16x32_f16 a[76:79], v[24:27], v[108:111], 0\n"
  "buffer_store_dwordx4 v[88:91], v243, s[40:43], s65 offen sc1\n"
  "v_mov_b64 v[134:135], v[132:133]\n"
  "v_mfma_f32_16x16x32_f16 a[88:91], v[28:31], v[108:111], 0\n"
  "ds_read_b64_tr_b16 v[56:57], v230 offset:0\n"
  "v_readlane_b32 s65, v244, 36\n"
  "v_mfma_f32_16x16x32_f16 a[56:59], v[32:35], v[120:123], 0\n"
  "s_add_i32 m0, s47, 12288\n"
  "ds_read_b128 v[108:111], v232 offset:6656\n"
  "v_mov_b64 v[138:139], v[136:137]\n"
  "v_mfma_f32_16x16x32_f16 a[68:71], v[36:39], v[120:123], 0\n"
  "s_add_i32 s64, s50, 4206592\n"
  "buffer_load_dwordx4 v237, s[40:43], s64 offen sc1 lds\n"
  "v_mfma_f32_16x16x32_f16 a[80:83], v[40:43], v[120:123], 0\n"
  "ds_read_b64_tr_b16 v[58:59], v230 offset:8192\n"
  "v_pk_add_f16 v80, v48, v64\n"
  "v_mfma_f32_16x16x32_f16 a[92:95], v[44:47], v[120:123], 0\n"
  "s_waitcnt lgkmcnt(4)\n"
  "ds_read_b64_tr_b16 v[72:73], v230 offset:256\n"
  "v_pk_add_f16 v81, v49, v65\n"
  "v_mfma_f32_16x16x32_f16 a[96:99], v[0:3], v[96:99], 0\n"
  "buffer_store_dwordx4 v[92:95], v243, s[40:43], s66 offen sc1\n"
  "v_pk_add_f16 v120, v96, v100\n"
  "v_mfma_f32_16x16x32_bf16 v[140:143], v[124:127], v[176:179], 0\n"
  "ds_read_b64_tr_b16 v[74:75], v230 offset:8448\n"
  "v_pk_add_f16 v121, v97, v101\n"
  "v_mfma_f32_16x16x32_f16 a[108:111], v[4:7], v[96:99], 0\n"
  "s_add_i32 m0, s47, 16384\n"
  "v_pk_add_f16 v122, v98, v102\n"
  "v_pk_add_f16 v123, v99, v103\n"
  "v_mfma_f32_16x16x32_f16 a[120:123], v[8:11], v[96:99], 0\n"
  "s_add_i32 s64, s50, 4194304\n"
  "buffer_load_dwordx4 v238, s[40:43], s64 offen sc1 lds\n"
  "v_readlane_b32 s66, v244, 37\n"
  "v_mfma_f32_16x16x32_f16 a[132:135], v[12:15], v[96:99], 0\n"
  "ds_read_b64_tr_b16 v[60:61], v231 offset:0\n"
  "v_cvt_pkrtz_f16_f32 v124, v140, v141\n"
  "v_mfma_f32_16x16x32_bf16 v[144:147], v[128:131], v[176:179], 0\n"
  "ds_read_b64_tr_b16 v[62:63], v231 offset:8192\n"
  "v_cvt_pkrtz_f16_f32 v125, v142, v143\n"
  "v_mfma_f32_16x16x32_f16 a[100:103], v[16:19], v[100:103], 0\n"
  "buffer_store_dwordx4 v[156:159], v243, s[40:43], s65 offen sc1\n"
  "v_and_b32 v124, 0xfff8fff8, v124\n"
  "v_mfma_f32_16x16x32_f16 a[112:115], v[20:23], v[100:103], 0\n"
  "ds_read_b128 v[96:99], v232 offset:0\n"
  "v_and_b32 v125, 0xfff8fff8, v125\n"
  "v_mfma_f32_16x16x32_f16 a[124:127], v[24:27], v[100:103], 0\n"
  "s_add_i32 m0, s47, 20480\n"
  "ds_read_b128 v[112:115], v232 offset:8192\n"
  "v_cvt_pkrtz_f16_f32 v126, v144, v145\n"
  "v_mfma_f32_16x16x32_bf16 v[148:151], v[132:135], v[176:179], 0\n"
  "s_add_i32 s64, s50, 4096\n"
  "buffer_load_dwordx4 v238, s[40:43], s64 offen sc1 lds\n"
  "v_cvt_pkrtz_f16_f32 v127, v146, v147\n"
  "v_mfma_f32_16x16x32_f16 a[136:139], v[28:31], v[100:103], 0\n"
  "s_waitcnt lgkmcnt(4)\n"
  "ds_write_b128 v252, a[144:147] offset:4096\n"
  "v_and_b32 v126, 0xfff8fff8, v126\n"
  "v_mfma_f32_16x16x32_f16 a[104:107], v[32:35], v[120:123], 0\n"
  "ds_read_b64_tr_b16 v[76:77], v231 offset:256\n"
  "v_and_b32 v127, 0xfff8fff8, v127\n"
  "v_mfma_f32_16x16x32_f16 a[116:119], v[36:39], v[120:123], 0\n"
  "buffer_store_dwordx4 v[160:163], v243, s[40:43], s66 offen sc1\n"
  "v_cvt_pkrtz_f16_f32 v128, v148, v149\n"
  "v_mfma_f32_16x16x32_bf16 v[152:155], v[136:139], v[176:179], 0\n"
  "ds_write_b128 v252, a[156:159] offset:16384\n"
  "v_cvt_pkrtz_f16_f32 v129, v150, v151\n"
  "v_mfma_f32_16x16x32_f16 a[128:131], v[40:43], v[120:123], 0\n"
  "s_add_i32 m0, s48, 24576\n"
  "ds_read_b128 v[100:103], v232 offset:512\n"
  "v_and_b32 v128, 0xfff8fff8, v128\n"
  "v_mfma_f32_16x16x32_f16 a[140:143], v[44:47], v[120:123], 0\n"
  "s_add_i32 s64, s50, 16384\n"
  "buffer_load_dwordx4 v238, s[40:43], s64 offen sc1 lds\n"
  "v_and_b32 v129, 0xfff8fff8, v129\n"
  "v_mfma_f32_16x16x32_f16 a[144:147], v[0:3], v[104:107], 0\n"
  "ds_read_b128 v[116:119], v232 offset:8704\n"
  "v_pk_add_f16 v120, v104, v108\n"
  "v_mfma_f32_16x16x32_f16 a[156:159], v[4:7], v[104:107], 0\n"
  "ds_write_b128 v252, a[168:171] offset:28672\n"
  "v_pk_add_f16 v121, v105, v109\n"
  "v_mfma_f32_16x16x32_f16 a[168:171], v[8:11], v[104:107], 0\n"
  "buffer_store_dwordx4 v[124:127], v242, s[40:43], s56 offen offset:0 sc1\n"
  "ds_read_b64_tr_b16 v[78:79], v231 offset:8448\n"
  "ds_write_b128 v252, a[180:183] offset:40960\n"
  "v_mfma_f32_16x16x32_f16 a[180:183], v[12:15], v[104:107], 0\n"
  "ds_write_b128 v252, a[148:151] offset:8192\n"
  "v_pk_add_f16 v122, v106, v110\n"
  "v_mfma_f32_16x16x32_f16 a[148:151], v[16:19], v[108:111], 0\n"
  "s_add_i32 m0, s47, 26624\n"
  "ds_write_b128 v252, a[160:163] offset:20480\n"
  "v_pk_add_f16 v123, v107, v111\n"
  "v_mfma_f32_16x16x32_f16 a[160:163], v[20:23], v[108:111], 0\n"
  "s_add_i32 s64, s50, 4214784\n"
  "buffer_load_dwordx4 v238, s[40:43], s64 offen sc1 lds\n"
  "ds_write_b128 v252, a[172:175] offset:32768\n"
  "v_mfma_f32_16x16x32_f16 a[172:175], v[24:27], v[108:111], 0\n"
  "ds_write_b128 v252, a[152:155] offset:12288\n"
  "v_cvt_pkrtz_f16_f32 v130, v152, v153\n"
  "v_mfma_f32_16x16x32_f16 a[184:187], v[28:31], v[108:111], 0\n"
  "s_add_i32 m0, s47, 30720\n"
  "ds_write_b128 v252, a[164:167] offset:24576\n"
  "v_cvt_pkrtz_f16_f32 v131, v154, v155\n"
  "v_mfma_f32_16x16x32_f16 a[152:155], v[32:35], v[120:123], 0\n"
  "s_add_i32 s64, s50, 32768\n"
  "buffer_load_dwordx4 v238, s[40:43], s64 offen sc1 lds\n"
  "ds_write_b128 v252, a[176:179] offset:36864\n"
  "v_mfma_f32_16x16x32_f16 a[164:167], v[36:39], v[120:123], 0\n"
  "s_waitcnt lgkmcnt(9)\n"
  "v_and_b32 v130, 0xfff8fff8, v130\n"
  "v_and_b32 v131, 0xfff8fff8, v131\n"
  "v_mfma_f32_16x16x32_f16 a[176:179], v[40:43], v[120:123], 0\n"
  "s_add_i32 s47, s44, s46\n"
  "s_waitcnt lgkmcnt(7)\n"
  "ds_read_b128 v[104:107], v232 offset:2048\n"
  "v_mfma_f32_16x16x32_f16 a[188:191], v[44:47], v[120:123], 0\n"
  "s_add_i32 s48, s47, s45\n"
  "buffer_store_dwordx4 v[128:131], v242, s[40:43], s56 offen offset:128 sc1\n"
  "v_pk_add_f16 v82, v50, v66\n"
  "v_mfma_f32_16x16x32_f16 a[192:195], v[0:3], v[112:115], 0\n"
  "s_add_i32 s46, s46, s49\n"
  "ds_read_b128 v[108:111], v232 offset:2560\n"
  "v_pk_add_f16 v120, v112, v116\n"
  "v_mfma_f32_16x16x32_f16 a[204:207], v[4:7], v[112:115], 0\n"
  "s_add_i32 s50, s50, 4194304\n"
  "v_pk_add_f16 v121, v113, v117\n"
  "v_pk_add_f16 v122, v114, v118\n"
  "v_mfma_f32_16x16x32_f16 a[216:219], v[8:11], v[112:115], 0\n"
  "v_pk_add_f16 v123, v115, v119\n"
  "v_pk_add_f16 v83, v51, v67\n"
  "v_mfma_f32_16x16x32_f16 a[228:231], v[12:15], v[112:115], 0\n"
  "v_pk_add_f16 v84, v52, v68\n"
  "v_pk_add_f16 v85, v53, v69\n"
  "v_mfma_f32_16x16x32_f16 a[196:199], v[16:19], v[116:119], 0\n"
  "v_pk_add_f16 v86, v54, v70\n"
  "v_pk_add_f16 v87, v55, v71\n"
  "v_mfma_f32_16x16x32_f16 a[208:211], v[20:23], v[116:119], 0\n"
  "v_pk_add_f16 v88, v56, v72\n"
  "v_pk_add_f16 v89, v57, v73\n"
  "v_mfma_f32_16x16x32_f16 a[220:223], v[24:27], v[116:119], 0\n"
  "v_pk_add_f16 v90, v58, v74\n"
  "v_pk_add_f16 v91, v59, v75\n"
  "v_mfma_f32_16x16x32_f16 a[232:235], v[28:31], v[116:119], 0\n"
  "v_pk_add_f16 v92, v60, v76\n"
  "v_pk_add_f16 v93, v61, v77\n"
  "v_mfma_f32_16x16x32_f16 a[200:203], v[32:35], v[120:123], 0\n"
  "v_pk_add_f16 v94, v62, v78\n"
  "v_pk_add_f16 v95, v63, v79\n"
  "s_waitcnt vmcnt(17) lgkmcnt(0)\n"
  "v_mfma_f32_16x16x32_f16 a[212:215], v[36:39], v[120:123], 0\n"
  "s_cmp_gt_u32 s62, 88\n"
  "s_cbranch_scc1 .Lpace_s0_None_%=\n"
  ".Lpaced_s0_None_%=:\n"
  "v_mfma_f32_16x16x32_f16 a[224:227], v[40:43], v[120:123], 0\n"
  "v_add_u32 v229, s49, v229\n"
  "v_add_u32 v230, s49, v230\n"
  "s_barrier\n"
  "v_mfma_f32_16x16x32_f16 a[236:239], v[44:47], v[120:123], 0\n"
  "v_add_u32 v231, s49, v231\n"
  "v_add_u32 v232, s49, v232\n"
  "v_mfma_f32_16x16x32_f16 a[0:3], v[48:51], v[96:99], a[0:3]\n"
  "v_pk_add_f16 v120, v96, v100\n"
  "v_pk_add_f16 v121, v97, v101\n"
  "v_mfma_f32_16x16x32_f16 a[12:15], v[52:55], v[96:99], a[12:15]\n"
  "v_pk_add_f16 v122, v98, v102\n"
  "v_pk_add_f16 v123, v99, v103\n"
  "v_mfma_f32_16x16x32_f16 a[24:27], v[56:59], v[96:99], a[24:27]\n"
  "s_and_b32 s65, s51, 0xffffe300\n"
  "ds_read_b64_tr_b16 v[0:1], v228 offset:0\n"
  "v_readlane_b32 s66, v244, 38\n"
  "v_mfma_f32_16x16x32_f16 a[36:39], v[60:63], v[96:99], a[36:39]\n"
  "buffer_load_dwordx4 v[164:167], v240, s[40:43], s59 offen sc1\n"
  "v_readlane_b32 s67, v244, 39\n"
  "v_mfma_f32_16x16x32_f16 a[4:7], v[64:67], v[100:103], a[4:7]\n"
  "s_cmp_eq_u32 s46, 73728\n"
  "s_cselect_b32 s49, s68, s69\n"
  "ds_read_b64_tr_b16 v[124:125], v233 offset:2048\n"
  "v_mfma_f32_16x16x32_f16 a[16:19], v[68:71], v[100:103], a[16:19]\n"
  "s_add_i32 m0, s44, 155648\n"
  "ds_read_b64_tr_b16 v[128:129], v234 offset:2048\n"
  "v_add_u32 v233, -45056, v233\n"
  "v_mfma_f32_16x16x32_f16 a[28:31], v[72:75], v[100:103], a[28:31]\n"
  "buffer_load_dwordx4 v239, s[40:43], s58 offen sc1 lds\n"
  "v_add_u32 v234, -45056, v234\n"
  "v_mfma_f32_16x16x32_f16 a[40:43], v[76:79], v[100:103], a[40:43]\n"
  "s_add_i32 s57, s57, 32768\n"
  "ds_read_b64_tr_b16 v[2:3], v228 offset:8192\n"
  "ds_read_b128 v[96:99], v232 offset:4096\n"
  "v_mfma_f32_16x16x32_f16 a[8:11], v[80:83], v[120:123], a[8:11]\n"
  "s_add_i32 m0, s47, 0\n"
  "s_lshr_b32 s59, s57, 9\n"
  "ds_read_b64_tr_b16 v[132:133], v235 offset:2048\n"
  "v_mfma_f32_16x16x32_f16 a[20:23], v[84:87], v[120:123], a[20:23]\n"
  "s_add_i32 s64, s50, 4194304\n"
  "buffer_load_dwordx4 v237, s[40:43], s64 offen sc1 lds\n"
  "v_add_u32 v235, -45056, v235\n"
  "v_mfma_f32_16x16x32_f16 a[32:35], v[88:91], v[120:123], a[32:35]\n"
  "s_add_i32 s53, s53, 8192\n"
  "s_lshr_b32 s52, s52, 1\n"
  "ds_read_b64_tr_b16 v[16:17], v228 offset:256\n"
  "v_mfma_f32_16x16x32_f16 a[44:47], v[92:95], v[120:123], a[44:47]\n"
  "s_bitcmp1_b32 s52, 0\n"
  "s_cselect_b32 s54, s53, 134217728\n"
  "ds_read_b128 v[100:103], v232 offset:4608\n"
  "v_mfma_f32_16x16x32_f16 a[48:51], v[48:51], v[104:107], a[48:51]\n"
  "s_add_i32 s51, s51, 8448\n"
  "buffer_store_dword v232, v245, s[40:43], s65 offen sc1\n"
  "v_pk_add_f16 v120, v104, v108\n"
  "v_mfma_f32_16x16x32_f16 a[60:63], v[52:55], v[104:107], a[60:63]\n"
  "ds_read_b64_tr_b16 v[136:137], v236 offset:2048\n"
  "v_pk_add_f16 v121, v105, v109\n"
  "v_mfma_f32_16x16x32_f16 a[72:75], v[56:59], v[104:107], a[72:75]\n"
  "ds_read_b64_tr_b16 v[18:19], v228 offset:8448\n"
  "v_pk_add_f16 v122, v106, v110\n"
  "v_mfma_f32_16x16x32_f16 a[84:87], v[60:63], v[104:107], a[84:87]\n"
  "buffer_store_dwordx4 a[240:243], v243, s[40:43], s66 offen sc1\n"
  "v_pk_add_f16 v123, v107, v111\n"
  "v_mfma_f32_16x16x32_f16 a[52:55], v[64:67], v[108:111], a[52:55]\n"
  "ds_read_b64_tr_b16 v[4:5], v229 offset:0\n"
  "v_readlane_b32 s66, v244, 40\n"
  "v_mfma_f32_16x16x32_f16 a[64:67], v[68:71], v[108:111], a[64:67]\n"
  "s_add_i32 m0, s47, 4096\n"
  "ds_read_b64_tr_b16 v[6:7], v229 offset:8192\n"
  "v_add_u32 v228, s49, v228\n"
  "v_mfma_f32_16x16x32_f16 a[76:79], v[72:75], v[108:111], a[76:79]\n"
  "s_add_i32 s64, s50, 4198400\n"
  "buffer_load_dwordx4 v237, s[40:43], s64 offen sc1 lds\n"
  "v_add_u32 v236, -45056, v236\n"
  "v_mfma_f32_16x16x32_f16 a[88:91], v[76:79], v[108:111], a[88:91]\n"
  "ds_read_b64_tr_b16 v[20:21], v229 offset:256\n"
  "ds_read_b64_tr_b16 v[22:23], v229 offset:8448\n"
  "v_mfma_f32_16x16x32_f16 a[56:59], v[80:83], v[120:123], a[56:59]\n"
  "s_waitcnt lgkmcnt(5)\n"
  "ds_read_b128 v[104:107], v232 offset:6144\n"
  "v_mov_b64 v[126:127], v[124:125]\n"
  "v_mfma_f32_16x16x32_f16 a[68:71], v[84:87], v[120:123], a[68:71]\n"
  "buffer_store_dwordx4 a[244:247], v243, s[40:43], s67 offen sc1\n"
  "v_mov_b64 v[130:131], v[128:129]\n"
  "v_mfma_f32_16x16x32_f16 a[80:83], v[88:91], v[120:123], a[80:83]\n"
  "s_waitcnt lgkmcnt(5)\n"
  "ds_read_b64_tr_b16 v[8:9], v230 offset:0\n"
  "v_readlane_b32 s67, v244, 41\n"
  "v_mfma_f32_16x16x32_f16 a[92:95], v[92:95], v[120:123], a[92:95]\n"
  "s_add_i32 m0, s47, 8192\n"
  "ds_read_b128 v[108:111], v232 offset:6656\n"
  "v_mov_b64 v[134:135], v[132:133]\n"
  "v_mfma_f32_16x16x32_f16 a[96:99], v[48:51], v[96:99], a[96:99]\n"
  "s_add_i32 s64, s50, 4202496\n"
  "buffer_load_dwordx4 v237, s[40:43], s64 offen sc1 lds\n"
  "v_pk_add_f16 v120, v96, v100\n"
  "v_mfma_f32_16x16x32_bf16 v[140:143], v[124:127], v[176:179], 0\n"
  "ds_read_b64_tr_b16 v[10:11], v230 offset:8192\n"
  "v_pk_add_f16 v121, v97, v101\n"
  "v_mfma_f32_16x16x32_f16 a[108:111], v[52:55], v[96:99], a[108:111]\n"
  "s_waitcnt lgkmcnt(4)\n"
  "ds_read_b64_tr_b16 v[24:25], v230 offset:256\n"
  "v_pk_add_f16 v122, v98, v102\n"
  "v_mfma_f32_16x16x32_f16 a[120:123], v[56:59], v[96:99], a[120:123]\n"
  "buffer_store_dwordx4 a[248:251], v243, s[40:43], s66 offen sc1\n"
  "v_pk_add_f16 v123, v99, v103\n"
  "v_mfma_f32_16x16x32_f16 a[132:135], v[60:63], v[96:99], a[132:135]\n"
  "ds_read_b64_tr_b16 v[26:27], v230 offset:8448\n"
  "v_mov_b64 v[138:139], v[136:137]\n"
  "v_mfma_f32_16x16x32_bf16 v[144:147], v[128:131], v[176:179], 0\n"
  "s_add_i32 m0, s47, 12288\n"
  "ds_read_b64_tr_b16 v[12:13], v231 offset:0\n"
  "v_cvt_pkrtz_f16_f32 v124, v140, v141\n"
  "v_mfma_f32_16x16x32_f16 a[100:103], v[64:67], v[100:103], a[100:103]\n"
  "s_add_i32 s64, s50, 4206592\n"
  "buffer_load_dwordx4 v237, s[40:43], s64 offen sc1 lds\n"
  "v_cvt_pkrtz_f16_f32 v125, v142, v143\n"
  "v_mfma_f32_16x16x32_f16 a[112:115], v[68:71], v[100:103], a[112:115]\n"
  "ds_read_b64_tr_b16 v[14:15], v231 offset:8192\n"
  "v_and_b32 v124, 0xfff8fff8, v124\n"
  "v_mfma_f32_16x16x32_f16 a[124:127], v[72:75], v[100:103], a[124:127]\n"
  "ds_read_b128 v[96:99], v232 offset:0\n"
  "v_cvt_pkrtz_f16_f32 v126, v144, v145\n"
  "v_mfma_f32_16x16x32_bf16 v[148:151], v[132:135], v[176:179], 0\n"
  "buffer_store_dwordx4 a[252:255], v243, s[40:43], s67 offen sc1\n"
  "v_cvt_pkrtz_f16_f32 v127, v146, v147\n"
  "v_mfma_f32_16x16x32_f16 a[136:139], v[76:79], v[100:103], a[136:139]\n"
  "ds_read_b128 v[112:115], v232 offset:8192\n"
  "v_and_b32 v125, 0xfff8fff8, v125\n"
  "v_mfma_f32_16x16x32_f16 a[104:107], v[80:83], v[120:123], a[104:107]\n"
  "s_add_i32 m0, s47, 16384\n"
  "ds_read_b64_tr_b16 v[28:29], v231 offset:256\n"
  "v_and_b32 v126, 0xfff8fff8, v126\n"
  "v_mfma_f32_16x16x32_f16 a[116:119], v[84:87], v[120:123], a[116:119]\n"
  "s_waitcnt lgkmcnt(8)\n"
  "s_add_i32 s64, s50, 4194304\n"
  "buffer_load_dwordx4 v238, s[40:43], s64 offen sc1 lds\n"
  "v_mfma_f32_16x16x32_bf16 v[152:155], v[136:139], v[176:179], 0\n"
  "s_waitcnt lgkmcnt(5)\n"
  "ds_read_b128 v[100:103], v232 offset:512\n"
  "v_and_b32 v127, 0xfff8fff8, v127\n"
  "v_mfma_f32_16x16x32_f16 a[128:131], v[88:91], v[120:123], a[128:131]\n"
  "s_add_i32 m0, s47, 20480\n"
  "ds_read_b128 v[116:119], v232 offset:8704\n"
  "v_cvt_pkrtz_f16_f32 v128, v148, v149\n"
  "v_mfma_f32_16x16x32_f16 a[140:143], v[92:95], v[120:123], a[140:143]\n"
  "s_add_i32 s64, s50, 4096\n"
  "buffer_load_dwordx4 v238, s[40:43], s64 offen sc1 lds\n"
  "v_cvt_pkrtz_f16_f32 v129, v150, v151\n"
  "v_mfma_f32_16x16x32_f16 a[144:147], v[48:51], v[104:107], a[144:147]\n"
  "ds_read_b64_tr_b16 v[30:31], v231 offset:8448\n"
  "v_pk_add_f16 v120, v104, v108\n"
  "v_mfma_f32_16x16x32_f16 a[156:159], v[52:55], v[104:107], a[156:159]\n"
  "s_add_i32 m0, s48, 24576\n"
  "v_pk_add_f16 v121, v105, v109\n"
  "v_pk_add_f16 v122, v106, v110\n"
  "v_mfma_f32_16x16x32_f16 a[168:171], v[56:59], v[104:107], a[168:171]\n"
  "s_add_i32 s64, s50, 16384\n"
  "buffer_load_dwordx4 v238, s[40:43], s64 offen sc1 lds\n"
  "v_pk_add_f16 v123, v107, v111\n"
  "v_mfma_f32_16x16x32_f16 a[180:183], v[60:63], v[104:107], a[180:183]\n"
  "ds_read_b128 v[104:107], v232 offset:2048\n"
  "v_cvt_pkrtz_f16_f32 v130, v152, v153\n"
  "v_mfma_f32_16x16x32_f16 a[148:151], v[64:67], v[108:111], a[148:151]\n"
  "v_cvt_pkrtz_f16_f32 v131, v154, v155\n"
  "v_and_b32 v128, 0xfff8fff8, v128\n"
  "v_mfma_f32_16x16x32_f16 a[160:163], v[68:71], v[108:111], a[160:163]\n"
  "buffer_store_dwordx4 v[124:127], v242, s[40:43], s56 offen offset:256 sc1\n"
  "v_and_b32 v129, 0xfff8fff8, v129\n"
  "v_mfma_f32_16x16x32_f16 a[172:175], v[72:75], v[108:111], a[172:175]\n"
  "v_and_b32 v130, 0xfff8fff8, v130\n"
  "v_and_b32 v131, 0xfff8fff8, v131\n"
  "v_mfma_f32_16x16x32_f16 a[184:187], v[76:79], v[108:111], a[184:187]\n"
  "s_add_i32 m0, s47, 26624\n"
  "s_waitcnt lgkmcnt(1)\n"
  "ds_read_b128 v[108:111], v232 offset:2560\n"
  "v_mfma_f32_16x16x32_f16 a[152:155], v[80:83], v[120:123], a[152:155]\n"
  "s_add_i32 s64, s50, 4214784\n"
  "buffer_load_dwordx4 v238, s[40:43], s64 offen sc1 lds\n"
  "v_pk_add_f16 v32, v0, v16\n"
  "v_mfma_f32_16x16x32_f16 a[164:167], v[84:87], v[120:123], a[164:167]\n"
  "v_pk_add_f16 v33, v1, v17\n"
  "v_pk_add_f16 v34, v2, v18\n"
  "v_mfma_f32_16x16x32_f16 a[176:179], v[88:91], v[120:123], a[176:179]\n"
  "s_add_i32 m0, s47, 30720\n"
  "v_pk_add_f16 v35, v3, v19\n"
  "v_pk_add_f16 v36, v4, v20\n"
  "v_mfma_f32_16x16x32_f16 a[188:191], v[92:95], v[120:123], a[188:191]\n"
  "s_add_i32 s64, s50, 32768\n"
  "buffer_load_dwordx4 v238, s[40:43], s64 offen sc1 lds\n"
  "v_pk_add_f16 v37, v5, v21\n"
  "v_mfma_f32_16x16x32_f16 a[192:195], v[48:51], v[112:115], a[192:195]\n"
  "s_add_i32 s47, s44, s46\n"
  "v_pk_add_f16 v120, v112, v116\n"
  "v_pk_add_f16 v121, v113, v117\n"
  "v_mfma_f32_16x16x32_f16 a[204:207], v[52:55], v[112:115], a[204:207]\n"
  "s_add_i32 s48, s47, s45\n"
  "v_pk_add_f16 v122, v114, v118\n"
  "v_pk_add_f16 v123, v115, v119\n"
  "v_mfma_f32_16x16x32_f16 a[216:219], v[56:59], v[112:115], a[216:219]\n"
  "s_add_i32 s46, s46, s49\n"
  "buffer_store_dwordx4 v[128:131], v242, s[40:43], s56 offen offset:384 sc1\n"
  "v_pk_add_f16 v38, v6, v22\n"
  "v_mfma_f32_16x16x32_f16 a[228:231], v[60:63], v[112:115], a[228:231]\n"
  "s_cmp_lt_u32 s57, 0x80000\n"
  "s_cselect_b32 s58, s57, 0xf0000000\n"
  "s_cselect_b32 s59, s59, 0xf0000000\n"
  "v_mfma_f32_16x16x32_f16 a[196:199], v[64:67], v[116:119], a[196:199]\n"
  "s_add_i32 s55, s55, 8404992\n"
  "v_pk_add_f16 v39, v7, v23\n"
  "v_pk_add_f16 v40, v8, v24\n"
  "v_mfma_f32_16x16x32_f16 a[208:211], v[68:71], v[116:119], a[208:211]\n"
  "s_add_i32 s50, s50, 4194304\n"
  "v_pk_add_f16 v41, v9, v25\n"
  "v_pk_add_f16 v42, v10, v26\n"
  "v_mfma_f32_16x16x32_f16 a[220:223], v[72:75], v[116:119], a[220:223]\n"
  "s_and_b32 s56, s55, 0xff80c000\n"
  "v_pk_add_f16 v43, v11, v27\n"
  "v_pk_add_f16 v44, v12, v28\n"
  "v_mfma_f32_16x16x32_f16 a[232:235], v[76:79], v[116:119], a[232:235]\n"
  "v_pk_add_f16 v45, v13, v29\n"
  "v_pk_add_f16 v46, v14, v30\n"
  "v_mfma_f32_16x16x32_f16 a[200:203], v[80:83], v[120:123], a[200:203]\n"
  "v_pk_add_f16 v47, v15, v31\n"
  "v_add_u32 v229, s49, v229\n"
  "s_waitcnt vmcnt(16) lgkmcnt(0)\n"
  "v_mfma_f32_16x16x32_f16 a[212:215], v[84:87], v[120:123], a[212:215]\n"
  "s_cmp_gt_u32 s62, 88\n"
  "s_cbranch_scc1 .Lpace_s1_None_%=\n"
  ".Lpaced_s1_None_%=:\n"
  "v_mfma_f32_16x16x32_f16 a[224:227], v[88:91], v[120:123], a[224:227]\n"
  "v_add_u32 v230, s49, v230\n"
  "v_add_u32 v231, s49, v231\n"
  "s_barrier\n"
  "v_mfma_f32_16x16x32_f16 a[236:239], v[92:95], v[120:123], a[236:239]\n"
  "v_add_u32 v232, s49, v232\n"
  "s_mov_b32 s63, 10\n"
  ".p2align 6\n"
  ".LloopP_%=:\n"
  "v_mfma_f32_16x16x32_f16 a[0:3], v[0:3], v[96:99], a[0:3]\n"
  "buffer_load_dword v247, v246, s[40:43], s54 offen sc1\n"
  "v_pk_add_f16 v120, v96, v100\n"
  "v_mfma_f32_16x16x32_f16 a[12:15], v[4:7], v[96:99], a[12:15]\n"
  "v_pk_add_f16 v121, v97, v101\n"
  "v_pk_add_f16 v122, v98, v102\n"
  "v_mfma_f32_16x16x32_f16 a[24:27], v[8:11], v[96:99], a[24:27]\n"
  "s_min_u32 s67, s60, 11\n"
  "s_lshl_b32 s67, s67, 2\n"
  "v_pk_add_f16 v123, v99, v103\n"
  "v_mfma_f32_16x16x32_f16 a[36:39], v[12:15], v[96:99], a[36:39]\n"
  "ds_read_b64_tr_b16 v[48:49], v228 offset:0\n"
  "v_readlane_b32 s66, v244, s60\n"
  "v_mfma_f32_16x16x32_f16 a[4:7], v[16:19], v[100:103], a[4:7]\n"
  "s_cmp_eq_u32 s46, 73728\n"
  "s_cselect_b32 s49, s68, s69\n"
  "buffer_load_dwordx4 v[168:171], v240, s[40:43], s59 offen sc1\n"
  "v_mfma_f32_16x16x32_f16 a[16:19], v[20:23], v[100:103], a[16:19]\n"
  "ds_read_b64_tr_b16 v[124:125], v233 offset:0\n"
  "ds_read_b64_tr_b16 v[128:129], v234 offset:0\n"
  "v_mfma_f32_16x16x32_f16 a[28:31], v[24:27], v[100:103], a[28:31]\n"
  "ds_read_b64_tr_b16 v[50:51], v228 offset:8192\n"
  "ds_read_b128 v[96:99], v232 offset:4096\n"
  "v_mfma_f32_16x16x32_f16 a[40:43], v[28:31], v[100:103], a[40:43]\n"
  "s_add_i32 m0, s44, 110592\n"
  "ds_read_b64_tr_b16 v[160:161], v241 offset:45056\n"
  "ds_read_b64_tr_b16 v[162:163], v241 offset:45568\n"
  "v_mfma_f32_16x16x32_f16 a[8:11], v[32:35], v[120:123], a[8:11]\n"
  "buffer_load_dwordx4 v239, s[40:43], s58 offen sc1 lds\n"
  "ds_read_b64_tr_b16 v[132:133], v235 offset:0\n"
  "v_mfma_f32_16x16x32_f16 a[20:23], v[36:39], v[120:123], a[20:23]\n"
  "s_add_i32 s57, s57, 32768\n"
  "ds_read_b64_tr_b16 v[64:65], v228 offset:256\n"
  "ds_read_b128 v[100:103], v232 offset:4608\n"
  "v_mfma_f32_16x16x32_f16 a[32:35], v[40:43], v[120:123], a[32:35]\n"
  "s_lshr_b32 s59, s57, 9\n"
  "s_add_i32 s60, s60, 1\n"
  "ds_read_b64_tr_b16 v[136:137], v236 offset:0\n"
  "v_mfma_f32_16x16x32_f16 a[44:47], v[44:47], v[120:123], a[44:47]\n"
  "s_add_i32 m0, s47, 0\n"
  "ds_read_b64_tr_b16 v[66:67], v228 offset:8448\n"
  "v_add_u32 v228, s49, v228\n"
  "v_mfma_f32_16x16x32_f16 a[48:51], v[0:3], v[104:107], a[48:51]\n"
  "s_add_i32 s64, s50, 4194304\n"
  "buffer_load_dwordx4 v237, s[40:43], s64 offen sc1 lds\n"
  "v_pk_add_f16 v120, v104, v108\n"
  "v_mfma_f32_16x16x32_f16 a[60:63], v[4:7], v[104:107], a[60:63]\n"
  "s_set_gpr_idx_on s67, gpr_idx(SRC0)\n"
  "v_mov_b64 v[156:157], v[180:181]\n"
  "v_mov_b64 v[158:159], v[182:183]\n"
  "s_set_gpr_idx_off\n"
  "v_mfma_f32_16x16x32_f16 a[72:75], v[8:11], v[104:107], a[72:75]\n"
  "s_waitcnt lgkmcnt(5)\n"
  "ds_read_b64_tr_b16 v[52:53], v229 offset:0\n"
  "v_pk_add_f16 v121, v105, v109\n"
  "v_mfma_f32_16x16x32_f16 a[84:87], v[12:15], v[104:107], a[84:87]\n"
  "v_pk_add_f16 v122, v106, v110\n"
  "v_pk_add_f16 v123, v107, v111\n"
  "v_mfma_f32_16x16x32_f16 a[52:55], v[16:19], v[108:111], a[52:55]\n"
  "buffer_store_dwordx4 v[156:159], v243, s[40:43], s66 offen sc1\n"
  "ds_read_b64_tr_b16 v[54:55], v229 offset:8192\n"
  "v_mfma_f32_16x16x32_f16 a[64:67], v[20:23], v[108:111], a[64:67]\n"
  "s_waitcnt lgkmcnt(3)\n"
  "ds_read_b64_tr_b16 v[68:69], v229 offset:256\n"
  "v_mov_b64 v[126:127], v[124:125]\n"
  "v_mfma_f32_16x16x32_f16 a[76:79], v[24:27], v[108:111], a[76:79]\n"
  "s_waitcnt lgkmcnt(3)\n"
  "ds_read_b64_tr_b16 v[70:71], v229 offset:8448\n"
  "v_mov_b64 v[130:131], v[128:129]\n"
  "v_mfma_f32_16x16x32_bf16 v[172:175], v[160:163], v[164:167], v[172:175]\n"
  "s_add_i32 m0, s47, 4096\n"
  "ds_read_b128 v[104:107], v232 offset:6144\n"
  "v_mov_b64 v[134:135], v[132:133]\n"
  "v_mfma_f32_16x16x32_f16 a[88:91], v[28:31], v[108:111], a[88:91]\n"
  "s_add_i32 s64, s50, 4198400\n"
  "buffer_load_dwordx4 v237, s[40:43], s64 offen sc1 lds\n"
  "v_mov_b64 v[138:139], v[136:137]\n"
  "v_mfma_f32_16x16x32_f16 a[56:59], v[32:35], v[120:123], a[56:59]\n"
  "ds_read_b64_tr_b16 v[56:57], v230 offset:0\n"
  "v_pk_add_f16 v80, v48, v64\n"
  "v_mfma_f32_16x16x32_f16 a[68:71], v[36:39], v[120:123], a[68:71]\n"
  "ds_read_b128 v[108:111], v232 offset:6656\n"
  "v_pk_add_f16 v81, v49, v65\n"
  "v_mfma_f32_16x16x32_f16 a[80:83], v[40:43], v[120:123], a[80:83]\n"
  "s_add_i32 m0, s47, 8192\n"
  "ds_read_b64_tr_b16 v[58:59], v230 offset:8192\n"
  "v_pk_add_f16 v82, v50, v66\n"
  "v_mfma_f32_16x16x32_f16 a[92:95], v[44:47], v[120:123], a[92:95]\n"
  "s_add_i32 s64, s50, 4202496\n"
  "buffer_load_dwordx4 v237, s[40:43], s64 offen sc1 lds\n"
  "v_pk_add_f16 v83, v51, v67\n"
  "v_mfma_f32_16x16x32_f16 a[96:99], v[0:3], v[96:99], a[96:99]\n"
  "s_waitcnt lgkmcnt(4)\n"
  "v_pk_add_f16 v120, v96, v100\n"
  "v_pk_add_f16 v121, v97, v101\n"
  "v_mfma_f32_16x16x32_bf16 v[140:143], v[124:127], v[176:179], 0\n"
  "ds_read_b64_tr_b16 v[72:73], v230 offset:256\n"
  "v_pk_add_f16 v84, v52, v68\n"
  "v_mfma_f32_16x16x32_f16 a[108:111], v[4:7], v[96:99], a[108:111]\n"
  "s_add_i32 m0, s47, 12288\n"
  "v_pk_add_f16 v122, v98, v102\n"
  "v_pk_add_f16 v123, v99, v103\n"
  "v_mfma_f32_16x16x32_f16 a[120:123], v[8:11], v[96:99], a[120:123]\n"
  "s_add_i32 s64, s50, 4206592\n"
  "buffer_load_dwordx4 v237, s[40:43], s64 offen sc1 lds\n"
  "v_pk_add_f16 v85, v53, v69\n"
  "v_mfma_f32_16x16x32_f16 a[132:135], v[12:15], v[96:99], a[132:135]\n"
  "ds_read_b64_tr_b16 v[74:75], v230 offset:8448\n"
  "v_cvt_pkrtz_f16_f32 v124, v140, v141\n"
  "v_mfma_f32_16x16x32_bf16 v[144:147], v[128:131], v[176:179], 0\n"
  "ds_read_b64_tr_b16 v[60:61], v231 offset:0\n"
  "v_cvt_pkrtz_f16_f32 v125, v142, v143\n"
  "v_mfma_f32_16x16x32_f16 a[100:103], v[16:19], v[100:103], a[100:103]\n"
  "s_add_i32 m0, s47, 16384\n"
  "ds_read_b64_tr_b16 v[62:63], v231 offset:8192\n"
  "v_and_b32 v124, 0xfff8fff8, v124\n"
  "v_mfma_f32_16x16x32_f16 a[112:115], v[20:23], v[100:103], a[112:115]\n"
  "s_add_i32 s64, s50, 4194304\n"
  "buffer_load_dwordx4 v238, s[40:43], s64 offen sc1 lds\n"
  "v_and_b32 v125, 0xfff8fff8, v125\n"
  "v_mfma_f32_16x16x32_f16 a[124:127], v[24:27], v[100:103], a[124:127]\n"
  "ds_read_b128 v[96:99], v232 offset:0\n"
  "v_cvt_pkrtz_f16_f32 v126, v144, v145\n"
  "v_mfma_f32_16x16x32_bf16 v[148:151], v[132:135], v[176:179], 0\n"
  "ds_read_b128 v[112:115], v232 offset:8192\n"
  "v_cvt_pkrtz_f16_f32 v127, v146, v147\n"
  "v_mfma_f32_16x16x32_f16 a[136:139], v[28:31], v[100:103], a[136:139]\n"
  "s_add_i32 m0, s47, 20480\n"
  "ds_read_b64_tr_b16 v[76:77], v231 offset:256\n"
  "v_and_b32 v126, 0xfff8fff8, v126\n"
  "v_mfma_f32_16x16x32_f16 a[104:107], v[32:35], v[120:123], a[104:107]\n"
  "s_add_i32 s64, s50, 4096\n"
  "buffer_load_dwordx4 v238, s[40:43], s64 offen sc1 lds\n"
  "v_and_b32 v127, 0xfff8fff8, v127\n"
  "v_mfma_f32_16x16x32_f16 a[116:119], v[36:39], v[120:123], a[116:119]\n"
  "s_waitcnt lgkmcnt(8)\n"
  "ds_read_b128 v[100:103], v232 offset:512\n"
  "v_cvt_pkrtz_f16_f32 v128, v148, v149\n"
  "v_mfma_f32_16x16x32_bf16 v[152:155], v[136:139], v[176:179], 0\n"
  "s_waitcnt lgkmcnt(6)\n"
  "ds_read_b128 v[116:119], v232 offset:8704\n"
  "v_cvt_pkrtz_f16_f32 v129, v150, v151\n"
  "v_mfma_f32_16x16x32_f16 a[128:131], v[40:43], v[120:123], a[128:131]\n"
  "s_add_i32 m0, s48, 24576\n"
  "ds_read_b64_tr_b16 v[78:79], v231 offset:8448\n"
  "v_and_b32 v128, 0xfff8fff8, v128\n"
  "v_mfma_f32_16x16x32_f16 a[140:143], v[44:47], v[120:123], a[140:143]\n"
  "s_add_i32 s64, s50, 16384\n"
  "buffer_load_dwordx4 v238, s[40:43], s64 offen sc1 lds\n"
  "v_and_b32 v129, 0xfff8fff8, v129\n"
  "v_mfma_f32_16x16x32_f16 a[144:147], v[0:3], v[104:107], a[144:147]\n"
  "v_pk_add_f16 v120, v104, v108\n"
  "v_pk_add_f16 v121, v105, v109\n"
  "v_mfma_f32_16x16x32_f16 a[156:159], v[4:7], v[104:107], a[156:159]\n"
  "v_pk_add_f16 v122, v106, v110\n"
  "v_pk_add_f16 v123, v107, v111\n"
  "v_mfma_f32_16x16x32_f16 a[168:171], v[8:11], v[104:107], a[168:171]\n"
  "v_cvt_pkrtz_f16_f32 v130, v152, v153\n"
  "v_cvt_pkrtz_f16_f32 v131, v154, v155\n"
  "v_mfma_f32_16x16x32_f16 a[180:183], v[12:15], v[104:107], a[180:183]\n"
  "buffer_store_dwordx4 v[124:127], v242, s[40:43], s56 offen offset:0 sc1\n"
  "v_and_b32 v130, 0xfff8fff8, v130\n"
  "v_mfma_f32_16x16x32_f16 a[148:151], v[16:19], v[108:111], a[148:151]\n"
  "ds_read_b128 v[104:107], v232 offset:2048\n"
  "v_and_b32 v131, 0xfff8fff8, v131\n"
  "v_mfma_f32_16x16x32_f16 a[160:163], v[20:23], v[108:111], a[160:163]\n"
  "s_cmp_lt_u32 s57, 0x80000\n"
  "s_cselect_b32 s58, s57, 0xf0000000\n"
  "s_cselect_b32 s59, s59, 0xf0000000\n"
  "v_mfma_f32_16x16x32_f16 a[172:175], v[24:27], v[108:111], a[172:175]\n"
  "s_add_i32 m0, s47, 26624\n"
  "s_waitcnt lgkmcnt(1)\n"
  "v_pk_add_f16 v86, v54, v70\n"
  "v_mfma_f32_16x16x32_f16 a[184:187], v[28:31], v[108:111], a[184:187]\n"
  "s_add_i32 s64, s50, 4214784\n"
  "buffer_load_dwordx4 v238, s[40:43], s64 offen sc1 lds\n"
  "v_pk_add_f16 v87, v55, v71\n"
  "v_mfma_f32_16x16x32_f16 a[152:155], v[32:35], v[120:123], a[152:155]\n"
  "ds_read_b128 v[108:111], v232 offset:2560\n"
  "v_pk_add_f16 v88, v56, v72\n"
  "v_mfma_f32_16x16x32_f16 a[164:167], v[36:39], v[120:123], a[164:167]\n"
  "v_pk_add_f16 v89, v57, v73\n"
  "v_pk_add_f16 v90, v58, v74\n"
  "v_mfma_f32_16x16x32_f16 a[176:179], v[40:43], v[120:123], a[176:179]\n"
  "s_add_i32 m0, s47, 30720\n"
  "v_pk_add_f16 v91, v59, v75\n"
  "v_pk_add_f16 v92, v60, v76\n"
  "v_mfma_f32_16x16x32_f16 a[188:191], v[44:47], v[120:123], a[188:191]\n"
  "s_add_i32 s64, s50, 32768\n"
  "buffer_load_dwordx4 v238, s[40:43], s64 offen sc1 lds\n"
  "v_pk_add_f16 v93, v61, v77\n"
  "v_mfma_f32_16x16x32_f16 a[192:195], v[0:3], v[112:115], a[192:195]\n"
  "s_add_i32 s47, s44, s46\n"
  "v_pk_add_f16 v120, v112, v116\n"
  "v_pk_add_f16 v121, v113, v117\n"
  "v_mfma_f32_16x16x32_f16 a[204:207], v[4:7], v[112:115], a[204:207]\n"
  "s_add_i32 s48, s47, s45\n"
  "v_pk_add_f16 v122, v114, v118\n"
  "v_pk_add_f16 v123, v115, v119\n"
  "v_mfma_f32_16x16x32_f16 a[216:219], v[8:11], v[112:115], a[216:219]\n"
  "s_add_i32 s46, s46, s49\n"
  "v_pk_add_f16 v94, v62, v78\n"
  "v_pk_add_f16 v95, v63, v79\n"
  "v_mfma_f32_16x16x32_f16 a[228:231], v[12:15], v[112:115], a[228:231]\n"
  "s_add_i32 s50, s50, 4194304\n"
  "buffer_store_dwordx4 v[128:131], v242, s[40:43], s56 offen offset:128 sc1\n"
  "v_add_u32 v229, s49, v229\n"
  "v_mfma_f32_16x16x32_f16 a[196:199], v[16:19], v[116:119], a[196:199]\n"
  "v_add_u32 v230, s49, v230\n"
  "v_add_u32 v231, s49, v231\n"
  "v_mfma_f32_16x16x32_f16 a[208:211], v[20:23], v[116:119], a[208:211]\n"
  "v_add_u32 v232, s49, v232\n"
  "v_mfma_f32_16x16x32_f16 a[220:223], v[24:27], v[116:119], a[220:223]\n"
  "v_mfma_f32_16x16x32_f16 a[232:235], v[28:31], v[116:119], a[232:235]\n"
  "v_mfma_f32_16x16x32_f16 a[200:203], v[32:35], v[120:123], a[200:203]\n"
  "s_waitcnt vmcnt(12) lgkmcnt(0)\n"
  "v_mfma_f32_16x16x32_f16 a[212:215], v[36:39], v[120:123], a[212:215]\n"
  "v_cmp_eq_u32 vcc, 0, v247\n"
  "s_cbranch_vccnz .Lspin_pe_None_%=\n"
  ".Lspun_pe_None_%=:\n"
  "s_cmp_gt_u32 s62, 88\n"
  "s_cbranch_scc1 .Lpace_pe_None_%=\n"
  ".Lpaced_pe_None_%=:\n"
  "v_mfma_f32_16x16x32_f16 a[224:227], v[40:43], v[120:123], a[224:227]\n"
  "s_barrier\n"
  "v_mfma_f32_16x16x32_f16 a[236:239], v[44:47], v[120:123], a[236:239]\n"
  "v_mfma_f32_16x16x32_f16 a[0:3], v[48:51], v[96:99], a[0:3]\n"
  "buffer_load_dwordx4 v[164:167], v240, s[40:43], s59 offen sc1\n"
  "v_pk_add_f16 v120, v96, v100\n"
  "v_mfma_f32_16x16x32_f16 a[12:15], v[52:55], v[96:99], a[12:15]\n"
  "v_pk_add_f16 v121, v97, v101\n"
  "v_pk_add_f16 v122, v98, v102\n"
  "v_mfma_f32_16x16x32_f16 a[24:27], v[56:59], v[96:99], a[24:27]\n"
  "s_min_u32 s67, s60, 11\n"
  "s_lshl_b32 s67, s67, 2\n"
  "v_pk_add_f16 v123, v99, v103\n"
  "v_mfma_f32_16x16x32_f16 a[36:39], v[60:63], v[96:99], a[36:39]\n"
  "s_add_i32 m0, s44, 155648\n"
  "s_and_b32 s65, s51, 0xffffe300\n"
  "ds_read_b64_tr_b16 v[0:1], v228 offset:0\n"
  "v_mfma_f32_16x16x32_f16 a[4:7], v[64:67], v[100:103], a[4:7]\n"
  "buffer_load_dwordx4 v239, s[40:43], s58 offen sc1 lds\n"
  "v_readlane_b32 s66, v244, s60\n"
  "v_mfma_f32_16x16x32_f16 a[16:19], v[68:71], v[100:103], a[16:19]\n"
  "s_cmp_eq_u32 s46, 73728\n"
  "s_cselect_b32 s49, s68, s69\n"
  "ds_read_b64_tr_b16 v[124:125], v233 offset:2048\n"
  "v_mfma_f32_16x16x32_f16 a[28:31], v[72:75], v[100:103], a[28:31]\n"
  "s_add_i32 s57, s57, 32768\n"
  "ds_read_b64_tr_b16 v[128:129], v234 offset:2048\n"
  "v_add_u32 v233, 4096, v233\n"
  "v_mfma_f32_16x16x32_f16 a[40:43], v[76:79], v[100:103], a[40:43]\n"
  "s_add_i32 m0, s47, 0\n"
  "s_lshr_b32 s59, s57, 9\n"
  "ds_read_b64_tr_b16 v[2:3], v228 offset:8192\n"
  "v_mfma_f32_16x16x32_f16 a[8:11], v[80:83], v[120:123], a[8:11]\n"
  "s_add_i32 s64, s50, 4194304\n"
  "buffer_load_dwordx4 v237, s[40:43], s64 offen sc1 lds\n"
  "v_add_u32 v234, 4096, v234\n"
  "v_mfma_f32_16x16x32_f16 a[20:23], v[84:87], v[120:123], a[20:23]\n"
  "ds_read_b128 v[96:99], v232 offset:4096\n"
  "ds_read_b64_tr_b16 v[160:161], v241 offset:0\n"
  "v_mfma_f32_16x16x32_f16 a[32:35], v[88:91], v[120:123], a[32:35]\n"
  "s_add_i32 s53, s53, 8192\n"
  "s_lshr_b32 s52, s52, 1\n"
  "ds_read_b64_tr_b16 v[162:163], v241 offset:512\n"
  "v_mfma_f32_16x16x32_f16 a[44:47], v[92:95], v[120:123], a[44:47]\n"
  "s_add_i32 s60, s60, 1\n"
  "ds_read_b64_tr_b16 v[132:133], v235 offset:2048\n"
  "v_add_u32 v235, 4096, v235\n"
  "v_mfma_f32_16x16x32_f16 a[48:51], v[48:51], v[104:107], a[48:51]\n"
  "s_add_i32 s51, s51, 8448\n"
  "buffer_store_dword v232, v245, s[40:43], s65 offen sc1\n"
  "ds_read_b64_tr_b16 v[16:17], v228 offset:256\n"
  "v_mfma_f32_16x16x32_f16 a[60:63], v[52:55], v[104:107], a[60:63]\n"
  "ds_read_b128 v[100:103], v232 offset:4608\n"
  "v_pk_add_f16 v120, v104, v108\n"
  "v_mfma_f32_16x16x32_f16 a[72:75], v[56:59], v[104:107], a[72:75]\n"
  "ds_read_b64_tr_b16 v[136:137], v236 offset:2048\n"
  "ds_read_b64_tr_b16 v[18:19], v228 offset:8448\n"
  "s_set_gpr_idx_on s67, gpr_idx(SRC0)\n"
  "v_mov_b64 v[156:157], v[180:181]\n"
  "v_mov_b64 v[158:159], v[182:183]\n"
  "s_set_gpr_idx_off\n"
  "v_mfma_f32_16x16x32_f16 a[84:87], v[60:63], v[104:107], a[84:87]\n"
  "ds_read_b64_tr_b16 v[4:5], v229 offset:0\n"
  "v_pk_add_f16 v121, v105, v109\n"
  "v_mfma_f32_16x16x32_f16 a[52:55], v[64:67], v[108:111], a[52:55]\n"
  "buffer_store_dwordx4 v[156:159], v243, s[40:43], s66 offen sc1\n"
  "v_pk_add_f16 v122, v106, v110\n"
  "v_mfma_f32_16x16x32_f16 a[64:67], v[68:71], v[108:111], a[64:67]\n"
  "s_waitcnt lgkmcnt(6)\n"
  "ds_read_b64_tr_b16 v[6:7], v229 offset:8192\n"
  "v_pk_add_f16 v123, v107, v111\n"
  "v_mfma_f32_16x16x32_f16 a[76:79], v[72:75], v[108:111], a[76:79]\n"
  "s_bitcmp1_b32 s52, 0\n"
  "s_cselect_b32 s54, s53, 134217728\n"
  "ds_read_b64_tr_b16 v[20:21], v229 offset:256\n"
  "v_mfma_f32_16x16x32_bf16 v[172:175], v[160:163], v[168:171], v[172:175]\n"
  "s_add_i32 m0, s47, 4096\n"
  "ds_read_b64_tr_b16 v[22:23], v229 offset:8448\n"
  "v_add_u32 v228, s49, v228\n"
  "v_mfma_f32_16x16x32_f16 a[88:91], v[76:79], v[108:111], a[88:91]\n"
  "s_add_i32 s64, s50, 4198400\n"
  "buffer_load_dwordx4 v237, s[40:43], s64 offen sc1 lds\n"
  "v_add_u32 v229, s49, v229\n"
  "v_mfma_f32_16x16x32_f16 a[56:59], v[80:83], v[120:123], a[56:59]\n"
  "s_waitcnt lgkmcnt(5)\n"
  "ds_read_b128 v[104:107], v232 offset:6144\n"
  "v_mov_b64 v[126:127], v[124:125]\n"
  "v_mfma_f32_16x16x32_f16 a[68:71], v[84:87], v[120:123], a[68:71]\n"
  "ds_read_b64_tr_b16 v[8:9], v230 offset:0\n"
  "v_mov_b64 v[130:131], v[128:129]\n"
  "v_mfma_f32_16x16x32_f16 a[80:83], v[88:91], v[120:123], a[80:83]\n"
  "s_add_i32 m0, s47, 8192\n"
  "ds_read_b128 v[108:111], v232 offset:6656\n"
  "v_mov_b64 v[134:135], v[132:133]\n"
  "v_mfma_f32_16x16x32_f16 a[92:95], v[92:95], v[120:123], a[92:95]\n"
  "s_add_i32 s64, s50, 4202496\n"
  "buffer_load_dwordx4 v237, s[40:43], s64 offen sc1 lds\n"
  "v_mov_b64 v[138:139], v[136:137]\n"
  "v_mfma_f32_16x16x32_f16 a[96:99], v[48:51], v[96:99], a[96:99]\n"
  "s_waitcnt lgkmcnt(7)\n"
  "ds_read_b64_tr_b16 v[10:11], v230 offset:8192\n"
  "v_pk_add_f16 v120, v96, v100\n"
  "v_mfma_f32_16x16x32_bf16 v[140:143], v[124:127], v[176:179], 0\n"
  "s_waitcnt lgkmcnt(4)\n"
  "ds_read_b64_tr_b16 v[24:25], v230 offset:256\n"
  "v_pk_add_f16 v121, v97, v101\n"
  "v_mfma_f32_16x16x32_f16 a[108:111], v[52:55], v[96:99], a[108:111]\n"
  "s_add_i32 m0, s47, 12288\n"
  "v_pk_add_f16 v122, v98, v102\n"
  "v_pk_add_f16 v123, v99, v103\n"
  "v_mfma_f32_16x16x32_f16 a[120:123], v[56:59], v[96:99], a[120:123]\n"
  "s_add_i32 s64, s50, 4206592\n"
  "buffer_load_dwordx4 v237, s[40:43], s64 offen sc1 lds\n"
  "v_pk_add_f16 v32, v0, v16\n"
  "v_mfma_f32_16x16x32_f16 a[132:135], v[60:63], v[96:99], a[132:135]\n"
  "ds_read_b64_tr_b16 v[26:27], v230 offset:8448\n"
  "v_cvt_pkrtz_f16_f32 v124, v140, v141\n"
  "v_mfma_f32_16x16x32_bf16 v[144:147], v[128:131], v[176:179], 0\n"
  "ds_read_b64_tr_b16 v[12:13], v231 offset:0\n"
  "v_cvt_pkrtz_f16_f32 v125, v142, v143\n"
  "v_mfma_f32_16x16x32_f16 a[100:103], v[64:67], v[100:103], a[100:103]\n"
  "s_add_i32 m0, s47, 16384\n"
  "ds_read_b64_tr_b16 v[14:15], v231 offset:8192\n"
  "v_and_b32 v124, 0xfff8fff8, v124\n"
  "v_mfma_f32_16x16x32_f16 a[112:115], v[68:71], v[100:103], a[112:115]\n"
  "s_add_i32 s64, s50, 4194304\n"
  "buffer_load_dwordx4 v238, s[40:43], s64 offen sc1 lds\n"
  "v_and_b32 v125, 0xfff8fff8, v125\n"
  "v_mfma_f32_16x16x32_f16 a[124:127], v[72:75], v[100:103], a[124:127]\n"
  "ds_read_b128 v[96:99], v232 offset:0\n"
  "v_cvt_pkrtz_f16_f32 v126, v144, v145\n"
  "v_mfma_f32_16x16x32_bf16 v[148:151], v[132:135], v[176:179], 0\n"
  "ds_read_b128 v[112:115], v232 offset:8192\n"
  "v_cvt_pkrtz_f16_f32 v127, v146, v147\n"
  "v_mfma_f32_16x16x32_f16 a[136:139], v[76:79], v[100:103], a[136:139]\n"
  "s_add_i32 m0, s47, 20480\n"
  "ds_read_b64_tr_b16 v[28:29], v231 offset:256\n"
  "v_and_b32 v126, 0xfff8fff8, v126\n"
  "v_mfma_f32_16x16x32_f16 a[104:107], v[80:83], v[120:123], a[104:107]\n"
  "s_add_i32 s64, s50, 4096\n"
  "buffer_load_dwordx4 v238, s[40:43], s64 offen sc1 lds\n"
  "v_and_b32 v127, 0xfff8fff8, v127\n"
  "v_mfma_f32_16x16x32_f16 a[116:119], v[84:87], v[120:123], a[116:119]\n"
  "s_waitcnt lgkmcnt(8)\n"
  "ds_read_b128 v[100:103], v232 offset:512\n"
  "v_cvt_pkrtz_f16_f32 v128, v148, v149\n"
  "v_mfma_f32_16x16x32_bf16 v[152:155], v[136:139], v[176:179], 0\n"
  "s_waitcnt lgkmcnt(6)\n"
  "ds_read_b128 v[116:119], v232 offset:8704\n"
  "v_cvt_pkrtz_f16_f32 v129, v150, v151\n"
  "v_mfma_f32_16x16x32_f16 a[128:131], v[88:91], v[120:123], a[128:131]\n"
  "s_add_i32 m0, s48, 24576\n"
  "ds_read_b64_tr_b16 v[30:31], v231 offset:8448\n"
  "v_and_b32 v128, 0xfff8fff8, v128\n"
  "v_mfma_f32_16x16x32_f16 a[140:143], v[92:95], v[120:123], a[140:143]\n"
  "s_add_i32 s64, s50, 16384\n"
  "buffer_load_dwordx4 v238, s[40:43], s64 offen sc1 lds\n"
  "v_and_b32 v129, 0xfff8fff8, v129\n"
  "v_mfma_f32_16x16x32_f16 a[144:147], v[48:51], v[104:107], a[144:147]\n"
  "v_pk_add_f16 v120, v104, v108\n"
  "v_pk_add_f16 v121, v105, v109\n"
  "v_mfma_f32_16x16x32_f16 a[156:159], v[52:55], v[104:107], a[156:159]\n"
  "v_pk_add_f16 v122, v106, v110\n"
  "v_pk_add_f16 v123, v107, v111\n"
  "v_mfma_f32_16x16x32_f16 a[168:171], v[56:59], v[104:107], a[168:171]\n"
  "v_cvt_pkrtz_f16_f32 v130, v152, v153\n"
  "v_cvt_pkrtz_f16_f32 v131, v154, v155\n"
  "v_mfma_f32_16x16x32_f16 a[180:183], v[60:63], v[104:107], a[180:183]\n"
  "buffer_store_dwordx4 v[124:127], v242, s[40:43], s56 offen offset:256 sc1\n"
  "v_and_b32 v130, 0xfff8fff8, v130\n"
  "v_mfma_f32_16x16x32_f16 a[148:151], v[64:67], v[108:111], a[148:151]\n"
  "ds_read_b128 v[104:107], v232 offset:2048\n"
  "v_and_b32 v131, 0xfff8fff8, v131\n"
  "v_mfma_f32_16x16x32_f16 a[160:163], v[68:71], v[108:111], a[160:163]\n"
  "s_cmp_lt_u32 s57, 0x80000\n"
  "s_cselect_b32 s58, s57, 0xf0000000\n"
  "s_cselect_b32 s59, s59, 0xf0000000\n"
  "v_mfma_f32_16x16x32_f16 a[172:175], v[72:75], v[108:111], a[172:175]\n"
  "s_add_i32 m0, s47, 26624\n"
  "s_waitcnt lgkmcnt(1)\n"
  "v_pk_add_f16 v33, v1, v17\n"
  "v_mfma_f32_16x16x32_f16 a[184:187], v[76:79], v[108:111], a[184:187]\n"
  "s_add_i32 s64, s50, 4214784\n"
  "buffer_load_dwordx4 v238, s[40:43], s64 offen sc1 lds\n"
  "v_pk_add_f16 v34, v2, v18\n"
  "v_mfma_f32_16x16x32_f16 a[152:155], v[80:83], v[120:123], a[152:155]\n"
  "ds_read_b128 v[108:111], v232 offset:2560\n"
  "v_pk_add_f16 v35, v3, v19\n"
  "v_mfma_f32_16x16x32_f16 a[164:167], v[84:87], v[120:123], a[164:167]\n"
  "v_pk_add_f16 v36, v4, v20\n"
  "v_pk_add_f16 v37, v5, v21\n"
  "v_mfma_f32_16x16x32_f16 a[176:179], v[88:91], v[120:123], a[176:179]\n"
  "s_add_i32 m0, s47, 30720\n"
  "v_pk_add_f16 v38, v6, v22\n"
  "v_pk_add_f16 v39, v7, v23\n"
  "v_mfma_f32_16x16x32_f16 a[188:191], v[92:95], v[120:123], a[188:191]\n"
  "s_add_i32 s64, s50, 32768\n"
  "buffer_load_dwordx4 v238, s[40:43], s64 offen sc1 lds\n"
  "v_pk_add_f16 v40, v8, v24\n"
  "v_mfma_f32_16x16x32_f16 a[192:195], v[48:51], v[112:115], a[192:195]\n"
  "s_add_i32 s47, s44, s46\n"
  "v_pk_add_f16 v120, v112, v116\n"
  "v_pk_add_f16 v121, v113, v117\n"
  "v_mfma_f32_16x16x32_f16 a[204:207], v[52:55], v[112:115], a[204:207]\n"
  "s_add_i32 s48, s47, s45\n"
  "v_pk_add_f16 v122, v114, v118\n"
  "v_pk_add_f16 v123, v115, v119\n"
  "v_mfma_f32_16x16x32_f16 a[216:219], v[56:59], v[112:115], a[216:219]\n"
  "s_add_i32 s46, s46, s49\n"
  "v_pk_add_f16 v41, v9, v25\n"
  "v_pk_add_f16 v42, v10, v26\n"
  "v_mfma_f32_16x16x32_f16 a[228:231], v[60:63], v[112:115], a[228:231]\n"
  "s_add_i32 s50, s50, 4194304\n"
  "buffer_store_dwordx4 v[128:131], v242, s[40:43], s56 offen offset:384 sc1\n"
  "v_pk_add_f16 v43, v11, v27\n"
  "v_mfma_f32_16x16x32_f16 a[196:199], v[64:67], v[116:119], a[196:199]\n"
  "s_add_i32 s55, s55, 8404992\n"
  "v_pk_add_f16 v44, v12, v28\n"
  "v_pk_add_f16 v45, v13, v29\n"
  "v_mfma_f32_16x16x32_f16 a[208:211], v[68:71], v[116:119], a[208:211]\n"
  "s_and_b32 s56, s55, 0xff80c000\n"
  "v_pk_add_f16 v46, v14, v30\n"
  "v_pk_add_f16 v47, v15, v31\n"
  "v_mfma_f32_16x16x32_f16 a[220:223], v[72:75], v[116:119], a[220:223]\n"
  "v_add_u32 v230, s49, v230\n"
  "v_add_u32 v231, s49, v231\n"
  "v_mfma_f32_16x16x32_f16 a[232:235], v[76:79], v[116:119], a[232:235]\n"
  "v_add_u32 v232, s49, v232\n"
  "v_add_u32 v236, 4096, v236\n"
  "v_mfma_f32_16x16x32_f16 a[200:203], v[80:83], v[120:123], a[200:203]\n"
  "s_waitcnt vmcnt(13) lgkmcnt(0)\n"
  "v_mfma_f32_16x16x32_f16 a[212:215], v[84:87], v[120:123], a[212:215]\n"
  "s_cmp_gt_u32 s62, 88\n"
  "s_cbranch_scc1 .Lpace_po_None_%=\n"
  ".Lpaced_po_None_%=:\n"
  "v_mfma_f32_16x16x32_f16 a[224:227], v[88:91], v[120:123], a[224:227]\n"
  "s_barrier\n"
  "v_mfma_f32_16x16x32_f16 a[236:239], v[92:95], v[120:123], a[236:239]\n"
  "s_sub_i32 s63, s63, 1\n"
  "s_cmp_lg_u32 s63, 0\n"
  "s_cbranch_scc1 .LloopP_%=\n"
  ".Lcf_t:\n"
  "s_cmp_eq_u32 s85, 0\n"
  "s_cbranch_scc1 .Lnt_%=\n"
  "s_getpc_b64 s[86:87]\n"
  "s_mov_b32 m0, s85\n"
  "s_nop 0\n"
  "global_load_lds_dword v251, s[86:87]\n"
  ".Lnt_%=:\n"
  "v_mfma_f32_16x16x32_f16 a[0:3], v[0:3], v[96:99], a[0:3]\n"
  "buffer_load_dword v247, v246, s[40:43], s54 offen sc1\n"
  "v_pk_add_f16 v120, v96, v100\n"
  "v_mfma_f32_16x16x32_f16 a[12:15], v[4:7], v[96:99], a[12:15]\n"
  "v_pk_add_f16 v121, v97, v101\n"
  "v_pk_add_f16 v122, v98, v102\n"
  "v_mfma_f32_16x16x32_f16 a[24:27], v[8:11], v[96:99], a[24:27]\n"
  "ds_read_b64_tr_b16 v[48:49], v228 offset:0\n"
  "v_pk_add_f16 v123, v99, v103\n"
  "v_mfma_f32_16x16x32_f16 a[36:39], v[12:15], v[96:99], a[36:39]\n"
  "s_add_i32 m0, s47, 0\n"
  "ds_read_b64_tr_b16 v[50:51], v228 offset:8192\n"
  "v_accvgpr_read_b32 v124, a0\n"
  "v_mfma_f32_16x16x32_f16 a[4:7], v[16:19], v[100:103], a[4:7]\n"
  "s_add_i32 s64, s50, 4194304\n"
  "buffer_load_dwordx4 v237, s[40:43], s64 offen sc1 lds\n"
  "v_accvgpr_read_b32 v125, a1\n"
  "v_mfma_f32_16x16x32_f16 a[16:19], v[20:23], v[100:103], a[16:19]\n"
  "s_cmp_eq_u32 s46, 73728\n"
  "s_cselect_b32 s49, s68, s69\n"
  "ds_read_b128 v[96:99], v232 offset:4096\n"
  "v_mfma_f32_16x16x32_f16 a[28:31], v[24:27], v[100:103], a[28:31]\n"
  "ds_read_b64_tr_b16 v[64:65], v228 offset:256\n"
  "v_accvgpr_read_b32 v126, a2\n"
  "v_mfma_f32_16x16x32_f16 a[40:43], v[28:31], v[100:103], a[40:43]\n"
  "s_add_i32 m0, s47, 4096\n"
  "ds_read_b128 v[100:103], v232 offset:4608\n"
  "v_accvgpr_read_b32 v127, a3\n"
  "v_mfma_f32_16x16x32_f16 a[8:11], v[32:35], v[120:123], a[8:11]\n"
  "s_add_i32 s64, s50, 4198400\n"
  "buffer_load_dwordx4 v237, s[40:43], s64 offen sc1 lds\n"
  "v_accvgpr_read_b32 v128, a4\n"
  "v_mfma_f32_16x16x32_f16 a[20:23], v[36:39], v[120:123], a[20:23]\n"
  "ds_read_b64_tr_b16 v[66:67], v228 offset:8448\n"
  "v_accvgpr_read_b32 v129, a5\n"
  "v_mfma_f32_16x16x32_f16 a[32:35], v[40:43], v[120:123], a[32:35]\n"
  "ds_read_b64_tr_b16 v[52:53], v229 offset:0\n"
  "v_accvgpr_read_b32 v130, a6\n"
  "v_mfma_f32_16x16x32_f16 a[44:47], v[44:47], v[120:123], a[44:47]\n"
  "s_add_i32 m0, s47, 8192\n"
  "ds_read_b64_tr_b16 v[54:55], v229 offset:8192\n"
  "v_accvgpr_read_b32 v131, a7\n"
  "v_mfma_f32_16x16x32_f16 a[48:51], v[0:3], v[104:107], a[48:51]\n"
  "s_add_i32 s64, s50, 4202496\n"
  "buffer_load_dwordx4 v237, s[40:43], s64 offen sc1 lds\n"
  "v_pk_add_f16 v120, v104, v108\n"
  "v_mfma_f32_16x16x32_f16 a[60:63], v[4:7], v[104:107], a[60:63]\n"
  "v_pk_add_f16 v121, v105, v109\n"
  "v_pk_add_f16 v122, v106, v110\n"
  "v_mfma_f32_16x16x32_f16 a[72:75], v[8:11], v[104:107], a[72:75]\n"
  "ds_read_b64_tr_b16 v[68:69], v229 offset:256\n"
  "v_pk_add_f16 v123, v107, v111\n"
  "v_mfma_f32_16x16x32_f16 a[84:87], v[12:15], v[104:107], a[84:87]\n"
  "s_add_i32 m0, s47, 12288\n"
  "ds_read_b64_tr_b16 v[70:71], v229 offset:8448\n"
  "v_accvgpr_read_b32 v132, a8\n"
  "v_mfma_f32_16x16x32_f16 a[52:55], v[16:19], v[108:111], a[52:55]\n"
  "s_add_i32 s64, s50, 4206592\n"
  "buffer_load_dwordx4 v237, s[40:43], s64 offen sc1 lds\n"
  "v_accvgpr_read_b32 v133, a9\n"
  "v_mfma_f32_16x16x32_f16 a[64:67], v[20:23], v[108:111], a[64:67]\n"
  "s_waitcnt lgkmcnt(4)\n"
  "ds_read_b128 v[104:107], v232 offset:6144\n"
  "v_accvgpr_read_b32 v134, a10\n"
  "v_mfma_f32_16x16x32_f16 a[76:79], v[24:27], v[108:111], a[76:79]\n"
  "ds_read_b64_tr_b16 v[56:57], v230 offset:0\n"
  "v_accvgpr_read_b32 v135, a11\n"
  "v_mfma_f32_16x16x32_f16 a[88:91], v[28:31], v[108:111], a[88:91]\n"
  "s_add_i32 m0, s47, 16384\n"
  "ds_read_b128 v[108:111], v232 offset:6656\n"
  "v_accvgpr_read_b32 v136, a12\n"
  "v_mfma_f32_16x16x32_f16 a[56:59], v[32:35], v[120:123], a[56:59]\n"
  "s_add_i32 s64, s50, 4194304\n"
  "buffer_load_dwordx4 v238, s[40:43], s64 offen sc1 lds\n"
  "v_accvgpr_read_b32 v137, a13\n"
  "v_mfma_f32_16x16x32_f16 a[68:71], v[36:39], v[120:123], a[68:71]\n"
  "ds_read_b64_tr_b16 v[58:59], v230 offset:8192\n"
  "v_accvgpr_read_b32 v138, a14\n"
  "v_mfma_f32_16x16x32_f16 a[80:83], v[40:43], v[120:123], a[80:83]\n"
  "s_waitcnt lgkmcnt(4)\n"
  "ds_read_b64_tr_b16 v[72:73], v230 offset:256\n"
  "v_accvgpr_read_b32 v139, a15\n"
  "v_mfma_f32_16x16x32_f16 a[92:95], v[44:47], v[120:123], a[92:95]\n"
  "s_add_i32 m0, s47, 20480\n"
  "ds_read_b64_tr_b16 v[74:75], v230 offset:8448\n"
  "v_accvgpr_read_b32 v140, a16\n"
  "v_mfma_f32_16x16x32_f16 a[96:99], v[0:3], v[96:99], a[96:99]\n"
  "s_add_i32 s64, s50, 4096\n"
  "buffer_load_dwordx4 v238, s[40:43], s64 offen sc1 lds\n"
  "v_pk_add_f16 v120, v96, v100\n"
  "v_mfma_f32_16x16x32_f16 a[108:111], v[4:7], v[96:99], a[108:111]\n"
  "v_pk_add_f16 v121, v97, v101\n"
  "v_pk_add_f16 v122, v98, v102\n"
  "v_mfma_f32_16x16x32_f16 a[120:123], v[8:11], v[96:99], a[120:123]\n"
  "ds_read_b64_tr_b16 v[60:61], v231 offset:0\n"
  "v_pk_add_f16 v123, v99, v103\n"
  "v_mfma_f32_16x16x32_f16 a[132:135], v[12:15], v[96:99], a[132:135]\n"
  "s_add_i32 m0, s48, 24576\n"
  "ds_read_b64_tr_b16 v[62:63], v231 offset:8192\n"
  "v_accvgpr_read_b32 v141, a17\n"
  "v_mfma_f32_16x16x32_f16 a[100:103], v[16:19], v[100:103], a[100:103]\n"
  "s_add_i32 s64, s50, 16384\n"
  "buffer_load_dwordx4 v238, s[40:43], s64 offen sc1 lds\n"
  "v_accvgpr_read_b32 v142, a18\n"
  "v_mfma_f32_16x16x32_f16 a[112:115], v[20:23], v[100:103], a[112:115]\n"
  "ds_read_b128 v[96:99], v232 offset:0\n"
  "v_accvgpr_read_b32 v143, a19\n"
  "v_mfma_f32_16x16x32_f16 a[124:127], v[24:27], v[100:103], a[124:127]\n"
  "s_waitcnt lgkmcnt(3)\n"
  "ds_read_b128 v[112:115], v232 offset:8192\n"
  "v_accvgpr_read_b32 v144, a20\n"
  "v_mfma_f32_16x16x32_f16 a[136:139], v[28:31], v[100:103], a[136:139]\n"
  "s_add_i32 m0, s47, 26624\n"
  "ds_read_b64_tr_b16 v[76:77], v231 offset:256\n"
  "v_accvgpr_read_b32 v145, a21\n"
  "v_mfma_f32_16x16x32_f16 a[104:107], v[32:35], v[120:123], a[104:107]\n"
  "s_add_i32 s64, s50, 4214784\n"
  "buffer_load_dwordx4 v238, s[40:43], s64 offen sc1 lds\n"
  "v_accvgpr_read_b32 v146, a22\n"
  "v_mfma_f32_16x16x32_f16 a[116:119], v[36:39], v[120:123], a[116:119]\n"
  "ds_read_b128 v[100:103], v232 offset:512\n"
  "v_accvgpr_read_b32 v147, a23\n"
  "v_mfma_f32_16x16x32_f16 a[128:131], v[40:43], v[120:123], a[128:131]\n"
  "ds_read_b128 v[116:119], v232 offset:8704\n"
  "v_accvgpr_read_b32 v148, a24\n"
  "v_mfma_f32_16x16x32_f16 a[140:143], v[44:47], v[120:123], a[140:143]\n"
  "s_add_i32 m0, s47, 30720\n"
  "ds_read_b64_tr_b16 v[78:79], v231 offset:8448\n"
  "v_accvgpr_read_b32 v149, a25\n"
  "v_mfma_f32_16x16x32_f16 a[144:147], v[0:3], v[104:107], a[144:147]\n"
  "s_add_i32 s64, s50, 32768\n"
  "buffer_load_dwordx4 v238, s[40:43], s64 offen sc1 lds\n"
  "v_pk_add_f16 v120, v104, v108\n"
  "v_mfma_f32_16x16x32_f16 a[156:159], v[4:7], v[104:107], a[156:159]\n"
  "v_pk_add_f16 v121, v105, v109\n"
  "v_pk_add_f16 v122, v106, v110\n"
  "v_mfma_f32_16x16x32_f16 a[168:171], v[8:11], v[104:107], a[168:171]\n"
  "v_pk_add_f16 v123, v107, v111\n"
  "v_accvgpr_read_b32 v150, a26\n"
  "v_mfma_f32_16x16x32_f16 a[180:183], v[12:15], v[104:107], a[180:183]\n"
  "ds_read_b128 v[104:107], v232 offset:2048\n"
  "v_accvgpr_read_b32 v151, a27\n"
  "v_mfma_f32_16x16x32_f16 a[148:151], v[16:19], v[108:111], a[148:151]\n"
  "s_add_i32 s47, s44, s46\n"
  "v_accvgpr_read_b32 v152, a28\n"
  "v_accvgpr_read_b32 v153, a29\n"
  "v_mfma_f32_16x16x32_f16 a[160:163], v[20:23], v[108:111], a[160:163]\n"
  "s_add_i32 s48, s47, s45\n"
  "v_accvgpr_read_b32 v154, a30\n"
  "v_accvgpr_read_b32 v155, a31\n"
  "v_mfma_f32_16x16x32_f16 a[172:175], v[24:27], v[108:111], a[172:175]\n"
  "s_waitcnt lgkmcnt(1)\n"
  "v_accvgpr_read_b32 v156, a32\n"
  "v_accvgpr_read_b32 v157, a33\n"
  "v_mfma_f32_16x16x32_f16 a[184:187], v[28:31], v[108:111], a[184:187]\n"
  "s_add_i32 s46, s46, s49\n"
  "ds_read_b128 v[108:111], v232 offset:2560\n"
  "v_accvgpr_read_b32 v158, a34\n"
  "v_mfma_f32_16x16x32_f16 a[152:155], v[32:35], v[120:123], a[152:155]\n"
  "s_add_i32 s50, s50, 4194304\n"
  "v_accvgpr_read_b32 v159, a35\n"
  "v_accvgpr_read_b32 v160, a36\n"
  "v_mfma_f32_16x16x32_f16 a[164:167], v[36:39], v[120:123], a[164:167]\n"
  "v_accvgpr_read_b32 v161, a37\n"
  "v_accvgpr_read_b32 v162, a38\n"
  "v_mfma_f32_16x16x32_f16 a[176:179], v[40:43], v[120:123], a[176:179]\n"
  "v_accvgpr_read_b32 v163, a39\n"
  "v_accvgpr_read_b32 v164, a40\n"
  "v_mfma_f32_16x16x32_f16 a[188:191], v[44:47], v[120:123], a[188:191]\n"
  "v_accvgpr_read_b32 v165, a41\n"
  "v_accvgpr_read_b32 v166, a42\n"
  "v_mfma_f32_16x16x32_f16 a[192:195], v[0:3], v[112:115], a[192:195]\n"
  "v_pk_add_f16 v120, v112, v116\n"
  "v_pk_add_f16 v121, v113, v117\n"
  "v_mfma_f32_16x16x32_f16 a[204:207], v[4:7], v[112:115], a[204:207]\n"
  "v_pk_add_f16 v122, v114, v118\n"
  "v_pk_add_f16 v123, v115, v119\n"
  "v_mfma_f32_16x16x32_f16 a[216:219], v[8:11], v[112:115], a[216:219]\n"
  "v_accvgpr_read_b32 v167, a43\n"
  "v_accvgpr_read_b32 v168, a44\n"
  "v_mfma_f32_16x16x32_f16 a[228:231], v[12:15], v[112:115], a[228:231]\n"
  "v_accvgpr_read_b32 v169, a45\n"
  "v_accvgpr_read_b32 v170, a46\n"
  "v_mfma_f32_16x16x32_f16 a[196:199], v[16:19], v[116:119], a[196:199]\n"
  "v_accvgpr_read_b32 v171, a47\n"
  "v_pk_add_f16 v80, v48, v64\n"
  "v_mfma_f32_16x16x32_f16 a[208:211], v[20:23], v[116:119], a[208:211]\n"
  "v_pk_add_f16 v81, v49, v65\n"
  "v_pk_add_f16 v82, v50, v66\n"
  "v_mfma_f32_16x16x32_f16 a[220:223], v[24:27], v[116:119], a[220:223]\n"
  "v_pk_add_f16 v83, v51, v67\n"
  "v_pk_add_f16 v84, v52, v68\n"
  "v_mfma_f32_16x16x32_f16 a[232:235], v[28:31], v[116:119], a[232:235]\n"
  "v_pk_add_f16 v85, v53, v69\n"
  "v_pk_add_f16 v86, v54, v70\n"
  "v_mfma_f32_16x16x32_f16 a[200:203], v[32:35], v[120:123], a[200:203]\n"
  "v_pk_add_f16 v87, v55, v71\n"
  "v_pk_add_f16 v88, v56, v72\n"
  "s_waitcnt vmcnt(9) lgkmcnt(0)\n"
  "v_mfma_f32_16x16x32_f16 a[212:215], v[36:39], v[120:123], a[212:215]\n"
  "v_cmp_eq_u32 vcc, 0, v247\n"
  "s_cbranch_vccnz .Lspin_s22_None_%=\n"
  ".Lspun_s22_None_%=:\n"
  "v_mfma_f32_16x16x32_f16 a[224:227], v[40:43], v[120:123], a[224:227]\n"
  "v_pk_add_f16 v89, v57, v73\n"
  "v_pk_add_f16 v90, v58, v74\n"
  "s_barrier\n"
  "v_mfma_f32_16x16x32_f16 a[236:239], v[44:47], v[120:123], a[236:239]\n"
  "v_pk_add_f16 v91, v59, v75\n"
  "v_pk_add_f16 v92, v60, v76\n"
  "v_pk_add_f16 v93, v61, v77\n"
  "v_pk_add_f16 v94, v62, v78\n"
  "v_pk_add_f16 v95, v63, v79\n"
  "v_add_u32 v228, s49, v228\n"
  "v_add_u32 v229, s49, v229\n"
  "v_add_u32 v230, s49, v230\n"
  "v_add_u32 v231, s49, v231\n"
  "v_add_u32 v232, s49, v232\n"
  "v_mfma_f32_16x16x32_f16 v[124:127], v[48:51], v[96:99], v[124:127]\n"
  "v_pk_add_f16 v120, v96, v100\n"
  "v_pk_add_f16 v121, v97, v101\n"
  "v_mfma_f32_16x16x32_f16 v[136:139], v[52:55], v[96:99], v[136:139]\n"
  "v_pk_add_f16 v122, v98, v102\n"
  "v_pk_add_f16 v123, v99, v103\n"
  "v_mfma_f32_16x16x32_f16 v[148:151], v[56:59], v[96:99], v[148:151]\n"
  "s_and_b32 s65, s51, 0xffffe300\n"
  "ds_read_b64_tr_b16 v[0:1], v228 offset:0\n"
  "ds_read_b64_tr_b16 v[2:3], v228 offset:8192\n"
  "v_mfma_f32_16x16x32_f16 v[160:163], v[60:63], v[96:99], v[160:163]\n"
  "s_add_i32 m0, s47, 0\n"
  "ds_read_b128 v[96:99], v232 offset:4096\n"
  "ds_read_b64_tr_b16 v[16:17], v228 offset:256\n"
  "v_mfma_f32_16x16x32_f16 v[128:131], v[64:67], v[100:103], v[128:131]\n"
  "s_add_i32 s64, s50, 4194304\n"
  "buffer_load_dwordx4 v237, s[40:43], s64 offen sc1 lds\n"
  "ds_read_b64_tr_b16 v[18:19], v228 offset:8448\n"
  "v_mfma_f32_16x16x32_f16 v[140:143], v[68:71], v[100:103], v[140:143]\n"
  "s_cmp_eq_u32 s46, 73728\n"
  "s_cselect_b32 s49, s68, s69\n"
  "ds_read_b64_tr_b16 v[4:5], v229 offset:0\n"
  "v_mfma_f32_16x16x32_f16 v[152:155], v[72:75], v[100:103], v[152:155]\n"
  "ds_read_b64_tr_b16 v[6:7], v229 offset:8192\n"
  "v_add_u32 v228, s49, v228\n"
  "v_mfma_f32_16x16x32_f16 v[164:167], v[76:79], v[100:103], v[164:167]\n"
  "ds_read_b128 v[100:103], v232 offset:4608\n"
  "ds_read_b64_tr_b16 v[20:21], v229 offset:256\n"
  "v_mfma_f32_16x16x32_f16 v[132:135], v[80:83], v[120:123], v[132:135]\n"
  "buffer_store_dword v232, v245, s[40:43], s65 offen sc1\n"
  "ds_read_b64_tr_b16 v[22:23], v229 offset:8448\n"
  "v_mfma_f32_16x16x32_f16 v[144:147], v[84:87], v[120:123], v[144:147]\n"
  "ds_read_b64_tr_b16 v[8:9], v230 offset:0\n"
  "v_add_u32 v229, s49, v229\n"
  "v_mfma_f32_16x16x32_f16 v[156:159], v[88:91], v[120:123], v[156:159]\n"
  "s_add_i32 s53, s53, 8192\n"
  "s_lshr_b32 s52, s52, 1\n"
  "ds_read_b64_tr_b16 v[10:11], v230 offset:8192\n"
  "v_mfma_f32_16x16x32_f16 v[168:171], v[92:95], v[120:123], v[168:171]\n"
  "s_add_i32 m0, s47, 4096\n"
  "s_waitcnt lgkmcnt(7)\n"
  "ds_read_b64_tr_b16 v[24:25], v230 offset:256\n"
  "v_mfma_f32_16x16x32_f16 a[48:51], v[48:51], v[104:107], a[48:51]\n"
  "s_add_i32 s64, s50, 4198400\n"
  "buffer_load_dwordx4 v237, s[40:43], s64 offen sc1 lds\n"
  "v_pk_add_f16 v120, v104, v108\n"
  "v_mfma_f32_16x16x32_f16 a[60:63], v[52:55], v[104:107], a[60:63]\n"
  "s_add_i32 s51, s51, 8448\n"
  "v_pk_add_f16 v121, v105, v109\n"
  "v_pk_add_f16 v122, v106, v110\n"
  "v_mfma_f32_16x16x32_f16 a[72:75], v[56:59], v[104:107], a[72:75]\n"
  "ds_read_b64_tr_b16 v[26:27], v230 offset:8448\n"
  "v_pk_add_f16 v123, v107, v111\n"
  "v_mfma_f32_16x16x32_f16 a[84:87], v[60:63], v[104:107], a[84:87]\n"
  "s_add_i32 m0, s47, 8192\n"
  "ds_read_b128 v[104:107], v232 offset:6144\n"
  "v_accvgpr_read_b32 v180, a48\n"
  "v_mfma_f32_16x16x32_f16 a[52:55], v[64:67], v[108:111], a[52:55]\n"
  "s_add_i32 s64, s50, 4202496\n"
  "buffer_load_dwordx4 v237, s[40:43], s64 offen sc1 lds\n"
  "v_accvgpr_read_b32 v181, a49\n"
  "v_mfma_f32_16x16x32_f16 a[64:67], v[68:71], v[108:111], a[64:67]\n"
  "s_bitcmp1_b32 s52, 0\n"
  "s_cselect_b32 s54, s53, 134217728\n"
  "ds_read_b64_tr_b16 v[12:13], v231 offset:0\n"
  "v_mfma_f32_16x16x32_f16 a[76:79], v[72:75], v[108:111], a[76:79]\n"
  "s_waitcnt lgkmcnt(6)\n"
  "ds_read_b64_tr_b16 v[14:15], v231 offset:8192\n"
  "v_accvgpr_read_b32 v182, a50\n"
  "v_mfma_f32_16x16x32_f16 a[88:91], v[76:79], v[108:111], a[88:91]\n"
  "s_add_i32 m0, s47, 12288\n"
  "ds_read_b128 v[108:111], v232 offset:6656\n"
  "v_accvgpr_read_b32 v183, a51\n"
  "v_mfma_f32_16x16x32_f16 a[56:59], v[80:83], v[120:123], a[56:59]\n"
  "s_add_i32 s64, s50, 4206592\n"
  "buffer_load_dwordx4 v237, s[40:43], s64 offen sc1 lds\n"
  "v_accvgpr_read_b32 v184, a52\n"
  "v_mfma_f32_16x16x32_f16 a[68:71], v[84:87], v[120:123], a[68:71]\n"
  "ds_read_b128 v[112:115], v232 offset:8192\n"
  "v_accvgpr_read_b32 v185, a53\n"
  "v_mfma_f32_16x16x32_f16 a[80:83], v[88:91], v[120:123], a[80:83]\n"
  "s_waitcnt lgkmcnt(5)\n"
  "ds_read_b64_tr_b16 v[28:29], v231 offset:256\n"
  "v_accvgpr_read_b32 v186, a54\n"
  "v_mfma_f32_16x16x32_f16 a[92:95], v[92:95], v[120:123], a[92:95]\n"
  "s_add_i32 m0, s47, 16384\n"
  "ds_read_b128 v[116:119], v232 offset:8704\n"
  "v_accvgpr_read_b32 v187, a55\n"
  "v_mfma_f32_16x16x32_f16 a[96:99], v[48:51], v[96:99], a[96:99]\n"
  "s_add_i32 s64, s50, 4194304\n"
  "buffer_load_dwordx4 v238, s[40:43], s64 offen sc1 lds\n"
  "v_pk_add_f16 v120, v96, v100\n"
  "v_mfma_f32_16x16x32_f16 a[108:111], v[52:55], v[96:99], a[108:111]\n"
  "v_pk_add_f16 v121, v97, v101\n"
  "v_pk_add_f16 v122, v98, v102\n"
  "v_mfma_f32_16x16x32_f16 a[120:123], v[56:59], v[96:99], a[120:123]\n"
  "ds_read_b64_tr_b16 v[30:31], v231 offset:8448\n"
  "v_pk_add_f16 v123, v99, v103\n"
  "v_mfma_f32_16x16x32_f16 a[132:135], v[60:63], v[96:99], a[132:135]\n"
  "s_add_i32 m0, s47, 20480\n"
  "ds_read_b128 v[96:99], v232 offset:0\n"
  "v_accvgpr_read_b32 v188, a56\n"
  "v_mfma_f32_16x16x32_f16 a[100:103], v[64:67], v[100:103], a[100:103]\n"
  "s_add_i32 s64, s50, 4096\n"
  "buffer_load_dwordx4 v238, s[40:43], s64 offen sc1 lds\n"
  "v_accvgpr_read_b32 v189, a57\n"
  "v_mfma_f32_16x16x32_f16 a[112:115], v[68:71], v[100:103], a[112:115]\n"
  "v_accvgpr_read_b32 v190, a58\n"
  "v_accvgpr_read_b32 v191, a59\n"
  "v_mfma_f32_16x16x32_f16 a[124:127], v[72:75], v[100:103], a[124:127]\n"
  "v_accvgpr_read_b32 v192, a60\n"
  "v_accvgpr_read_b32 v193, a61\n"
  "v_mfma_f32_16x16x32_f16 a[136:139], v[76:79], v[100:103], a[136:139]\n"
  "s_add_i32 m0, s48, 24576\n"
  "ds_read_b128 v[100:103], v232 offset:512\n"
  "v_accvgpr_read_b32 v194, a62\n"
  "v_mfma_f32_16x16x32_f16 a[104:107], v[80:83], v[120:123], a[104:107]\n"
  "s_add_i32 s64, s50, 16384\n"
  "buffer_load_dwordx4 v238, s[40:43], s64 offen sc1 lds\n"
  "v_accvgpr_read_b32 v195, a63\n"
  "v_mfma_f32_16x16x32_f16 a[116:119], v[84:87], v[120:123], a[116:119]\n"
  "s_waitcnt lgkmcnt(6)\n"
  "v_accvgpr_read_b32 v196, a64\n"
  "v_accvgpr_read_b32 v197, a65\n"
  "v_mfma_f32_16x16x32_f16 a[128:131], v[88:91], v[120:123], a[128:131]\n"
  "s_waitcnt lgkmcnt(2)\n"
  "v_accvgpr_read_b32 v198, a66\n"
  "v_accvgpr_read_b32 v199, a67\n"
  "v_mfma_f32_16x16x32_f16 a[140:143], v[92:95], v[120:123], a[140:143]\n"
  "s_add_i32 m0, s47, 26624\n"
  "v_accvgpr_read_b32 v200, a68\n"
  "v_accvgpr_read_b32 v201, a69\n"
  "v_mfma_f32_16x16x32_f16 a[144:147], v[48:51], v[104:107], a[144:147]\n"
  "s_add_i32 s64, s50, 4214784\n"
  "buffer_load_dwordx4 v238, s[40:43], s64 offen sc1 lds\n"
  "v_pk_add_f16 v120, v104, v108\n"
  "v_mfma_f32_16x16x32_f16 a[156:159], v[52:55], v[104:107], a[156:159]\n"
  "v_pk_add_f16 v121, v105, v109\n"
  "v_pk_add_f16 v122, v106, v110\n"
  "v_mfma_f32_16x16x32_f16 a[168:171], v[56:59], v[104:107], a[168:171]\n"
  "v_pk_add_f16 v123, v107, v111\n"
  "v_accvgpr_read_b32 v202, a70\n"
  "v_mfma_f32_16x16x32_f16 a[180:183], v[60:63], v[104:107], a[180:183]\n"
  "s_add_i32 m0, s47, 30720\n"
  "ds_read_b128 v[104:107], v232 offset:2048\n"
  "v_accvgpr_read_b32 v203, a71\n"
  "v_mfma_f32_16x16x32_f16 a[148:151], v[64:67], v[108:111], a[148:151]\n"
  "s_add_i32 s64, s50, 32768\n"
  "buffer_load_dwordx4 v238, s[40:43], s64 offen sc1 lds\n"
  "v_accvgpr_read_b32 v204, a72\n"
  "v_mfma_f32_16x16x32_f16 a[160:163], v[68:71], v[108:111], a[160:163]\n"
  "s_add_i32 s47, s44, s46\n"
  "v_accvgpr_read_b32 v205, a73\n"
  "v_accvgpr_read_b32 v206, a74\n"
  "v_mfma_f32_16x16x32_f16 a[172:175], v[72:75], v[108:111], a[172:175]\n"
  "s_add_i32 s48, s47, s45\n"
  "v_accvgpr_read_b32 v207, a75\n"
  "v_accvgpr_read_b32 v208, a76\n"
  "v_mfma_f32_16x16x32_f16 a[184:187], v[76:79], v[108:111], a[184:187]\n"
  "s_add_i32 s46, s46, s49\n"
  "ds_read_b128 v[108:111], v232 offset:2560\n"
  "v_accvgpr_read_b32 v209, a77\n"
  "v_mfma_f32_16x16x32_f16 a[152:155], v[80:83], v[120:123], a[152:155]\n"
  "s_add_i32 s50, s50, 4194304\n"
  "v_accvgpr_read_b32 v210, a78\n"
  "v_accvgpr_read_b32 v211, a79\n"
  "v_mfma_f32_16x16x32_f16 a[164:167], v[84:87], v[120:123], a[164:167]\n"
  "v_accvgpr_read_b32 v212, a80\n"
  "v_accvgpr_read_b32 v213, a81\n"
  "v_mfma_f32_16x16x32_f16 a[176:179], v[88:91], v[120:123], a[176:179]\n"
  "v_accvgpr_read_b32 v214, a82\n"
  "v_accvgpr_read_b32 v215, a83\n"
  "v_mfma_f32_16x16x32_f16 a[188:191], v[92:95], v[120:123], a[188:191]\n"
  "v_accvgpr_read_b32 v216, a84\n"
  "v_accvgpr_read_b32 v217, a85\n"
  "v_mfma_f32_16x16x32_f16 a[192:195], v[48:51], v[112:115], a[192:195]\n"
  "v_pk_add_f16 v120, v112, v116\n"
  "v_pk_add_f16 v121, v113, v117\n"
  "v_mfma_f32_16x16x32_f16 a[204:207], v[52:55], v[112:115], a[204:207]\n"
  "v_pk_add_f16 v122, v114, v118\n"
  "v_pk_add_f16 v123, v115, v119\n"
  "v_mfma_f32_16x16x32_f16 a[216:219], v[56:59], v[112:115], a[216:219]\n"
  "v_accvgpr_read_b32 v218, a86\n"
  "v_accvgpr_read_b32 v219, a87\n"
  "v_mfma_f32_16x16x32_f16 a[228:231], v[60:63], v[112:115], a[228:231]\n"
  "v_accvgpr_read_b32 v220, a88\n"
  "v_accvgpr_read_b32 v221, a89\n"
  "v_mfma_f32_16x16x32_f16 a[196:199], v[64:67], v[116:119], a[196:199]\n"
  "v_accvgpr_read_b32 v222, a90\n"
  "v_accvgpr_read_b32 v223, a91\n"
  "v_mfma_f32_16x16x32_f16 a[208:211], v[68:71], v[116:119], a[208:211]\n"
  "v_accvgpr_read_b32 v224, a92\n"
  "v_accvgpr_read_b32 v225, a93\n"
  "v_mfma_f32_16x16x32_f16 a[220:223], v[72:75], v[116:119], a[220:223]\n"
  "v_accvgpr_read_b32 v226, a94\n"
  "v_accvgpr_read_b32 v227, a95\n"
  "v_mfma_f32_16x16x32_f16 a[232:235], v[76:79], v[116:119], a[232:235]\n"
  "v_pk_add_f16 v32, v0, v16\n"
  "v_pk_add_f16 v33, v1, v17\n"
  "v_mfma_f32_16x16x32_f16 a[200:203], v[80:83], v[120:123], a[200:203]\n"
  "v_pk_add_f16 v34, v2, v18\n"
  "v_pk_add_f16 v35, v3, v19\n"
  "s_waitcnt vmcnt(10) lgkmcnt(0)\n"
  "v_mfma_f32_16x16x32_f16 a[212:215], v[84:87], v[120:123], a[212:215]\n"
  "v_pk_add_f16 v36, v4, v20\n"
  "v_pk_add_f16 v37, v5, v21\n"
  "v_mfma_f32_16x16x32_f16 a[224:227], v[88:91], v[120:123], a[224:227]\n"
  "v_pk_add_f16 v38, v6, v22\n"
  "v_pk_add_f16 v39, v7, v23\n"
  "s_barrier\n"
  "v_mfma_f32_16x16x32_f16 a[236:239], v[92:95], v[120:123], a[236:239]\n"
  "v_pk_add_f16 v40, v8, v24\n"
  "v_pk_add_f16 v41, v9, v25\n"
  "v_pk_add_f16 v42, v10, v26\n"
  "v_pk_add_f16 v43, v11, v27\n"
  "v_pk_add_f16 v44, v12, v28\n"
  "v_pk_add_f16 v45, v13, v29\n"
  "v_pk_add_f16 v46, v14, v30\n"
  "v_pk_add_f16 v47, v15, v31\n"
  "v_add_u32 v230, s49, v230\n"
  "v_add_u32 v231, s49, v231\n"
  "v_add_u32 v232, s49, v232\n"
  "s_mov_b32 s63, 2\n"
  ".p2align 6\n"
  ".LloopN_%=:\n"
  "v_mfma_f32_16x16x32_f16 v[124:127], v[0:3], v[96:99], v[124:127]\n"
  "buffer_load_dword v247, v246, s[40:43], s54 offen sc1\n"
  "v_pk_add_f16 v120, v96, v100\n"
  "v_mfma_f32_16x16x32_f16 v[136:139], v[4:7], v[96:99], v[136:139]\n"
  "v_pk_add_f16 v121, v97, v101\n"
  "v_pk_add_f16 v122, v98, v102\n"
  "v_mfma_f32_16x16x32_f16 v[148:151], v[8:11], v[96:99], v[148:151]\n"
  "ds_read_b64_tr_b16 v[48:49], v228 offset:0\n"
  "v_pk_add_f16 v123, v99, v103\n"
  "v_mfma_f32_16x16x32_f16 v[160:163], v[12:15], v[96:99], v[160:163]\n"
  "s_add_i32 m0, s47, 0\n"
  "ds_read_b64_tr_b16 v[50:51], v228 offset:8192\n"
  "ds_read_b128 v[96:99], v232 offset:4096\n"
  "v_mfma_f32_16x16x32_f16 v[128:131], v[16:19], v[100:103], v[128:131]\n"
  "s_add_i32 s64, s50, 4194304\n"
  "buffer_load_dwordx4 v237, s[40:43], s64 offen sc1 lds\n"
  "ds_read_b64_tr_b16 v[64:65], v228 offset:256\n"
  "v_mfma_f32_16x16x32_f16 v[140:143], v[20:23], v[100:103], v[140:143]\n"
  "s_cmp_eq_u32 s46, 73728\n"
  "s_cselect_b32 s49, s68, s69\n"
  "ds_read_b64_tr_b16 v[66:67], v228 offset:8448\n"
  "v_mfma_f32_16x16x32_f16 v[152:155], v[24:27], v[100:103], v[152:155]\n"
  "ds_read_b64_tr_b16 v[52:53], v229 offset:0\n"
  "v_add_u32 v228, s49, v228\n"
  "v_mfma_f32_16x16x32_f16 v[164:167], v[28:31], v[100:103], v[164:167]\n"
  "s_add_i32 m0, s47, 4096\n"
  "ds_read_b128 v[100:103], v232 offset:4608\n"
  "ds_read_b64_tr_b16 v[54:55], v229 offset:8192\n"
  "v_mfma_f32_16x16x32_f16 v[132:135], v[32:35], v[120:123], v[132:135]\n"
  "s_add_i32 s64, s50, 4198400\n"
  "buffer_load_dwordx4 v237, s[40:43], s64 offen sc1 lds\n"
  "ds_read_b64_tr_b16 v[68:69], v229 offset:256\n"
  "v_mfma_f32_16x16x32_f16 v[144:147], v[36:39], v[120:123], v[144:147]\n"
  "ds_read_b64_tr_b16 v[70:71], v229 offset:8448\n"
  "v_add_u32 v229, s49, v229\n"
  "v_mfma_f32_16x16x32_f16 v[156:159], v[40:43], v[120:123], v[156:159]\n"
  "ds_read_b64_tr_b16 v[56:57], v230 offset:0\n"
  "ds_read_b64_tr_b16 v[58:59], v230 offset:8192\n"
  "v_mfma_f32_16x16x32_f16 v[168:171], v[44:47], v[120:123], v[168:171]\n"
  "s_add_i32 m0, s47, 8192\n"
  "ds_read_b64_tr_b16 v[72:73], v230 offset:256\n"
  "ds_read_b64_tr_b16 v[74:75], v230 offset:8448\n"
  "v_mfma_f32_16x16x32_f16 v[180:183], v[0:3], v[104:107], v[180:183]\n"
  "s_add_i32 s64, s50, 4202496\n"
  "buffer_load_dwordx4 v237, s[40:43], s64 offen sc1 lds\n"
  "v_pk_add_f16 v120, v104, v108\n"
  "v_mfma_f32_16x16x32_f16 v[192:195], v[4:7], v[104:107], v[192:195]\n"
  "s_waitcnt lgkmcnt(9)\n"
  "v_pk_add_f16 v121, v105, v109\n"
  "v_pk_add_f16 v122, v106, v110\n"
  "v_mfma_f32_16x16x32_f16 v[204:207], v[8:11], v[104:107], v[204:207]\n"
  "ds_read_b64_tr_b16 v[60:61], v231 offset:0\n"
  "v_pk_add_f16 v123, v107, v111\n"
  "v_mfma_f32_16x16x32_f16 v[216:219], v[12:15], v[104:107], v[216:219]\n"
  "s_add_i32 m0, s47, 12288\n"
  "ds_read_b128 v[104:107], v232 offset:6144\n"
  "v_pk_add_f16 v80, v48, v64\n"
  "v_mfma_f32_16x16x32_f16 v[184:187], v[16:19], v[108:111], v[184:187]\n"
  "s_waitcnt lgkmcnt(6)\n"
  "s_add_i32 s64, s50, 4206592\n"
  "buffer_load_dwordx4 v237, s[40:43], s64 offen sc1 lds\n"
  "v_mfma_f32_16x16x32_f16 v[196:199], v[20:23], v[108:111], v[196:199]\n"
  "ds_read_b64_tr_b16 v[62:63], v231 offset:8192\n"
  "v_pk_add_f16 v81, v49, v65\n"
  "v_mfma_f32_16x16x32_f16 v[208:211], v[24:27], v[108:111], v[208:211]\n"
  "s_waitcnt lgkmcnt(3)\n"
  "ds_read_b128 v[112:115], v232 offset:8192\n"
  "v_pk_add_f16 v82, v50, v66\n"
  "v_mfma_f32_16x16x32_f16 v[220:223], v[28:31], v[108:111], v[220:223]\n"
  "s_add_i32 m0, s47, 16384\n"
  "ds_read_b128 v[108:111], v232 offset:6656\n"
  "v_pk_add_f16 v83, v51, v67\n"
  "v_mfma_f32_16x16x32_f16 v[188:191], v[32:35], v[120:123], v[188:191]\n"
  "s_add_i32 s64, s50, 4194304\n"
  "buffer_load_dwordx4 v238, s[40:43], s64 offen sc1 lds\n"
  "v_pk_add_f16 v84, v52, v68\n"
  "v_mfma_f32_16x16x32_f16 v[200:203], v[36:39], v[120:123], v[200:203]\n"
  "ds_read_b64_tr_b16 v[76:77], v231 offset:256\n"
  "v_pk_add_f16 v85, v53, v69\n"
  "v_mfma_f32_16x16x32_f16 v[212:215], v[40:43], v[120:123], v[212:215]\n"
  "ds_read_b128 v[116:119], v232 offset:8704\n"
  "v_pk_add_f16 v86, v54, v70\n"
  "v_mfma_f32_16x16x32_f16 v[224:227], v[44:47], v[120:123], v[224:227]\n"
  "s_add_i32 m0, s47, 20480\n"
  "ds_read_b64_tr_b16 v[78:79], v231 offset:8448\n"
  "v_pk_add_f16 v87, v55, v71\n"
  "v_mfma_f32_16x16x32_f16 a[96:99], v[0:3], v[96:99], a[96:99]\n"
  "s_add_i32 s64, s50, 4096\n"
  "buffer_load_dwordx4 v238, s[40:43], s64 offen sc1 lds\n"
  "v_pk_add_f16 v120, v96, v100\n"
  "v_mfma_f32_16x16x32_f16 a[108:111], v[4:7], v[96:99], a[108:111]\n"
  "v_pk_add_f16 v121, v97, v101\n"
  "v_pk_add_f16 v122, v98, v102\n"
  "v_mfma_f32_16x16x32_f16 a[120:123], v[8:11], v[96:99], a[120:123]\n"
  "v_pk_add_f16 v123, v99, v103\n"
  "v_pk_add_f16 v88, v56, v72\n"
  "v_mfma_f32_16x16x32_f16 a[132:135], v[12:15], v[96:99], a[132:135]\n"
  "s_add_i32 m0, s48, 24576\n"
  "ds_read_b128 v[96:99], v232 offset:0\n"
  "v_pk_add_f16 v89, v57, v73\n"
  "v_mfma_f32_16x16x32_f16 a[100:103], v[16:19], v[100:103], a[100:103]\n"
  "s_add_i32 s64, s50, 16384\n"
  "buffer_load_dwordx4 v238, s[40:43], s64 offen sc1 lds\n"
  "v_pk_add_f16 v90, v58, v74\n"
  "v_mfma_f32_16x16x32_f16 a[112:115], v[20:23], v[100:103], a[112:115]\n"
  "v_pk_add_f16 v91, v59, v75\n"
  "v_add_u32 v230, s49, v230\n"
  "v_mfma_f32_16x16x32_f16 a[124:127], v[24:27], v[100:103], a[124:127]\n"
  "s_waitcnt lgkmcnt(1)\n"
  "v_pk_add_f16 v92, v60, v76\n"
  "v_pk_add_f16 v93, v61, v77\n"
  "v_mfma_f32_16x16x32_f16 a[136:139], v[28:31], v[100:103], a[136:139]\n"
  "s_add_i32 m0, s47, 26624\n"
  "ds_read_b128 v[100:103], v232 offset:512\n"
  "v_pk_add_f16 v94, v62, v78\n"
  "v_mfma_f32_16x16x32_f16 a[104:107], v[32:35], v[120:123], a[104:107]\n"
  "s_add_i32 s64, s50, 4214784\n"
  "buffer_load_dwordx4 v238, s[40:43], s64 offen sc1 lds\n"
  "v_pk_add_f16 v95, v63, v79\n"
  "v_mfma_f32_16x16x32_f16 a[116:119], v[36:39], v[120:123], a[116:119]\n"
  "v_add_u32 v231, s49, v231\n"
  "v_mfma_f32_16x16x32_f16 a[128:131], v[40:43], v[120:123], a[128:131]\n"
  "v_mfma_f32_16x16x32_f16 a[140:143], v[44:47], v[120:123], a[140:143]\n"
  "s_add_i32 m0, s47, 30720\n"
  "v_mfma_f32_16x16x32_f16 a[144:147], v[0:3], v[104:107], a[144:147]\n"
  "s_add_i32 s64, s50, 32768\n"
  "buffer_load_dwordx4 v238, s[40:43], s64 offen sc1 lds\n"
  "v_pk_add_f16 v120, v104, v108\n"
  "v_mfma_f32_16x16x32_f16 a[156:159], v[4:7], v[104:107], a[156:159]\n"
  "v_pk_add_f16 v121, v105, v109\n"
  "v_pk_add_f16 v122, v106, v110\n"
  "v_mfma_f32_16x16x32_f16 a[168:171], v[8:11], v[104:107], a[168:171]\n"
  "v_pk_add_f16 v123, v107, v111\n"
  "v_mfma_f32_16x16x32_f16 a[180:183], v[12:15], v[104:107], a[180:183]\n"
  "ds_read_b128 v[104:107], v232 offset:2048\n"
  "v_mfma_f32_16x16x32_f16 a[148:151], v[16:19], v[108:111], a[148:151]\n"
  "s_add_i32 s47, s44, s46\n"
  "s_add_i32 s50, s50, 4194304\n"
  "v_mfma_f32_16x16x32_f16 a[160:163], v[20:23], v[108:111], a[160:163]\n"
  "s_add_i32 s48, s47, s45\n"
  "s_add_i32 s46, s46, s49\n"
  "v_mfma_f32_16x16x32_f16 a[172:175], v[24:27], v[108:111], a[172:175]\n"
  "v_mfma_f32_16x16x32_f16 a[184:187], v[28:31], v[108:111], a[184:187]\n"
  "ds_read_b128 v[108:111], v232 offset:2560\n"
  "v_add_u32 v232, s49, v232\n"
  "v_mfma_f32_16x16x32_f16 a[152:155], v[32:35], v[120:123], a[152:155]\n"
  "v_mfma_f32_16x16x32_f16 a[164:167], v[36:39], v[120:123], a[164:167]\n"
  "v_mfma_f32_16x16x32_f16 a[176:179], v[40:43], v[120:123], a[176:179]\n"
  "v_mfma_f32_16x16x32_f16 a[188:191], v[44:47], v[120:123], a[188:191]\n"
  "v_mfma_f32_16x16x32_f16 a[192:195], v[0:3], v[112:115], a[192:195]\n"
  "v_pk_add_f16 v120, v112, v116\n"
  "v_pk_add_f16 v121, v113, v117\n"
  "v_mfma_f32_16x16x32_f16 a[204:207], v[4:7], v[112:115], a[204:207]\n"
  "v_pk_add_f16 v122, v114, v118\n"
  "v_pk_add_f16 v123, v115, v119\n"
  "v_mfma_f32_16x16x32_f16 a[216:219], v[8:11], v[112:115], a[216:219]\n"
  "v_mfma_f32_16x16x32_f16 a[228:231], v[12:15], v[112:115], a[228:231]\n"
  "v_mfma_f32_16x16x32_f16 a[196:199], v[16:19], v[116:119], a[196:199]\n"
  "v_mfma_f32_16x16x32_f16 a[208:211], v[20:23], v[116:119], a[208:211]\n"
  "v_mfma_f32_16x16x32_f16 a[220:223], v[24:27], v[116:119], a[220:223]\n"
  "v_mfma_f32_16x16x32_f16 a[232:235], v[28:31], v[116:119], a[232:235]\n"
  "v_mfma_f32_16x16x32_f16 a[200:203], v[32:35], v[120:123], a[200:203]\n"
  "s_waitcnt vmcnt(9) lgkmcnt(0)\n"
  "v_mfma_f32_16x16x32_f16 a[212:215], v[36:39], v[120:123], a[212:215]\n"
  "v_cmp_eq_u32 vcc, 0, v247\n"
  "s_cbranch_vccnz .Lspin_ne_None_%=\n"
  ".Lspun_ne_None_%=:\n"
  "v_mfma_f32_16x16x32_f16 a[224:227], v[40:43], v[120:123], a[224:227]\n"
  "s_barrier\n"
  "v_mfma_f32_16x16x32_f16 a[236:239], v[44:47], v[120:123], a[236:239]\n"
  "v_mfma_f32_16x16x32_f16 v[124:127], v[48:51], v[96:99], v[124:127]\n"
  "v_pk_add_f16 v120, v96, v100\n"
  "v_pk_add_f16 v121, v97, v101\n"
  "v_mfma_f32_16x16x32_f16 v[136:139], v[52:55], v[96:99], v[136:139]\n"
  "v_pk_add_f16 v122, v98, v102\n"
  "v_pk_add_f16 v123, v99, v103\n"
  "v_mfma_f32_16x16x32_f16 v[148:151], v[56:59], v[96:99], v[148:151]\n"
  "s_and_b32 s65, s51, 0xffffe300\n"
  "ds_read_b64_tr_b16 v[0:1], v228 offset:0\n"
  "ds_read_b64_tr_b16 v[2:3], v228 offset:8192\n"
  "v_mfma_f32_16x16x32_f16 v[160:163], v[60:63], v[96:99], v[160:163]\n"
  "s_add_i32 m0, s47, 0\n"
  "ds_read_b128 v[96:99], v232 offset:4096\n"
  "ds_read_b64_tr_b16 v[16:17], v228 offset:256\n"
  "v_mfma_f32_16x16x32_f16 v[128:131], v[64:67], v[100:103], v[128:131]\n"
  "s_add_i32 s64, s50, 4194304\n"
  "buffer_load_dwordx4 v237, s[40:43], s64 offen sc1 lds\n"
  "ds_read_b64_tr_b16 v[18:19], v228 offset:8448\n"
  "v_mfma_f32_16x16x32_f16 v[140:143], v[68:71], v[100:103], v[140:143]\n"
  "s_cmp_eq_u32 s46, 73728\n"
  "s_cselect_b32 s49, s68, s69\n"
  "ds_read_b64_tr_b16 v[4:5], v229 offset:0\n"
  "v_mfma_f32_16x16x32_f16 v[152:155], v[72:75], v[100:103], v[152:155]\n"
  "ds_read_b64_tr_b16 v[6:7], v229 offset:8192\n"
  "v_add_u32 v228, s49, v228\n"
  "v_mfma_f32_16x16x32_f16 v[164:167], v[76:79], v[100:103], v[164:167]\n"
  "ds_read_b128 v[100:103], v232 offset:4608\n"
  "ds_read_b64_tr_b16 v[20:21], v229 offset:256\n"
  "v_mfma_f32_16x16x32_f16 v[132:135], v[80:83], v[120:123], v[132:135]\n"
  "buffer_store_dword v232, v245, s[40:43], s65 offen sc1\n"
  "ds_read_b64_tr_b16 v[22:23], v229 offset:8448\n"
  "v_mfma_f32_16x16x32_f16 v[144:147], v[84:87], v[120:123], v[144:147]\n"
  "ds_read_b64_tr_b16 v[8:9], v230 offset:0\n"
  "v_add_u32 v229, s49, v229\n"
  "v_mfma_f32_16x16x32_f16 v[156:159], v[88:91], v[120:123], v[156:159]\n"
  "s_add_i32 s53, s53, 8192\n"
  "s_lshr_b32 s52, s52, 1\n"
  "ds_read_b64_tr_b16 v[10:11], v230 offset:8192\n"
  "v_mfma_f32_16x16x32_f16 v[168:171], v[92:95], v[120:123], v[168:171]\n"
  "s_add_i32 m0, s47, 4096\n"
  "s_waitcnt lgkmcnt(7)\n"
  "ds_read_b64_tr_b16 v[24:25], v230 offset:256\n"
  "v_mfma_f32_16x16x32_f16 v[180:183], v[48:51], v[104:107], v[180:183]\n"
  "s_add_i32 s64, s50, 4198400\n"
  "buffer_load_dwordx4 v237, s[40:43], s64 offen sc1 lds\n"
  "v_pk_add_f16 v120, v104, v108\n"
  "v_mfma_f32_16x16x32_f16 v[192:195], v[52:55], v[104:107], v[192:195]\n"
  "s_add_i32 s51, s51, 8448\n"
  "v_pk_add_f16 v121, v105, v109\n"
  "v_pk_add_f16 v122, v106, v110\n"
  "v_mfma_f32_16x16x32_f16 v[204:207], v[56:59], v[104:107], v[204:207]\n"
  "ds_read_b64_tr_b16 v[26:27], v230 offset:8448\n"
  "v_pk_add_f16 v123, v107, v111\n"
  "v_mfma_f32_16x16x32_f16 v[216:219], v[60:63], v[104:107], v[216:219]\n"
  "s_add_i32 m0, s47, 8192\n"
  "s_waitcnt lgkmcnt(4)\n"
  "ds_read_b128 v[104:107], v232 offset:6144\n"
  "v_mfma_f32_16x16x32_f16 v[184:187], v[64:67], v[108:111], v[184:187]\n"
  "s_add_i32 s64, s50, 4202496\n"
  "buffer_load_dwordx4 v237, s[40:43], s64 offen sc1 lds\n"
  "v_pk_add_f16 v32, v0, v16\n"
  "v_mfma_f32_16x16x32_f16 v[196:199], v[68:71], v[108:111], v[196:199]\n"
  "s_bitcmp1_b32 s52, 0\n"
  "s_cselect_b32 s54, s53, 134217728\n"
  "ds_read_b64_tr_b16 v[12:13], v231 offset:0\n"
  "v_mfma_f32_16x16x32_f16 v[208:211], v[72:75], v[108:111], v[208:211]\n"
  "ds_read_b64_tr_b16 v[14:15], v231 offset:8192\n"
  "v_pk_add_f16 v33, v1, v17\n"
  "v_mfma_f32_16x16x32_f16 v[220:223], v[76:79], v[108:111], v[220:223]\n"
  "s_add_i32 m0, s47, 12288\n"
  "ds_read_b128 v[108:111], v232 offset:6656\n"
  "v_pk_add_f16 v34, v2, v18\n"
  "v_mfma_f32_16x16x32_f16 v[188:191], v[80:83], v[120:123], v[188:191]\n"
  "s_add_i32 s64, s50, 4206592\n"
  "buffer_load_dwordx4 v237, s[40:43], s64 offen sc1 lds\n"
  "v_pk_add_f16 v35, v3, v19\n"
  "v_mfma_f32_16x16x32_f16 v[200:203], v[84:87], v[120:123], v[200:203]\n"
  "s_waitcnt lgkmcnt(4)\n"
  "ds_read_b128 v[112:115], v232 offset:8192\n"
  "v_mfma_f32_16x16x32_f16 v[212:215], v[88:91], v[120:123], v[212:215]\n"
  "ds_read_b64_tr_b16 v[28:29], v231 offset:256\n"
  "v_pk_add_f16 v36, v4, v20\n"
  "v_mfma_f32_16x16x32_f16 v[224:227], v[92:95], v[120:123], v[224:227]\n"
  "s_add_i32 m0, s47, 16384\n"
  "ds_read_b128 v[116:119], v232 offset:8704\n"
  "v_pk_add_f16 v37, v5, v21\n"
  "v_mfma_f32_16x16x32_f16 a[96:99], v[48:51], v[96:99], a[96:99]\n"
  "s_add_i32 s64, s50, 4194304\n"
  "buffer_load_dwordx4 v238, s[40:43], s64 offen sc1 lds\n"
  "v_pk_add_f16 v120, v96, v100\n"
  "v_mfma_f32_16x16x32_f16 a[108:111], v[52:55], v[96:99], a[108:111]\n"
  "v_pk_add_f16 v121, v97, v101\n"
  "v_pk_add_f16 v122, v98, v102\n"
  "v_mfma_f32_16x16x32_f16 a[120:123], v[56:59], v[96:99], a[120:123]\n"
  "ds_read_b64_tr_b16 v[30:31], v231 offset:8448\n"
  "v_pk_add_f16 v123, v99, v103\n"
  "v_mfma_f32_16x16x32_f16 a[132:135], v[60:63], v[96:99], a[132:135]\n"
  "s_add_i32 m0, s47, 20480\n"
  "ds_read_b128 v[96:99], v232 offset:0\n"
  "v_pk_add_f16 v38, v6, v22\n"
  "v_mfma_f32_16x16x32_f16 a[100:103], v[64:67], v[100:103], a[100:103]\n"
  "s_add_i32 s64, s50, 4096\n"
  "buffer_load_dwordx4 v238, s[40:43], s64 offen sc1 lds\n"
  "v_pk_add_f16 v39, v7, v23\n"
  "v_mfma_f32_16x16x32_f16 a[112:115], v[68:71], v[100:103], a[112:115]\n"
  "v_pk_add_f16 v40, v8, v24\n"
  "v_pk_add_f16 v41, v9, v25\n"
  "v_mfma_f32_16x16x32_f16 a[124:127], v[72:75], v[100:103], a[124:127]\n"
  "v_pk_add_f16 v42, v10, v26\n"
  "v_pk_add_f16 v43, v11, v27\n"
  "v_mfma_f32_16x16x32_f16 a[136:139], v[76:79], v[100:103], a[136:139]\n"
  "s_add_i32 m0, s48, 24576\n"
  "ds_read_b128 v[100:103], v232 offset:512\n"
  "v_add_u32 v230, s49, v230\n"
  "v_mfma_f32_16x16x32_f16 a[104:107], v[80:83], v[120:123], a[104:107]\n"
  "s_add_i32 s64, s50, 16384\n"
  "buffer_load_dwordx4 v238, s[40:43], s64 offen sc1 lds\n"
  "v_add_u32 v231, s49, v231\n"
  "v_mfma_f32_16x16x32_f16 a[116:119], v[84:87], v[120:123], a[116:119]\n"
  "s_waitcnt lgkmcnt(6)\n"
  "s_waitcnt lgkmcnt(2)\n"
  "v_pk_add_f16 v44, v12, v28\n"
  "v_mfma_f32_16x16x32_f16 a[128:131], v[88:91], v[120:123], a[128:131]\n"
  "v_pk_add_f16 v45, v13, v29\n"
  "v_pk_add_f16 v46, v14, v30\n"
  "v_mfma_f32_16x16x32_f16 a[140:143], v[92:95], v[120:123], a[140:143]\n"
  "s_add_i32 m0, s47, 26624\n"
  "v_pk_add_f16 v47, v15, v31\n"
  "v_mfma_f32_16x16x32_f16 a[144:147], v[48:51], v[104:107], a[144:147]\n"
  "s_add_i32 s64, s50, 4214784\n"
  "buffer_load_dwordx4 v238, s[40:43], s64 offen sc1 lds\n"
  "v_pk_add_f16 v120, v104, v108\n"
  "v_mfma_f32_16x16x32_f16 a[156:159], v[52:55], v[104:107], a[156:159]\n"
  "v_pk_add_f16 v121, v105, v109\n"
  "v_pk_add_f16 v122, v106, v110\n"
  "v_mfma_f32_16x16x32_f16 a[168:171], v[56:59], v[104:107], a[168:171]\n"
  "v_pk_add_f16 v123, v107, v111\n"
  "v_mfma_f32_16x16x32_f16 a[180:183], v[60:63], v[104:107], a[180:183]\n"
  "s_add_i32 m0, s47, 30720\n"
  "ds_read_b128 v[104:107], v232 offset:2048\n"
  "v_mfma_f32_16x16x32_f16 a[148:151], v[64:67], v[108:111], a[148:151]\n"
  "s_add_i32 s64, s50, 32768\n"
  "buffer_load_dwordx4 v238, s[40:43], s64 offen sc1 lds\n"
  "v_mfma_f32_16x16x32_f16 a[160:163], v[68:71], v[108:111], a[160:163]\n"
  "s_add_i32 s47, s44, s46\n"
  "s_add_i32 s50, s50, 4194304\n"
  "v_mfma_f32_16x16x32_f16 a[172:175], v[72:75], v[108:111], a[172:175]\n"
  "s_add_i32 s48, s47, s45\n"
  "s_add_i32 s46, s46, s49\n"
  "v_mfma_f32_16x16x32_f16 a[184:187], v[76:79], v[108:111], a[184:187]\n"
  "ds_read_b128 v[108:111], v232 offset:2560\n"
  "v_add_u32 v232, s49, v232\n"
  "v_mfma_f32_16x16x32_f16 a[152:155], v[80:83], v[120:123], a[152:155]\n"
  "v_mfma_f32_16x16x32_f16 a[164:167], v[84:87], v[120:123], a[164:167]\n"
  "v_mfma_f32_16x16x32_f16 a[176:179], v[88:91], v[120:123], a[176:179]\n"
  "v_mfma_f32_16x16x32_f16 a[188:191], v[92:95], v[120:123], a[188:191]\n"
  "v_mfma_f32_16x16x32_f16 a[192:195], v[48:51], v[112:115], a[192:195]\n"
  "v_pk_add_f16 v120, v112, v116\n"
  "v_pk_add_f16 v121, v113, v117\n"
  "v_mfma_f32_16x16x32_f16 a[204:207], v[52:55], v[112:115], a[204:207]\n"
  "v_pk_add_f16 v122, v114, v118\n"
  "v_pk_add_f16 v123, v115, v119\n"
  "v_mfma_f32_16x16x32_f16 a[216:219], v[56:59], v[112:115], a[216:219]\n"
  "v_mfma_f32_16x16x32_f16 a[228:231], v[60:63], v[112:115], a[228:231]\n"
  "v_mfma_f32_16x16x32_f16 a[196:199], v[64:67], v[116:119], a[196:199]\n"
  "v_mfma_f32_16x16x32_f16 a[208:211], v[68:71], v[116:119], a[208:211]\n"
  "v_mfma_f32_16x16x32_f16 a[220:223], v[72:75], v[116:119], a[220:223]\n"
  "v_mfma_f32_16x16x32_f16 a[232:235], v[76:79], v[116:119], a[232:235]\n"
  "v_mfma_f32_16x16x32_f16 a[200:203], v[80:83], v[120:123], a[200:203]\n"
  "s_waitcnt vmcnt(10) lgkmcnt(0)\n"
  "v_mfma_f32_16x16x32_f16 a[212:215], v[84:87], v[120:123], a[212:215]\n"
  "v_mfma_f32_16x16x32_f16 a[224:227], v[88:91], v[120:123], a[224:227]\n"
  "s_barrier\n"
  "v_mfma_f32_16x16x32_f16 a[236:239], v[92:95], v[120:123], a[236:239]\n"
  "s_sub_i32 s63, s63, 1\n"
  "s_cmp_lg_u32 s63, 0\n"
  "s_cbranch_scc1 .LloopN_%=\n"
  "v_mfma_f32_16x16x32_f16 v[124:127], v[0:3], v[96:99], v[124:127]\n"
  "s_add_i32 s67, s44, s45\n"
  "buffer_load_dword v247, v246, s[40:43], s54 offen sc1\n"
  "v_pk_add_f16 v120, v96, v100\n"
  "v_mfma_f32_16x16x32_f16 v[136:139], v[4:7], v[96:99], v[136:139]\n"
  "v_pk_add_f16 v121, v97, v101\n"
  "v_pk_add_f16 v122, v98, v102\n"
  "v_mfma_f32_16x16x32_f16 v[148:151], v[8:11], v[96:99], v[148:151]\n"
  "ds_read_b64_tr_b16 v[48:49], v228 offset:0\n"
  "v_pk_add_f16 v123, v99, v103\n"
  "v_mfma_f32_16x16x32_f16 v[160:163], v[12:15], v[96:99], v[160:163]\n"
  "s_add_i32 m0, s47, 0\n"
  "ds_read_b64_tr_b16 v[50:51], v228 offset:8192\n"
  "ds_read_b128 v[96:99], v232 offset:4096\n"
  "v_mfma_f32_16x16x32_f16 v[128:131], v[16:19], v[100:103], v[128:131]\n"
  "s_add_i32 s64, s50, 4194304\n"
  "buffer_load_dwordx4 v237, s[40:43], s64 offen sc1 lds\n"
  "ds_read_b64_tr_b16 v[64:65], v228 offset:256\n"
  "v_mfma_f32_16x16x32_f16 v[140:143], v[20:23], v[100:103], v[140:143]\n"
  "s_cmp_eq_u32 s46, 73728\n"
  "s_cselect_b32 s49, s68, s69\n"
  "ds_read_b64_tr_b16 v[66:67], v228 offset:8448\n"
  "v_mfma_f32_16x16x32_f16 v[152:155], v[24:27], v[100:103], v[152:155]\n"
  "ds_read_b64_tr_b16 v[52:53], v229 offset:0\n"
  "v_add_u32 v228, s49, v228\n"
  "v_mfma_f32_16x16x32_f16 v[164:167], v[28:31], v[100:103], v[164:167]\n"
  "s_add_i32 m0, s47, 4096\n"
  "ds_read_b128 v[100:103], v232 offset:4608\n"
  "ds_read_b64_tr_b16 v[54:55], v229 offset:8192\n"
  "v_mfma_f32_16x16x32_f16 v[132:135], v[32:35], v[120:123], v[132:135]\n"
  "s_add_i32 s64, s50, 4198400\n"
  "buffer_load_dwordx4 v237, s[40:43], s64 offen sc1 lds\n"
  "ds_read_b64_tr_b16 v[68:69], v229 offset:256\n"
  "v_mfma_f32_16x16x32_f16 v[144:147], v[36:39], v[120:123], v[144:147]\n"
  "ds_read_b64_tr_b16 v[70:71], v229 offset:8448\n"
  "v_add_u32 v229, s49, v229\n"
  "v_mfma_f32_16x16x32_f16 v[156:159], v[40:43], v[120:123], v[156:159]\n"
  "ds_read_b64_tr_b16 v[56:57], v230 offset:0\n"
  "ds_read_b64_tr_b16 v[58:59], v230 offset:8192\n"
  "v_mfma_f32_16x16x32_f16 v[168:171], v[44:47], v[120:123], v[168:171]\n"
  "s_add_i32 m0, s47, 8192\n"
  "ds_read_b64_tr_b16 v[72:73], v230 offset:256\n"
  "ds_read_b64_tr_b16 v[74:75], v230 offset:8448\n"
  "v_mfma_f32_16x16x32_f16 v[180:183], v[0:3], v[104:107], v[180:183]\n"
  "s_add_i32 s64, s50, 4202496\n"
  "buffer_load_dwordx4 v237, s[40:43], s64 offen sc1 lds\n"
  "v_pk_add_f16 v120, v104, v108\n"
  "v_mfma_f32_16x16x32_f16 v[192:195], v[4:7], v[104:107], v[192:195]\n"
  "s_waitcnt lgkmcnt(9)\n"
  "v_pk_add_f16 v121, v105, v109\n"
  "v_pk_add_f16 v122, v106, v110\n"
  "v_mfma_f32_16x16x32_f16 v[204:207], v[8:11], v[104:107], v[204:207]\n"
  "ds_read_b64_tr_b16 v[60:61], v231 offset:0\n"
  "v_pk_add_f16 v123, v107, v111\n"
  "v_mfma_f32_16x16x32_f16 v[216:219], v[12:15], v[104:107], v[216:219]\n"
  "s_add_i32 m0, s47, 12288\n"
  "ds_read_b128 v[104:107], v232 offset:6144\n"
  "v_pk_add_f16 v80, v48, v64\n"
  "v_mfma_f32_16x16x32_f16 v[184:187], v[16:19], v[108:111], v[184:187]\n"
  "s_waitcnt lgkmcnt(6)\n"
  "s_add_i32 s64, s50, 4206592\n"
  "buffer_load_dwordx4 v237, s[40:43], s64 offen sc1 lds\n"
  "v_mfma_f32_16x16x32_f16 v[196:199], v[20:23], v[108:111], v[196:199]\n"
  "ds_read_b64_tr_b16 v[62:63], v231 offset:8192\n"
  "v_pk_add_f16 v81, v49, v65\n"
  "v_mfma_f32_16x16x32_f16 v[208:211], v[24:27], v[108:111], v[208:211]\n"
  "s_waitcnt lgkmcnt(3)\n"
  "ds_read_b128 v[112:115], v232 offset:8192\n"
  "v_pk_add_f16 v82, v50, v66\n"
  "v_mfma_f32_16x16x32_f16 v[220:223], v[28:31], v[108:111], v[220:223]\n"
  "s_add_i32 m0, s47, 16384\n"
  "ds_read_b128 v[108:111], v232 offset:6656\n"
  "v_pk_add_f16 v83, v51, v67\n"
  "v_mfma_f32_16x16x32_f16 v[188:191], v[32:35], v[120:123], v[188:191]\n"
  "s_add_i32 s64, s50, 4194304\n"
  "buffer_load_dwordx4 v238, s[40:43], s64 offen sc1 lds\n"
  "v_pk_add_f16 v84, v52, v68\n"
  "v_mfma_f32_16x16x32_f16 v[200:203], v[36:39], v[120:123], v[200:203]\n"
  "ds_read_b64_tr_b16 v[76:77], v231 offset:256\n"
  "v_pk_add_f16 v85, v53, v69\n"
  "v_mfma_f32_16x16x32_f16 v[212:215], v[40:43], v[120:123], v[212:215]\n"
  "ds_read_b128 v[116:119], v232 offset:8704\n"
  "v_pk_add_f16 v86, v54, v70\n"
  "v_mfma_f32_16x16x32_f16 v[224:227], v[44:47], v[120:123], v[224:227]\n"
  "s_add_i32 m0, s47, 20480\n"
  "ds_read_b64_tr_b16 v[78:79], v231 offset:8448\n"
  "v_pk_add_f16 v87, v55, v71\n"
  "v_mfma_f32_16x16x32_f16 a[96:99], v[0:3], v[96:99], a[96:99]\n"
  "s_add_i32 s64, s50, 4096\n"
  "buffer_load_dwordx4 v238, s[40:43], s64 offen sc1 lds\n"
  "v_pk_add_f16 v120, v96, v100\n"
  "v_mfma_f32_16x16x32_f16 a[108:111], v[4:7], v[96:99], a[108:111]\n"
  "v_pk_add_f16 v121, v97, v101\n"
  "v_pk_add_f16 v122, v98, v102\n"
  "v_mfma_f32_16x16x32_f16 a[120:123], v[8:11], v[96:99], a[120:123]\n"
  "ds_write_b128 v253, v[172:175]\n"
  "v_pk_add_f16 v123, v99, v103\n"
  "v_mfma_f32_16x16x32_f16 a[132:135], v[12:15], v[96:99], a[132:135]\n"
  "s_add_i32 m0, s48, 24576\n"
  "ds_read_b128 v[96:99], v232 offset:0\n"
  "v_pk_add_f16 v88, v56, v72\n"
  "v_mfma_f32_16x16x32_f16 a[100:103], v[16:19], v[100:103], a[100:103]\n"
  "s_add_i32 s64, s50, 16384\n"
  "buffer_load_dwordx4 v238, s[40:43], s64 offen sc1 lds\n"
  "v_pk_add_f16 v89, v57, v73\n"
  "v_mfma_f32_16x16x32_f16 a[112:115], v[20:23], v[100:103], a[112:115]\n"
  "v_pk_add_f16 v90, v58, v74\n"
  "v_pk_add_f16 v91, v59, v75\n"
  "v_mfma_f32_16x16x32_f16 a[124:127], v[24:27], v[100:103], a[124:127]\n"
  "s_waitcnt lgkmcnt(2)\n"
  "v_pk_add_f16 v92, v60, v76\n"
  "v_pk_add_f16 v93, v61, v77\n"
  "v_mfma_f32_16x16x32_f16 a[136:139], v[28:31], v[100:103], a[136:139]\n"
  "s_add_i32 m0, s47, 26624\n"
  "ds_read_b128 v[100:103], v232 offset:512\n"
  "v_pk_add_f16 v94, v62, v78\n"
  "v_mfma_f32_16x16x32_f16 a[104:107], v[32:35], v[120:123], a[104:107]\n"
  "s_add_i32 s64, s50, 4214784\n"
  "buffer_load_dwordx4 v238, s[40:43], s64 offen sc1 lds\n"
  "v_pk_add_f16 v95, v63, v79\n"
  "v_mfma_f32_16x16x32_f16 a[116:119], v[36:39], v[120:123], a[116:119]\n"
  "v_add_u32 v230, s49, v230\n"
  "v_add_u32 v231, s49, v231\n"
  "v_mfma_f32_16x16x32_f16 a[128:131], v[40:43], v[120:123], a[128:131]\n"
  "v_mfma_f32_16x16x32_f16 a[140:143], v[44:47], v[120:123], a[140:143]\n"
  "s_add_i32 m0, s47, 30720\n"
  "v_mfma_f32_16x16x32_f16 a[144:147], v[0:3], v[104:107], a[144:147]\n"
  "s_add_i32 s64, s50, 32768\n"
  "buffer_load_dwordx4 v238, s[40:43], s64 offen sc1 lds\n"
  "v_pk_add_f16 v120, v104, v108\n"
  "v_mfma_f32_16x16x32_f16 a[156:159], v[4:7], v[104:107], a[156:159]\n"
  "v_pk_add_f16 v121, v105, v109\n"
  "v_pk_add_f16 v122, v106, v110\n"
  "v_mfma_f32_16x16x32_f16 a[168:171], v[8:11], v[104:107], a[168:171]\n"
  "v_pk_add_f16 v123, v107, v111\n"
  "v_mfma_f32_16x16x32_f16 a[180:183], v[12:15], v[104:107], a[180:183]\n"
  "s_add_i32 m0, s44, 114688\n"
  "ds_read_b128 v[104:107], v232 offset:2048\n"
  "v_mfma_f32_16x16x32_f16 a[148:151], v[16:19], v[108:111], a[148:151]\n"
  "s_add_i32 s47, s44, s46\n"
  "s_add_i32 s64, s50, 4198400\n"
  "buffer_load_dwordx4 v238, s[40:43], s64 offen sc1 lds\n"
  "v_mfma_f32_16x16x32_f16 a[160:163], v[20:23], v[108:111], a[160:163]\n"
  "s_add_i32 s48, s47, s45\n"
  "s_add_i32 s46, s46, s49\n"
  "v_mfma_f32_16x16x32_f16 a[172:175], v[24:27], v[108:111], a[172:175]\n"
  "v_mfma_f32_16x16x32_f16 a[184:187], v[28:31], v[108:111], a[184:187]\n"
  "s_add_i32 m0, s67, 118784\n"
  "ds_read_b128 v[108:111], v232 offset:2560\n"
  "v_add_u32 v232, s49, v232\n"
  "v_mfma_f32_16x16x32_f16 a[152:155], v[32:35], v[120:123], a[152:155]\n"
  "s_add_i32 s64, s50, 4210688\n"
  "buffer_load_dwordx4 v238, s[40:43], s64 offen sc1 lds\n"
  "v_mfma_f32_16x16x32_f16 a[164:167], v[36:39], v[120:123], a[164:167]\n"
  "v_mfma_f32_16x16x32_f16 a[176:179], v[40:43], v[120:123], a[176:179]\n"
  "v_mfma_f32_16x16x32_f16 a[188:191], v[44:47], v[120:123], a[188:191]\n"
  "s_add_i32 m0, s44, 124928\n"
  "v_mfma_f32_16x16x32_f16 a[192:195], v[0:3], v[112:115], a[192:195]\n"
  "s_add_i32 s64, s50, 4227072\n"
  "buffer_load_dwordx4 v238, s[40:43], s64 offen sc1 lds\n"
  "v_pk_add_f16 v120, v112, v116\n"
  "v_mfma_f32_16x16x32_f16 a[204:207], v[4:7], v[112:115], a[204:207]\n"
  "s_add_i32 s50, s50, 4194304\n"
  "v_pk_add_f16 v121, v113, v117\n"
  "v_pk_add_f16 v122, v114, v118\n"
  "v_mfma_f32_16x16x32_f16 a[216:219], v[8:11], v[112:115], a[216:219]\n"
  "v_pk_add_f16 v123, v115, v119\n"
  "v_mfma_f32_16x16x32_f16 a[228:231], v[12:15], v[112:115], a[228:231]\n"
  "v_mfma_f32_16x16x32_f16 a[196:199], v[16:19], v[116:119], a[196:199]\n"
  "v_mfma_f32_16x16x32_f16 a[208:211], v[20:23], v[116:119], a[208:211]\n"
  "v_mfma_f32_16x16x32_f16 a[220:223], v[24:27], v[116:119], a[220:223]\n"
  "v_mfma_f32_16x16x32_f16 a[232:235], v[28:31], v[116:119], a[232:235]\n"
  "v_mfma_f32_16x16x32_f16 a[200:203], v[32:35], v[120:123], a[200:203]\n"
  "s_waitcnt vmcnt(12) lgkmcnt(0)\n"
  "v_mfma_f32_16x16x32_f16 a[212:215], v[36:39], v[120:123], a[212:215]\n"
  "v_cmp_eq_u32 vcc, 0, v247\n"
  "s_cbranch_vccnz .Lspin_s28_None_%=\n"
  ".Lspun_s28_None_%=:\n"
  "v_mfma_f32_16x16x32_f16 a[224:227], v[40:43], v[120:123], a[224:227]\n"
  "s_barrier\n"
  "v_mfma_f32_16x16x32_f16 a[236:239], v[44:47], v[120:123], a[236:239]\n"
  "v_mfma_f32_16x16x32_f16 v[124:127], v[48:51], v[96:99], v[124:127]\n"
  "v_mbcnt_lo_u32_b32 v252, -1, 0\n"
  "v_mbcnt_hi_u32_b32 v252, -1, v252\n"
  "v_mfma_f32_16x16x32_f16 v[136:139], v[52:55], v[96:99], v[136:139]\n"
  "v_pk_add_f16 v120, v96, v100\n"
  "v_pk_add_f16 v121, v97, v101\n"
  "v_mfma_f32_16x16x32_f16 v[148:151], v[56:59], v[96:99], v[148:151]\n"
  "v_lshlrev_b32 v252, 7, v252\n"
  "v_pk_add_f16 v122, v98, v102\n"
  "v_mfma_f32_16x16x32_f16 v[160:163], v[60:63], v[96:99], v[160:163]\n"
  "s_mov_b32 m0, 147456\n"
  "v_pk_add_f16 v123, v99, v103\n"
  "v_add_u32 v252, s45, v252\n"
  "v_mfma_f32_16x16x32_f16 v[128:131], v[64:67], v[100:103], v[128:131]\n"
  "s_cmp_eq_u32 s46, 73728\n"
  "s_cselect_b32 s49, s68, s69\n"
  "global_load_lds_dword v[248:249], off\n"
  "v_mfma_f32_16x16x32_f16 v[140:143], v[68:71], v[100:103], v[140:143]\n"
  "ds_read_b64_tr_b16 v[0:1], v228 offset:0\n"
  "ds_read_b64_tr_b16 v[2:3], v228 offset:8192\n"
  "v_mfma_f32_16x16x32_f16 v[152:155], v[72:75], v[100:103], v[152:155]\n"
  "ds_read_b128 v[96:99], v232 offset:4096\n"
  "ds_read_b64_tr_b16 v[16:17], v228 offset:256\n"
  "v_mfma_f32_16x16x32_f16 v[164:167], v[76:79], v[100:103], v[164:167]\n"
  "ds_read_b128 v[100:103], v232 offset:4608\n"
  "ds_read_b64_tr_b16 v[18:19], v228 offset:8448\n"
  "v_mfma_f32_16x16x32_f16 v[132:135], v[80:83], v[120:123], v[132:135]\n"
  "ds_read_b64_tr_b16 v[4:5], v229 offset:0\n"
  "v_add_u32 v228, s49, v228\n"
  "v_mfma_f32_16x16x32_f16 v[144:147], v[84:87], v[120:123], v[144:147]\n"
  "ds_read_b64_tr_b16 v[6:7], v229 offset:8192\n"
  "ds_read_b64_tr_b16 v[20:21], v229 offset:256\n"
  "v_mfma_f32_16x16x32_f16 v[156:159], v[88:91], v[120:123], v[156:159]\n"
  "ds_read_b64_tr_b16 v[22:23], v229 offset:8448\n"
  "v_add_u32 v229, s49, v229\n"
  "v_mfma_f32_16x16x32_f16 v[168:171], v[92:95], v[120:123], v[168:171]\n"
  "s_mov_b32 m0, 144384\n"
  "ds_read_b64_tr_b16 v[8:9], v230 offset:0\n"
  "ds_read_b64_tr_b16 v[10:11], v230 offset:8192\n"
  "v_mfma_f32_16x16x32_f16 v[180:183], v[48:51], v[104:107], v[180:183]\n"
  "global_load_lds_dword v252, s[88:89]\n"
  "v_pk_add_f16 v120, v104, v108\n"
  "v_mfma_f32_16x16x32_f16 v[192:195], v[52:55], v[104:107], v[192:195]\n"
  "v_pk_add_f16 v121, v105, v109\n"
  "v_pk_add_f16 v122, v106, v110\n"
  "v_mfma_f32_16x16x32_f16 v[204:207], v[56:59], v[104:107], v[204:207]\n"
  "s_waitcnt lgkmcnt(6)\n"
  "ds_read_b64_tr_b16 v[24:25], v230 offset:256\n"
  "v_pk_add_f16 v123, v107, v111\n"
  "v_mfma_f32_16x16x32_f16 v[216:219], v[60:63], v[104:107], v[216:219]\n"
  "ds_read_b128 v[104:107], v232 offset:6144\n"
  "v_pk_add_f16 v32, v0, v16\n"
  "v_mfma_f32_16x16x32_f16 v[184:187], v[64:67], v[108:111], v[184:187]\n"
  "ds_read_b64_tr_b16 v[26:27], v230 offset:8448\n"
  "v_pk_add_f16 v33, v1, v17\n"
  "v_mfma_f32_16x16x32_f16 v[196:199], v[68:71], v[108:111], v[196:199]\n"
  "s_waitcnt lgkmcnt(5)\n"
  "ds_read_b64_tr_b16 v[12:13], v231 offset:0\n"
  "v_pk_add_f16 v34, v2, v18\n"
  "v_mfma_f32_16x16x32_f16 v[208:211], v[72:75], v[108:111], v[208:211]\n"
  "ds_read_b64_tr_b16 v[14:15], v231 offset:8192\n"
  "v_pk_add_f16 v35, v3, v19\n"
  "v_mfma_f32_16x16x32_f16 v[220:223], v[76:79], v[108:111], v[220:223]\n"
  "ds_read_b128 v[108:111], v232 offset:6656\n"
  "v_pk_add_f16 v36, v4, v20\n"
  "v_mfma_f32_16x16x32_f16 v[188:191], v[80:83], v[120:123], v[188:191]\n"
  "ds_read_b128 v[112:115], v232 offset:8192\n"
  "v_pk_add_f16 v37, v5, v21\n"
  "v_mfma_f32_16x16x32_f16 v[200:203], v[84:87], v[120:123], v[200:203]\n"
  "ds_read_b64_tr_b16 v[28:29], v231 offset:256\n"
  "v_pk_add_f16 v38, v6, v22\n"
  "v_mfma_f32_16x16x32_f16 v[212:215], v[88:91], v[120:123], v[212:215]\n"
  "ds_read_b128 v[116:119], v232 offset:8704\n"
  "v_pk_add_f16 v39, v7, v23\n"
  "v_mfma_f32_16x16x32_f16 v[224:227], v[92:95], v[120:123], v[224:227]\n"
  "s_waitcnt lgkmcnt(6)\n"
  "ds_read_b64_tr_b16 v[30:31], v231 offset:8448\n"
  "v_pk_add_f16 v40, v8, v24\n"
  "v_mfma_f32_16x16x32_f16 a[96:99], v[48:51], v[96:99], a[96:99]\n"
  "v_pk_add_f16 v120, v96, v100\n"
  "v_pk_add_f16 v121, v97, v101\n"
  "v_mfma_f32_16x16x32_f16 a[108:111], v[52:55], v[96:99], a[108:111]\n"
  "v_pk_add_f16 v122, v98, v102\n"
  "v_pk_add_f16 v123, v99, v103\n"
  "v_mfma_f32_16x16x32_f16 a[120:123], v[56:59], v[96:99], a[120:123]\n"
  "v_pk_add_f16 v41, v9, v25\n"
  "v_pk_add_f16 v42, v10, v26\n"
  "v_mfma_f32_16x16x32_f16 a[132:135], v[60:63], v[96:99], a[132:135]\n"
  "ds_read_b128 v[96:99], v232 offset:0\n"
  "v_pk_add_f16 v43, v11, v27\n"
  "v_mfma_f32_16x16x32_f16 a[100:103], v[64:67], v[100:103], a[100:103]\n"
  "v_add_u32 v230, s49, v230\n"
  "v_add_u32 v231, s49, v231\n"
  "v_mfma_f32_16x16x32_f16 a[112:115], v[68:71], v[100:103], a[112:115]\n"
  "v_mfma_f32_16x16x32_f16 a[124:127], v[72:75], v[100:103], a[124:127]\n"
  "s_waitcnt lgkmcnt(1)\n"
  "v_pk_add_f16 v44, v12, v28\n"
  "v_pk_add_f16 v45, v13, v29\n"
  "v_mfma_f32_16x16x32_f16 a[136:139], v[76:79], v[100:103], a[136:139]\n"
  "ds_read_b128 v[100:103], v232 offset:512\n"
  "v_pk_add_f16 v46, v14, v30\n"
  "v_mfma_f32_16x16x32_f16 a[104:107], v[80:83], v[120:123], a[104:107]\n"
  "v_pk_add_f16 v47, v15, v31\n"
  "v_mfma_f32_16x16x32_f16 a[116:119], v[84:87], v[120:123], a[116:119]\n"
  "v_mfma_f32_16x16x32_f16 a[128:131], v[88:91], v[120:123], a[128:131]\n"
  "v_mfma_f32_16x16x32_f16 a[140:143], v[92:95], v[120:123], a[140:143]\n"
  "v_mfma_f32_16x16x32_f16 a[144:147], v[48:51], v[104:107], a[144:147]\n"
  "v_pk_add_f16 v120, v104, v108\n"
  "v_pk_add_f16 v121, v105, v109\n"
  "v_mfma_f32_16x16x32_f16 a[156:159], v[52:55], v[104:107], a[156:159]\n"
  "v_pk_add_f16 v122, v106, v110\n"
  "v_pk_add_f16 v123, v107, v111\n"
  "v_mfma_f32_16x16x32_f16 a[168:171], v[56:59], v[104:107], a[168:171]\n"
  "v_mfma_f32_16x16x32_f16 a[180:183], v[60:63], v[104:107], a[180:183]\n"
  "ds_read_b128 v[104:107], v232 offset:2048\n"
  "v_mfma_f32_16x16x32_f16 a[148:151], v[64:67], v[108:111], a[148:151]\n"
  "s_add_i32 s47, s44, s46\n"
  "s_add_i32 s50, s50, 4194304\n"
  "v_mfma_f32_16x16x32_f16 a[160:163], v[68:71], v[108:111], a[160:163]\n"
  "s_add_i32 s48, s47, s45\n"
  "s_add_i32 s46, s46, s49\n"
  "v_mfma_f32_16x16x32_f16 a[172:175], v[72:75], v[108:111], a[172:175]\n"
  "v_mfma_f32_16x16x32_f16 a[184:187], v[76:79], v[108:111], a[184:187]\n"
  "ds_read_b128 v[108:111], v232 offset:2560\n"
  "v_add_u32 v232, s49, v232\n"
  "v_mfma_f32_16x16x32_f16 a[152:155], v[80:83], v[120:123], a[152:155]\n"
  "v_mfma_f32_16x16x32_f16 a[164:167], v[84:87], v[120:123], a[164:167]\n"
  "v_mfma_f32_16x16x32_f16 a[176:179], v[88:91], v[120:123], a[176:179]\n"
  "v_mfma_f32_16x16x32_f16 a[188:191], v[92:95], v[120:123], a[188:191]\n"
  "v_mfma_f32_16x16x32_f16 a[192:195], v[48:51], v[112:115], a[192:195]\n"
  "v_pk_add_f16 v120, v112, v116\n"
  "v_pk_add_f16 v121, v113, v117\n"
  "v_mfma_f32_16x16x32_f16 a[204:207], v[52:55], v[112:115], a[204:207]\n"
  "v_pk_add_f16 v122, v114, v118\n"
  "v_pk_add_f16 v123, v115, v119\n"
  "v_mfma_f32_16x16x32_f16 a[216:219], v[56:59], v[112:115], a[216:219]\n"
  "v_mfma_f32_16x16x32_f16 a[228:231], v[60:63], v[112:115], a[228:231]\n"
  "v_mfma_f32_16x16x32_f16 a[196:199], v[64:67], v[116:119], a[196:199]\n"
  "v_mfma_f32_16x16x32_f16 a[208:211], v[68:71], v[116:119], a[208:211]\n"
  "v_mfma_f32_16x16x32_f16 a[220:223], v[72:75], v[116:119], a[220:223]\n"
  "v_mfma_f32_16x16x32_f16 a[232:235], v[76:79], v[116:119], a[232:235]\n"
  "v_mfma_f32_16x16x32_f16 a[200:203], v[80:83], v[120:123], a[200:203]\n"
  "s_waitcnt vmcnt(2) lgkmcnt(0)\n"
  "v_mfma_f32_16x16x32_f16 a[212:215], v[84:87], v[120:123], a[212:215]\n"
  "v_mfma_f32_16x16x32_f16 a[224:227], v[88:91], v[120:123], a[224:227]\n"
  "s_barrier\n"
  "v_mfma_f32_16x16x32_f16 a[236:239], v[92:95], v[120:123], a[236:239]\n"
  "v_mfma_f32_16x16x32_f16 v[124:127], v[0:3], v[96:99], v[124:127]\n"
  "v_pk_add_f16 v120, v96, v100\n"
  "v_pk_add_f16 v121, v97, v101\n"
  "v_mfma_f32_16x16x32_f16 v[136:139], v[4:7], v[96:99], v[136:139]\n"
  "v_pk_add_f16 v122, v98, v102\n"
  "v_pk_add_f16 v123, v99, v103\n"
  "v_mfma_f32_16x16x32_f16 v[148:151], v[8:11], v[96:99], v[148:151]\n"
  "ds_read_b128 v[112:115], v232 offset:0\n"
  "v_add_u32 v242, 57344, v232\n"
  "v_mfma_f32_16x16x32_f16 v[160:163], v[12:15], v[96:99], v[160:163]\n"
  "s_mov_b32 s66, 0x55555555\n"
  "s_mov_b32 s67, 0x55555555\n"
  "ds_read_b128 v[116:119], v232 offset:512\n"
  "v_mfma_f32_16x16x32_f16 v[128:131], v[16:19], v[100:103], v[128:131]\n"
  "ds_read_b64_tr_b16 v[48:49], v228 offset:0\n"
  "ds_read_b64_tr_b16 v[50:51], v228 offset:8192\n"
  "v_mfma_f32_16x16x32_f16 v[140:143], v[20:23], v[100:103], v[140:143]\n"
  "ds_read_b64_tr_b16 v[64:65], v228 offset:256\n"
  "ds_read_b64_tr_b16 v[66:67], v228 offset:8448\n"
  "v_mfma_f32_16x16x32_f16 v[152:155], v[24:27], v[100:103], v[152:155]\n"
  "ds_read_b64_tr_b16 v[52:53], v229 offset:0\n"
  "ds_read_b64_tr_b16 v[54:55], v229 offset:8192\n"
  "v_mfma_f32_16x16x32_f16 v[164:167], v[28:31], v[100:103], v[164:167]\n"
  "ds_read_b64_tr_b16 v[68:69], v229 offset:256\n"
  "ds_read_b64_tr_b16 v[70:71], v229 offset:8448\n"
  "v_mfma_f32_16x16x32_f16 v[132:135], v[32:35], v[120:123], v[132:135]\n"
  "ds_read_b64_tr_b16 v[56:57], v230 offset:0\n"
  "ds_read_b64_tr_b16 v[58:59], v230 offset:8192\n"
  "v_mfma_f32_16x16x32_f16 v[144:147], v[36:39], v[120:123], v[144:147]\n"
  "ds_read_b64_tr_b16 v[72:73], v230 offset:256\n"
  "ds_read_b64_tr_b16 v[74:75], v230 offset:8448\n"
  "v_mfma_f32_16x16x32_f16 v[156:159], v[40:43], v[120:123], v[156:159]\n"
  "ds_read_b64_tr_b16 v[60:61], v231 offset:0\n"
  "ds_read_b64_tr_b16 v[62:63], v231 offset:8192\n"
  "v_mfma_f32_16x16x32_f16 v[168:171], v[44:47], v[120:123], v[168:171]\n"
  "ds_read_b64_tr_b16 v[76:77], v231 offset:256\n"
  "ds_read_b64_tr_b16 v[78:79], v231 offset:8448\n"
  "v_mfma_f32_16x16x32_f16 v[180:183], v[0:3], v[104:107], v[180:183]\n"
  "s_waitcnt lgkmcnt(12)\n"
  "v_pk_add_f16 v120, v104, v108\n"
  "v_pk_add_f16 v121, v105, v109\n"
  "v_mfma_f32_16x16x32_f16 v[192:195], v[4:7], v[104:107], v[192:195]\n"
  "v_pk_add_f16 v122, v106, v110\n"
  "v_pk_add_f16 v123, v107, v111\n"
  "v_mfma_f32_16x16x32_f16 v[204:207], v[8:11], v[104:107], v[204:207]\n"
  "s_waitcnt lgkmcnt(8)\n"
  "v_pk_add_f16 v80, v48, v64\n"
  "v_pk_add_f16 v81, v49, v65\n"
  "v_mfma_f32_16x16x32_f16 v[216:219], v[12:15], v[104:107], v[216:219]\n"
  "v_pk_add_f16 v82, v50, v66\n"
  "v_pk_add_f16 v83, v51, v67\n"
  "v_mfma_f32_16x16x32_f16 v[184:187], v[16:19], v[108:111], v[184:187]\n"
  "s_waitcnt lgkmcnt(4)\n"
  "ds_read_b128 v[96:99], v232 offset:2048\n"
  "v_pk_add_f16 v84, v52, v68\n"
  "v_mfma_f32_16x16x32_f16 v[196:199], v[20:23], v[108:111], v[196:199]\n"
  "ds_read_b128 v[100:103], v232 offset:2560\n"
  "v_pk_add_f16 v85, v53, v69\n"
  "v_mfma_f32_16x16x32_f16 v[208:211], v[24:27], v[108:111], v[208:211]\n"
  "s_waitcnt lgkmcnt(2)\n"
  "v_pk_add_f16 v86, v54, v70\n"
  "v_pk_add_f16 v87, v55, v71\n"
  "v_mfma_f32_16x16x32_f16 v[220:223], v[28:31], v[108:111], v[220:223]\n"
  "v_pk_add_f16 v88, v56, v72\n"
  "v_pk_add_f16 v89, v57, v73\n"
  "v_mfma_f32_16x16x32_f16 v[188:191], v[32:35], v[120:123], v[188:191]\n"
  "v_pk_add_f16 v90, v58, v74\n"
  "v_pk_add_f16 v91, v59, v75\n"
  "v_mfma_f32_16x16x32_f16 v[200:203], v[36:39], v[120:123], v[200:203]\n"
  "v_pk_add_f16 v92, v60, v76\n"
  "v_pk_add_f16 v93, v61, v77\n"
  "v_mfma_f32_16x16x32_f16 v[212:215], v[40:43], v[120:123], v[212:215]\n"
  "v_pk_add_f16 v94, v62, v78\n"
  "v_pk_add_f16 v95, v63, v79\n"
  "v_mfma_f32_16x16x32_f16 v[224:227], v[44:47], v[120:123], v[224:227]\n"
  "v_mfma_f32_16x16x32_f16 v[124:127], v[48:51], v[112:115], v[124:127]\n"
  "v_pk_add_f16 v120, v112, v116\n"
  "v_pk_add_f16 v121, v113, v117\n"
  "v_mfma_f32_16x16x32_f16 v[136:139], v[52:55], v[112:115], v[136:139]\n"
  "v_pk_add_f16 v122, v114, v118\n"
  "v_pk_add_f16 v123, v115, v119\n"
  "v_mfma_f32_16x16x32_f16 v[128:131], v[64:67], v[116:119], v[128:131]\n"
  "ds_read_b128 v[104:107], v232 offset:4096\n"
  "ds_read_b128 v[108:111], v232 offset:4608\n"
  "v_mfma_f32_16x16x32_f16 v[140:143], v[68:71], v[116:119], v[140:143]\n"
  "v_mfma_f32_16x16x32_f16 v[132:135], v[80:83], v[120:123], v[132:135]\n"
  "v_mfma_f32_16x16x32_f16 v[144:147], v[84:87], v[120:123], v[144:147]\n"
  "v_mfma_f32_16x16x32_f16 v[148:151], v[56:59], v[112:115], v[148:151]\n"
  "v_mfma_f32_16x16x32_f16 v[160:163], v[60:63], v[112:115], v[160:163]\n"
  "v_fmac_f32 v124, s76, v132\n"
  "v_fmac_f32 v128, s76, v132\n"
  "v_mfma_f32_16x16x32_f16 v[152:155], v[72:75], v[116:119], v[152:155]\n"
  "s_waitcnt lgkmcnt(2)\n"
  "v_fmac_f32 v125, s76, v133\n"
  "v_fmac_f32 v129, s76, v133\n"
  "v_mfma_f32_16x16x32_f16 v[164:167], v[76:79], v[116:119], v[164:167]\n"
  "v_fmac_f32 v126, s76, v134\n"
  "v_fmac_f32 v130, s76, v134\n"
  "v_mfma_f32_16x16x32_f16 v[156:159], v[88:91], v[120:123], v[156:159]\n"
  "v_fmac_f32 v127, s76, v135\n"
  "v_fmac_f32 v131, s76, v135\n"
  "v_mfma_f32_16x16x32_f16 v[168:171], v[92:95], v[120:123], v[168:171]\n"
  "v_cvt_pk_bf16_f32 v172, v124, v125\n"
  "v_cvt_pk_bf16_f32 v173, v126, v127\n"
  "v_mfma_f32_16x16x32_f16 v[180:183], v[48:51], v[96:99], v[180:183]\n"
  "v_pk_add_f16 v120, v96, v100\n"
  "v_pk_add_f16 v121, v97, v101\n"
  "v_mfma_f32_16x16x32_f16 v[192:195], v[52:55], v[96:99], v[192:195]\n"
  "v_pk_add_f16 v122, v98, v102\n"
  "v_pk_add_f16 v123, v99, v103\n"
  "v_mfma_f32_16x16x32_f16 v[184:187], v[64:67], v[100:103], v[184:187]\n"
  "ds_read_b128 v[112:115], v242 offset:4096\n"
  "v_cvt_pk_bf16_f32 v176, v128, v129\n"
  "v_cvt_pk_bf16_f32 v177, v130, v131\n"
  "v_fmac_f32 v136, s76, v144\n"
  "v_fmac_f32 v140, s76, v144\n"
  "v_mfma_f32_16x16x32_f16 v[196:199], v[68:71], v[100:103], v[196:199]\n"
  "ds_read_b128 v[116:119], v242 offset:4608\n"
  "v_fmac_f32 v137, s76, v145\n"
  "v_fmac_f32 v141, s76, v145\n"
  "v_fmac_f32 v138, s76, v146\n"
  "v_fmac_f32 v142, s76, v146\n"
  "v_mfma_f32_16x16x32_f16 v[188:191], v[80:83], v[120:123], v[188:191]\n"
  "v_fmac_f32 v139, s76, v147\n"
  "v_fmac_f32 v143, s76, v147\n"
  "v_cvt_pk_bf16_f32 v174, v136, v137\n"
  "v_cvt_pk_bf16_f32 v175, v138, v139\n"
  "v_cvt_pk_bf16_f32 v178, v140, v141\n"
  "v_mfma_f32_16x16x32_f16 v[200:203], v[84:87], v[120:123], v[200:203]\n"
  "v_cvt_pk_bf16_f32 v179, v142, v143\n"
  "v_fmac_f32 v148, s76, v156\n"
  "v_fmac_f32 v152, s76, v156\n"
  "v_fmac_f32 v149, s76, v157\n"
  "v_fmac_f32 v153, s76, v157\n"
  "v_mfma_f32_16x16x32_f16 v[204:207], v[56:59], v[96:99], v[204:207]\n"
  "s_mov_b64 vcc, s[66:67]\n"
  "v_cndmask_b32_dpp v234, v176, v172, vcc quad_perm:[1,0,3,2] row_mask:0xf bank_mask:0xf\n"
  "v_cndmask_b32_dpp v235, v177, v173, vcc quad_perm:[1,0,3,2] row_mask:0xf bank_mask:0xf\n"
  "v_cndmask_b32_dpp v236, v178, v174, vcc quad_perm:[1,0,3,2] row_mask:0xf bank_mask:0xf\n"
  "v_cndmask_b32_dpp v237, v179, v175, vcc quad_perm:[1,0,3,2] row_mask:0xf bank_mask:0xf\n"
  "v_fmac_f32 v150, s76, v158\n"
  "v_mfma_f32_16x16x32_f16 v[216:219], v[60:63], v[96:99], v[216:219]\n"
  "s_add_i32 s64, s80, 0\n"
  "buffer_store_dwordx4 v[234:237], v250, s[72:75], s64 offen sc1\n"
  "s_not_b64 vcc, vcc\n"
  "v_cndmask_b32_dpp v238, v172, v176, vcc quad_perm:[1,0,3,2] row_mask:0xf bank_mask:0xf\n"
  "v_cndmask_b32_dpp v239, v173, v177, vcc quad_perm:[1,0,3,2] row_mask:0xf bank_mask:0xf\n"
  "v_cndmask_b32_dpp v240, v174, v178, vcc quad_perm:[1,0,3,2] row_mask:0xf bank_mask:0xf\n"
  "v_cndmask_b32_dpp v241, v175, v179, vcc quad_perm:[1,0,3,2] row_mask:0xf bank_mask:0xf\n"
  "v_mfma_f32_16x16x32_f16 v[208:211], v[72:75], v[100:103], v[208:211]\n"
  "s_waitcnt lgkmcnt(2)\n"
  "s_add_i32 s65, s80, 65536\n"
  "buffer_store_dwordx4 v[238:241], v250, s[72:75], s65 offen sc1\n"
  "v_fmac_f32 v154, s76, v158\n"
  "v_fmac_f32 v151, s76, v159\n"
  "v_fmac_f32 v155, s76, v159\n"
  "v_cvt_pk_bf16_f32 v172, v148, v149\n"
  "v_mfma_f32_16x16x32_f16 v[220:223], v[76:79], v[100:103], v[220:223]\n"
  "v_cvt_pk_bf16_f32 v173, v150, v151\n"
  "v_cvt_pk_bf16_f32 v176, v152, v153\n"
  "v_cvt_pk_bf16_f32 v177, v154, v155\n"
  "v_fmac_f32 v160, s76, v168\n"
  "v_fmac_f32 v164, s76, v168\n"
  "v_mfma_f32_16x16x32_f16 v[212:215], v[88:91], v[120:123], v[212:215]\n"
  "v_fmac_f32 v161, s76, v169\n"
  "v_fmac_f32 v165, s76, v169\n"
  "v_fmac_f32 v162, s76, v170\n"
  "v_fmac_f32 v166, s76, v170\n"
  "v_fmac_f32 v163, s76, v171\n"
  "v_mfma_f32_16x16x32_f16 v[224:227], v[92:95], v[120:123], v[224:227]\n"
  "v_fmac_f32 v167, s76, v171\n"
  "v_cvt_pk_bf16_f32 v174, v160, v161\n"
  "v_cvt_pk_bf16_f32 v175, v162, v163\n"
  "v_cvt_pk_bf16_f32 v178, v164, v165\n"
  "v_cvt_pk_bf16_f32 v179, v166, v167\n"
  "v_mfma_f32_16x16x32_f16 a[96:99], v[0:3], v[104:107], a[96:99]\n"
  "ds_read_b128 v[96:99], v232 offset:6144\n"
  "v_pk_add_f16 v120, v104, v108\n"
  "v_pk_add_f16 v121, v105, v109\n"
  "v_pk_add_f16 v122, v106, v110\n"
  "v_pk_add_f16 v123, v107, v111\n"
  "v_mfma_f32_16x16x32_f16 a[108:111], v[4:7], v[104:107], a[108:111]\n"
  "ds_read_b128 v[100:103], v232 offset:6656\n"
  "s_mov_b64 vcc, s[66:67]\n"
  "v_cndmask_b32_dpp v234, v176, v172, vcc quad_perm:[1,0,3,2] row_mask:0xf bank_mask:0xf\n"
  "v_cndmask_b32_dpp v235, v177, v173, vcc quad_perm:[1,0,3,2] row_mask:0xf bank_mask:0xf\n"
  "v_cndmask_b32_dpp v236, v178, v174, vcc quad_perm:[1,0,3,2] row_mask:0xf bank_mask:0xf\n"
  "v_cndmask_b32_dpp v237, v179, v175, vcc quad_perm:[1,0,3,2] row_mask:0xf bank_mask:0xf\n"
  "v_mfma_f32_16x16x32_f16 a[120:123], v[8:11], v[104:107], a[120:123]\n"
  "s_add_i32 s64, s80, 4096\n"
  "buffer_store_dwordx4 v[234:237], v250, s[72:75], s64 offen sc1\n"
  "s_not_b64 vcc, vcc\n"
  "v_cndmask_b32_dpp v238, v172, v176, vcc quad_perm:[1,0,3,2] row_mask:0xf bank_mask:0xf\n"
  "v_cndmask_b32_dpp v239, v173, v177, vcc quad_perm:[1,0,3,2] row_mask:0xf bank_mask:0xf\n"
  "v_cndmask_b32_dpp v240, v174, v178, vcc quad_perm:[1,0,3,2] row_mask:0xf bank_mask:0xf\n"
  "v_cndmask_b32_dpp v241, v175, v179, vcc quad_perm:[1,0,3,2] row_mask:0xf bank_mask:0xf\n"
  "v_mfma_f32_16x16x32_f16 a[132:135], v[12:15], v[104:107], a[132:135]\n"
  "s_add_i32 s65, s80, 69632\n"
  "buffer_store_dwordx4 v[238:241], v250, s[72:75], s65 offen sc1\n"
  "v_fmac_f32 v180, s76, v188\n"
  "v_fmac_f32 v184, s76, v188\n"
  "v_fmac_f32 v181, s76, v189\n"
  "v_fmac_f32 v185, s76, v189\n"
  "v_mfma_f32_16x16x32_f16 a[100:103], v[16:19], v[108:111], a[100:103]\n"
  "v_fmac_f32 v182, s76, v190\n"
  "v_fmac_f32 v186, s76, v190\n"
  "v_fmac_f32 v183, s76, v191\n"
  "v_fmac_f32 v187, s76, v191\n"
  "v_cvt_pk_bf16_f32 v172, v180, v181\n"
  "v_mfma_f32_16x16x32_f16 a[112:115], v[20:23], v[108:111], a[112:115]\n"
  "v_cvt_pk_bf16_f32 v173, v182, v183\n"
  "v_cvt_pk_bf16_f32 v176, v184, v185\n"
  "v_cvt_pk_bf16_f32 v177, v186, v187\n"
  "v_fmac_f32 v192, s76, v200\n"
  "v_fmac_f32 v196, s76, v200\n"
  "v_mfma_f32_16x16x32_f16 a[124:127], v[24:27], v[108:111], a[124:127]\n"
  "v_fmac_f32 v193, s76, v201\n"
  "v_fmac_f32 v197, s76, v201\n"
  "v_fmac_f32 v194, s76, v202\n"
  "v_fmac_f32 v198, s76, v202\n"
  "v_fmac_f32 v195, s76, v203\n"
  "v_mfma_f32_16x16x32_f16 a[136:139], v[28:31], v[108:111], a[136:139]\n"
  "v_fmac_f32 v199, s76, v203\n"
  "v_cvt_pk_bf16_f32 v174, v192, v193\n"
  "v_cvt_pk_bf16_f32 v175, v194, v195\n"
  "v_cvt_pk_bf16_f32 v178, v196, v197\n"
  "v_cvt_pk_bf16_f32 v179, v198, v199\n"
  "v_mfma_f32_16x16x32_f16 a[104:107], v[32:35], v[120:123], a[104:107]\n"
  "s_waitcnt lgkmcnt(2)\n"
  "s_mov_b64 vcc, s[66:67]\n"
  "v_cndmask_b32_dpp v234, v176, v172, vcc quad_perm:[1,0,3,2] row_mask:0xf bank_mask:0xf\n"
  "v_cndmask_b32_dpp v235, v177, v173, vcc quad_perm:[1,0,3,2] row_mask:0xf bank_mask:0xf\n"
  "v_cndmask_b32_dpp v236, v178, v174, vcc quad_perm:[1,0,3,2] row_mask:0xf bank_mask:0xf\n"
  "v_cndmask_b32_dpp v237, v179, v175, vcc quad_perm:[1,0,3,2] row_mask:0xf bank_mask:0xf\n"
  "v_fmac_f32 v204, s76, v212\n"
  "v_mfma_f32_16x16x32_f16 a[116:119], v[36:39], v[120:123], a[116:119]\n"
  "s_add_i32 s64, s81, 0\n"
  "buffer_store_dwordx4 v[234:237], v250, s[72:75], s64 offen sc1\n"
  "s_not_b64 vcc, vcc\n"
  "v_cndmask_b32_dpp v238, v172, v176, vcc quad_perm:[1,0,3,2] row_mask:0xf bank_mask:0xf\n"
  "v_cndmask_b32_dpp v239, v173, v177, vcc quad_perm:[1,0,3,2] row_mask:0xf bank_mask:0xf\n"
  "v_cndmask_b32_dpp v240, v174, v178, vcc quad_perm:[1,0,3,2] row_mask:0xf bank_mask:0xf\n"
  "v_cndmask_b32_dpp v241, v175, v179, vcc quad_perm:[1,0,3,2] row_mask:0xf bank_mask:0xf\n"
  "v_mfma_f32_16x16x32_f16 a[128:131], v[40:43], v[120:123], a[128:131]\n"
  "s_add_i32 s65, s81, 65536\n"
  "buffer_store_dwordx4 v[238:241], v250, s[72:75], s65 offen sc1\n"
  "v_fmac_f32 v208, s76, v212\n"
  "v_fmac_f32 v205, s76, v213\n"
  "v_fmac_f32 v209, s76, v213\n"
  "v_fmac_f32 v206, s76, v214\n"
  "v_mfma_f32_16x16x32_f16 a[140:143], v[44:47], v[120:123], a[140:143]\n"
  "v_fmac_f32 v210, s76, v214\n"
  "v_fmac_f32 v207, s76, v215\n"
  "v_fmac_f32 v211, s76, v215\n"
  "v_cvt_pk_bf16_f32 v172, v204, v205\n"
  "v_cvt_pk_bf16_f32 v173, v206, v207\n"
  "v_mfma_f32_16x16x32_f16 a[96:99], v[48:51], v[112:115], a[96:99]\n"
  "ds_read_b128 v[104:107], v242 offset:6144\n"
  "v_pk_add_f16 v120, v112, v116\n"
  "v_pk_add_f16 v121, v113, v117\n"
  "v_pk_add_f16 v122, v114, v118\n"
  "v_pk_add_f16 v123, v115, v119\n"
  "v_mfma_f32_16x16x32_f16 a[108:111], v[52:55], v[112:115], a[108:111]\n"
  "ds_read_b128 v[108:111], v242 offset:6656\n"
  "v_cvt_pk_bf16_f32 v176, v208, v209\n"
  "v_cvt_pk_bf16_f32 v177, v210, v211\n"
  "v_fmac_f32 v216, s76, v224\n"
  "v_fmac_f32 v220, s76, v224\n"
  "v_mfma_f32_16x16x32_f16 a[100:103], v[64:67], v[116:119], a[100:103]\n"
  "v_fmac_f32 v217, s76, v225\n"
  "v_fmac_f32 v221, s76, v225\n"
  "v_fmac_f32 v218, s76, v226\n"
  "v_fmac_f32 v222, s76, v226\n"
  "v_fmac_f32 v219, s76, v227\n"
  "v_mfma_f32_16x16x32_f16 a[112:115], v[68:71], v[116:119], a[112:115]\n"
  "v_fmac_f32 v223, s76, v227\n"
  "v_cvt_pk_bf16_f32 v174, v216, v217\n"
  "v_cvt_pk_bf16_f32 v175, v218, v219\n"
  "v_cvt_pk_bf16_f32 v178, v220, v221\n"
  "v_cvt_pk_bf16_f32 v179, v222, v223\n"
  "v_mfma_f32_16x16x32_f16 a[104:107], v[80:83], v[120:123], a[104:107]\n"
  "s_mov_b64 vcc, s[66:67]\n"
  "v_cndmask_b32_dpp v234, v176, v172, vcc quad_perm:[1,0,3,2] row_mask:0xf bank_mask:0xf\n"
  "v_cndmask_b32_dpp v235, v177, v173, vcc quad_perm:[1,0,3,2] row_mask:0xf bank_mask:0xf\n"
  "v_cndmask_b32_dpp v236, v178, v174, vcc quad_perm:[1,0,3,2] row_mask:0xf bank_mask:0xf\n"
  "v_cndmask_b32_dpp v237, v179, v175, vcc quad_perm:[1,0,3,2] row_mask:0xf bank_mask:0xf\n"
  "v_accvgpr_read_b32 v124, a96\n"
  "v_mfma_f32_16x16x32_f16 a[116:119], v[84:87], v[120:123], a[116:119]\n"
  "s_add_i32 s64, s81, 4096\n"
  "buffer_store_dwordx4 v[234:237], v250, s[72:75], s64 offen sc1\n"
  "s_not_b64 vcc, vcc\n"
  "v_cndmask_b32_dpp v238, v172, v176, vcc quad_perm:[1,0,3,2] row_mask:0xf bank_mask:0xf\n"
  "v_cndmask_b32_dpp v239, v173, v177, vcc quad_perm:[1,0,3,2] row_mask:0xf bank_mask:0xf\n"
  "v_cndmask_b32_dpp v240, v174, v178, vcc quad_perm:[1,0,3,2] row_mask:0xf bank_mask:0xf\n"
  "v_cndmask_b32_dpp v241, v175, v179, vcc quad_perm:[1,0,3,2] row_mask:0xf bank_mask:0xf\n"
  "v_mfma_f32_16x16x32_f16 a[120:123], v[56:59], v[112:115], a[120:123]\n"
  "s_add_i32 s65, s81, 69632\n"
  "buffer_store_dwordx4 v[238:241], v250, s[72:75], s65 offen sc1\n"
  "v_accvgpr_read_b32 v125, a97\n"
  "v_accvgpr_read_b32 v126, a98\n"
  "v_accvgpr_read_b32 v127, a99\n"
  "v_accvgpr_read_b32 v128, a100\n"
  "v_mfma_f32_16x16x32_f16 a[132:135], v[60:63], v[112:115], a[132:135]\n"
  "v_accvgpr_read_b32 v129, a101\n"
  "v_accvgpr_read_b32 v130, a102\n"
  "v_accvgpr_read_b32 v131, a103\n"
  "v_accvgpr_read_b32 v132, a104\n"
  "v_accvgpr_read_b32 v133, a105\n"
  "v_mfma_f32_16x16x32_f16 a[124:127], v[72:75], v[116:119], a[124:127]\n"
  "s_waitcnt lgkmcnt(2)\n"
  "v_accvgpr_read_b32 v134, a106\n"
  "v_accvgpr_read_b32 v135, a107\n"
  "v_fmac_f32 v124, s76, v132\n"
  "v_fmac_f32 v128, s76, v132\n"
  "v_fmac_f32 v125, s76, v133\n"
  "v_mfma_f32_16x16x32_f16 a[136:139], v[76:79], v[116:119], a[136:139]\n"
  "v_fmac_f32 v129, s76, v133\n"
  "v_fmac_f32 v126, s76, v134\n"
  "v_fmac_f32 v130, s76, v134\n"
  "v_fmac_f32 v127, s76, v135\n"
  "v_fmac_f32 v131, s76, v135\n"
  "v_mfma_f32_16x16x32_f16 a[128:131], v[88:91], v[120:123], a[128:131]\n"
  "v_cvt_pk_bf16_f32 v172, v124, v125\n"
  "v_cvt_pk_bf16_f32 v173, v126, v127\n"
  "v_cvt_pk_bf16_f32 v176, v128, v129\n"
  "v_cvt_pk_bf16_f32 v177, v130, v131\n"
  "v_accvgpr_read_b32 v136, a108\n"
  "v_mfma_f32_16x16x32_f16 a[140:143], v[92:95], v[120:123], a[140:143]\n"
  "v_accvgpr_read_b32 v137, a109\n"
  "v_accvgpr_read_b32 v138, a110\n"
  "v_accvgpr_read_b32 v139, a111\n"
  "v_accvgpr_read_b32 v140, a112\n"
  "v_accvgpr_read_b32 v141, a113\n"
  "v_mfma_f32_16x16x32_f16 a[144:147], v[0:3], v[96:99], a[144:147]\n"
  "ds_read_b128 v[112:115], v232 offset:8192\n"
  "v_pk_add_f16 v120, v96, v100\n"
  "v_pk_add_f16 v121, v97, v101\n"
  "v_pk_add_f16 v122, v98, v102\n"
  "v_pk_add_f16 v123, v99, v103\n"
  "v_mfma_f32_16x16x32_f16 a[156:159], v[4:7], v[96:99], a[156:159]\n"
  "ds_read_b128 v[116:119], v232 offset:8704\n"
  "v_accvgpr_read_b32 v142, a114\n"
  "v_accvgpr_read_b32 v143, a115\n"
  "v_accvgpr_read_b32 v144, a116\n"
  "v_accvgpr_read_b32 v145, a117\n"
  "v_mfma_f32_16x16x32_f16 a[168:171], v[8:11], v[96:99], a[168:171]\n"
  "v_accvgpr_read_b32 v146, a118\n"
  "v_accvgpr_read_b32 v147, a119\n"
  "v_fmac_f32 v136, s76, v144\n"
  "v_fmac_f32 v140, s76, v144\n"
  "v_fmac_f32 v137, s76, v145\n"
  "v_mfma_f32_16x16x32_f16 a[180:183], v[12:15], v[96:99], a[180:183]\n"
  "v_fmac_f32 v141, s76, v145\n"
  "v_fmac_f32 v138, s76, v146\n"
  "v_fmac_f32 v142, s76, v146\n"
  "v_fmac_f32 v139, s76, v147\n"
  "v_fmac_f32 v143, s76, v147\n"
  "v_mfma_f32_16x16x32_f16 a[148:151], v[16:19], v[100:103], a[148:151]\n"
  "v_cvt_pk_bf16_f32 v174, v136, v137\n"
  "v_cvt_pk_bf16_f32 v175, v138, v139\n"
  "v_cvt_pk_bf16_f32 v178, v140, v141\n"
  "v_cvt_pk_bf16_f32 v179, v142, v143\n"
  "v_accvgpr_read_b32 v148, a120\n"
  "v_mfma_f32_16x16x32_f16 a[160:163], v[20:23], v[100:103], a[160:163]\n"
  "s_mov_b64 vcc, s[66:67]\n"
  "v_cndmask_b32_dpp v234, v176, v172, vcc quad_perm:[1,0,3,2] row_mask:0xf bank_mask:0xf\n"
  "v_cndmask_b32_dpp v235, v177, v173, vcc quad_perm:[1,0,3,2] row_mask:0xf bank_mask:0xf\n"
  "v_cndmask_b32_dpp v236, v178, v174, vcc quad_perm:[1,0,3,2] row_mask:0xf bank_mask:0xf\n"
  "v_cndmask_b32_dpp v237, v179, v175, vcc quad_perm:[1,0,3,2] row_mask:0xf bank_mask:0xf\n"
  "v_accvgpr_read_b32 v149, a121\n"
  "v_mfma_f32_16x16x32_f16 a[172:175], v[24:27], v[100:103], a[172:175]\n"
  "s_add_i32 s64, s82, 0\n"
  "buffer_store_dwordx4 v[234:237], v250, s[72:75], s64 offen sc1\n"
  "s_not_b64 vcc, vcc\n"
  "v_cndmask_b32_dpp v238, v172, v176, vcc quad_perm:[1,0,3,2] row_mask:0xf bank_mask:0xf\n"
  "v_cndmask_b32_dpp v239, v173, v177, vcc quad_perm:[1,0,3,2] row_mask:0xf bank_mask:0xf\n"
  "v_cndmask_b32_dpp v240, v174, v178, vcc quad_perm:[1,0,3,2] row_mask:0xf bank_mask:0xf\n"
  "v_cndmask_b32_dpp v241, v175, v179, vcc quad_perm:[1,0,3,2] row_mask:0xf bank_mask:0xf\n"
  "v_mfma_f32_16x16x32_f16 a[184:187], v[28:31], v[100:103], a[184:187]\n"
  "s_add_i32 s65, s82, 65536\n"
  "buffer_store_dwordx4 v[238:241], v250, s[72:75], s65 offen sc1\n"
  "v_accvgpr_read_b32 v150, a122\n"
  "v_accvgpr_read_b32 v151, a123\n"
  "v_accvgpr_read_b32 v152, a124\n"
  "v_accvgpr_read_b32 v153, a125\n"
  "v_mfma_f32_16x16x32_f16 a[152:155], v[32:35], v[120:123], a[152:155]\n"
  "s_waitcnt lgkmcnt(2)\n"
  "v_accvgpr_read_b32 v154, a126\n"
  "v_accvgpr_read_b32 v155, a127\n"
  "v_accvgpr_read_b32 v156, a128\n"
  "v_accvgpr_read_b32 v157, a129\n"
  "v_accvgpr_read_b32 v158, a130\n"
  "v_mfma_f32_16x16x32_f16 a[164:167], v[36:39], v[120:123], a[164:167]\n"
  "v_accvgpr_read_b32 v159, a131\n"
  "v_fmac_f32 v148, s76, v156\n"
  "v_fmac_f32 v152, s76, v156\n"
  "v_fmac_f32 v149, s76, v157\n"
  "v_fmac_f32 v153, s76, v157\n"
  "v_mfma_f32_16x16x32_f16 a[176:179], v[40:43], v[120:123], a[176:179]\n"
  "v_fmac_f32 v150, s76, v158\n"
  "v_fmac_f32 v154, s76, v158\n"
  "v_fmac_f32 v151, s76, v159\n"
  "v_fmac_f32 v155, s76, v159\n"
  "v_cvt_pk_bf16_f32 v172, v148, v149\n"
  "v_mfma_f32_16x16x32_f16 a[188:191], v[44:47], v[120:123], a[188:191]\n"
  "v_cvt_pk_bf16_f32 v173, v150, v151\n"
  "v_cvt_pk_bf16_f32 v176, v152, v153\n"
  "v_cvt_pk_bf16_f32 v177, v154, v155\n"
  "v_accvgpr_read_b32 v160, a132\n"
  "v_accvgpr_read_b32 v161, a133\n"
  "v_mfma_f32_16x16x32_f16 a[144:147], v[48:51], v[104:107], a[144:147]\n"
  "ds_read_b128 v[96:99], v242 offset:8192\n"
  "v_pk_add_f16 v120, v104, v108\n"
  "v_pk_add_f16 v121, v105, v109\n"
  "v_pk_add_f16 v122, v106, v110\n"
  "v_pk_add_f16 v123, v107, v111\n"
  "v_mfma_f32_16x16x32_f16 a[156:159], v[52:55], v[104:107], a[156:159]\n"
  "ds_read_b128 v[100:103], v242 offset:8704\n"
  "v_accvgpr_read_b32 v162, a134\n"
  "v_accvgpr_read_b32 v163, a135\n"
  "v_accvgpr_read_b32 v164, a136\n"
  "v_accvgpr_read_b32 v165, a137\n"
  "v_mfma_f32_16x16x32_f16 a[148:151], v[64:67], v[108:111], a[148:151]\n"
  "v_accvgpr_read_b32 v166, a138\n"
  "v_accvgpr_read_b32 v167, a139\n"
  "v_accvgpr_read_b32 v168, a140\n"
  "v_accvgpr_read_b32 v169, a141\n"
  "v_accvgpr_read_b32 v170, a142\n"
  "v_mfma_f32_16x16x32_f16 a[160:163], v[68:71], v[108:111], a[160:163]\n"
  "v_accvgpr_read_b32 v171, a143\n"
  "v_fmac_f32 v160, s76, v168\n"
  "v_fmac_f32 v164, s76, v168\n"
  "v_fmac_f32 v161, s76, v169\n"
  "v_fmac_f32 v165, s76, v169\n"
  "v_mfma_f32_16x16x32_f16 a[152:155], v[80:83], v[120:123], a[152:155]\n"
  "v_fmac_f32 v162, s76, v170\n"
  "v_fmac_f32 v166, s76, v170\n"
  "v_fmac_f32 v163, s76, v171\n"
  "v_fmac_f32 v167, s76, v171\n"
  "v_cvt_pk_bf16_f32 v174, v160, v161\n"
  "v_mfma_f32_16x16x32_f16 a[164:167], v[84:87], v[120:123], a[164:167]\n"
  "v_cvt_pk_bf16_f32 v175, v162, v163\n"
  "v_cvt_pk_bf16_f32 v178, v164, v165\n"
  "v_cvt_pk_bf16_f32 v179, v166, v167\n"
  "v_accvgpr_read_b32 v180, a144\n"
  "v_accvgpr_read_b32 v181, a145\n"
  "v_mfma_f32_16x16x32_f16 a[168:171], v[56:59], v[104:107], a[168:171]\n"
  "s_mov_b64 vcc, s[66:67]\n"
  "v_cndmask_b32_dpp v234, v176, v172, vcc quad_perm:[1,0,3,2] row_mask:0xf bank_mask:0xf\n"
  "v_cndmask_b32_dpp v235, v177, v173, vcc quad_perm:[1,0,3,2] row_mask:0xf bank_mask:0xf\n"
  "v_cndmask_b32_dpp v236, v178, v174, vcc quad_perm:[1,0,3,2] row_mask:0xf bank_mask:0xf\n"
  "v_cndmask_b32_dpp v237, v179, v175, vcc quad_perm:[1,0,3,2] row_mask:0xf bank_mask:0xf\n"
  "v_accvgpr_read_b32 v182, a146\n"
  "v_mfma_f32_16x16x32_f16 a[180:183], v[60:63], v[104:107], a[180:183]\n"
  "s_add_i32 s64, s82, 4096\n"
  "buffer_store_dwordx4 v[234:237], v250, s[72:75], s64 offen sc1\n"
  "s_not_b64 vcc, vcc\n"
  "v_cndmask_b32_dpp v238, v172, v176, vcc quad_perm:[1,0,3,2] row_mask:0xf bank_mask:0xf\n"
  "v_cndmask_b32_dpp v239, v173, v177, vcc quad_perm:[1,0,3,2] row_mask:0xf bank_mask:0xf\n"
  "v_cndmask_b32_dpp v240, v174, v178, vcc quad_perm:[1,0,3,2] row_mask:0xf bank_mask:0xf\n"
  "v_cndmask_b32_dpp v241, v175, v179, vcc quad_perm:[1,0,3,2] row_mask:0xf bank_mask:0xf\n"
  "v_mfma_f32_16x16x32_f16 a[172:175], v[72:75], v[108:111], a[172:175]\n"
  "s_waitcnt lgkmcnt(2)\n"
  "s_add_i32 s65, s82, 69632\n"
  "buffer_store_dwordx4 v[238:241], v250, s[72:75], s65 offen sc1\n"
  "v_accvgpr_read_b32 v183, a147\n"
  "v_accvgpr_read_b32 v184, a148\n"
  "v_accvgpr_read_b32 v185, a149\n"
  "v_accvgpr_read_b32 v186, a150\n"
  "v_mfma_f32_16x16x32_f16 a[184:187], v[76:79], v[108:111], a[184:187]\n"
  "v_accvgpr_read_b32 v187, a151\n"
  "v_accvgpr_read_b32 v188, a152\n"
  "v_accvgpr_read_b32 v189, a153\n"
  "v_accvgpr_read_b32 v190, a154\n"
  "v_accvgpr_read_b32 v191, a155\n"
  "v_mfma_f32_16x16x32_f16 a[176:179], v[88:91], v[120:123], a[176:179]\n"
  "v_fmac_f32 v180, s76, v188\n"
  "v_fmac_f32 v184, s76, v188\n"
  "v_fmac_f32 v181, s76, v189\n"
  "v_fmac_f32 v185, s76, v189\n"
  "v_fmac_f32 v182, s76, v190\n"
  "v_mfma_f32_16x16x32_f16 a[188:191], v[92:95], v[120:123], a[188:191]\n"
  "v_fmac_f32 v186, s76, v190\n"
  "v_fmac_f32 v183, s76, v191\n"
  "v_fmac_f32 v187, s76, v191\n"
  "v_cvt_pk_bf16_f32 v172, v180, v181\n"
  "v_cvt_pk_bf16_f32 v173, v182, v183\n"
  "v_mfma_f32_16x16x32_f16 a[192:195], v[0:3], v[112:115], a[192:195]\n"
  "v_pk_add_f16 v120, v112, v116\n"
  "v_pk_add_f16 v121, v113, v117\n"
  "v_pk_add_f16 v122, v114, v118\n"
  "v_pk_add_f16 v123, v115, v119\n"
  "v_cvt_pk_bf16_f32 v176, v184, v185\n"
  "v_mfma_f32_16x16x32_f16 a[204:207], v[4:7], v[112:115], a[204:207]\n"
  "v_cvt_pk_bf16_f32 v177, v186, v187\n"
  "v_accvgpr_read_b32 v192, a156\n"
  "v_accvgpr_read_b32 v193, a157\n"
  "v_accvgpr_read_b32 v194, a158\n"
  "v_accvgpr_read_b32 v195, a159\n"
  "v_mfma_f32_16x16x32_f16 a[216:219], v[8:11], v[112:115], a[216:219]\n"
  "v_accvgpr_read_b32 v196, a160\n"
  "v_accvgpr_read_b32 v197, a161\n"
  "v_accvgpr_read_b32 v198, a162\n"
  "v_accvgpr_read_b32 v199, a163\n"
  "v_accvgpr_read_b32 v200, a164\n"
  "v_mfma_f32_16x16x32_f16 a[228:231], v[12:15], v[112:115], a[228:231]\n"
  "v_accvgpr_read_b32 v201, a165\n"
  "v_accvgpr_read_b32 v202, a166\n"
  "v_accvgpr_read_b32 v203, a167\n"
  "v_fmac_f32 v192, s76, v200\n"
  "v_fmac_f32 v196, s76, v200\n"
  "v_mfma_f32_16x16x32_f16 a[196:199], v[16:19], v[116:119], a[196:199]\n"
  "v_fmac_f32 v193, s76, v201\n"
  "v_fmac_f32 v197, s76, v201\n"
  "v_fmac_f32 v194, s76, v202\n"
  "v_fmac_f32 v198, s76, v202\n"
  "v_fmac_f32 v195, s76, v203\n"
  "v_mfma_f32_16x16x32_f16 a[208:211], v[20:23], v[116:119], a[208:211]\n"
  "v_fmac_f32 v199, s76, v203\n"
  "v_cvt_pk_bf16_f32 v174, v192, v193\n"
  "v_cvt_pk_bf16_f32 v175, v194, v195\n"
  "v_cvt_pk_bf16_f32 v178, v196, v197\n"
  "v_cvt_pk_bf16_f32 v179, v198, v199\n"
  "v_mfma_f32_16x16x32_f16 a[220:223], v[24:27], v[116:119], a[220:223]\n"
  "s_mov_b64 vcc, s[66:67]\n"
  "v_cndmask_b32_dpp v234, v176, v172, vcc quad_perm:[1,0,3,2] row_mask:0xf bank_mask:0xf\n"
  "v_cndmask_b32_dpp v235, v177, v173, vcc quad_perm:[1,0,3,2] row_mask:0xf bank_mask:0xf\n"
  "v_cndmask_b32_dpp v236, v178, v174, vcc quad_perm:[1,0,3,2] row_mask:0xf bank_mask:0xf\n"
  "v_cndmask_b32_dpp v237, v179, v175, vcc quad_perm:[1,0,3,2] row_mask:0xf bank_mask:0xf\n"
  "v_accvgpr_read_b32 v204, a168\n"
  "v_mfma_f32_16x16x32_f16 a[232:235], v[28:31], v[116:119], a[232:235]\n"
  "s_add_i32 s64, s83, 0\n"
  "buffer_store_dwordx4 v[234:237], v250, s[72:75], s64 offen sc1\n"
  "s_not_b64 vcc, vcc\n"
  "v_cndmask_b32_dpp v238, v172, v176, vcc quad_perm:[1,0,3,2] row_mask:0xf bank_mask:0xf\n"
  "v_cndmask_b32_dpp v239, v173, v177, vcc quad_perm:[1,0,3,2] row_mask:0xf bank_mask:0xf\n"
  "v_cndmask_b32_dpp v240, v174, v178, vcc quad_perm:[1,0,3,2] row_mask:0xf bank_mask:0xf\n"
  "v_cndmask_b32_dpp v241, v175, v179, vcc quad_perm:[1,0,3,2] row_mask:0xf bank_mask:0xf\n"
  "v_mfma_f32_16x16x32_f16 a[200:203], v[32:35], v[120:123], a[200:203]\n"
  "s_waitcnt lgkmcnt(0)\n"
  "s_add_i32 s65, s83, 65536\n"
  "buffer_store_dwordx4 v[238:241], v250, s[72:75], s65 offen sc1\n"
  "v_accvgpr_read_b32 v205, a169\n"
  "v_accvgpr_read_b32 v206, a170\n"
  "v_accvgpr_read_b32 v207, a171\n"
  "v_accvgpr_read_b32 v208, a172\n"
  "v_mfma_f32_16x16x32_f16 a[212:215], v[36:39], v[120:123], a[212:215]\n"
  "v_accvgpr_read_b32 v209, a173\n"
  "v_accvgpr_read_b32 v210, a174\n"
  "v_accvgpr_read_b32 v211, a175\n"
  "v_accvgpr_read_b32 v212, a176\n"
  "v_accvgpr_read_b32 v213, a177\n"
  "v_mfma_f32_16x16x32_f16 a[224:227], v[40:43], v[120:123], a[224:227]\n"
  "v_accvgpr_read_b32 v214, a178\n"
  "v_accvgpr_read_b32 v215, a179\n"
  "v_fmac_f32 v204, s76, v212\n"
  "v_fmac_f32 v208, s76, v212\n"
  "v_fmac_f32 v205, s76, v213\n"
  "v_mfma_f32_16x16x32_f16 a[236:239], v[44:47], v[120:123], a[236:239]\n"
  "v_fmac_f32 v209, s76, v213\n"
  "v_fmac_f32 v206, s76, v214\n"
  "v_fmac_f32 v210, s76, v214\n"
  "v_fmac_f32 v207, s76, v215\n"
  "v_fmac_f32 v211, s76, v215\n"
  "v_mfma_f32_16x16x32_f16 a[192:195], v[48:51], v[96:99], a[192:195]\n"
  "v_pk_add_f16 v120, v96, v100\n"
  "v_pk_add_f16 v121, v97, v101\n"
  "v_pk_add_f16 v122, v98, v102\n"
  "v_pk_add_f16 v123, v99, v103\n"
  "v_cvt_pk_bf16_f32 v172, v204, v205\n"
  "v_mfma_f32_16x16x32_f16 a[204:207], v[52:55], v[96:99], a[204:207]\n"
  "v_cvt_pk_bf16_f32 v173, v206, v207\n"
  "v_cvt_pk_bf16_f32 v176, v208, v209\n"
  "v_cvt_pk_bf16_f32 v177, v210, v211\n"
  "v_accvgpr_read_b32 v216, a180\n"
  "v_accvgpr_read_b32 v217, a181\n"
  "v_mfma_f32_16x16x32_f16 a[196:199], v[64:67], v[100:103], a[196:199]\n"
  "v_accvgpr_read_b32 v218, a182\n"
  "v_accvgpr_read_b32 v219, a183\n"
  "v_accvgpr_read_b32 v220, a184\n"
  "v_accvgpr_read_b32 v221, a185\n"
  "v_accvgpr_read_b32 v222, a186\n"
  "v_mfma_f32_16x16x32_f16 a[208:211], v[68:71], v[100:103], a[208:211]\n"
  "v_accvgpr_read_b32 v223, a187\n"
  "v_accvgpr_read_b32 v224, a188\n"
  "v_accvgpr_read_b32 v225, a189\n"
  "v_accvgpr_read_b32 v226, a190\n"
  "v_accvgpr_read_b32 v227, a191\n"
  "v_mfma_f32_16x16x32_f16 a[200:203], v[80:83], v[120:123], a[200:203]\n"
  "v_fmac_f32 v216, s76, v224\n"
  "v_fmac_f32 v220, s76, v224\n"
  "v_fmac_f32 v217, s76, v225\n"
  "v_fmac_f32 v221, s76, v225\n"
  "v_fmac_f32 v218, s76, v226\n"
  "v_mfma_f32_16x16x32_f16 a[212:215], v[84:87], v[120:123], a[212:215]\n"
  "v_fmac_f32 v222, s76, v226\n"
  "v_fmac_f32 v219, s76, v227\n"
  "v_fmac_f32 v223, s76, v227\n"
  "v_cvt_pk_bf16_f32 v174, v216, v217\n"
  "v_cvt_pk_bf16_f32 v175, v218, v219\n"
  "v_mfma_f32_16x16x32_f16 a[216:219], v[56:59], v[96:99], a[216:219]\n"
  "v_cvt_pk_bf16_f32 v178, v220, v221\n"
  "v_cvt_pk_bf16_f32 v179, v222, v223\n"
  "v_accvgpr_read_b32 v124, a192\n"
  "v_accvgpr_read_b32 v125, a193\n"
  "v_accvgpr_read_b32 v126, a194\n"
  "v_mfma_f32_16x16x32_f16 a[228:231], v[60:63], v[96:99], a[228:231]\n"
  "s_mov_b64 vcc, s[66:67]\n"
  "v_cndmask_b32_dpp v234, v176, v172, vcc quad_perm:[1,0,3,2] row_mask:0xf bank_mask:0xf\n"
  "v_cndmask_b32_dpp v235, v177, v173, vcc quad_perm:[1,0,3,2] row_mask:0xf bank_mask:0xf\n"
  "v_cndmask_b32_dpp v236, v178, v174, vcc quad_perm:[1,0,3,2] row_mask:0xf bank_mask:0xf\n"
  "v_cndmask_b32_dpp v237, v179, v175, vcc quad_perm:[1,0,3,2] row_mask:0xf bank_mask:0xf\n"
  "v_accvgpr_read_b32 v127, a195\n"
  "v_mfma_f32_16x16x32_f16 a[220:223], v[72:75], v[100:103], a[220:223]\n"
  "s_add_i32 s64, s83, 4096\n"
  "buffer_store_dwordx4 v[234:237], v250, s[72:75], s64 offen sc1\n"
  "s_not_b64 vcc, vcc\n"
  "v_cndmask_b32_dpp v238, v172, v176, vcc quad_perm:[1,0,3,2] row_mask:0xf bank_mask:0xf\n"
  "v_cndmask_b32_dpp v239, v173, v177, vcc quad_perm:[1,0,3,2] row_mask:0xf bank_mask:0xf\n"
  "v_cndmask_b32_dpp v240, v174, v178, vcc quad_perm:[1,0,3,2] row_mask:0xf bank_mask:0xf\n"
  "v_cndmask_b32_dpp v241, v175, v179, vcc quad_perm:[1,0,3,2] row_mask:0xf bank_mask:0xf\n"
  "v_mfma_f32_16x16x32_f16 a[232:235], v[76:79], v[100:103], a[232:235]\n"
  "s_add_i32 s65, s83, 69632\n"
  "buffer_store_dwordx4 v[238:241], v250, s[72:75], s65 offen sc1\n"
  "v_accvgpr_read_b32 v128, a196\n"
  "v_accvgpr_read_b32 v129, a197\n"
  "v_accvgpr_read_b32 v130, a198\n"
  "v_accvgpr_read_b32 v131, a199\n"
  "v_mfma_f32_16x16x32_f16 a[224:227], v[88:91], v[120:123], a[224:227]\n"
  "v_accvgpr_read_b32 v132, a200\n"
  "v_accvgpr_read_b32 v133, a201\n"
  "v_accvgpr_read_b32 v134, a202\n"
  "v_accvgpr_read_b32 v135, a203\n"
  "v_pk_fma_f32 v[124:125], v[132:133], s[76:77], v[124:125]\n"
  "v_mfma_f32_16x16x32_f16 a[236:239], v[92:95], v[120:123], a[236:239]\n"
  "v_pk_fma_f32 v[128:129], v[132:133], s[76:77], v[128:129]\n"
  "v_pk_fma_f32 v[126:127], v[134:135], s[76:77], v[126:127]\n"
  "v_pk_fma_f32 v[130:131], v[134:135], s[76:77], v[130:131]\n"
  "v_cvt_pk_bf16_f32 v172, v124, v125\n"
  "v_cvt_pk_bf16_f32 v173, v126, v127\n"
  "v_cvt_pk_bf16_f32 v176, v128, v129\n"
  "v_cvt_pk_bf16_f32 v177, v130, v131\n"
  "v_accvgpr_read_b32 v136, a204\n"
  "v_accvgpr_read_b32 v137, a205\n"
  "v_accvgpr_read_b32 v138, a206\n"
  "v_accvgpr_read_b32 v139, a207\n"
  "v_accvgpr_read_b32 v140, a208\n"
  "v_accvgpr_read_b32 v141, a209\n"
  "v_accvgpr_read_b32 v142, a210\n"
  "v_accvgpr_read_b32 v143, a211\n"
  "v_accvgpr_read_b32 v144, a212\n"
  "v_accvgpr_read_b32 v145, a213\n"
  "v_accvgpr_read_b32 v146, a214\n"
  "v_accvgpr_read_b32 v147, a215\n"
  "v_pk_fma_f32 v[136:137], v[144:145], s[76:77], v[136:137]\n"
  "v_pk_fma_f32 v[140:141], v[144:145], s[76:77], v[140:141]\n"
  "v_pk_fma_f32 v[138:139], v[146:147], s[76:77], v[138:139]\n"
  "v_pk_fma_f32 v[142:143], v[146:147], s[76:77], v[142:143]\n"
  "v_cvt_pk_bf16_f32 v174, v136, v137\n"
  "v_cvt_pk_bf16_f32 v175, v138, v139\n"
  "v_cvt_pk_bf16_f32 v178, v140, v141\n"
  "v_cvt_pk_bf16_f32 v179, v142, v143\n"
  "v_accvgpr_read_b32 v148, a216\n"
  "v_accvgpr_read_b32 v149, a217\n"
  "v_accvgpr_read_b32 v150, a218\n"
  "s_mov_b64 vcc, s[66:67]\n"
  "v_cndmask_b32_dpp v234, v176, v172, vcc quad_perm:[1,0,3,2] row_mask:0xf bank_mask:0xf\n"
  "v_cndmask_b32_dpp v235, v177, v173, vcc quad_perm:[1,0,3,2] row_mask:0xf bank_mask:0xf\n"
  "v_cndmask_b32_dpp v236, v178, v174, vcc quad_perm:[1,0,3,2] row_mask:0xf bank_mask:0xf\n"
  "v_cndmask_b32_dpp v237, v179, v175, vcc quad_perm:[1,0,3,2] row_mask:0xf bank_mask:0xf\n"
  "v_accvgpr_read_b32 v151, a219\n"
  "s_add_i32 s64, s84, 0\n"
  "buffer_store_dwordx4 v[234:237], v250, s[72:75], s64 offen sc1\n"
  "s_not_b64 vcc, vcc\n"
  "v_cndmask_b32_dpp v238, v172, v176, vcc quad_perm:[1,0,3,2] row_mask:0xf bank_mask:0xf\n"
  "v_cndmask_b32_dpp v239, v173, v177, vcc quad_perm:[1,0,3,2] row_mask:0xf bank_mask:0xf\n"
  "v_cndmask_b32_dpp v240, v174, v178, vcc quad_perm:[1,0,3,2] row_mask:0xf bank_mask:0xf\n"
  "v_cndmask_b32_dpp v241, v175, v179, vcc quad_perm:[1,0,3,2] row_mask:0xf bank_mask:0xf\n"
  "s_add_i32 s65, s84, 65536\n"
  "buffer_store_dwordx4 v[238:241], v250, s[72:75], s65 offen sc1\n"
  "v_accvgpr_read_b32 v152, a220\n"
  "v_accvgpr_read_b32 v153, a221\n"
  "v_accvgpr_read_b32 v154, a222\n"
  "v_accvgpr_read_b32 v155, a223\n"
  "v_accvgpr_read_b32 v156, a224\n"
  "v_accvgpr_read_b32 v157, a225\n"
  "v_accvgpr_read_b32 v158, a226\n"
  "v_accvgpr_read_b32 v159, a227\n"
  "v_pk_fma_f32 v[148:149], v[156:157], s[76:77], v[148:149]\n"
  "v_pk_fma_f32 v[152:153], v[156:157], s[76:77], v[152:153]\n"
  "v_pk_fma_f32 v[150:151], v[158:159], s[76:77], v[150:151]\n"
  "v_pk_fma_f32 v[154:155], v[158:159], s[76:77], v[154:155]\n"
  "v_cvt_pk_bf16_f32 v172, v148, v149\n"
  "v_cvt_pk_bf16_f32 v173, v150, v151\n"
  "v_cvt_pk_bf16_f32 v176, v152, v153\n"
  "v_cvt_pk_bf16_f32 v177, v154, v155\n"
  "v_accvgpr_read_b32 v160, a228\n"
  "v_accvgpr_read_b32 v161, a229\n"
  "v_accvgpr_read_b32 v162, a230\n"
  "v_accvgpr_read_b32 v163, a231\n"
  "v_accvgpr_read_b32 v164, a232\n"
  "v_accvgpr_read_b32 v165, a233\n"
  "v_accvgpr_read_b32 v166, a234\n"
  "v_accvgpr_read_b32 v167, a235\n"
  "v_accvgpr_read_b32 v168, a236\n"
  "v_accvgpr_read_b32 v169, a237\n"
  "v_accvgpr_read_b32 v170, a238\n"
  "v_accvgpr_read_b32 v171, a239\n"
  "v_pk_fma_f32 v[160:161], v[168:169], s[76:77], v[160:161]\n"
  "v_pk_fma_f32 v[164:165], v[168:169], s[76:77], v[164:165]\n"
  "v_pk_fma_f32 v[162:163], v[170:171], s[76:77], v[162:163]\n"
  "v_pk_fma_f32 v[166:167], v[170:171], s[76:77], v[166:167]\n"
  "v_cvt_pk_bf16_f32 v174, v160, v161\n"
  "v_cvt_pk_bf16_f32 v175, v162, v163\n"
  "v_cvt_pk_bf16_f32 v178, v164, v165\n"
  "v_cvt_pk_bf16_f32 v179, v166, v167\n"
  "s_mov_b64 vcc, s[66:67]\n"
  "v_cndmask_b32_dpp v234, v176, v172, vcc quad_perm:[1,0,3,2] row_mask:0xf bank_mask:0xf\n"
  "v_cndmask_b32_dpp v235, v177, v173, vcc quad_perm:[1,0,3,2] row_mask:0xf bank_mask:0xf\n"
  "v_cndmask_b32_dpp v236, v178, v174, vcc quad_perm:[1,0,3,2] row_mask:0xf bank_mask:0xf\n"
  "v_cndmask_b32_dpp v237, v179, v175, vcc quad_perm:[1,0,3,2] row_mask:0xf bank_mask:0xf\n"
  "s_add_i32 s64, s84, 4096\n"
  "buffer_store_dwordx4 v[234:237], v250, s[72:75], s64 offen sc1\n"
  "s_not_b64 vcc, vcc\n"
  "v_cndmask_b32_dpp v238, v172, v176, vcc quad_perm:[1,0,3,2] row_mask:0xf bank_mask:0xf\n"
  "v_cndmask_b32_dpp v239, v173, v177, vcc quad_perm:[1,0,3,2] row_mask:0xf bank_mask:0xf\n"
  "v_cndmask_b32_dpp v240, v174, v178, vcc quad_perm:[1,0,3,2] row_mask:0xf bank_mask:0xf\n"
  "v_cndmask_b32_dpp v241, v175, v179, vcc quad_perm:[1,0,3,2] row_mask:0xf bank_mask:0xf\n"
  "s_add_i32 s65, s84, 69632\n"
  "buffer_store_dwordx4 v[238:241], v250, s[72:75], s65 offen sc1\n"
  "s_waitcnt vmcnt(20)\n"
  "s_branch .Lend_%=\n"
  ".Lspin_pro_%=:\n"
  "s_mov_b32 s64, 0\n"
  ".Lspin2_pro_%=:\n"
  "s_sleep 3\n"
  "buffer_load_dword v247, v246, s[40:43], s54 offen sc1\n"
  "s_add_u32 s64, s64, 1\n"
  "s_waitcnt vmcnt(0)\n"
  "s_cmp_gt_u32 s64, 0x200000\n"
  "s_cbranch_scc1 .Lspun_pro_%=\n"
  "v_cmp_eq_u32 vcc, 0, v247\n"
  "s_cbranch_vccnz .Lspin2_pro_%=\n"
  "s_branch .Lspun_pro_%=\n"
  ".Lredo_%=:\n"
  "s_bfe_u32 s64, %[ctl], 0x80010\n"
  "s_add_u32 s64, s64, 7\n"
  "s_max_u32 s64, s64, s70\n"
  "s_sub_u32 s64, s64, 7\n"
  "s_min_u32 s64, s64, 220\n"
  "s_lshr_b32 s65, %[ctl], 24\n"
  "s_add_u32 s65, s65, 9\n"
  "s_max_u32 s65, s65, s71\n"
  "s_sub_u32 s65, s65, 9\n"
  "s_min_u32 s65, s65, 220\n"
  "s_lshl_b32 s64, s64, 16\n"
  "s_lshl_b32 s65, s65, 24\n"
  "s_or_b32 s64, s64, s65\n"
  "s_and_b32 s65, %[ctl], 255\n"
  "s_or_b32 s64, s64, s65\n"
  "s_bfe_u32 s65, %[ctl], 0x30008\n"
  "s_lshl_b32 s65, s65, 10\n"
  "s_or_b32 s64, s64, s65\n"
  "s_or_b32 s64, s64, 768\n"
  "s_bfe_u32 s65, %[ctl], 0x2000d\n"
  "s_lshl_b32 s65, s65, 6\n"
  "v_mbcnt_lo_u32_b32 v0, -1, 0\n"
  "v_mbcnt_hi_u32_b32 v0, -1, v0\n"
  "v_add_u32 v0, s65, v0\n"
  "s_mov_b64 s[0:1], %[ka]\n"
  "s_mov_b32 s2, s64\n"
  "s_waitcnt vmcnt(0)\n"
  "s_barrier\n"
  "s_branch conv_fft\n"
  ".Lpace_s0_None_%=:\n"
  "s_memrealtime s[70:71]\n"
  "s_waitcnt lgkmcnt(0)\n"
  "s_sub_i32 s64, s70, s61\n"
  "s_cmp_lt_i32 s64, 0\n"
  "s_cbranch_scc0 .Lpace2_s0_None_%=\n"
  "s_sleep 1\n"
  "s_branch .Lpace_s0_None_%=\n"
  ".Lpace2_s0_None_%=:\n"
  "s_add_i32 s61, s70, s62\n"
  "s_add_i32 s61, s61, 8\n"
  "s_sub_i32 s62, s62, 14\n"
  "s_branch .Lpaced_s0_None_%=\n"
  ".Lpace_s1_None_%=:\n"
  "s_memrealtime s[70:71]\n"
  "s_waitcnt lgkmcnt(0)\n"
  "s_sub_i32 s64, s70, s61\n"
  "s_cmp_lt_i32 s64, 0\n"
  "s_cbranch_scc0 .Lpace2_s1_None_%=\n"
  "s_sleep 1\n"
  "s_branch .Lpace_s1_None_%=\n"
  ".Lpace2_s1_None_%=:\n"
  "s_add_i32 s61, s70, s62\n"
  "s_add_i32 s61, s61, 0\n"
  "s_sub_i32 s62, s62, 14\n"
  "s_branch .Lpaced_s1_None_%=\n"
  ".Lspin_pe_None_%=:\n"
  "s_mov_b32 s64, 0\n"
  ".Lspin2_pe_None_%=:\n"
  "s_sleep 3\n"
  "buffer_load_dword v247, v246, s[40:43], s54 offen sc1\n"
  "s_add_u32 s64, s64, 1\n"
  "s_waitcnt vmcnt(0)\n"
  "s_cmp_gt_u32 s64, 0x200000\n"
  "s_cbranch_scc1 .Lspun_pe_None_%=\n"
  "v_cmp_eq_u32 vcc, 0, v247\n"
  "s_cbranch_vccnz .Lspin2_pe_None_%=\n"
  "s_branch .Lspun_pe_None_%=\n"
  ".Lpace_pe_None_%=:\n"
  "s_memrealtime s[70:71]\n"
  "s_waitcnt lgkmcnt(0)\n"
  "s_sub_i32 s64, s70, s61\n"
  "s_cmp_lt_i32 s64, 0\n"
  "s_cbranch_scc0 .Lpace2_pe_None_%=\n"
  "s_sleep 1\n"
  "s_branch .Lpace_pe_None_%=\n"
  ".Lpace2_pe_None_%=:\n"
  "s_add_i32 s61, s70, s62\n"
  "s_add_i32 s61, s61, 8\n"
  "s_sub_i32 s62, s62, 14\n"
  "s_branch .Lpaced_pe_None_%=\n"
  ".Lpace_po_None_%=:\n"
  "s_memrealtime s[70:71]\n"
  "s_waitcnt lgkmcnt(0)\n"
  "s_sub_i32 s64, s70, s61\n"
  "s_cmp_lt_i32 s64, 0\n"
  "s_cbranch_scc0 .Lpace2_po_None_%=\n"
  "s_sleep 1\n"
  "s_branch .Lpace_po_None_%=\n"
  ".Lpace2_po_None_%=:\n"
  "s_add_i32 s61, s70, s62\n"
  "s_add_i32 s61, s61, 0\n"
  "s_sub_i32 s62, s62, 14\n"
  "s_branch .Lpaced_po_None_%=\n"
  ".Lspin_s22_None_%=:\n"
  "s_mov_b32 s64, 0\n"
  ".Lspin2_s22_None_%=:\n"
  "s_sleep 3\n"
  "buffer_load_dword v247, v246, s[40:43], s54 offen sc1\n"
  "s_add_u32 s64, s64, 1\n"
  "s_waitcnt vmcnt(0)\n"
  "s_cmp_gt_u32 s64, 0x200000\n"
  "s_cbranch_scc1 .Lspun_s22_None_%=\n"
  "v_cmp_eq_u32 vcc, 0, v247\n"
  "s_cbranch_vccnz .Lspin2_s22_None_%=\n"
  "s_branch .Lspun_s22_None_%=\n"
  ".Lspin_ne_None_%=:\n"
  "s_mov_b32 s64, 0\n"
  ".Lspin2_ne_None_%=:\n"
  "s_sleep 3\n"
  "buffer_load_dword v247, v246, s[40:43], s54 offen sc1\n"
  "s_add_u32 s64, s64, 1\n"
  "s_waitcnt vmcnt(0)\n"
  "s_cmp_gt_u32 s64, 0x200000\n"
  "s_cbranch_scc1 .Lspun_ne_None_%=\n"
  "v_cmp_eq_u32 vcc, 0, v247\n"
  "s_cbranch_vccnz .Lspin2_ne_None_%=\n"
  "s_branch .Lspun_ne_None_%=\n"
  ".Lspin_s28_None_%=:\n"
  "s_mov_b32 s64, 0\n"
  ".Lspin2_s28_None_%=:\n"
  "s_sleep 3\n"
  "buffer_load_dword v247, v246, s[40:43], s54 offen sc1\n"
  "s_add_u32 s64, s64, 1\n"
  "s_waitcnt vmcnt(0)\n"
  "s_cmp_gt_u32 s64, 0x200000\n"
  "s_cbranch_scc1 .Lspun_s28_None_%=\n"
  "v_cmp_eq_u32 vcc, 0, v247\n"
  "s_cbranch_vccnz .Lspin2_s28_None_%=\n"
  "s_branch .Lspun_s28_None_%=\n"
  ".Lend_%=:\n"
    : "={v[180:211]}"(o3), "={v[212:227]}"(o4), "={v[176:179]}"(o5), "={v[80:95]}"(o0), "={v[156:163]}"(o1),
      "={v[124:125]}"(o6), "={v247}"(o7)
    : "{v[180:211]}"(h0), "{v[212:227]}"(h1), "{v[176:179]}"(hd.vb), "{v[80:95]}"(st0), "{v[156:163]}"(st1), "{s[40:43]}"(cx.rs),
      "{v[124:125]}"(hd.ov), "{v247}"(hd.pollv), [nw] "i"(NW), [ctl] "s"(hd.ctl), [rec] "s"(hd.rec), [ka] "s"(__builtin_amdgcn_kernarg_segment_ptr())
    : "v0", "v1", "v2", "v3", "v4", "v5", "v6", "v7", "v8", "v9", "v10", "v11", "v12", "v13", "v14", "v15", "v16", "v17", "v18", "v19", "v20", "v21", "v22", "v23", "v24", "v25", "v26", "v27", "v28", "v29", "v30", "v31", "v32", "v33", "v34", "v35", "v36", "v37", "v38", "v39", "v40", "v41", "v42", "v43", "v44", "v45", "v46", "v47", "v48", "v49", "v50", "v51", "v52", "v53", "v54", "v55", "v56", "v57", "v58", "v59", "v60", "v61", "v62", "v63", "v64", "v65", "v66", "v67", "v68", "v69", "v70", "v71", "v72", "v73", "v74", "v75", "v76", "v77", "v78", "v79", "v96", "v97", "v98", "v99", "v100", "v101", "v102", "v103", "v104", "v105", "v106", "v107", "v108", "v109", "v110", "v111", "v112", "v113", "v114", "v115", "v116", "v117", "v118", "v119", "v120", "v121", "v122", "v123", "v126", "v127", "v128", "v129", "v130", "v131", "v132", "v133", "v134", "v135", "v136", "v137", "v138", "v139", "v140", "v141", "v142", "v143", "v144", "v145", "v146", "v147", "v148", "v149", "v150", "v151", "v152", "v153", "v154", "v155", "v164", "v165", "v166", "v167", "v168", "v169", "v170", "v171", "v172", "v173", "v174", "v175", "v228", "v229", "v230", "v231", "v232", "v233", "v234", "v235", "v236", "v237", "v238", "v239", "v240", "v241", "v242", "v243", "v244", "v245", "v246", "v248", "v249", "v250", "v251", "v252", "v253", "v254", "v255", "a0", "a1", "a2", "a3", "a4", "a5", "a6", "a7", "a8", "a9", "a10", "a11", "a12", "a13", "a14", "a15", "a16", "a17", "a18", "a19", "a20", "a21", "a22", "a23", "a24", "a25", "a26", "a27", "a28", "a29", "a30", "a31", "a32", "a33", "a34", "a35", "a36", "a37", "a38", "a39", "a40", "a41", "a42", "a43", "a44", "a45", "a46", "a47", "a48", "a49", "a50", "a51", "a52", "a53", "a54", "a55", "a56", "a57", "a58", "a59", "a60", "a61", "a62", "a63", "a64", "a65", "a66", "a67", "a68", "a69", "a70", "a71", "a72", "a73", "a74", "a75", "a76", "a77", "a78", "a79", "a80", "a81", "a82", "a83", "a84", "a85", "a86", "a87", "a88", "a89", "a90", "a91", "a92", "a93", "a94", "a95", "a96", "a97", "a98", "a99", "a100", "a101", "a102", "a103", "a104", "a105", "a106", "a107", "a108", "a109", "a110", "a111", "a112", "a113", "a114", "a115", "a116", "a117", "a118", "a119", "a120", "a121", "a122", "a123", "a124", "a125", "a126", "a127", "a128", "a129", "a130", "a131", "a132", "a133", "a134", "a135", "a136", "a137", "a138", "a139", "a140", "a141", "a142", "a143", "a144", "a145", "a146", "a147", "a148", "a149", "a150", "a151", "a152", "a153", "a154", "a155", "a156", "a157", "a158", "a159", "a160", "a161", "a162", "a163", "a164", "a165", "a166", "a167", "a168", "a169", "a170", "a171", "a172", "a173", "a174", "a175", "a176", "a177", "a178", "a179", "a180", "a181", "a182", "a183", "a184", "a185", "a186", "a187", "a188", "a189", "a190", "a191", "a192", "a193", "a194", "a195", "a196", "a197", "a198", "a199", "a200", "a201", "a202", "a203", "a204", "a205", "a206", "a207", "a208", "a209", "a210", "a211", "a212", "a213", "a214", "a215", "a216", "a217", "a218", "a219", "a220", "a221", "a222", "a223", "a224", "a225", "a226", "a227", "a228", "a229", "a230", "a231", "a232", "a233", "a234", "a235", "a236", "a237", "a238", "a239", "a240", "a241", "a242", "a243", "a244", "a245", "a246", "a247", "a248", "a249", "a250", "a251", "a252", "a253", "a254", "a255", "s44", "s45", "s46", "s47", "s48", "s49", "s50", "s51", "s52", "s53", "s54", "s55", "s56", "s57", "s58", "s59", "s60", "s61", "s62", "s63", "s64", "s65", "s66", "s67", "s68", "s69", "s70", "s71", "s72", "s73", "s74", "s75", "s76", "s77", "s78", "s79", "s80", "s81", "s82", "s83", "s84", "s85", "s86", "s87", "s88", "s89", "vcc", "scc", "m0", "memory");
}

__device__ __forceinline__ void imfma0(f32x4& d, const bf16x8& a, const bf16x8& b, const f32x4& c) {
  asm volatile("v_mfma_f32_16x16x32_bf16 %0, %1, %2, %3" : "=&v"(d) : "v"(a), "a"(b), "v"(c));
}
__device__ __forceinline__ void imfma1(f32x4& d, const bf16x8& a, const bf16x8& b) {
  asm volatile("v_mfma_f32_16x16x32_bf16 %0, %1, %2, %0" : "+v"(d) : "v"(a), "a"(b));
}
#define DPPADD(d, s, CTRL, BANKS) asm volatile("v_add_f32_dpp %0, %1, %1 " CTRL " row_mask:0xf bank_mask:" BANKS : "+v"(d) : "v"(s))
template <int CTRL>
__device__ __forceinline__ float dpp_add(float v) {
  asm("" : "+v"(v));
  return v + __builtin_bit_cast(float, __builtin_amdgcn_update_dpp(0, __builtin_bit_cast(int, v), CTRL, 0xf, 0xf, true));
}

extern "C" __global__ __launch_bounds__(256, 1) void conv_fft(char* ws, const unsigned short* __restrict__ x,
                                                   const unsigned short* __restrict__ w,
                                                   const unsigned short* __restrict__ gamma,
                                                   const unsigned short* __restrict__ beta,
                                                   unsigned short* __restrict__ out) {
  __shared__ __attribute__((aligned(16))) char smem[160 * 1024];
  const unsigned lds0 = (unsigned)(uintptr_t)(lds_void*)smem;
  const int wv = __builtin_amdgcn_readfirstlane(threadIdx.x >> 6);
  Ctx cx;

  {
    unsigned len;
    asm volatile("s_getpc_b64 %0\n.Lcf_a:\n\ts_mov_b32 %1, .Lcf_b-.Lcf_a" : "=s"(cx.pc), "=s"(len));
    cx.wv = wv;
    cx.pfo = len;
    unsigned xcc;
    asm volatile("s_getreg_b32 %0, hwreg(HW_REG_XCC_ID, 0, 3)" : "=s"(xcc));
    unsigned id = blockIdx.x;
    asm volatile("" : "+s"(id));
    cx.id = id;
    cx.vb = (int)((id & 248u) | (id & 256u ? (id >> 10) & 7u : xcc));
    const char* pb = ws + WS_PUB;
    u32x4 &sx = cx.sx, &sw = cx.sw;
    cx.xcc = xcc;
    if ((id | wv) == 0) {
      const unsigned l = threadIdx.x;
      const u32x4 z = {0u, 0u, 0u, 0u};
      {
        const unsigned so = l * (31u * 1024u + 16u) + (4u << 20);
        asm volatile("global_load_dwordx4 %0, %2, %3 nt\n\tglobal_load_dwordx4 %1, %4, %5 nt"
                     : "=&v"(sx), "=&v"(sw) : "v"(so + (l & 3u) * (8u << 20)), "s"(x), "v"(so + (l & 1u) * (6u << 20)), "s"(w) : "memory");
      }
      {
#pragma unroll
        for (int k = 0; k < 2; ++k)
          asm volatile("global_store_dwordx4 %0, %1, %2 sc1" :: "v"(32768 + (l & 7) * 16 + (l & 8) * 512 + (l & 16) * 1024 + (l & 32) * 32 + k * 8192), "v"(z), "s"(pb) : "memory");
        asm volatile("global_store_dwordx4 %0, %1, %2 sc1" :: "v"(l * 16 + 1024), "v"(z), "s"(pb) : "memory");
      }
    }
    if (id) code_touch(cx);
    SB0;
  }
  cx.smem = smem;
  cx.ws = ws;
  cx.gamma = gamma;
  cx.beta = beta;
  run12(cx, x, w);
  ws = cx.ws;
  const int bid = cx.vb, idx = bid >> 3;
  const int slot = (bid & 7) * 4 + (idx >> 3), b = (idx >> 2) & 1, nt = idx & 3;
  const int lane = (int)__builtin_amdgcn_mbcnt_hi(~0u, __builtin_amdgcn_mbcnt_lo(~0u, 0u));
  const int c64i = nt * 2 + ((bid >> 2) & 1), rg = (bid & 1) * 8 + (slot & 1) * 4 + (bid & 2) + (idx >> 4);
  const int bs = 10 * rg, hw = wv & 1, par = wv >> 1;
  const int g = lane >> 4, qq = (lane >> 2) & 3, pp = lane & 3, fr = lane & 15;
  const int lend = min(HOP * (bs + 10), L_);
  const u32x4 rsf = make_rsrc(ws + WS_GRP + nt * 256, 16 * 1024);
  unsigned fw = (unsigned)((lane + (lane & 4)) * 1024 + (b * 32 + slot) * 4);
  unsigned fp = (unsigned)(((bid & 7) + (bid & 4)) * 1024 + (b * 32 + ((lane & 31) ^ ((lane & 31) == slot))) * 4);
  f32x4 y[30], ye[2], cin[2], npv[2];
  {
    const int ee = fr & 1 ? cx.es : -2000;
#pragma unroll
    for (int s = 0; s < 2; ++s) {
      cin[s] = *reinterpret_cast<const f32x4*>(smem + CBUF + (par + (fr & 14)) * 256 + hw * 128 + g * 32 + s * 16);
#pragma unroll
      for (int r = 0; r < 4; ++r) cin[s][r] = __builtin_amdgcn_ldexpf(cin[s][r], ee);
    }
  }
  char* my = smem + wv * 20480;
  unsigned src = (unsigned)(uintptr_t)(lds_void*)my + (unsigned)((4 * g + qq) * 64 + ((g & 1) << 5) + pp * 8), src1 = src ^ 32u;
  unsigned vo = (unsigned)((lane >> 2) * 64 + (((lane & 3) ^ ((lane >> 3) & 2)) << 4)), vp = (unsigned)(g * 8);
  const u32x4 rsy = make_rsrc(ws + WS_Y, ROWS * 65536);
  unsigned so[6];
  {
    const unsigned t0 = (unsigned)(b * NBLK * 65536 + (c64i * 2 + hw) * 4096);
#pragma unroll
    for (int i = 0; i < 6; ++i) {
      const unsigned t = __builtin_amdgcn_readfirstlane(i < 5 ? t0 + (unsigned)(bs + par + 2 * i) * 65536 : t0);
      so[i] = t ^ ((t >> 8) & 8192u);
      asm volatile("" : "+s"(so[i]));
    }
  }
  asm volatile("" : "+v"(cin[0]), "+v"(cin[1]), "+v"(src), "+v"(src1), "+v"(vo), "+v"(vp), "+v"(fw), "+v"(fp));
  SB0;
  wait_vm<0>();
  __builtin_amdgcn_s_barrier();
  unsigned fv = 1u;
  if (wv == 1 && lane < 8) asm volatile("buffer_store_dword %0, %1, %2, 0 offen sc1" :: "v"(fv), "v"(fw), "s"(rsf) : "memory");
  if (wv == 0) asm volatile("buffer_load_dword %0, %1, %2, 0 offen sc1" : "=v"(fv) : "v"(fp), "s"(rsf) : "memory");
  bf16x8 gb[16];
  {
    const unsigned l16 = (unsigned)lane * 16u;
    const char* gt = (const char*)&TAB.G[0][0][0][0][0];
#pragma unroll
    for (int i = 0; i < 16; ++i)
      asm volatile("global_load_dwordx4 %0, %1, %2 offset:%3" : "=a"(gb[i]) : "v"(l16), "s"(gt + (i >> 2) * 4096), "i"((i & 3) * 1024) : "memory");
  }
  SB0;
  constexpr float GC = 0.4686686930532063f, GA = 4.91222953926424f, GL = -1.0933596685052493f;
  float gam = GC * bf_lo(*reinterpret_cast<const unsigned short*>(smem + GAM + wv * 32 + fr * 2));
  float bet = GC * bf_lo(*reinterpret_cast<const unsigned short*>(smem + GAM + 128 + wv * 32 + fr * 2));
  float eps = fmaxf(__builtin_amdgcn_ldexpf(1e-5f, 2 * cx.es), 1e-30f);
  const int l0 = HOP * (bs + par) + fr;
  const int le = fr < 10 ? HOP * (bs + par + 2 * (fr >> 1)) + 48 + (fr & 1) : 1 << 20;
  unsigned oo = (unsigned)((l0 * D_ + (c64i * 2 + hw) * 32 + 8 * g) * 2), oe = (unsigned)((le * D_ + (c64i * 2 + hw) * 32 + 8 * g) * 2);
  float v4[4];
#pragma unroll
  for (int tt = 0; tt < 4; ++tt) v4[tt] = (tt < 3 ? l0 + 8 * HOP + 16 * tt : le) < lend ? 1.f : 0.f;
  unsigned long long* stats = reinterpret_cast<unsigned long long*>(ws + WS_STATS);
  const u32x4 rss = make_rsrc(stats + (b * 32 + (c64i & 1) * 4) * 256 + nt * 64, 65536);
  unsigned sp = (unsigned)((g * 256 + 16 * wv + fr) * 8);
  const unsigned red = lds0 + 8 * 12288, aff = red + 4096;
  unsigned rw_ = red + (unsigned)((par * 64 + hw * 32 + 8 * g + (fr & 7)) * 8 + (fr >> 3) * 4), aw_ = aff + (unsigned)(((g & 1) * 64 + 16 * wv + fr) * 4);
  unsigned ar_ = aff + (unsigned)((hw * 32 + 8 * g) * 4);
  asm volatile("" : "+v"(gam), "+v"(bet), "+v"(eps), "+v"(oo), "+v"(oe), "+v"(sp), "+v"(rw_), "+v"(aw_), "+v"(ar_), "+v"(v4[0]), "+v"(v4[1]), "+v"(v4[2]), "+v"(v4[3]));
  SB0;
  if (wv == 0) {
    wait_vm<16>();
    asm volatile("" : "+v"(fv));
    for (unsigned spins = 0; !__all(fv != 0u) && spins < (1u << 21); ++spins)
      asm volatile("s_sleep 1\n\tbuffer_load_dword %0, %1, %2, 0 offen sc1\n\ts_waitcnt vmcnt(0)" : "=v"(fv) : "v"(fp), "s"(rsf) : "memory");
  }
  __builtin_amdgcn_s_barrier();
  u32x2 pv[2];
  asm volatile("buffer_load_dwordx2 %0, %2, %3, %4 offen sc1\n\tbuffer_load_dwordx2 %1, %2, %3, %4 offen offset:32 sc1"
               : "=&v"(pv[0]), "=&v"(pv[1]) : "v"(vp), "s"(rsy), "s"(so[5]) : "memory");
#pragma unroll
  for (int i = 0; i < 5; ++i)
    asm volatile("s_mov_b32 m0, %0\n\ts_nop 0\n\tbuffer_load_dwordx4 %1, %2, %3 offen sc1 lds\n\tbuffer_load_dwordx4 %1, %2, %3 offen offset:1024 sc1 lds\n\t"
                 "buffer_load_dwordx4 %1, %2, %3 offen offset:2048 sc1 lds\n\tbuffer_load_dwordx4 %1, %2, %3 offen offset:3072 sc1 lds"
                 :: "s"(lds0 + wv * 20480 + i * 4096), "v"(vo), "s"(rsy), "s"(so[i]) : "memory");
  float s1[8], s2[8];
  {
    bf16x8 a[2][4];
    auto rd = [&](int i, int q) __attribute__((always_inline)) {
      const lds_s16x4* p = (const lds_s16x4*)(uintptr_t)((q & 2 ? src1 : src) + i * 4096 + (q & 1) * 2048);
      a[i & 1][q] = cat(__builtin_amdgcn_ds_read_tr16_b64_v4i16((lds_s16x4*)p), __builtin_amdgcn_ds_read_tr16_b64_v4i16((lds_s16x4*)p + 128));
    };
    f32x4 t3[2];
    auto sums = [&](int cp, int q) __attribute__((always_inline)) {
      const int i = cp >> 3, s = (cp >> 2) & 1, tt = cp & 3, e = 4 * s + q;
      if (tt == 3) {
        if (i) ye[s][q] = (fr >> 1) == i ? t3[s][q] : ye[s][q];
        asm volatile("" : "+v"(ye[s]));
        return;
      }
      float d = y[6 * i + 3 * s + tt][q];
      if (i == 4) d *= v4[tt];
      if (i == 0 && tt == 0) { s1[e] = d; s2[e] = d * d; }
      else { s1[e] += d; s2[e] = __builtin_fmaf(d, d, s2[e]); }
      asm volatile("" : "+v"(s1[e]), "+v"(s2[e]));
    };
    wait_vm<16>();
    SB0;
#pragma unroll
    for (int q = 0; q < 4; ++q) { rd(0, q); SB0; }
    asm volatile("" : "+v"(pv[0]), "+v"(pv[1]));
#pragma unroll
    for (int s = 0; s < 2; ++s) {
      npv[s] = f32x4{bf_lo(pv[s][0]), bf_hi(pv[s][0]), bf_lo(pv[s][1]), bf_hi(pv[s][1])} * (-1.0f / 64.0f);
      cin[s] += npv[s];
    }
    asm volatile("" : "+v"(npv[0]), "+v"(npv[1]), "+v"(cin[0]), "+v"(cin[1]));
    asm volatile("s_nop 1");
    SB0;
    sfor<0, 42>([&](auto cc) __attribute__((always_inline)) {
      constexpr int c = cc.v, i = c >> 3, is = (c >> 2) & 1, tt = c & 3;
      if constexpr ((c & 7) == 4 && i < 4) wait_vm<12 - 4 * i>();
      sfor<0, 4>([&](auto qq_) __attribute__((always_inline)) {
        constexpr int q = qq_.v;
        if constexpr (c < 40) {
          f32x4& d = tt < 3 ? y[6 * i + 3 * is + (tt < 3 ? tt : 0)] : i ? t3[is] : ye[is];
          if constexpr (q == 0) imfma0(d, a[i & 1][2 * is], gb[4 * tt + 1], tt == 3 ? cin[is] : npv[is]);
          else imfma1(d, a[i & 1][2 * is + (q >> 1)], gb[4 * tt + (q ^ 1)]);
        }
        SB0;
        if constexpr (((c & 7) == 4 || (c & 7) == 5) && i < 4) { if (q & 1) rd(i + 1, (c & 1) * 2 + (q >> 1)); }
        if constexpr (c >= 2) sums(c - 2, q);
        SB0;
      });
    });
    asm volatile("" :: "v"(npv[0]), "v"(npv[1]), "v"(cin[0]), "v"(cin[1]));
#pragma unroll
    for (int e = 0; e < 8; ++e) {
      const float d = v4[3] != 0.f ? ye[e >> 2][e & 3] : 0.f;
      s1[e] += d;
      s2[e] = __builtin_fmaf(d, d, s2[e]);
    }
  }
  {
    float v[8];
#pragma unroll
    for (int j = 0; j < 8; ++j) { v[j] = s1[j]; }
#pragma unroll
    for (int j = 0; j < 8; ++j) DPPADD(v[j], s1[j], "row_ror:8", "0x3");
#pragma unroll
    for (int j = 0; j < 8; ++j) DPPADD(v[j], s2[j], "row_ror:8", "0xc");
    asm volatile("s_nop 0");
#pragma unroll
    for (int j = 0; j < 4; ++j) DPPADD(v[j], v[j], "row_half_mirror", "0x5");
#pragma unroll
    for (int j = 0; j < 4; ++j) DPPADD(v[j], v[j + 4], "row_half_mirror", "0xa");
    asm volatile("s_nop 0");
#pragma unroll
    for (int j = 0; j < 4; ++j) v[j] = dpp_add<0x4E>(v[j]);
    SB0;
#pragma unroll
    for (int j = 0; j < 4; ++j) v[j] = dpp_add<0xB1>(v[j]);
    const float w0 = fr & 1 ? v[1] : v[0], w1 = fr & 1 ? v[3] : v[2];
    *(__attribute__((address_space(3))) float*)(uintptr_t)rw_ = fr & 2 ? w1 : w0;
  }
  asm volatile("s_waitcnt lgkmcnt(0)" ::: "memory");
  __builtin_amdgcn_s_barrier();
  if (wv == 0) {
    typedef __attribute__((address_space(3))) f32x2 lds_f32x2;
    const f32x2 sm = *(lds_f32x2*)(uintptr_t)(red + lane * 8) + *(lds_f32x2*)(uintptr_t)(red + 512 + lane * 8);
    u32x2 pk = {__float_as_uint(sm[0]), __float_as_uint(sm[1])};
    if (pk[0] == ~0u) pk[0] = ~1u;
    asm volatile("buffer_store_dwordx2 %0, %1, %2, %3 offen sc1" :: "v"(pk), "v"(lane * 8), "s"(rss), "s"((rg + (rg & 12)) * 2048) : "memory");
  }
  {
    u32x2 pk[4];
#define PDLY 10
    __builtin_amdgcn_s_sleep(PDLY);
    for (unsigned spins = 0;; ++spins) {
      asm volatile("buffer_load_dwordx2 %0, %4, %5, 0 offen sc1\n\tbuffer_load_dwordx2 %1, %4, %5, %6 offen sc1\n\tbuffer_load_dwordx2 %2, %4, %5, %7 offen sc1\n\t"
                   "buffer_load_dwordx2 %3, %4, %5, %8 offen sc1\n\ts_waitcnt vmcnt(0)"
                   : "=&v"(pk[0]), "=&v"(pk[1]), "=&v"(pk[2]), "=&v"(pk[3]) : "v"(sp), "s"(rss), "s"(16384), "s"(32768), "s"(49152) : "memory");
      if (__all(max(max(pk[0][0], pk[1][0]), max(pk[2][0], pk[3][0])) != ~0u) || spins > (1u << 20)) break;
      __builtin_amdgcn_s_sleep(1);
    }
    float a = 0.f, q = 0.f;
#pragma unroll
    for (int k = 0; k < 4; ++k) { a += __uint_as_float(pk[k][0]); q += __uint_as_float(pk[k][1]); }
    auto r0 = __builtin_amdgcn_permlane16_swap(__float_as_uint(a), __float_as_uint(q), false, false);
    const unsigned c0 = __float_as_uint(__uint_as_float(r0[0]) + __uint_as_float(r0[1]));
    auto r1 = __builtin_amdgcn_permlane32_swap(c0, c0, false, false);
    const unsigned c1 = __float_as_uint(__uint_as_float(r1[0]) + __uint_as_float(r1[1]));
    auto r2 = __builtin_amdgcn_permlane16_swap(c1, c1, false, false);
    const float mean = __uint_as_float(r2[0]) * (1.0f / (float)L_);
    const float var = fmaxf(__builtin_fmaf(-mean, mean, __uint_as_float(r2[1]) * (1.0f / (float)L_)), 0.f);
    const float gg = gam * __builtin_amdgcn_rsqf(var + eps);
    *(__attribute__((address_space(3))) float*)(uintptr_t)aw_ = g & 1 ? __builtin_fmaf(-mean, gg, bet) : gg;
    asm volatile("s_waitcnt lgkmcnt(0)" ::: "memory");
    __builtin_amdgcn_s_barrier();
  }
  {
    const u32x4 ro = make_rsrc(out + (size_t)b * L_ * D_, lend * (D_ * 2));
    f32x4 ga[2], be[2];
#pragma unroll
    for (int s = 0; s < 2; ++s) {
      typedef __attribute__((address_space(3))) f32x4 lds_f32x4;
      ga[s] = *(lds_f32x4*)(uintptr_t)(ar_ + 16 * s);
      be[s] = *(lds_f32x4*)(uintptr_t)(ar_ + 256 + 16 * s);
    }
    f32x4 kk1 = {GA, GA, GA, GA}, kk2 = {GL, GL, GL, GL}, kk3 = {GC, GC, GC, GC};
    asm volatile("" : "+v"(kk1), "+v"(kk2), "+v"(kk3));
#pragma unroll
    for (int n = 0; n < 16; ++n) {
      const int i = n / 3, tt = n % 3;
      f32x4 o[2];
#pragma unroll
      for (int s = 0; s < 2; ++s) {
        const f32x4 v = (n < 15 ? y[(6 * i + 3 * s + tt) % 30] : ye[s]) * ga[s] + be[s];
        const f32x4 a = kk2 - v * (v * v + kk1);
        f32x4 e;
#pragma unroll
        for (int r = 0; r < 4; ++r) e[r] = __builtin_amdgcn_exp2f(a[r]);
        e += kk3;
#pragma unroll
        for (int r = 0; r < 4; ++r) e[r] = __builtin_amdgcn_rcpf(e[r]);
        o[s] = v * e;
      }
      asm volatile("buffer_store_dwordx4 %0, %1, %2, %3 offen sc1 nt\n\ts_nop 1"
                   :: "v"(pack8(o[0], o[1])), "v"(n < 15 ? oo : oe), "s"(ro), "s"(n < 15 ? (2 * HOP * i + 16 * tt) * (D_ * 2) : 0) : "memory");
      SB0;
    }
  }

  asm volatile(".Lcf_b:\n\ts_nop 0" ::: "memory");
}

extern "C" void kernel_launch(void* const* d_in, const int* in_sizes, int n_in,
                              void* d_out, int out_size, void* d_ws, size_t ws_size,
                              hipStream_t stream) {
  const unsigned short* x = (const unsigned short*)d_in[0];
  const unsigned short* w = (const unsigned short*)d_in[1];
  const unsigned short* gamma = (const unsigned short*)d_in[2];
  const unsigned short* beta = (const unsigned short*)d_in[3];
  char* ws = (char*)(((uintptr_t)d_ws + (2u << 20) - 1) & ~(uintptr_t)((2u << 20) - 1));
  conv_fft<<<256, 256, 0, stream>>>(ws, x, w, gamma, beta, (unsigned short*)d_out);
}
```
